```python
import torch

def _softmax(x, axis=-1):
    return torch.softmax(x, dim=axis)
def _gelu(x):
    return torch.nn.functional.gelu(x, approximate="tanh")
def _silu(x):
    return torch.nn.functional.silu(x)
def _sigmoid(x):
    return torch.sigmoid(x)
def _layernorm(x, axis=-1, eps=1e-5):
    m = x.mean(dim=axis, keepdim=True)
    v = x.var(dim=axis, keepdim=True, unbiased=False)
    return (x - m) / torch.sqrt(v + eps)
def _scatter_add(tgt, idx, src, axis=0):
    return tgt.clone().index_add_(axis, idx, src)
def _segment_sum(src, seg, n):
    out = torch.zeros((n,) + src.shape[1:], dtype=src.dtype)
    return out.index_add_(0, seg, src)
def _pad(x, pw):
    flat = tuple(v for pair in reversed(pw) for v in pair)
    return torch.nn.functional.pad(x, flat)

kernel_name = 'concat_heads__gqa_block_cached__gqa_block_cached__near_ridge'

def setup_inputs():

    _w = (lambda a: (lambda t: t.to(torch.bfloat16) if t.is_floating_point() else t)(torch.as_tensor(a)))
    def _A_setup():
        B, Sq, D = 8, 256, 640
        Hq, Hkv, Sk, Dh = 32, 32, 1024, 128
        assert Hq % Hkv == 0, (Hq, Hkv)
        return {
            "x": _w((lambda *s: torch.randn(*s, dtype=torch.bfloat16) * 0.8)(B, Sq, D)),
            "k_cache": _w((lambda *s: torch.randn(*s, dtype=torch.bfloat16) * 0.8)(B, Hkv, Sk, Dh)),
            "v_cache": _w((lambda *s: torch.randn(*s, dtype=torch.bfloat16) * 0.05)(B, Hkv, Sk, Dh)),
            "wq": _w((lambda *s: torch.randn(*s, dtype=torch.bfloat16) * 0.8)(D, Hq * Dh) * (D ** -0.5)),
            "wo": _w((lambda *s: torch.randn(*s, dtype=torch.bfloat16) * 0.05)(Hq * Dh, D) * ((Hq * Dh) ** -0.5)),
        }
    _A_in = _A_setup()

    def _B_setup():
        B, Sq, D = 8, 256, 640
        Hq, Hkv, Sk, Dh = 32, 32, 1024, 128
        assert Hq % Hkv == 0, (Hq, Hkv)
        return {
            "x": _w((lambda *s: torch.randn(*s, dtype=torch.bfloat16) * 0.8)(B, Sq, D)),
            "k_cache": _w((lambda *s: torch.randn(*s, dtype=torch.bfloat16) * 0.8)(B, Hkv, Sk, Dh)),
            "v_cache": _w((lambda *s: torch.randn(*s, dtype=torch.bfloat16) * 0.05)(B, Hkv, Sk, Dh)),
            "wq": _w((lambda *s: torch.randn(*s, dtype=torch.bfloat16) * 0.8)(D, Hq * Dh) * (D ** -0.5)),
            "wo": _w((lambda *s: torch.randn(*s, dtype=torch.bfloat16) * 0.05)(Hq * Dh, D) * ((Hq * Dh) ** -0.5)),
        }
    _B_in = _B_setup()

    return {
        "A_x": _A_in["x"],
        "A_k_cache": _A_in["k_cache"],
        "A_v_cache": _A_in["v_cache"],
        "A_wq": _A_in["wq"],
        "A_wo": _A_in["wo"],
        "B_x": _B_in["x"],
        "B_k_cache": _B_in["k_cache"],
        "B_v_cache": _B_in["v_cache"],
        "B_wq": _B_in["wq"],
        "B_wo": _B_in["wo"],
    }

def reference(A_x, A_k_cache, A_v_cache, A_wq, A_wo, B_x, B_k_cache, B_v_cache, B_wq, B_wo):
    def _A_ref(x, k_cache, v_cache, wq, wo):
        B, Sq, D = x.shape
        Hq, Dh = 32, 128
        Hkv = k_cache.shape[1]
        rep = Hq // Hkv
        q = (lambda x, *d: x.permute(*d))((x @ wq).reshape(B, Sq, Hq, Dh), 0, 2, 1, 3)
        k = (lambda x, n, axis: torch.repeat_interleave(x, n, dim=axis))(k_cache, rep, axis=1)
        v = (lambda x, n, axis: torch.repeat_interleave(x, n, dim=axis))(v_cache, rep, axis=1)
        scores = torch.einsum("bhqd,bhkd->bhqk", q, k) * (Dh ** -0.5)
        p = _softmax(scores, axis=-1)
        o = torch.einsum("bhqk,bhkd->bhqd", p, v)
        return (lambda x, *d: x.permute(*d))(o, 0, 2, 1, 3).reshape(B, Sq, Hq * Dh) @ wo

    def _B_ref(x, k_cache, v_cache, wq, wo):
        B, Sq, D = x.shape
        Hq, Dh = 32, 128
        Hkv = k_cache.shape[1]
        rep = Hq // Hkv
        q = (lambda x, *d: x.permute(*d))((x @ wq).reshape(B, Sq, Hq, Dh), 0, 2, 1, 3)
        k = (lambda x, n, axis: torch.repeat_interleave(x, n, dim=axis))(k_cache, rep, axis=1)
        v = (lambda x, n, axis: torch.repeat_interleave(x, n, dim=axis))(v_cache, rep, axis=1)
        scores = torch.einsum("bhqd,bhkd->bhqk", q, k) * (Dh ** -0.5)
        p = _softmax(scores, axis=-1)
        o = torch.einsum("bhqk,bhkd->bhqd", p, v)
        return (lambda x, *d: x.permute(*d))(o, 0, 2, 1, 3).reshape(B, Sq, Hq * Dh) @ wo

    a = _A_ref(A_x, A_k_cache, A_v_cache, A_wq, A_wo)
    b = _B_ref(B_x, B_k_cache, B_v_cache, B_wq, B_wo)
    return torch.concatenate([a, b], axis=-1)
```

```cpp
#include <hip/hip_runtime.h>
#include <hip/hip_bf16.h>
typedef unsigned long long u64;
#define CLOB "v0","v1","v2","v3","v4","v5","v6","v7","v8","v9","v10","v11","v12","v13","v14","v15","v16","v17","v18","v19","v20","v21","v22","v23","v24","v25","v26","v27","v28","v29","v30","v31","v32","v33","v34","v35","v36","v37","v38","v39","v40","v41","v42","v43","v44","v45","v46","v47","v48","v49","v50","v51","v52","v53","v54","v55","v56","v57","v58","v59","v60","v61","v62","v63","v64","v65","v66","v67","v68","v69","v70","v71","v72","v73","v74","v75","v76","v77","v78","v79","v80","v81","v82","v83","v84","v85","v86","v87","v88","v89","v90","v91","v92","v93","v94","v95","v96","v97","v98","v99","v100","v101","v102","v103","v104","v105","v106","v107","v108","v109","v110","v111","v112","v113","v114","v115","v116","v117","v118","v119","v120","v121","v122","v123","v124","v125","v126","v127","v128","v129","v130","v131","v132","v133","v134","v135","v136","v137","v138","v139","v140","v141","v142","v143","v144","v145","v146","v147","v148","v149","v150","v151","v152","v153","v154","v155","v156","v157","v158","v159","v160","v161","v162","v163","v164","v165","v166","v167","v168","v169","v170","v171","v172","v173","v174","v175","v176","v177","v178","v179","v180","v181","v182","v183","v184","v185","v186","v187","v188","v189","v190","v191","v192","v193","v194","v195","v196","v197","v198","v199","v200","v201","v202","v203","v204","v205","v206","v207","v208","v209","v210","v211","v212","v213","v214","v215","v216","v217","v218","v219","v220","v221","v222","v223","v224","v225","v226","v227","v228","v229","v230","v231","v232","v233","v234","v235","v236","v237","v238","v239","v240","v241","v242","v243","v244","v245","v246","v247","v248","v249","v250","v251","v252","v253","v254","v255","a0","a1","a2","a3","a4","a5","a6","a7","a8","a9","a10","a11","a12","a13","a14","a15","a16","a17","a18","a19","a20","a21","a22","a23","a24","a25","a26","a27","a28","a29","a30","a31","a32","a33","a34","a35","a36","a37","a38","a39","a40","a41","a42","a43","a44","a45","a46","a47","a48","a49","a50","a51","a52","a53","a54","a55","a56","a57","a58","a59","a60","a61","a62","a63","a64","a65","a66","a67","a68","a69","a70","a71","a72","a73","a74","a75","a76","a77","a78","a79","a80","a81","a82","a83","a84","a85","a86","a87","a88","a89","a90","a91","a92","a93","a94","a95","a96","a97","a98","a99","a100","a101","a102","a103","a104","a105","a106","a107","a108","a109","a110","a111","a112","a113","a114","a115","a116","a117","a118","a119","a120","a121","a122","a123","a124","a125","a126","a127","a128","a129","a130","a131","a132","a133","a134","a135","a136","a137","a138","a139","a140","a141","a142","a143","a144","a145","a146","a147","a148","a149","a150","a151","a152","a153","a154","a155","a156","a157","a158","a159","a160","a161","a162","a163","a164","a165","a166","a167","a168","a169","a170","a171","a172","a173","a174","a175","a176","a177","a178","a179","a180","a181","a182","a183","a184","a185","a186","a187","a188","a189","a190","a191","a192","a193","a194","a195","a196","a197","a198","a199","a200","a201","a202","a203","a204","a205","a206","a207","a208","a209","a210","a211","a212","a213","a214","a215","a216","a217","a218","a219","a220","a221","a222","a223","a224","a225","a226","a227","a228","a229","a230","a231","a232","a233","a234","a235","a236","a237","a238","a239","a240","a241","a242","a243","a244","a245","a246","a247","a248","a249","a250","a251","a252","a253","a254","a255","s40","s41","s42","s43","s44","s45","s46","s47","s48","s49","s50","s51","s52","s53","s54","s55","s56","s57","s58","s59","s60","s61","s62","s63","s64","s65","s66","s67","s68","s69","s70","s71","s72","s73","s74","s75","s76","s77","s78","s79","s80","s81","s82","s83","s84","s85","s86","s87","s88","s89","s90","s91","s92","s93","s94","s95","vcc","scc","m0","memory"

extern "C" __global__ __launch_bounds__(256, 1) void k_attn(const char* xA, const char* kA, const char* vA, const char* wqA, const char* woA,
                                                            const char* xB, const char* kB, const char* vB, const char* wqB, const char* woB,
                                                            char* ows, char* wws, char* out, char* cnt) {
  const unsigned bid = blockIdx.x, xcd = bid & 7u, idx = bid >> 3;
  const unsigned br = xcd >> 2, b = 4u * (xcd & 1u) + (idx & 3u), hp = 8u * ((xcd >> 1) & 1u) + (idx >> 2), kk = b * 16u + hp;
  const unsigned wave = __builtin_amdgcn_readfirstlane(threadIdx.x >> 6);
  const u64 kvo = (u64)(b * 32u + 2u * hp) * 262144u;
  const u64 pk = (u64)(br ? kB : kA) + kvo, pv = (u64)(br ? vB : vA) + kvo;
  const u64 px = (u64)(br ? xB : xA) + (u64)b * 327680u, pw = (u64)(br ? wqB : wqA) + hp * 512u;
  const u64 po = (u64)ows + (u64)(br * 8u + b) * 2097152u + hp * 65536u;
  const u64 ps = (u64)(br ? woB : woA) + kk * 40960u, pd = (u64)wws + (u64)br * 5242880u + kk * 5120u;
  const unsigned rb = 4u * xcd + (idx >> 3), t = idx & 7u;
  const u64 qo = (u64)ows + (u64)rb * 1048576u, qw = (u64)wws + (u64)(br * 8u + t) * 655360u;
  const u64 pc = (u64)out + (u64)(rb & 15u) * (128u * 2560u) + br * 1280u + t * 160u;
  const unsigned lid = (xcd & 3u) * 32u + idx;
  const u64 pn = (u64)cnt + br * 512u, pm = pn + lid * 4u, rows = (u64)cnt + 4096u + br * 131072u;
  const u64 pr = rows + lid * 1024u, p0 = rows + lid * 4u, p1 = rows + 512u + hp * 4u, m1 = 0xffull << (8u * b);
  asm volatile(
      "s_mov_b64 s[48:49], %3\n"
      "s_and_b32 s49, s49, 0xffff\n"
      "s_mov_b32 s50, 327680\n"
      "s_mov_b32 s51, 0x00020000\n"
      "s_mov_b64 s[52:53], %4\n"
      "s_and_b32 s53, s53, 0xffff\n"
      "s_mov_b32 s54, 5235200\n"
      "s_mov_b32 s55, 0x00020000\n"
      "s_mov_b32 s80, %0\n"
      "s_lshl_b32 s78, s80, 12\n"
      "s_lshl_b32 s79, s80, 13\n"
      "v_mbcnt_lo_u32_b32 v255, -1, 0\n"
      "v_mbcnt_hi_u32_b32 v255, -1, v255\n"
      "v_lshrrev_b32 v0, 4, v255\n"
      "v_and_b32 v1, 15, v255\n"
      "v_lshrrev_b32 v2, 3, v255\n"
      "v_and_b32 v3, 7, v255\n"
      "v_lshlrev_b32 v4, 2, v0\n"
      "v_xor_b32 v4, v4, v1\n"
      "s_lshr_b32 s81, s80, 1\n"
      "s_lshl_b32 s81, s81, 8\n"
      "s_and_b32 s82, s80, 1\n"
      "s_lshl_b32 s82, s82, 17\n"
      "s_add_u32 s81, s81, s82\n"
      "s_mul_i32 s82, s80, 81920\n"
      "s_movk_i32 s83, 1280\n"
      "v_mul_u32_u24 v7, s83, v2\n"
      "v_add_u32 v7, s82, v7\n"
      "v_lshlrev_b32 v5, 4, v4\n"
      "v_lshl_add_u32 v6, v0, 13, v5\n"
      "v_add_u32 v220, s81, v6\n"
      "v_add_u32 v5, 0, v0\n"
      "v_xor_b32 v5, v5, v3\n"
      "v_lshl_add_u32 v8, v5, 4, v7\n"
      "v_add_u32 v5, 4, v0\n"
      "v_xor_b32 v5, v5, v3\n"
      "v_lshl_add_u32 v9, v5, 4, v7\n"
      "v_add_u32 v224, 0, v8\n"
      "v_add_u32 v225, 9216, v9\n"
      "v_add_u32 v226, 18432, v8\n"
      "v_add_u32 v227, 27648, v9\n"
      "v_add_u32 v221, 31744, v220\n"
      "v_add_u32 v222, 63488, v220\n"
      "v_add_u32 v223, 95232, v220\n"
      "s_add_u32 m0, s79, 131072\n"
      "s_mov_b32 s83, 0\n"
      "buffer_load_dwordx4 v224, s[48:51], s83 offen offset:0 nt lds\n"
      "buffer_load_dwordx4 v225, s[48:51], s83 offen offset:1024 nt lds\n"
      "buffer_load_dwordx4 v226, s[48:51], s83 offen offset:2048 nt lds\n"
      "buffer_load_dwordx4 v227, s[48:51], s83 offen offset:3072 nt lds\n"
      "s_add_u32 m0, s79, 135168\n"
      "s_mov_b32 s83, 40960\n"
      "buffer_load_dwordx4 v224, s[48:51], s83 offen offset:0 nt lds\n"
      "buffer_load_dwordx4 v225, s[48:51], s83 offen offset:1024 nt lds\n"
      "buffer_load_dwordx4 v226, s[48:51], s83 offen offset:2048 nt lds\n"
      "buffer_load_dwordx4 v227, s[48:51], s83 offen offset:3072 nt lds\n"
      "s_add_u32 m0, s78, 65536\n"
      "s_mov_b32 s83, 0\n"
      "buffer_load_dwordx4 v220, s[52:55], s83 offen offset:0 nt lds\n"
      "buffer_load_dwordx4 v221, s[52:55], s83 offen offset:1024 nt lds\n"
      "buffer_load_dwordx4 v222, s[52:55], s83 offen offset:2048 nt lds\n"
      "buffer_load_dwordx4 v223, s[52:55], s83 offen offset:3072 nt lds\n"
      "s_add_u32 m0, s78, 81920\n"
      "s_mov_b32 s83, 262144\n"
      "buffer_load_dwordx4 v220, s[52:55], s83 offen offset:0 nt lds\n"
      "buffer_load_dwordx4 v221, s[52:55], s83 offen offset:1024 nt lds\n"
      "buffer_load_dwordx4 v222, s[52:55], s83 offen offset:2048 nt lds\n"
      "buffer_load_dwordx4 v223, s[52:55], s83 offen offset:3072 nt lds\n"
      "s_mov_b64 s[92:93], %12\n"
      "s_bfe_u32 s92, s92, 0x50002\n"
      "s_lshl_b32 s92, s92, 2\n"
      "s_add_u32 s92, s92, s80\n"
      "s_lshl_b32 s92, s92, 10\n"
      "s_min_u32 s92, s92, END_%=-PC0_%=-1024\n"
      "s_getpc_b64 s[94:95]\n"
      "PC0_%=:\n"
      "v_lshl_add_u32 v140, v255, 4, s92\n"
      "global_load_dwordx4 v[136:139], v140, s[94:95]\n"
      "s_mov_b64 s[40:41], %1\n"
      "s_and_b32 s41, s41, 0xffff\n"
      "s_mov_b32 s42, 524288\n"
      "s_mov_b32 s43, 0x00020000\n"
      "s_mov_b64 s[44:45], %2\n"
      "s_and_b32 s45, s45, 0xffff\n"
      "s_mov_b32 s46, 524288\n"
      "s_mov_b32 s47, 0x00020000\n"
      "s_mov_b64 s[56:57], %5\n"
      "s_and_b32 s57, s57, 0xffff\n"
      "s_mov_b32 s58, 2097152\n"
      "s_mov_b32 s59, 0x00020000\n"
      "s_mov_b64 s[60:61], %6\n"
      "s_and_b32 s61, s61, 0xffff\n"
      "s_mov_b32 s62, 40960\n"
      "s_mov_b32 s63, 0x00020000\n"
      "s_mov_b64 s[64:65], %7\n"
      "s_and_b32 s65, s65, 0xffff\n"
      "s_mov_b32 s66, 5242880\n"
      "s_mov_b32 s67, 0x00020000\n"
      "s_mov_b32 s72, 0x3e0293ee\n"
      "s_mov_b32 s73, 0x3e0293ee\n"
      "s_mov_b32 s74, 0x47800000\n"
      "s_mov_b32 s71, 0\n"
      "v_add_u32 v5, 0, v0\n"
      "v_xor_b32 v6, v5, v1\n"
      "v_lshlrev_b32 v6, 4, v6\n"
      "v_lshl_add_u32 v6, v0, 8, v6\n"
      "v_add_u32 v196, s78, v6\n"
      "v_lshlrev_b32 v5, 1, v5\n"
      "v_xor_b32 v6, v5, v1\n"
      "v_lshlrev_b32 v6, 4, v6\n"
      "v_lshl_add_u32 v6, v0, 8, v6\n"
      "v_add_u32 v208, s78, v6\n"
      "v_add_u32 v5, 4, v0\n"
      "v_xor_b32 v6, v5, v1\n"
      "v_lshlrev_b32 v6, 4, v6\n"
      "v_lshl_add_u32 v6, v0, 8, v6\n"
      "v_add_u32 v197, s78, v6\n"
      "v_lshlrev_b32 v5, 1, v5\n"
      "v_xor_b32 v6, v5, v1\n"
      "v_lshlrev_b32 v6, 4, v6\n"
      "v_lshl_add_u32 v6, v0, 8, v6\n"
      "v_add_u32 v209, s78, v6\n"
      "v_add_u32 v5, 8, v0\n"
      "v_xor_b32 v6, v5, v1\n"
      "v_lshlrev_b32 v6, 4, v6\n"
      "v_lshl_add_u32 v6, v0, 8, v6\n"
      "v_add_u32 v198, s78, v6\n"
      "v_add_u32 v5, 12, v0\n"
      "v_xor_b32 v6, v5, v1\n"
      "v_lshlrev_b32 v6, 4, v6\n"
      "v_lshl_add_u32 v6, v0, 8, v6\n"
      "v_add_u32 v199, s78, v6\n"
      "v_bfe_u32 v4, v255, 2, 2\n"
      "v_and_b32 v5, 3, v255\n"
      "v_xor_b32 v7, v0, v1\n"
      "v_lshrrev_b32 v8, 1, v1\n"
      "v_xor_b32 v8, v8, v0\n"
      "v_lshlrev_b32 v9, 7, v1\n"
      "v_add_u32 v9, s79, v9\n"
      "v_lshlrev_b32 v252, 4, v255\n"
      "v_lshl_add_u32 v11, v0, 2, v4\n"
      "v_and_b32 v12, 7, v11\n"
      "v_lshlrev_b32 v12, 1, v12\n"
      "v_lshrrev_b32 v13, 1, v5\n"
      "v_or_b32 v12, v12, v13\n"
      "v_and_b32 v13, 1, v5\n"
      "v_lshlrev_b32 v13, 3, v13\n"
      "v_lshl_add_u32 v13, v11, 8, v13\n"
      "v_add_u32 v13, 65536, v13\n"
      "v_lshl_add_u32 v14, v0, 3, v4\n"
      "v_lshlrev_b32 v14, 8, v14\n"
      "v_add_u32 v14, 32768, v14\n"
      "v_lshl_or_b32 v15, v4, 2, v5\n"
      "v_xor_b32 v10, 0, v7\n"
      "v_lshlrev_b32 v10, 4, v10\n"
      "v_lshl_add_u32 v192, v1, 8, v10\n"
      "v_xor_b32 v10, 0, v15\n"
      "v_lshl_add_u32 v212, v10, 4, v14\n"
      "v_xor_b32 v10, 4, v7\n"
      "v_lshlrev_b32 v10, 4, v10\n"
      "v_lshl_add_u32 v193, v1, 8, v10\n"
      "v_xor_b32 v10, 4, v15\n"
      "v_lshl_add_u32 v213, v10, 4, v14\n"
      "v_xor_b32 v10, 8, v7\n"
      "v_lshlrev_b32 v10, 4, v10\n"
      "v_lshl_add_u32 v194, v1, 8, v10\n"
      "v_xor_b32 v10, 8, v15\n"
      "v_lshl_add_u32 v214, v10, 4, v14\n"
      "v_xor_b32 v10, 12, v7\n"
      "v_lshlrev_b32 v10, 4, v10\n"
      "v_lshl_add_u32 v195, v1, 8, v10\n"
      "v_xor_b32 v10, 12, v15\n"
      "v_lshl_add_u32 v215, v10, 4, v14\n"
      "v_xor_b32 v10, 0, v8\n"
      "v_lshl_add_u32 v216, v10, 4, v9\n"
      "v_add_u32 v218, 98304, v216\n"
      "v_xor_b32 v10, 4, v8\n"
      "v_lshl_add_u32 v217, v10, 4, v9\n"
      "v_add_u32 v219, 98304, v217\n"
      "v_xor_b32 v10, 0, v12\n"
      "v_lshl_add_u32 v200, v10, 4, v13\n"
      "v_xor_b32 v10, 2, v12\n"
      "v_lshl_add_u32 v201, v10, 4, v13\n"
      "v_xor_b32 v10, 4, v12\n"
      "v_lshl_add_u32 v202, v10, 4, v13\n"
      "v_xor_b32 v10, 6, v12\n"
      "v_lshl_add_u32 v203, v10, 4, v13\n"
      "v_xor_b32 v10, 8, v12\n"
      "v_lshl_add_u32 v204, v10, 4, v13\n"
      "v_xor_b32 v10, 10, v12\n"
      "v_lshl_add_u32 v205, v10, 4, v13\n"
      "v_xor_b32 v10, 12, v12\n"
      "v_lshl_add_u32 v206, v10, 4, v13\n"
      "v_xor_b32 v10, 14, v12\n"
      "v_lshl_add_u32 v207, v10, 4, v13\n"
      "s_lshr_b32 s81, s80, 1\n"
      "s_lshl_b32 s81, s81, 20\n"
      "s_and_b32 s82, s80, 1\n"
      "s_lshl_b32 s82, s82, 12\n"
      "s_add_u32 s81, s81, s82\n"
      "v_add_u32 v254, s81, v252\n"
      "v_mul_u32_u24 v10, 160, v11\n"
      "v_lshl_add_u32 v10, v5, 3, v10\n"
      "v_add_u32 v10, s79, v10\n"
      "v_add_u32 v253, 131072, v10\n"
      "v_cmp_eq_u32 vcc, 0, v5\n"
      "v_mov_b32 v10, 0x3f803f80\n"
      "s_mul_i32 s85, s80, 160\n"
      "s_mul_i32 s86, s80, 655360\n"
      "s_mov_b32 s91, 0\n"
      "s_mov_b32 s75, 0\n"
      "v_cndmask_b32 v228, 0, v10, vcc\n"
      "v_cndmask_b32 v229, 0, v10, vcc\n"
      "v_cndmask_b32 v230, 0, v10, vcc\n"
      "v_cndmask_b32 v231, 0, v10, vcc\n"
      "s_waitcnt vmcnt(5)\n"
      "s_barrier\n"
      "ds_read_b128 a[192:195], v218 offset:32768\n"
      "ds_read_b128 a[196:199], v218 offset:34816\n"
      "ds_read_b128 a[200:203], v218 offset:36864\n"
      "ds_read_b128 a[204:207], v218 offset:38912\n"
      "ds_read_b64_tr_b16 a[128:129], v212 offset:32768\n"
      "ds_read_b64_tr_b16 a[130:131], v212 offset:33792\n"
      "ds_read_b64_tr_b16 a[132:133], v212 offset:32776\n"
      "ds_read_b64_tr_b16 a[134:135], v212 offset:33800\n"
      "ds_read_b64_tr_b16 a[136:137], v213 offset:32768\n"
      "ds_read_b64_tr_b16 a[138:139], v213 offset:33792\n"
      "ds_read_b64_tr_b16 a[140:141], v213 offset:32776\n"
      "ds_read_b64_tr_b16 a[142:143], v213 offset:33800\n"
      "ds_read_b64_tr_b16 a[144:145], v214 offset:32768\n"
      "ds_read_b64_tr_b16 a[146:147], v214 offset:33792\n"
      "ds_read_b64_tr_b16 a[148:149], v214 offset:32776\n"
      "ds_read_b64_tr_b16 a[150:151], v214 offset:33800\n"
      "ds_read_b64_tr_b16 a[152:153], v215 offset:32768\n"
      "ds_read_b64_tr_b16 a[154:155], v215 offset:33792\n"
      "ds_read_b64_tr_b16 a[156:157], v215 offset:32776\n"
      "ds_read_b64_tr_b16 a[158:159], v215 offset:33800\n"
      "s_waitcnt lgkmcnt(0)\n"
      "v_mfma_f32_16x16x32_bf16 v[0:3], a[128:131], a[192:195], 0\n"
      "ds_read_b64_tr_b16 a[160:161], v212 offset:40960\n"
      "ds_read_b64_tr_b16 a[162:163], v212 offset:41984\n"
      "v_mfma_f32_16x16x32_bf16 v[32:35], a[128:131], a[196:199], 0\n"
      "ds_read_b64_tr_b16 a[164:165], v212 offset:40968\n"
      "ds_read_b64_tr_b16 a[166:167], v212 offset:41992\n"
      "v_mfma_f32_16x16x32_bf16 v[64:67], a[128:131], a[200:203], 0\n"
      "s_add_u32 m0, s79, 0\n"
      "s_mov_b32 s83, 128\n"
      "v_mfma_f32_16x16x32_bf16 v[96:99], a[128:131], a[204:207], 0\n"
      "ds_read_b64_tr_b16 a[168:169], v213 offset:40960\n"
      "ds_read_b64_tr_b16 a[170:171], v213 offset:41984\n"
      "v_mfma_f32_16x16x32_bf16 v[4:7], a[132:135], a[192:195], 0\n"
      "ds_read_b64_tr_b16 a[172:173], v213 offset:40968\n"
      "ds_read_b64_tr_b16 a[174:175], v213 offset:41992\n"
      "v_mfma_f32_16x16x32_bf16 v[36:39], a[132:135], a[196:199], 0\n"
      "buffer_load_dwordx4 v224, s[48:51], s83 offen offset:0 nt lds\n"
      "v_mfma_f32_16x16x32_bf16 v[68:71], a[132:135], a[200:203], 0\n"
      "ds_read_b64_tr_b16 a[176:177], v214 offset:40960\n"
      "v_mfma_f32_16x16x32_bf16 v[100:103], a[132:135], a[204:207], 0\n"
      "ds_read_b64_tr_b16 a[178:179], v214 offset:41984\n"
      "v_mfma_f32_16x16x32_bf16 v[8:11], a[136:139], a[192:195], 0\n"
      "buffer_load_dwordx4 v225, s[48:51], s83 offen offset:1024 nt lds\n"
      "v_mfma_f32_16x16x32_bf16 v[40:43], a[136:139], a[196:199], 0\n"
      "ds_read_b64_tr_b16 a[180:181], v214 offset:40968\n"
      "v_mfma_f32_16x16x32_bf16 v[72:75], a[136:139], a[200:203], 0\n"
      "ds_read_b64_tr_b16 a[182:183], v214 offset:41992\n"
      "v_mfma_f32_16x16x32_bf16 v[104:107], a[136:139], a[204:207], 0\n"
      "buffer_load_dwordx4 v226, s[48:51], s83 offen offset:2048 nt lds\n"
      "v_mfma_f32_16x16x32_bf16 v[12:15], a[140:143], a[192:195], 0\n"
      "ds_read_b64_tr_b16 a[184:185], v215 offset:40960\n"
      "v_mfma_f32_16x16x32_bf16 v[44:47], a[140:143], a[196:199], 0\n"
      "ds_read_b64_tr_b16 a[186:187], v215 offset:41984\n"
      "v_mfma_f32_16x16x32_bf16 v[76:79], a[140:143], a[200:203], 0\n"
      "buffer_load_dwordx4 v227, s[48:51], s83 offen offset:3072 nt lds\n"
      "v_mfma_f32_16x16x32_bf16 v[108:111], a[140:143], a[204:207], 0\n"
      "ds_read_b64_tr_b16 a[188:189], v215 offset:40968\n"
      "v_mfma_f32_16x16x32_bf16 v[16:19], a[144:147], a[192:195], 0\n"
      "ds_read_b64_tr_b16 a[190:191], v215 offset:41992\n"
      "v_mfma_f32_16x16x32_bf16 v[48:51], a[144:147], a[196:199], 0\n"
      "s_add_u32 m0, s79, 4096\n"
      "s_mov_b32 s83, 41088\n"
      "v_mfma_f32_16x16x32_bf16 v[80:83], a[144:147], a[200:203], 0\n"
      "v_mfma_f32_16x16x32_bf16 v[112:115], a[144:147], a[204:207], 0\n"
      "v_mfma_f32_16x16x32_bf16 v[20:23], a[148:151], a[192:195], 0\n"
      "buffer_load_dwordx4 v224, s[48:51], s83 offen offset:0 nt lds\n"
      "v_mfma_f32_16x16x32_bf16 v[52:55], a[148:151], a[196:199], 0\n"
      "v_mfma_f32_16x16x32_bf16 v[84:87], a[148:151], a[200:203], 0\n"
      "v_mfma_f32_16x16x32_bf16 v[116:119], a[148:151], a[204:207], 0\n"
      "buffer_load_dwordx4 v225, s[48:51], s83 offen offset:1024 nt lds\n"
      "v_mfma_f32_16x16x32_bf16 v[24:27], a[152:155], a[192:195], 0\n"
      "v_mfma_f32_16x16x32_bf16 v[56:59], a[152:155], a[196:199], 0\n"
      "v_mfma_f32_16x16x32_bf16 v[88:91], a[152:155], a[200:203], 0\n"
      "buffer_load_dwordx4 v226, s[48:51], s83 offen offset:2048 nt lds\n"
      "v_mfma_f32_16x16x32_bf16 v[120:123], a[152:155], a[204:207], 0\n"
      "v_mfma_f32_16x16x32_bf16 v[28:31], a[156:159], a[192:195], 0\n"
      "v_mfma_f32_16x16x32_bf16 v[60:63], a[156:159], a[196:199], 0\n"
      "buffer_load_dwordx4 v227, s[48:51], s83 offen offset:3072 nt lds\n"
      "v_mfma_f32_16x16x32_bf16 v[92:95], a[156:159], a[200:203], 0\n"
      "v_mfma_f32_16x16x32_bf16 v[124:127], a[156:159], a[204:207], 0\n"
      "s_waitcnt vmcnt(9) lgkmcnt(0)\n"
      "s_barrier\n"
      "v_mfma_f32_16x16x32_bf16 a[0:3], a[160:163], a[192:195], 0\n"
      "ds_read_b128 a[208:211], v219 offset:32768\n"
      "ds_read_b128 a[212:215], v219 offset:34816\n"
      "v_mfma_f32_16x16x32_bf16 a[32:35], a[160:163], a[196:199], 0\n"
      "ds_read_b128 a[216:219], v219 offset:36864\n"
      "ds_read_b128 a[220:223], v219 offset:38912\n"
      "v_mfma_f32_16x16x32_bf16 a[64:67], a[160:163], a[200:203], 0\n"
      "s_add_u32 m0, s78, 32768\n"
      "s_mov_b32 s83, 524288\n"
      "v_mfma_f32_16x16x32_bf16 a[96:99], a[160:163], a[204:207], 0\n"
      "ds_read_b64_tr_b16 a[128:129], v212 offset:49152\n"
      "ds_read_b64_tr_b16 a[130:131], v212 offset:50176\n"
      "v_mfma_f32_16x16x32_bf16 a[4:7], a[164:167], a[192:195], 0\n"
      "ds_read_b64_tr_b16 a[132:133], v212 offset:49160\n"
      "ds_read_b64_tr_b16 a[134:135], v212 offset:50184\n"
      "v_mfma_f32_16x16x32_bf16 a[36:39], a[164:167], a[196:199], 0\n"
      "buffer_load_dwordx4 v220, s[52:55], s83 offen offset:0 nt lds\n"
      "v_mfma_f32_16x16x32_bf16 a[68:71], a[164:167], a[200:203], 0\n"
      "ds_read_b64_tr_b16 a[136:137], v213 offset:49152\n"
      "v_mfma_f32_16x16x32_bf16 a[100:103], a[164:167], a[204:207], 0\n"
      "ds_read_b64_tr_b16 a[138:139], v213 offset:50176\n"
      "v_mfma_f32_16x16x32_bf16 a[8:11], a[168:171], a[192:195], 0\n"
      "buffer_load_dwordx4 v221, s[52:55], s83 offen offset:1024 nt lds\n"
      "v_mfma_f32_16x16x32_bf16 a[40:43], a[168:171], a[196:199], 0\n"
      "ds_read_b64_tr_b16 a[140:141], v213 offset:49160\n"
      "v_mfma_f32_16x16x32_bf16 a[72:75], a[168:171], a[200:203], 0\n"
      "ds_read_b64_tr_b16 a[142:143], v213 offset:50184\n"
      "v_mfma_f32_16x16x32_bf16 a[104:107], a[168:171], a[204:207], 0\n"
      "buffer_load_dwordx4 v222, s[52:55], s83 offen offset:2048 nt lds\n"
      "v_mfma_f32_16x16x32_bf16 a[12:15], a[172:175], a[192:195], 0\n"
      "ds_read_b64_tr_b16 a[144:145], v214 offset:49152\n"
      "v_mfma_f32_16x16x32_bf16 a[44:47], a[172:175], a[196:199], 0\n"
      "ds_read_b64_tr_b16 a[146:147], v214 offset:50176\n"
      "v_mfma_f32_16x16x32_bf16 a[76:79], a[172:175], a[200:203], 0\n"
      "buffer_load_dwordx4 v223, s[52:55], s83 offen offset:3072 nt lds\n"
      "v_mfma_f32_16x16x32_bf16 a[108:111], a[172:175], a[204:207], 0\n"
      "ds_read_b64_tr_b16 a[148:149], v214 offset:49160\n"
      "v_mfma_f32_16x16x32_bf16 a[16:19], a[176:179], a[192:195], 0\n"
      "ds_read_b64_tr_b16 a[150:151], v214 offset:50184\n"
      "v_mfma_f32_16x16x32_bf16 a[48:51], a[176:179], a[196:199], 0\n"
      "s_add_u32 m0, s78, 49152\n"
      "s_mov_b32 s83, 786432\n"
      "v_mfma_f32_16x16x32_bf16 a[80:83], a[176:179], a[200:203], 0\n"
      "ds_read_b64_tr_b16 a[152:153], v215 offset:49152\n"
      "v_mfma_f32_16x16x32_bf16 a[112:115], a[176:179], a[204:207], 0\n"
      "ds_read_b64_tr_b16 a[154:155], v215 offset:50176\n"
      "v_mfma_f32_16x16x32_bf16 a[20:23], a[180:183], a[192:195], 0\n"
      "buffer_load_dwordx4 v220, s[52:55], s83 offen offset:0 nt lds\n"
      "v_mfma_f32_16x16x32_bf16 a[52:55], a[180:183], a[196:199], 0\n"
      "ds_read_b64_tr_b16 a[156:157], v215 offset:49160\n"
      "v_mfma_f32_16x16x32_bf16 a[84:87], a[180:183], a[200:203], 0\n"
      "ds_read_b64_tr_b16 a[158:159], v215 offset:50184\n"
      "v_mfma_f32_16x16x32_bf16 a[116:119], a[180:183], a[204:207], 0\n"
      "buffer_load_dwordx4 v221, s[52:55], s83 offen offset:1024 nt lds\n"
      "v_mfma_f32_16x16x32_bf16 a[24:27], a[184:187], a[192:195], 0\n"
      "v_mfma_f32_16x16x32_bf16 a[56:59], a[184:187], a[196:199], 0\n"
      "v_mfma_f32_16x16x32_bf16 a[88:91], a[184:187], a[200:203], 0\n"
      "buffer_load_dwordx4 v222, s[52:55], s83 offen offset:2048 nt lds\n"
      "v_mfma_f32_16x16x32_bf16 a[120:123], a[184:187], a[204:207], 0\n"
      "v_mfma_f32_16x16x32_bf16 a[28:31], a[188:191], a[192:195], 0\n"
      "v_mfma_f32_16x16x32_bf16 a[60:63], a[188:191], a[196:199], 0\n"
      "buffer_load_dwordx4 v223, s[52:55], s83 offen offset:3072 nt lds\n"
      "v_mfma_f32_16x16x32_bf16 a[92:95], a[188:191], a[200:203], 0\n"
      "v_mfma_f32_16x16x32_bf16 a[124:127], a[188:191], a[204:207], 0\n"
      "s_waitcnt lgkmcnt(0)\n"
      "v_mfma_f32_16x16x32_bf16 v[0:3], a[128:131], a[208:211], v[0:3]\n"
      "ds_read_b64_tr_b16 a[160:161], v212 offset:57344\n"
      "ds_read_b64_tr_b16 a[162:163], v212 offset:58368\n"
      "v_mfma_f32_16x16x32_bf16 v[32:35], a[128:131], a[212:215], v[32:35]\n"
      "ds_read_b64_tr_b16 a[164:165], v212 offset:57352\n"
      "ds_read_b64_tr_b16 a[166:167], v212 offset:58376\n"
      "v_mfma_f32_16x16x32_bf16 v[64:67], a[128:131], a[216:219], v[64:67]\n"
      "s_add_u32 m0, s79, 98304\n"
      "s_mov_b32 s83, 256\n"
      "v_mfma_f32_16x16x32_bf16 v[96:99], a[128:131], a[220:223], v[96:99]\n"
      "ds_read_b64_tr_b16 a[168:169], v213 offset:57344\n"
      "ds_read_b64_tr_b16 a[170:171], v213 offset:58368\n"
      "v_mfma_f32_16x16x32_bf16 v[4:7], a[132:135], a[208:211], v[4:7]\n"
      "ds_read_b64_tr_b16 a[172:173], v213 offset:57352\n"
      "ds_read_b64_tr_b16 a[174:175], v213 offset:58376\n"
      "v_mfma_f32_16x16x32_bf16 v[36:39], a[132:135], a[212:215], v[36:39]\n"
      "buffer_load_dwordx4 v224, s[48:51], s83 offen offset:0 nt lds\n"
      "v_mfma_f32_16x16x32_bf16 v[68:71], a[132:135], a[216:219], v[68:71]\n"
      "ds_read_b64_tr_b16 a[176:177], v214 offset:57344\n"
      "v_mfma_f32_16x16x32_bf16 v[100:103], a[132:135], a[220:223], v[100:103]\n"
      "ds_read_b64_tr_b16 a[178:179], v214 offset:58368\n"
      "v_mfma_f32_16x16x32_bf16 v[8:11], a[136:139], a[208:211], v[8:11]\n"
      "buffer_load_dwordx4 v225, s[48:51], s83 offen offset:1024 nt lds\n"
      "v_mfma_f32_16x16x32_bf16 v[40:43], a[136:139], a[212:215], v[40:43]\n"
      "ds_read_b64_tr_b16 a[180:181], v214 offset:57352\n"
      "v_mfma_f32_16x16x32_bf16 v[72:75], a[136:139], a[216:219], v[72:75]\n"
      "ds_read_b64_tr_b16 a[182:183], v214 offset:58376\n"
      "v_mfma_f32_16x16x32_bf16 v[104:107], a[136:139], a[220:223], v[104:107]\n"
      "buffer_load_dwordx4 v226, s[48:51], s83 offen offset:2048 nt lds\n"
      "v_mfma_f32_16x16x32_bf16 v[12:15], a[140:143], a[208:211], v[12:15]\n"
      "ds_read_b64_tr_b16 a[184:185], v215 offset:57344\n"
      "v_mfma_f32_16x16x32_bf16 v[44:47], a[140:143], a[212:215], v[44:47]\n"
      "ds_read_b64_tr_b16 a[186:187], v215 offset:58368\n"
      "v_mfma_f32_16x16x32_bf16 v[76:79], a[140:143], a[216:219], v[76:79]\n"
      "buffer_load_dwordx4 v227, s[48:51], s83 offen offset:3072 nt lds\n"
      "v_mfma_f32_16x16x32_bf16 v[108:111], a[140:143], a[220:223], v[108:111]\n"
      "ds_read_b64_tr_b16 a[188:189], v215 offset:57352\n"
      "v_mfma_f32_16x16x32_bf16 v[16:19], a[144:147], a[208:211], v[16:19]\n"
      "ds_read_b64_tr_b16 a[190:191], v215 offset:58376\n"
      "v_mfma_f32_16x16x32_bf16 v[48:51], a[144:147], a[212:215], v[48:51]\n"
      "s_add_u32 m0, s79, 102400\n"
      "s_mov_b32 s83, 41216\n"
      "v_mfma_f32_16x16x32_bf16 v[80:83], a[144:147], a[216:219], v[80:83]\n"
      "v_mfma_f32_16x16x32_bf16 v[112:115], a[144:147], a[220:223], v[112:115]\n"
      "v_mfma_f32_16x16x32_bf16 v[20:23], a[148:151], a[208:211], v[20:23]\n"
      "buffer_load_dwordx4 v224, s[48:51], s83 offen offset:0 nt lds\n"
      "v_mfma_f32_16x16x32_bf16 v[52:55], a[148:151], a[212:215], v[52:55]\n"
      "v_mfma_f32_16x16x32_bf16 v[84:87], a[148:151], a[216:219], v[84:87]\n"
      "v_mfma_f32_16x16x32_bf16 v[116:119], a[148:151], a[220:223], v[116:119]\n"
      "buffer_load_dwordx4 v225, s[48:51], s83 offen offset:1024 nt lds\n"
      "v_mfma_f32_16x16x32_bf16 v[24:27], a[152:155], a[208:211], v[24:27]\n"
      "v_mfma_f32_16x16x32_bf16 v[56:59], a[152:155], a[212:215], v[56:59]\n"
      "v_mfma_f32_16x16x32_bf16 v[88:91], a[152:155], a[216:219], v[88:91]\n"
      "buffer_load_dwordx4 v226, s[48:51], s83 offen offset:2048 nt lds\n"
      "v_mfma_f32_16x16x32_bf16 v[120:123], a[152:155], a[220:223], v[120:123]\n"
      "v_mfma_f32_16x16x32_bf16 v[28:31], a[156:159], a[208:211], v[28:31]\n"
      "v_mfma_f32_16x16x32_bf16 v[60:63], a[156:159], a[212:215], v[60:63]\n"
      "buffer_load_dwordx4 v227, s[48:51], s83 offen offset:3072 nt lds\n"
      "v_mfma_f32_16x16x32_bf16 v[92:95], a[156:159], a[216:219], v[92:95]\n"
      "v_mfma_f32_16x16x32_bf16 v[124:127], a[156:159], a[220:223], v[124:127]\n"
      "s_waitcnt vmcnt(12) lgkmcnt(0)\n"
      "s_barrier\n"
      "v_mfma_f32_16x16x32_bf16 a[0:3], a[160:163], a[208:211], a[0:3]\n"
      "ds_read_b128 a[192:195], v216 offset:0\n"
      "ds_read_b128 a[196:199], v216 offset:2048\n"
      "v_mfma_f32_16x16x32_bf16 a[32:35], a[160:163], a[212:215], a[32:35]\n"
      "ds_read_b128 a[200:203], v216 offset:4096\n"
      "ds_read_b128 a[204:207], v216 offset:6144\n"
      "v_mfma_f32_16x16x32_bf16 a[64:67], a[160:163], a[216:219], a[64:67]\n"
      "s_add_u32 m0, s78, 65536\n"
      "s_mov_b32 s83, 1048576\n"
      "v_mfma_f32_16x16x32_bf16 a[96:99], a[160:163], a[220:223], a[96:99]\n"
      "ds_read_b64_tr_b16 a[128:129], v212 offset:0\n"
      "ds_read_b64_tr_b16 a[130:131], v212 offset:1024\n"
      "v_mfma_f32_16x16x32_bf16 a[4:7], a[164:167], a[208:211], a[4:7]\n"
      "ds_read_b64_tr_b16 a[132:133], v212 offset:8\n"
      "ds_read_b64_tr_b16 a[134:135], v212 offset:1032\n"
      "v_mfma_f32_16x16x32_bf16 a[36:39], a[164:167], a[212:215], a[36:39]\n"
      "buffer_load_dwordx4 v220, s[52:55], s83 offen offset:0 nt lds\n"
      "v_mfma_f32_16x16x32_bf16 a[68:71], a[164:167], a[216:219], a[68:71]\n"
      "ds_read_b64_tr_b16 a[136:137], v213 offset:0\n"
      "v_mfma_f32_16x16x32_bf16 a[100:103], a[164:167], a[220:223], a[100:103]\n"
      "ds_read_b64_tr_b16 a[138:139], v213 offset:1024\n"
      "v_mfma_f32_16x16x32_bf16 a[8:11], a[168:171], a[208:211], a[8:11]\n"
      "buffer_load_dwordx4 v221, s[52:55], s83 offen offset:1024 nt lds\n"
      "v_mfma_f32_16x16x32_bf16 a[40:43], a[168:171], a[212:215], a[40:43]\n"
      "ds_read_b64_tr_b16 a[140:141], v213 offset:8\n"
      "v_mfma_f32_16x16x32_bf16 a[72:75], a[168:171], a[216:219], a[72:75]\n"
      "ds_read_b64_tr_b16 a[142:143], v213 offset:1032\n"
      "v_mfma_f32_16x16x32_bf16 a[104:107], a[168:171], a[220:223], a[104:107]\n"
      "buffer_load_dwordx4 v222, s[52:55], s83 offen offset:2048 nt lds\n"
      "v_mfma_f32_16x16x32_bf16 a[12:15], a[172:175], a[208:211], a[12:15]\n"
      "ds_read_b64_tr_b16 a[144:145], v214 offset:0\n"
      "v_mfma_f32_16x16x32_bf16 a[44:47], a[172:175], a[212:215], a[44:47]\n"
      "ds_read_b64_tr_b16 a[146:147], v214 offset:1024\n"
      "v_mfma_f32_16x16x32_bf16 a[76:79], a[172:175], a[216:219], a[76:79]\n"
      "buffer_load_dwordx4 v223, s[52:55], s83 offen offset:3072 nt lds\n"
      "v_mfma_f32_16x16x32_bf16 a[108:111], a[172:175], a[220:223], a[108:111]\n"
      "ds_read_b64_tr_b16 a[148:149], v214 offset:8\n"
      "v_mfma_f32_16x16x32_bf16 a[16:19], a[176:179], a[208:211], a[16:19]\n"
      "ds_read_b64_tr_b16 a[150:151], v214 offset:1032\n"
      "v_mfma_f32_16x16x32_bf16 a[48:51], a[176:179], a[212:215], a[48:51]\n"
      "s_add_u32 m0, s79, 131072\n"
      "s_mov_b32 s83, 384\n"
      "v_mfma_f32_16x16x32_bf16 a[80:83], a[176:179], a[216:219], a[80:83]\n"
      "ds_read_b64_tr_b16 a[152:153], v215 offset:0\n"
      "v_mfma_f32_16x16x32_bf16 a[112:115], a[176:179], a[220:223], a[112:115]\n"
      "ds_read_b64_tr_b16 a[154:155], v215 offset:1024\n"
      "v_mfma_f32_16x16x32_bf16 a[20:23], a[180:183], a[208:211], a[20:23]\n"
      "buffer_load_dwordx4 v224, s[48:51], s83 offen offset:0 nt lds\n"
      "v_mfma_f32_16x16x32_bf16 a[52:55], a[180:183], a[212:215], a[52:55]\n"
      "ds_read_b64_tr_b16 a[156:157], v215 offset:8\n"
      "v_mfma_f32_16x16x32_bf16 a[84:87], a[180:183], a[216:219], a[84:87]\n"
      "ds_read_b64_tr_b16 a[158:159], v215 offset:1032\n"
      "v_mfma_f32_16x16x32_bf16 a[116:119], a[180:183], a[220:223], a[116:119]\n"
      "buffer_load_dwordx4 v225, s[48:51], s83 offen offset:1024 nt lds\n"
      "v_mfma_f32_16x16x32_bf16 a[24:27], a[184:187], a[208:211], a[24:27]\n"
      "v_mfma_f32_16x16x32_bf16 a[56:59], a[184:187], a[212:215], a[56:59]\n"
      "v_mfma_f32_16x16x32_bf16 a[88:91], a[184:187], a[216:219], a[88:91]\n"
      "buffer_load_dwordx4 v226, s[48:51], s83 offen offset:2048 nt lds\n"
      "v_mfma_f32_16x16x32_bf16 a[120:123], a[184:187], a[220:223], a[120:123]\n"
      "v_mfma_f32_16x16x32_bf16 a[28:31], a[188:191], a[208:211], a[28:31]\n"
      "v_mfma_f32_16x16x32_bf16 a[60:63], a[188:191], a[212:215], a[60:63]\n"
      "buffer_load_dwordx4 v227, s[48:51], s83 offen offset:3072 nt lds\n"
      "v_mfma_f32_16x16x32_bf16 a[92:95], a[188:191], a[216:219], a[92:95]\n"
      "v_mfma_f32_16x16x32_bf16 a[124:127], a[188:191], a[220:223], a[124:127]\n"
      "s_waitcnt lgkmcnt(0)\n"
      "v_mfma_f32_16x16x32_bf16 v[0:3], a[128:131], a[192:195], v[0:3]\n"
      "ds_read_b64_tr_b16 a[160:161], v212 offset:8192\n"
      "ds_read_b64_tr_b16 a[162:163], v212 offset:9216\n"
      "v_mfma_f32_16x16x32_bf16 v[32:35], a[128:131], a[196:199], v[32:35]\n"
      "ds_read_b64_tr_b16 a[164:165], v212 offset:8200\n"
      "ds_read_b64_tr_b16 a[166:167], v212 offset:9224\n"
      "v_mfma_f32_16x16x32_bf16 v[64:67], a[128:131], a[200:203], v[64:67]\n"
      "s_add_u32 m0, s78, 81920\n"
      "s_mov_b32 s83, 1310720\n"
      "v_mfma_f32_16x16x32_bf16 v[96:99], a[128:131], a[204:207], v[96:99]\n"
      "ds_read_b64_tr_b16 a[168:169], v213 offset:8192\n"
      "ds_read_b64_tr_b16 a[170:171], v213 offset:9216\n"
      "v_mfma_f32_16x16x32_bf16 v[4:7], a[132:135], a[192:195], v[4:7]\n"
      "ds_read_b64_tr_b16 a[172:173], v213 offset:8200\n"
      "ds_read_b64_tr_b16 a[174:175], v213 offset:9224\n"
      "v_mfma_f32_16x16x32_bf16 v[36:39], a[132:135], a[196:199], v[36:39]\n"
      "buffer_load_dwordx4 v220, s[52:55], s83 offen offset:0 nt lds\n"
      "v_mfma_f32_16x16x32_bf16 v[68:71], a[132:135], a[200:203], v[68:71]\n"
      "ds_read_b64_tr_b16 a[176:177], v214 offset:8192\n"
      "v_mfma_f32_16x16x32_bf16 v[100:103], a[132:135], a[204:207], v[100:103]\n"
      "ds_read_b64_tr_b16 a[178:179], v214 offset:9216\n"
      "v_mfma_f32_16x16x32_bf16 v[8:11], a[136:139], a[192:195], v[8:11]\n"
      "buffer_load_dwordx4 v221, s[52:55], s83 offen offset:1024 nt lds\n"
      "v_mfma_f32_16x16x32_bf16 v[40:43], a[136:139], a[196:199], v[40:43]\n"
      "ds_read_b64_tr_b16 a[180:181], v214 offset:8200\n"
      "v_mfma_f32_16x16x32_bf16 v[72:75], a[136:139], a[200:203], v[72:75]\n"
      "ds_read_b64_tr_b16 a[182:183], v214 offset:9224\n"
      "v_mfma_f32_16x16x32_bf16 v[104:107], a[136:139], a[204:207], v[104:107]\n"
      "buffer_load_dwordx4 v222, s[52:55], s83 offen offset:2048 nt lds\n"
      "v_mfma_f32_16x16x32_bf16 v[12:15], a[140:143], a[192:195], v[12:15]\n"
      "ds_read_b64_tr_b16 a[184:185], v215 offset:8192\n"
      "v_mfma_f32_16x16x32_bf16 v[44:47], a[140:143], a[196:199], v[44:47]\n"
      "ds_read_b64_tr_b16 a[186:187], v215 offset:9216\n"
      "v_mfma_f32_16x16x32_bf16 v[76:79], a[140:143], a[200:203], v[76:79]\n"
      "buffer_load_dwordx4 v223, s[52:55], s83 offen offset:3072 nt lds\n"
      "v_mfma_f32_16x16x32_bf16 v[108:111], a[140:143], a[204:207], v[108:111]\n"
      "ds_read_b64_tr_b16 a[188:189], v215 offset:8200\n"
      "v_mfma_f32_16x16x32_bf16 v[16:19], a[144:147], a[192:195], v[16:19]\n"
      "ds_read_b64_tr_b16 a[190:191], v215 offset:9224\n"
      "v_mfma_f32_16x16x32_bf16 v[48:51], a[144:147], a[196:199], v[48:51]\n"
      "v_mfma_f32_16x16x32_bf16 v[80:83], a[144:147], a[200:203], v[80:83]\n"
      "v_mfma_f32_16x16x32_bf16 v[112:115], a[144:147], a[204:207], v[112:115]\n"
      "v_mfma_f32_16x16x32_bf16 v[20:23], a[148:151], a[192:195], v[20:23]\n"
      "v_mfma_f32_16x16x32_bf16 v[52:55], a[148:151], a[196:199], v[52:55]\n"
      "v_mfma_f32_16x16x32_bf16 v[84:87], a[148:151], a[200:203], v[84:87]\n"
      "v_mfma_f32_16x16x32_bf16 v[116:119], a[148:151], a[204:207], v[116:119]\n"
      "v_mfma_f32_16x16x32_bf16 v[24:27], a[152:155], a[192:195], v[24:27]\n"
      "v_mfma_f32_16x16x32_bf16 v[56:59], a[152:155], a[196:199], v[56:59]\n"
      "v_mfma_f32_16x16x32_bf16 v[88:91], a[152:155], a[200:203], v[88:91]\n"
      "v_mfma_f32_16x16x32_bf16 v[120:123], a[152:155], a[204:207], v[120:123]\n"
      "v_mfma_f32_16x16x32_bf16 v[28:31], a[156:159], a[192:195], v[28:31]\n"
      "v_mfma_f32_16x16x32_bf16 v[60:63], a[156:159], a[196:199], v[60:63]\n"
      "v_mfma_f32_16x16x32_bf16 v[92:95], a[156:159], a[200:203], v[92:95]\n"
      "v_mfma_f32_16x16x32_bf16 v[124:127], a[156:159], a[204:207], v[124:127]\n"
      "s_waitcnt vmcnt(20) lgkmcnt(0)\n"
      "s_barrier\n"
      "v_mfma_f32_16x16x32_bf16 a[0:3], a[160:163], a[192:195], a[0:3]\n"
      "ds_read_b128 a[208:211], v217 offset:0\n"
      "ds_read_b128 a[212:215], v217 offset:2048\n"
      "v_mfma_f32_16x16x32_bf16 a[32:35], a[160:163], a[196:199], a[32:35]\n"
      "ds_read_b128 a[216:219], v217 offset:4096\n"
      "ds_read_b128 a[220:223], v217 offset:6144\n"
      "v_mfma_f32_16x16x32_bf16 a[64:67], a[160:163], a[200:203], a[64:67]\n"
      "s_add_u32 m0, s79, 135168\n"
      "s_mov_b32 s83, 41344\n"
      "v_mfma_f32_16x16x32_bf16 a[96:99], a[160:163], a[204:207], a[96:99]\n"
      "ds_read_b64_tr_b16 a[128:129], v212 offset:16384\n"
      "ds_read_b64_tr_b16 a[130:131], v212 offset:17408\n"
      "v_mfma_f32_16x16x32_bf16 a[4:7], a[164:167], a[192:195], a[4:7]\n"
      "ds_read_b64_tr_b16 a[132:133], v212 offset:16392\n"
      "ds_read_b64_tr_b16 a[134:135], v212 offset:17416\n"
      "v_mfma_f32_16x16x32_bf16 a[36:39], a[164:167], a[196:199], a[36:39]\n"
      "buffer_load_dwordx4 v224, s[48:51], s83 offen offset:0 nt lds\n"
      "v_mfma_f32_16x16x32_bf16 a[68:71], a[164:167], a[200:203], a[68:71]\n"
      "ds_read_b64_tr_b16 a[136:137], v213 offset:16384\n"
      "v_mfma_f32_16x16x32_bf16 a[100:103], a[164:167], a[204:207], a[100:103]\n"
      "ds_read_b64_tr_b16 a[138:139], v213 offset:17408\n"
      "v_mfma_f32_16x16x32_bf16 a[8:11], a[168:171], a[192:195], a[8:11]\n"
      "buffer_load_dwordx4 v225, s[48:51], s83 offen offset:1024 nt lds\n"
      "v_mfma_f32_16x16x32_bf16 a[40:43], a[168:171], a[196:199], a[40:43]\n"
      "ds_read_b64_tr_b16 a[140:141], v213 offset:16392\n"
      "v_mfma_f32_16x16x32_bf16 a[72:75], a[168:171], a[200:203], a[72:75]\n"
      "ds_read_b64_tr_b16 a[142:143], v213 offset:17416\n"
      "v_mfma_f32_16x16x32_bf16 a[104:107], a[168:171], a[204:207], a[104:107]\n"
      "buffer_load_dwordx4 v226, s[48:51], s83 offen offset:2048 nt lds\n"
      "v_mfma_f32_16x16x32_bf16 a[12:15], a[172:175], a[192:195], a[12:15]\n"
      "ds_read_b64_tr_b16 a[144:145], v214 offset:16384\n"
      "v_mfma_f32_16x16x32_bf16 a[44:47], a[172:175], a[196:199], a[44:47]\n"
      "ds_read_b64_tr_b16 a[146:147], v214 offset:17408\n"
      "v_mfma_f32_16x16x32_bf16 a[76:79], a[172:175], a[200:203], a[76:79]\n"
      "buffer_load_dwordx4 v227, s[48:51], s83 offen offset:3072 nt lds\n"
      "v_mfma_f32_16x16x32_bf16 a[108:111], a[172:175], a[204:207], a[108:111]\n"
      "ds_read_b64_tr_b16 a[148:149], v214 offset:16392\n"
      "v_mfma_f32_16x16x32_bf16 a[16:19], a[176:179], a[192:195], a[16:19]\n"
      "ds_read_b64_tr_b16 a[150:151], v214 offset:17416\n"
      "v_mfma_f32_16x16x32_bf16 a[48:51], a[176:179], a[196:199], a[48:51]\n"
      "s_add_u32 m0, s78, 32768\n"
      "s_mov_b32 s83, 1572864\n"
      "v_mfma_f32_16x16x32_bf16 a[80:83], a[176:179], a[200:203], a[80:83]\n"
      "ds_read_b64_tr_b16 a[152:153], v215 offset:16384\n"
      "v_mfma_f32_16x16x32_bf16 a[112:115], a[176:179], a[204:207], a[112:115]\n"
      "ds_read_b64_tr_b16 a[154:155], v215 offset:17408\n"
      "v_mfma_f32_16x16x32_bf16 a[20:23], a[180:183], a[192:195], a[20:23]\n"
      "buffer_load_dwordx4 v220, s[52:55], s83 offen offset:0 nt lds\n"
      "v_mfma_f32_16x16x32_bf16 a[52:55], a[180:183], a[196:199], a[52:55]\n"
      "ds_read_b64_tr_b16 a[156:157], v215 offset:16392\n"
      "v_mfma_f32_16x16x32_bf16 a[84:87], a[180:183], a[200:203], a[84:87]\n"
      "ds_read_b64_tr_b16 a[158:159], v215 offset:17416\n"
      "v_mfma_f32_16x16x32_bf16 a[116:119], a[180:183], a[204:207], a[116:119]\n"
      "buffer_load_dwordx4 v221, s[52:55], s83 offen offset:1024 nt lds\n"
      "v_mfma_f32_16x16x32_bf16 a[24:27], a[184:187], a[192:195], a[24:27]\n"
      "v_mfma_f32_16x16x32_bf16 a[56:59], a[184:187], a[196:199], a[56:59]\n"
      "v_mfma_f32_16x16x32_bf16 a[88:91], a[184:187], a[200:203], a[88:91]\n"
      "buffer_load_dwordx4 v222, s[52:55], s83 offen offset:2048 nt lds\n"
      "v_mfma_f32_16x16x32_bf16 a[120:123], a[184:187], a[204:207], a[120:123]\n"
      "v_mfma_f32_16x16x32_bf16 a[28:31], a[188:191], a[192:195], a[28:31]\n"
      "v_mfma_f32_16x16x32_bf16 a[60:63], a[188:191], a[196:199], a[60:63]\n"
      "buffer_load_dwordx4 v223, s[52:55], s83 offen offset:3072 nt lds\n"
      "v_mfma_f32_16x16x32_bf16 a[92:95], a[188:191], a[200:203], a[92:95]\n"
      "v_mfma_f32_16x16x32_bf16 a[124:127], a[188:191], a[204:207], a[124:127]\n"
      "s_waitcnt lgkmcnt(0)\n"
      "v_mfma_f32_16x16x32_bf16 v[0:3], a[128:131], a[208:211], v[0:3]\n"
      "ds_read_b64_tr_b16 a[160:161], v212 offset:24576\n"
      "ds_read_b64_tr_b16 a[162:163], v212 offset:25600\n"
      "v_mfma_f32_16x16x32_bf16 v[32:35], a[128:131], a[212:215], v[32:35]\n"
      "ds_read_b64_tr_b16 a[164:165], v212 offset:24584\n"
      "ds_read_b64_tr_b16 a[166:167], v212 offset:25608\n"
      "v_mfma_f32_16x16x32_bf16 v[64:67], a[128:131], a[216:219], v[64:67]\n"
      "v_lshlrev_b32 v134, 3, v255\n"
      "global_load_dwordx2 v[128:129], v134, %13 sc1\n"
      "v_mfma_f32_16x16x32_bf16 v[96:99], a[128:131], a[220:223], v[96:99]\n"
      "ds_read_b64_tr_b16 a[168:169], v213 offset:24576\n"
      "ds_read_b64_tr_b16 a[170:171], v213 offset:25600\n"
      "v_mfma_f32_16x16x32_bf16 v[4:7], a[132:135], a[208:211], v[4:7]\n"
      "ds_read_b64_tr_b16 a[172:173], v213 offset:24584\n"
      "ds_read_b64_tr_b16 a[174:175], v213 offset:25608\n"
      "v_mfma_f32_16x16x32_bf16 v[36:39], a[132:135], a[212:215], v[36:39]\n"
      "v_lshrrev_b32 v135, 1, v134\n"
      "global_load_dword v130, v135, %13 offset:512 sc1\n"
      "v_mfma_f32_16x16x32_bf16 v[68:71], a[132:135], a[216:219], v[68:71]\n"
      "ds_read_b64_tr_b16 a[176:177], v214 offset:24576\n"
      "v_mfma_f32_16x16x32_bf16 v[100:103], a[132:135], a[220:223], v[100:103]\n"
      "ds_read_b64_tr_b16 a[178:179], v214 offset:25600\n"
      "v_mfma_f32_16x16x32_bf16 v[8:11], a[136:139], a[208:211], v[8:11]\n"
      "v_mov_b32 v133, 0\n"
      "global_load_dword v131, v133, %12 sc1\n"
      "v_mfma_f32_16x16x32_bf16 v[40:43], a[136:139], a[212:215], v[40:43]\n"
      "ds_read_b64_tr_b16 a[180:181], v214 offset:24584\n"
      "v_mfma_f32_16x16x32_bf16 v[72:75], a[136:139], a[216:219], v[72:75]\n"
      "ds_read_b64_tr_b16 a[182:183], v214 offset:25608\n"
      "v_mfma_f32_16x16x32_bf16 v[104:107], a[136:139], a[220:223], v[104:107]\n"
      "ds_read_b64_tr_b16 a[184:185], v215 offset:24576\n"
      "v_mfma_f32_16x16x32_bf16 v[12:15], a[140:143], a[208:211], v[12:15]\n"
      "ds_read_b64_tr_b16 a[186:187], v215 offset:25600\n"
      "v_mfma_f32_16x16x32_bf16 v[44:47], a[140:143], a[212:215], v[44:47]\n"
      "ds_read_b64_tr_b16 a[188:189], v215 offset:24584\n"
      "v_mfma_f32_16x16x32_bf16 v[76:79], a[140:143], a[216:219], v[76:79]\n"
      "ds_read_b64_tr_b16 a[190:191], v215 offset:25608\n"
      "v_mfma_f32_16x16x32_bf16 v[108:111], a[140:143], a[220:223], v[108:111]\n"
      "v_mfma_f32_16x16x32_bf16 v[16:19], a[144:147], a[208:211], v[16:19]\n"
      "v_mfma_f32_16x16x32_bf16 v[48:51], a[144:147], a[212:215], v[48:51]\n"
      "v_mfma_f32_16x16x32_bf16 v[80:83], a[144:147], a[216:219], v[80:83]\n"
      "v_mfma_f32_16x16x32_bf16 v[112:115], a[144:147], a[220:223], v[112:115]\n"
      "v_mfma_f32_16x16x32_bf16 v[20:23], a[148:151], a[208:211], v[20:23]\n"
      "v_mfma_f32_16x16x32_bf16 v[52:55], a[148:151], a[212:215], v[52:55]\n"
      "v_mfma_f32_16x16x32_bf16 v[84:87], a[148:151], a[216:219], v[84:87]\n"
      "v_mfma_f32_16x16x32_bf16 v[116:119], a[148:151], a[220:223], v[116:119]\n"
      "v_mfma_f32_16x16x32_bf16 v[24:27], a[152:155], a[208:211], v[24:27]\n"
      "v_mfma_f32_16x16x32_bf16 v[56:59], a[152:155], a[212:215], v[56:59]\n"
      "v_mfma_f32_16x16x32_bf16 v[88:91], a[152:155], a[216:219], v[88:91]\n"
      "v_mfma_f32_16x16x32_bf16 v[120:123], a[152:155], a[220:223], v[120:123]\n"
      "v_mfma_f32_16x16x32_bf16 v[28:31], a[156:159], a[208:211], v[28:31]\n"
      "v_mfma_f32_16x16x32_bf16 v[60:63], a[156:159], a[212:215], v[60:63]\n"
      "v_mfma_f32_16x16x32_bf16 v[92:95], a[156:159], a[216:219], v[92:95]\n"
      "v_mfma_f32_16x16x32_bf16 v[124:127], a[156:159], a[220:223], v[124:127]\n"
      "s_waitcnt vmcnt(19) lgkmcnt(0)\n"
      "s_barrier\n"
      "v_mfma_f32_16x16x32_bf16 a[0:3], a[160:163], a[208:211], a[0:3]\n"
      "ds_read_b128 a[192:195], v218 offset:0\n"
      "ds_read_b128 a[196:199], v218 offset:2048\n"
      "v_mfma_f32_16x16x32_bf16 a[32:35], a[160:163], a[212:215], a[32:35]\n"
      "ds_read_b128 a[200:203], v218 offset:4096\n"
      "ds_read_b128 a[204:207], v218 offset:6144\n"
      "v_mfma_f32_16x16x32_bf16 a[64:67], a[160:163], a[216:219], a[64:67]\n"
      "s_add_u32 m0, s79, 0\n"
      "s_mov_b32 s83, 512\n"
      "v_mfma_f32_16x16x32_bf16 a[96:99], a[160:163], a[220:223], a[96:99]\n"
      "ds_read_b64_tr_b16 a[128:129], v212 offset:32768\n"
      "ds_read_b64_tr_b16 a[130:131], v212 offset:33792\n"
      "v_mfma_f32_16x16x32_bf16 a[4:7], a[164:167], a[208:211], a[4:7]\n"
      "ds_read_b64_tr_b16 a[132:133], v212 offset:32776\n"
      "ds_read_b64_tr_b16 a[134:135], v212 offset:33800\n"
      "v_mfma_f32_16x16x32_bf16 a[36:39], a[164:167], a[212:215], a[36:39]\n"
      "buffer_load_dwordx4 v224, s[48:51], s83 offen offset:0 nt lds\n"
      "v_mfma_f32_16x16x32_bf16 a[68:71], a[164:167], a[216:219], a[68:71]\n"
      "ds_read_b64_tr_b16 a[136:137], v213 offset:32768\n"
      "v_mfma_f32_16x16x32_bf16 a[100:103], a[164:167], a[220:223], a[100:103]\n"
      "ds_read_b64_tr_b16 a[138:139], v213 offset:33792\n"
      "v_mfma_f32_16x16x32_bf16 a[8:11], a[168:171], a[208:211], a[8:11]\n"
      "buffer_load_dwordx4 v225, s[48:51], s83 offen offset:1024 nt lds\n"
      "v_mfma_f32_16x16x32_bf16 a[40:43], a[168:171], a[212:215], a[40:43]\n"
      "ds_read_b64_tr_b16 a[140:141], v213 offset:32776\n"
      "v_mfma_f32_16x16x32_bf16 a[72:75], a[168:171], a[216:219], a[72:75]\n"
      "ds_read_b64_tr_b16 a[142:143], v213 offset:33800\n"
      "v_mfma_f32_16x16x32_bf16 a[104:107], a[168:171], a[220:223], a[104:107]\n"
      "buffer_load_dwordx4 v226, s[48:51], s83 offen offset:2048 nt lds\n"
      "v_mfma_f32_16x16x32_bf16 a[12:15], a[172:175], a[208:211], a[12:15]\n"
      "ds_read_b64_tr_b16 a[144:145], v214 offset:32768\n"
      "v_mfma_f32_16x16x32_bf16 a[44:47], a[172:175], a[212:215], a[44:47]\n"
      "ds_read_b64_tr_b16 a[146:147], v214 offset:33792\n"
      "v_mfma_f32_16x16x32_bf16 a[76:79], a[172:175], a[216:219], a[76:79]\n"
      "buffer_load_dwordx4 v227, s[48:51], s83 offen offset:3072 nt lds\n"
      "v_mfma_f32_16x16x32_bf16 a[108:111], a[172:175], a[220:223], a[108:111]\n"
      "ds_read_b64_tr_b16 a[148:149], v214 offset:32776\n"
      "v_mfma_f32_16x16x32_bf16 a[16:19], a[176:179], a[208:211], a[16:19]\n"
      "ds_read_b64_tr_b16 a[150:151], v214 offset:33800\n"
      "v_mfma_f32_16x16x32_bf16 a[48:51], a[176:179], a[212:215], a[48:51]\n"
      "ds_read_b64_tr_b16 a[152:153], v215 offset:32768\n"
      "v_mfma_f32_16x16x32_bf16 a[80:83], a[176:179], a[216:219], a[80:83]\n"
      "ds_read_b64_tr_b16 a[154:155], v215 offset:33792\n"
      "v_mfma_f32_16x16x32_bf16 a[112:115], a[176:179], a[220:223], a[112:115]\n"
      "ds_read_b64_tr_b16 a[156:157], v215 offset:32776\n"
      "v_mfma_f32_16x16x32_bf16 a[20:23], a[180:183], a[208:211], a[20:23]\n"
      "ds_read_b64_tr_b16 a[158:159], v215 offset:33800\n"
      "v_mfma_f32_16x16x32_bf16 a[52:55], a[180:183], a[212:215], a[52:55]\n"
      "v_mfma_f32_16x16x32_bf16 a[84:87], a[180:183], a[216:219], a[84:87]\n"
      "v_mfma_f32_16x16x32_bf16 a[116:119], a[180:183], a[220:223], a[116:119]\n"
      "v_mfma_f32_16x16x32_bf16 a[24:27], a[184:187], a[208:211], a[24:27]\n"
      "v_mfma_f32_16x16x32_bf16 a[56:59], a[184:187], a[212:215], a[56:59]\n"
      "v_mfma_f32_16x16x32_bf16 a[88:91], a[184:187], a[216:219], a[88:91]\n"
      "v_mfma_f32_16x16x32_bf16 a[120:123], a[184:187], a[220:223], a[120:123]\n"
      "v_mfma_f32_16x16x32_bf16 a[28:31], a[188:191], a[208:211], a[28:31]\n"
      "v_mfma_f32_16x16x32_bf16 a[60:63], a[188:191], a[212:215], a[60:63]\n"
      "v_mfma_f32_16x16x32_bf16 a[92:95], a[188:191], a[216:219], a[92:95]\n"
      "v_mfma_f32_16x16x32_bf16 a[124:127], a[188:191], a[220:223], a[124:127]\n"
      "s_waitcnt lgkmcnt(0)\n"
      "v_mfma_f32_16x16x32_bf16 v[0:3], a[128:131], a[192:195], v[0:3]\n"
      "ds_read_b64_tr_b16 a[160:161], v212 offset:40960\n"
      "ds_read_b64_tr_b16 a[162:163], v212 offset:41984\n"
      "v_mfma_f32_16x16x32_bf16 v[32:35], a[128:131], a[196:199], v[32:35]\n"
      "ds_read_b64_tr_b16 a[164:165], v212 offset:40968\n"
      "ds_read_b64_tr_b16 a[166:167], v212 offset:41992\n"
      "v_mfma_f32_16x16x32_bf16 v[64:67], a[128:131], a[200:203], v[64:67]\n"
      "s_add_u32 m0, s78, 49152\n"
      "s_mov_b32 s83, 1835008\n"
      "v_mfma_f32_16x16x32_bf16 v[96:99], a[128:131], a[204:207], v[96:99]\n"
      "ds_read_b64_tr_b16 a[168:169], v213 offset:40960\n"
      "ds_read_b64_tr_b16 a[170:171], v213 offset:41984\n"
      "v_mfma_f32_16x16x32_bf16 v[4:7], a[132:135], a[192:195], v[4:7]\n"
      "ds_read_b64_tr_b16 a[172:173], v213 offset:40968\n"
      "ds_read_b64_tr_b16 a[174:175], v213 offset:41992\n"
      "v_mfma_f32_16x16x32_bf16 v[36:39], a[132:135], a[196:199], v[36:39]\n"
      "buffer_load_dwordx4 v220, s[52:55], s83 offen offset:0 nt lds\n"
      "v_mfma_f32_16x16x32_bf16 v[68:71], a[132:135], a[200:203], v[68:71]\n"
      "ds_read_b64_tr_b16 a[176:177], v214 offset:40960\n"
      "v_mfma_f32_16x16x32_bf16 v[100:103], a[132:135], a[204:207], v[100:103]\n"
      "ds_read_b64_tr_b16 a[178:179], v214 offset:41984\n"
      "v_mfma_f32_16x16x32_bf16 v[8:11], a[136:139], a[192:195], v[8:11]\n"
      "buffer_load_dwordx4 v221, s[52:55], s83 offen offset:1024 nt lds\n"
      "v_mfma_f32_16x16x32_bf16 v[40:43], a[136:139], a[196:199], v[40:43]\n"
      "ds_read_b64_tr_b16 a[180:181], v214 offset:40968\n"
      "v_mfma_f32_16x16x32_bf16 v[72:75], a[136:139], a[200:203], v[72:75]\n"
      "ds_read_b64_tr_b16 a[182:183], v214 offset:41992\n"
      "v_mfma_f32_16x16x32_bf16 v[104:107], a[136:139], a[204:207], v[104:107]\n"
      "buffer_load_dwordx4 v222, s[52:55], s83 offen offset:2048 nt lds\n"
      "v_mfma_f32_16x16x32_bf16 v[12:15], a[140:143], a[192:195], v[12:15]\n"
      "ds_read_b64_tr_b16 a[184:185], v215 offset:40960\n"
      "v_mfma_f32_16x16x32_bf16 v[44:47], a[140:143], a[196:199], v[44:47]\n"
      "ds_read_b64_tr_b16 a[186:187], v215 offset:41984\n"
      "v_mfma_f32_16x16x32_bf16 v[76:79], a[140:143], a[200:203], v[76:79]\n"
      "buffer_load_dwordx4 v223, s[52:55], s83 offen offset:3072 nt lds\n"
      "v_mfma_f32_16x16x32_bf16 v[108:111], a[140:143], a[204:207], v[108:111]\n"
      "ds_read_b64_tr_b16 a[188:189], v215 offset:40968\n"
      "v_mfma_f32_16x16x32_bf16 v[16:19], a[144:147], a[192:195], v[16:19]\n"
      "ds_read_b64_tr_b16 a[190:191], v215 offset:41992\n"
      "v_mfma_f32_16x16x32_bf16 v[48:51], a[144:147], a[196:199], v[48:51]\n"
      "v_mfma_f32_16x16x32_bf16 v[80:83], a[144:147], a[200:203], v[80:83]\n"
      "v_mfma_f32_16x16x32_bf16 v[112:115], a[144:147], a[204:207], v[112:115]\n"
      "v_mfma_f32_16x16x32_bf16 v[20:23], a[148:151], a[192:195], v[20:23]\n"
      "v_mfma_f32_16x16x32_bf16 v[52:55], a[148:151], a[196:199], v[52:55]\n"
      "v_mfma_f32_16x16x32_bf16 v[84:87], a[148:151], a[200:203], v[84:87]\n"
      "v_mfma_f32_16x16x32_bf16 v[116:119], a[148:151], a[204:207], v[116:119]\n"
      "v_mfma_f32_16x16x32_bf16 v[24:27], a[152:155], a[192:195], v[24:27]\n"
      "v_mfma_f32_16x16x32_bf16 v[56:59], a[152:155], a[196:199], v[56:59]\n"
      "v_mfma_f32_16x16x32_bf16 v[88:91], a[152:155], a[200:203], v[88:91]\n"
      "v_mfma_f32_16x16x32_bf16 v[120:123], a[152:155], a[204:207], v[120:123]\n"
      "v_mfma_f32_16x16x32_bf16 v[28:31], a[156:159], a[192:195], v[28:31]\n"
      "v_mfma_f32_16x16x32_bf16 v[60:63], a[156:159], a[196:199], v[60:63]\n"
      "v_mfma_f32_16x16x32_bf16 v[92:95], a[156:159], a[200:203], v[92:95]\n"
      "v_mfma_f32_16x16x32_bf16 v[124:127], a[156:159], a[204:207], v[124:127]\n"
      "s_waitcnt vmcnt(19) lgkmcnt(0)\n"
      "s_barrier\n"
      "v_mfma_f32_16x16x32_bf16 a[0:3], a[160:163], a[192:195], a[0:3]\n"
      "ds_read_b128 a[208:211], v219 offset:0\n"
      "ds_read_b128 a[212:215], v219 offset:2048\n"
      "v_mfma_f32_16x16x32_bf16 a[32:35], a[160:163], a[196:199], a[32:35]\n"
      "ds_read_b128 a[216:219], v219 offset:4096\n"
      "ds_read_b128 a[220:223], v219 offset:6144\n"
      "v_mfma_f32_16x16x32_bf16 a[64:67], a[160:163], a[200:203], a[64:67]\n"
      "s_add_u32 m0, s79, 4096\n"
      "s_mov_b32 s83, 41472\n"
      "v_mfma_f32_16x16x32_bf16 a[96:99], a[160:163], a[204:207], a[96:99]\n"
      "ds_read_b64_tr_b16 a[128:129], v212 offset:49152\n"
      "ds_read_b64_tr_b16 a[130:131], v212 offset:50176\n"
      "v_mfma_f32_16x16x32_bf16 a[4:7], a[164:167], a[192:195], a[4:7]\n"
      "ds_read_b64_tr_b16 a[132:133], v212 offset:49160\n"
      "ds_read_b64_tr_b16 a[134:135], v212 offset:50184\n"
      "v_mfma_f32_16x16x32_bf16 a[36:39], a[164:167], a[196:199], a[36:39]\n"
      "buffer_load_dwordx4 v224, s[48:51], s83 offen offset:0 nt lds\n"
      "v_mfma_f32_16x16x32_bf16 a[68:71], a[164:167], a[200:203], a[68:71]\n"
      "ds_read_b64_tr_b16 a[136:137], v213 offset:49152\n"
      "v_mfma_f32_16x16x32_bf16 a[100:103], a[164:167], a[204:207], a[100:103]\n"
      "ds_read_b64_tr_b16 a[138:139], v213 offset:50176\n"
      "v_mfma_f32_16x16x32_bf16 a[8:11], a[168:171], a[192:195], a[8:11]\n"
      "buffer_load_dwordx4 v225, s[48:51], s83 offen offset:1024 nt lds\n"
      "v_mfma_f32_16x16x32_bf16 a[40:43], a[168:171], a[196:199], a[40:43]\n"
      "ds_read_b64_tr_b16 a[140:141], v213 offset:49160\n"
      "v_mfma_f32_16x16x32_bf16 a[72:75], a[168:171], a[200:203], a[72:75]\n"
      "ds_read_b64_tr_b16 a[142:143], v213 offset:50184\n"
      "v_mfma_f32_16x16x32_bf16 a[104:107], a[168:171], a[204:207], a[104:107]\n"
      "buffer_load_dwordx4 v226, s[48:51], s83 offen offset:2048 nt lds\n"
      "v_mfma_f32_16x16x32_bf16 a[12:15], a[172:175], a[192:195], a[12:15]\n"
      "ds_read_b64_tr_b16 a[144:145], v214 offset:49152\n"
      "v_mfma_f32_16x16x32_bf16 a[44:47], a[172:175], a[196:199], a[44:47]\n"
      "ds_read_b64_tr_b16 a[146:147], v214 offset:50176\n"
      "v_mfma_f32_16x16x32_bf16 a[76:79], a[172:175], a[200:203], a[76:79]\n"
      "buffer_load_dwordx4 v227, s[48:51], s83 offen offset:3072 nt lds\n"
      "v_mfma_f32_16x16x32_bf16 a[108:111], a[172:175], a[204:207], a[108:111]\n"
      "ds_read_b64_tr_b16 a[148:149], v214 offset:49160\n"
      "v_mfma_f32_16x16x32_bf16 a[16:19], a[176:179], a[192:195], a[16:19]\n"
      "ds_read_b64_tr_b16 a[150:151], v214 offset:50184\n"
      "v_mfma_f32_16x16x32_bf16 a[48:51], a[176:179], a[196:199], a[48:51]\n"
      "ds_read_b64_tr_b16 a[152:153], v215 offset:49152\n"
      "v_mfma_f32_16x16x32_bf16 a[80:83], a[176:179], a[200:203], a[80:83]\n"
      "ds_read_b64_tr_b16 a[154:155], v215 offset:50176\n"
      "v_mfma_f32_16x16x32_bf16 a[112:115], a[176:179], a[204:207], a[112:115]\n"
      "ds_read_b64_tr_b16 a[156:157], v215 offset:49160\n"
      "v_mfma_f32_16x16x32_bf16 a[20:23], a[180:183], a[192:195], a[20:23]\n"
      "ds_read_b64_tr_b16 a[158:159], v215 offset:50184\n"
      "v_mfma_f32_16x16x32_bf16 a[52:55], a[180:183], a[196:199], a[52:55]\n"
      "v_mfma_f32_16x16x32_bf16 a[84:87], a[180:183], a[200:203], a[84:87]\n"
      "v_mfma_f32_16x16x32_bf16 a[116:119], a[180:183], a[204:207], a[116:119]\n"
      "v_mfma_f32_16x16x32_bf16 a[24:27], a[184:187], a[192:195], a[24:27]\n"
      "v_mfma_f32_16x16x32_bf16 a[56:59], a[184:187], a[196:199], a[56:59]\n"
      "v_mfma_f32_16x16x32_bf16 a[88:91], a[184:187], a[200:203], a[88:91]\n"
      "v_mfma_f32_16x16x32_bf16 a[120:123], a[184:187], a[204:207], a[120:123]\n"
      "v_mfma_f32_16x16x32_bf16 a[28:31], a[188:191], a[192:195], a[28:31]\n"
      "v_mfma_f32_16x16x32_bf16 a[60:63], a[188:191], a[196:199], a[60:63]\n"
      "v_mfma_f32_16x16x32_bf16 a[92:95], a[188:191], a[200:203], a[92:95]\n"
      "v_mfma_f32_16x16x32_bf16 a[124:127], a[188:191], a[204:207], a[124:127]\n"
      "s_waitcnt lgkmcnt(0)\n"
      "v_mfma_f32_16x16x32_bf16 v[0:3], a[128:131], a[208:211], v[0:3]\n"
      "ds_read_b64_tr_b16 a[160:161], v212 offset:57344\n"
      "ds_read_b64_tr_b16 a[162:163], v212 offset:58368\n"
      "v_mfma_f32_16x16x32_bf16 v[32:35], a[128:131], a[212:215], v[32:35]\n"
      "ds_read_b64_tr_b16 a[164:165], v212 offset:57352\n"
      "ds_read_b64_tr_b16 a[166:167], v212 offset:58376\n"
      "v_mfma_f32_16x16x32_bf16 v[64:67], a[128:131], a[216:219], v[64:67]\n"
      "s_add_u32 m0, s78, 65536\n"
      "s_mov_b32 s83, 2097152\n"
      "v_mfma_f32_16x16x32_bf16 v[96:99], a[128:131], a[220:223], v[96:99]\n"
      "ds_read_b64_tr_b16 a[168:169], v213 offset:57344\n"
      "ds_read_b64_tr_b16 a[170:171], v213 offset:58368\n"
      "v_mfma_f32_16x16x32_bf16 v[4:7], a[132:135], a[208:211], v[4:7]\n"
      "ds_read_b64_tr_b16 a[172:173], v213 offset:57352\n"
      "ds_read_b64_tr_b16 a[174:175], v213 offset:58376\n"
      "v_mfma_f32_16x16x32_bf16 v[36:39], a[132:135], a[212:215], v[36:39]\n"
      "buffer_load_dwordx4 v220, s[52:55], s83 offen offset:0 nt lds\n"
      "v_mfma_f32_16x16x32_bf16 v[68:71], a[132:135], a[216:219], v[68:71]\n"
      "ds_read_b64_tr_b16 a[176:177], v214 offset:57344\n"
      "v_mfma_f32_16x16x32_bf16 v[100:103], a[132:135], a[220:223], v[100:103]\n"
      "ds_read_b64_tr_b16 a[178:179], v214 offset:58368\n"
      "v_mfma_f32_16x16x32_bf16 v[8:11], a[136:139], a[208:211], v[8:11]\n"
      "buffer_load_dwordx4 v221, s[52:55], s83 offen offset:1024 nt lds\n"
      "v_mfma_f32_16x16x32_bf16 v[40:43], a[136:139], a[212:215], v[40:43]\n"
      "ds_read_b64_tr_b16 a[180:181], v214 offset:57352\n"
      "v_mfma_f32_16x16x32_bf16 v[72:75], a[136:139], a[216:219], v[72:75]\n"
      "ds_read_b64_tr_b16 a[182:183], v214 offset:58376\n"
      "v_mfma_f32_16x16x32_bf16 v[104:107], a[136:139], a[220:223], v[104:107]\n"
      "buffer_load_dwordx4 v222, s[52:55], s83 offen offset:2048 nt lds\n"
      "v_mfma_f32_16x16x32_bf16 v[12:15], a[140:143], a[208:211], v[12:15]\n"
      "ds_read_b64_tr_b16 a[184:185], v215 offset:57344\n"
      "v_mfma_f32_16x16x32_bf16 v[44:47], a[140:143], a[212:215], v[44:47]\n"
      "ds_read_b64_tr_b16 a[186:187], v215 offset:58368\n"
      "v_mfma_f32_16x16x32_bf16 v[76:79], a[140:143], a[216:219], v[76:79]\n"
      "buffer_load_dwordx4 v223, s[52:55], s83 offen offset:3072 nt lds\n"
      "v_mfma_f32_16x16x32_bf16 v[108:111], a[140:143], a[220:223], v[108:111]\n"
      "ds_read_b64_tr_b16 a[188:189], v215 offset:57352\n"
      "v_mfma_f32_16x16x32_bf16 v[16:19], a[144:147], a[208:211], v[16:19]\n"
      "ds_read_b64_tr_b16 a[190:191], v215 offset:58376\n"
      "v_mfma_f32_16x16x32_bf16 v[48:51], a[144:147], a[212:215], v[48:51]\n"
      "v_mfma_f32_16x16x32_bf16 v[80:83], a[144:147], a[216:219], v[80:83]\n"
      "v_mfma_f32_16x16x32_bf16 v[112:115], a[144:147], a[220:223], v[112:115]\n"
      "v_mfma_f32_16x16x32_bf16 v[20:23], a[148:151], a[208:211], v[20:23]\n"
      "v_mfma_f32_16x16x32_bf16 v[52:55], a[148:151], a[212:215], v[52:55]\n"
      "v_mfma_f32_16x16x32_bf16 v[84:87], a[148:151], a[216:219], v[84:87]\n"
      "v_mfma_f32_16x16x32_bf16 v[116:119], a[148:151], a[220:223], v[116:119]\n"
      "v_mfma_f32_16x16x32_bf16 v[24:27], a[152:155], a[208:211], v[24:27]\n"
      "v_mfma_f32_16x16x32_bf16 v[56:59], a[152:155], a[212:215], v[56:59]\n"
      "v_mfma_f32_16x16x32_bf16 v[88:91], a[152:155], a[216:219], v[88:91]\n"
      "v_mfma_f32_16x16x32_bf16 v[120:123], a[152:155], a[220:223], v[120:123]\n"
      "v_mfma_f32_16x16x32_bf16 v[28:31], a[156:159], a[208:211], v[28:31]\n"
      "v_mfma_f32_16x16x32_bf16 v[60:63], a[156:159], a[212:215], v[60:63]\n"
      "v_mfma_f32_16x16x32_bf16 v[92:95], a[156:159], a[216:219], v[92:95]\n"
      "v_mfma_f32_16x16x32_bf16 v[124:127], a[156:159], a[220:223], v[124:127]\n"
      "s_waitcnt vmcnt(19) lgkmcnt(0)\n"
      "s_barrier\n"
      "v_mfma_f32_16x16x32_bf16 a[0:3], a[160:163], a[208:211], a[0:3]\n"
      "ds_read_b128 a[192:195], v218 offset:32768\n"
      "ds_read_b128 a[196:199], v218 offset:34816\n"
      "v_mfma_f32_16x16x32_bf16 a[32:35], a[160:163], a[212:215], a[32:35]\n"
      "ds_read_b128 a[200:203], v218 offset:36864\n"
      "ds_read_b128 a[204:207], v218 offset:38912\n"
      "v_mfma_f32_16x16x32_bf16 a[64:67], a[160:163], a[216:219], a[64:67]\n"
      "s_add_u32 m0, s79, 98304\n"
      "s_mov_b32 s83, 640\n"
      "v_mfma_f32_16x16x32_bf16 a[96:99], a[160:163], a[220:223], a[96:99]\n"
      "ds_read_b64_tr_b16 a[128:129], v212 offset:0\n"
      "ds_read_b64_tr_b16 a[130:131], v212 offset:1024\n"
      "v_mfma_f32_16x16x32_bf16 a[4:7], a[164:167], a[208:211], a[4:7]\n"
      "ds_read_b64_tr_b16 a[132:133], v212 offset:8\n"
      "ds_read_b64_tr_b16 a[134:135], v212 offset:1032\n"
      "v_mfma_f32_16x16x32_bf16 a[36:39], a[164:167], a[212:215], a[36:39]\n"
      "buffer_load_dwordx4 v224, s[48:51], s83 offen offset:0 nt lds\n"
      "v_mfma_f32_16x16x32_bf16 a[68:71], a[164:167], a[216:219], a[68:71]\n"
      "ds_read_b64_tr_b16 a[136:137], v213 offset:0\n"
      "v_mfma_f32_16x16x32_bf16 a[100:103], a[164:167], a[220:223], a[100:103]\n"
      "ds_read_b64_tr_b16 a[138:139], v213 offset:1024\n"
      "v_mfma_f32_16x16x32_bf16 a[8:11], a[168:171], a[208:211], a[8:11]\n"
      "buffer_load_dwordx4 v225, s[48:51], s83 offen offset:1024 nt lds\n"
      "v_mfma_f32_16x16x32_bf16 a[40:43], a[168:171], a[212:215], a[40:43]\n"
      "ds_read_b64_tr_b16 a[140:141], v213 offset:8\n"
      "v_mfma_f32_16x16x32_bf16 a[72:75], a[168:171], a[216:219], a[72:75]\n"
      "ds_read_b64_tr_b16 a[142:143], v213 offset:1032\n"
      "v_mfma_f32_16x16x32_bf16 a[104:107], a[168:171], a[220:223], a[104:107]\n"
      "buffer_load_dwordx4 v226, s[48:51], s83 offen offset:2048 nt lds\n"
      "v_mfma_f32_16x16x32_bf16 a[12:15], a[172:175], a[208:211], a[12:15]\n"
      "ds_read_b64_tr_b16 a[144:145], v214 offset:0\n"
      "v_mfma_f32_16x16x32_bf16 a[44:47], a[172:175], a[212:215], a[44:47]\n"
      "ds_read_b64_tr_b16 a[146:147], v214 offset:1024\n"
      "v_mfma_f32_16x16x32_bf16 a[76:79], a[172:175], a[216:219], a[76:79]\n"
      "buffer_load_dwordx4 v227, s[48:51], s83 offen offset:3072 nt lds\n"
      "v_mfma_f32_16x16x32_bf16 a[108:111], a[172:175], a[220:223], a[108:111]\n"
      "ds_read_b64_tr_b16 a[148:149], v214 offset:8\n"
      "v_mfma_f32_16x16x32_bf16 a[16:19], a[176:179], a[208:211], a[16:19]\n"
      "ds_read_b64_tr_b16 a[150:151], v214 offset:1032\n"
      "v_mfma_f32_16x16x32_bf16 a[48:51], a[176:179], a[212:215], a[48:51]\n"
      "ds_read_b64_tr_b16 a[152:153], v215 offset:0\n"
      "v_mfma_f32_16x16x32_bf16 a[80:83], a[176:179], a[216:219], a[80:83]\n"
      "ds_read_b64_tr_b16 a[154:155], v215 offset:1024\n"
      "v_mfma_f32_16x16x32_bf16 a[112:115], a[176:179], a[220:223], a[112:115]\n"
      "ds_read_b64_tr_b16 a[156:157], v215 offset:8\n"
      "v_mfma_f32_16x16x32_bf16 a[20:23], a[180:183], a[208:211], a[20:23]\n"
      "ds_read_b64_tr_b16 a[158:159], v215 offset:1032\n"
      "v_mfma_f32_16x16x32_bf16 a[52:55], a[180:183], a[212:215], a[52:55]\n"
      "v_mfma_f32_16x16x32_bf16 a[84:87], a[180:183], a[216:219], a[84:87]\n"
      "v_mfma_f32_16x16x32_bf16 a[116:119], a[180:183], a[220:223], a[116:119]\n"
      "v_mfma_f32_16x16x32_bf16 a[24:27], a[184:187], a[208:211], a[24:27]\n"
      "v_mfma_f32_16x16x32_bf16 a[56:59], a[184:187], a[212:215], a[56:59]\n"
      "v_mfma_f32_16x16x32_bf16 a[88:91], a[184:187], a[216:219], a[88:91]\n"
      "v_mfma_f32_16x16x32_bf16 a[120:123], a[184:187], a[220:223], a[120:123]\n"
      "v_mfma_f32_16x16x32_bf16 a[28:31], a[188:191], a[208:211], a[28:31]\n"
      "v_mfma_f32_16x16x32_bf16 a[60:63], a[188:191], a[212:215], a[60:63]\n"
      "v_mfma_f32_16x16x32_bf16 a[92:95], a[188:191], a[216:219], a[92:95]\n"
      "v_mfma_f32_16x16x32_bf16 a[124:127], a[188:191], a[220:223], a[124:127]\n"
      "s_waitcnt lgkmcnt(0)\n"
      "v_mfma_f32_16x16x32_bf16 v[0:3], a[128:131], a[192:195], v[0:3]\n"
      "ds_read_b64_tr_b16 a[160:161], v212 offset:8192\n"
      "ds_read_b64_tr_b16 a[162:163], v212 offset:9216\n"
      "v_mfma_f32_16x16x32_bf16 v[32:35], a[128:131], a[196:199], v[32:35]\n"
      "ds_read_b64_tr_b16 a[164:165], v212 offset:8200\n"
      "ds_read_b64_tr_b16 a[166:167], v212 offset:9224\n"
      "v_mfma_f32_16x16x32_bf16 v[64:67], a[128:131], a[200:203], v[64:67]\n"
      "s_add_u32 m0, s78, 81920\n"
      "s_mov_b32 s83, 2359296\n"
      "v_mfma_f32_16x16x32_bf16 v[96:99], a[128:131], a[204:207], v[96:99]\n"
      "ds_read_b64_tr_b16 a[168:169], v213 offset:8192\n"
      "ds_read_b64_tr_b16 a[170:171], v213 offset:9216\n"
      "v_mfma_f32_16x16x32_bf16 v[4:7], a[132:135], a[192:195], v[4:7]\n"
      "ds_read_b64_tr_b16 a[172:173], v213 offset:8200\n"
      "ds_read_b64_tr_b16 a[174:175], v213 offset:9224\n"
      "v_mfma_f32_16x16x32_bf16 v[36:39], a[132:135], a[196:199], v[36:39]\n"
      "buffer_load_dwordx4 v220, s[52:55], s83 offen offset:0 nt lds\n"
      "v_mfma_f32_16x16x32_bf16 v[68:71], a[132:135], a[200:203], v[68:71]\n"
      "ds_read_b64_tr_b16 a[176:177], v214 offset:8192\n"
      "v_mfma_f32_16x16x32_bf16 v[100:103], a[132:135], a[204:207], v[100:103]\n"
      "ds_read_b64_tr_b16 a[178:179], v214 offset:9216\n"
      "v_mfma_f32_16x16x32_bf16 v[8:11], a[136:139], a[192:195], v[8:11]\n"
      "buffer_load_dwordx4 v221, s[52:55], s83 offen offset:1024 nt lds\n"
      "v_mfma_f32_16x16x32_bf16 v[40:43], a[136:139], a[196:199], v[40:43]\n"
      "ds_read_b64_tr_b16 a[180:181], v214 offset:8200\n"
      "v_mfma_f32_16x16x32_bf16 v[72:75], a[136:139], a[200:203], v[72:75]\n"
      "ds_read_b64_tr_b16 a[182:183], v214 offset:9224\n"
      "v_mfma_f32_16x16x32_bf16 v[104:107], a[136:139], a[204:207], v[104:107]\n"
      "buffer_load_dwordx4 v222, s[52:55], s83 offen offset:2048 nt lds\n"
      "v_mfma_f32_16x16x32_bf16 v[12:15], a[140:143], a[192:195], v[12:15]\n"
      "ds_read_b64_tr_b16 a[184:185], v215 offset:8192\n"
      "v_mfma_f32_16x16x32_bf16 v[44:47], a[140:143], a[196:199], v[44:47]\n"
      "ds_read_b64_tr_b16 a[186:187], v215 offset:9216\n"
      "v_mfma_f32_16x16x32_bf16 v[76:79], a[140:143], a[200:203], v[76:79]\n"
      "buffer_load_dwordx4 v223, s[52:55], s83 offen offset:3072 nt lds\n"
      "v_mfma_f32_16x16x32_bf16 v[108:111], a[140:143], a[204:207], v[108:111]\n"
      "ds_read_b64_tr_b16 a[188:189], v215 offset:8200\n"
      "v_mfma_f32_16x16x32_bf16 v[16:19], a[144:147], a[192:195], v[16:19]\n"
      "ds_read_b64_tr_b16 a[190:191], v215 offset:9224\n"
      "v_mfma_f32_16x16x32_bf16 v[48:51], a[144:147], a[196:199], v[48:51]\n"
      "v_mfma_f32_16x16x32_bf16 v[80:83], a[144:147], a[200:203], v[80:83]\n"
      "v_mfma_f32_16x16x32_bf16 v[112:115], a[144:147], a[204:207], v[112:115]\n"
      "v_mfma_f32_16x16x32_bf16 v[20:23], a[148:151], a[192:195], v[20:23]\n"
      "v_mfma_f32_16x16x32_bf16 v[52:55], a[148:151], a[196:199], v[52:55]\n"
      "v_mfma_f32_16x16x32_bf16 v[84:87], a[148:151], a[200:203], v[84:87]\n"
      "v_mfma_f32_16x16x32_bf16 v[116:119], a[148:151], a[204:207], v[116:119]\n"
      "v_mfma_f32_16x16x32_bf16 v[24:27], a[152:155], a[192:195], v[24:27]\n"
      "v_mfma_f32_16x16x32_bf16 v[56:59], a[152:155], a[196:199], v[56:59]\n"
      "v_mfma_f32_16x16x32_bf16 v[88:91], a[152:155], a[200:203], v[88:91]\n"
      "v_mfma_f32_16x16x32_bf16 v[120:123], a[152:155], a[204:207], v[120:123]\n"
      "v_mfma_f32_16x16x32_bf16 v[28:31], a[156:159], a[192:195], v[28:31]\n"
      "v_mfma_f32_16x16x32_bf16 v[60:63], a[156:159], a[196:199], v[60:63]\n"
      "v_mfma_f32_16x16x32_bf16 v[92:95], a[156:159], a[200:203], v[92:95]\n"
      "v_mfma_f32_16x16x32_bf16 v[124:127], a[156:159], a[204:207], v[124:127]\n"
      "s_waitcnt vmcnt(16) lgkmcnt(0)\n"
      "s_barrier\n"
      "v_mfma_f32_16x16x32_bf16 a[0:3], a[160:163], a[192:195], a[0:3]\n"
      "ds_read_b128 a[208:211], v219 offset:32768\n"
      "ds_read_b128 a[212:215], v219 offset:34816\n"
      "v_mfma_f32_16x16x32_bf16 a[32:35], a[160:163], a[196:199], a[32:35]\n"
      "ds_read_b128 a[216:219], v219 offset:36864\n"
      "ds_read_b128 a[220:223], v219 offset:38912\n"
      "v_mfma_f32_16x16x32_bf16 a[64:67], a[160:163], a[200:203], a[64:67]\n"
      "s_add_u32 m0, s79, 102400\n"
      "s_mov_b32 s83, 41600\n"
      "v_mfma_f32_16x16x32_bf16 a[96:99], a[160:163], a[204:207], a[96:99]\n"
      "ds_read_b64_tr_b16 a[128:129], v212 offset:16384\n"
      "ds_read_b64_tr_b16 a[130:131], v212 offset:17408\n"
      "v_mfma_f32_16x16x32_bf16 a[4:7], a[164:167], a[192:195], a[4:7]\n"
      "ds_read_b64_tr_b16 a[132:133], v212 offset:16392\n"
      "ds_read_b64_tr_b16 a[134:135], v212 offset:17416\n"
      "v_mfma_f32_16x16x32_bf16 a[36:39], a[164:167], a[196:199], a[36:39]\n"
      "buffer_load_dwordx4 v224, s[48:51], s83 offen offset:0 nt lds\n"
      "v_mfma_f32_16x16x32_bf16 a[68:71], a[164:167], a[200:203], a[68:71]\n"
      "ds_read_b64_tr_b16 a[136:137], v213 offset:16384\n"
      "v_mfma_f32_16x16x32_bf16 a[100:103], a[164:167], a[204:207], a[100:103]\n"
      "ds_read_b64_tr_b16 a[138:139], v213 offset:17408\n"
      "v_mfma_f32_16x16x32_bf16 a[8:11], a[168:171], a[192:195], a[8:11]\n"
      "buffer_load_dwordx4 v225, s[48:51], s83 offen offset:1024 nt lds\n"
      "v_mfma_f32_16x16x32_bf16 a[40:43], a[168:171], a[196:199], a[40:43]\n"
      "ds_read_b64_tr_b16 a[140:141], v213 offset:16392\n"
      "v_mfma_f32_16x16x32_bf16 a[72:75], a[168:171], a[200:203], a[72:75]\n"
      "ds_read_b64_tr_b16 a[142:143], v213 offset:17416\n"
      "v_mfma_f32_16x16x32_bf16 a[104:107], a[168:171], a[204:207], a[104:107]\n"
      "buffer_load_dwordx4 v226, s[48:51], s83 offen offset:2048 nt lds\n"
      "v_mfma_f32_16x16x32_bf16 a[12:15], a[172:175], a[192:195], a[12:15]\n"
      "ds_read_b64_tr_b16 a[144:145], v214 offset:16384\n"
      "v_mfma_f32_16x16x32_bf16 a[44:47], a[172:175], a[196:199], a[44:47]\n"
      "ds_read_b64_tr_b16 a[146:147], v214 offset:17408\n"
      "v_mfma_f32_16x16x32_bf16 a[76:79], a[172:175], a[200:203], a[76:79]\n"
      "buffer_load_dwordx4 v227, s[48:51], s83 offen offset:3072 nt lds\n"
      "v_mfma_f32_16x16x32_bf16 a[108:111], a[172:175], a[204:207], a[108:111]\n"
      "ds_read_b64_tr_b16 a[148:149], v214 offset:16392\n"
      "v_mfma_f32_16x16x32_bf16 a[16:19], a[176:179], a[192:195], a[16:19]\n"
      "ds_read_b64_tr_b16 a[150:151], v214 offset:17416\n"
      "v_mfma_f32_16x16x32_bf16 a[48:51], a[176:179], a[196:199], a[48:51]\n"
      "ds_read_b64_tr_b16 a[152:153], v215 offset:16384\n"
      "v_mfma_f32_16x16x32_bf16 a[80:83], a[176:179], a[200:203], a[80:83]\n"
      "ds_read_b64_tr_b16 a[154:155], v215 offset:17408\n"
      "v_mfma_f32_16x16x32_bf16 a[112:115], a[176:179], a[204:207], a[112:115]\n"
      "ds_read_b64_tr_b16 a[156:157], v215 offset:16392\n"
      "v_mfma_f32_16x16x32_bf16 a[20:23], a[180:183], a[192:195], a[20:23]\n"
      "ds_read_b64_tr_b16 a[158:159], v215 offset:17416\n"
      "v_mfma_f32_16x16x32_bf16 a[52:55], a[180:183], a[196:199], a[52:55]\n"
      "v_mfma_f32_16x16x32_bf16 a[84:87], a[180:183], a[200:203], a[84:87]\n"
      "v_mfma_f32_16x16x32_bf16 a[116:119], a[180:183], a[204:207], a[116:119]\n"
      "v_mfma_f32_16x16x32_bf16 a[24:27], a[184:187], a[192:195], a[24:27]\n"
      "v_mfma_f32_16x16x32_bf16 a[56:59], a[184:187], a[196:199], a[56:59]\n"
      "v_mfma_f32_16x16x32_bf16 a[88:91], a[184:187], a[200:203], a[88:91]\n"
      "v_mfma_f32_16x16x32_bf16 a[120:123], a[184:187], a[204:207], a[120:123]\n"
      "v_mfma_f32_16x16x32_bf16 a[28:31], a[188:191], a[192:195], a[28:31]\n"
      "v_mfma_f32_16x16x32_bf16 a[60:63], a[188:191], a[196:199], a[60:63]\n"
      "v_mfma_f32_16x16x32_bf16 a[92:95], a[188:191], a[200:203], a[92:95]\n"
      "v_mfma_f32_16x16x32_bf16 a[124:127], a[188:191], a[204:207], a[124:127]\n"
      "s_waitcnt lgkmcnt(0)\n"
      "v_mfma_f32_16x16x32_bf16 v[0:3], a[128:131], a[208:211], v[0:3]\n"
      "ds_read_b64_tr_b16 a[160:161], v212 offset:24576\n"
      "ds_read_b64_tr_b16 a[162:163], v212 offset:25600\n"
      "v_mfma_f32_16x16x32_bf16 v[32:35], a[128:131], a[212:215], v[32:35]\n"
      "ds_read_b64_tr_b16 a[164:165], v212 offset:24584\n"
      "ds_read_b64_tr_b16 a[166:167], v212 offset:25608\n"
      "v_mfma_f32_16x16x32_bf16 v[64:67], a[128:131], a[216:219], v[64:67]\n"
      "s_add_u32 m0, s78, 32768\n"
      "s_mov_b32 s83, 2621440\n"
      "v_mfma_f32_16x16x32_bf16 v[96:99], a[128:131], a[220:223], v[96:99]\n"
      "ds_read_b64_tr_b16 a[168:169], v213 offset:24576\n"
      "ds_read_b64_tr_b16 a[170:171], v213 offset:25600\n"
      "v_mfma_f32_16x16x32_bf16 v[4:7], a[132:135], a[208:211], v[4:7]\n"
      "ds_read_b64_tr_b16 a[172:173], v213 offset:24584\n"
      "ds_read_b64_tr_b16 a[174:175], v213 offset:25608\n"
      "v_mfma_f32_16x16x32_bf16 v[36:39], a[132:135], a[212:215], v[36:39]\n"
      "buffer_load_dwordx4 v220, s[52:55], s83 offen offset:0 nt lds\n"
      "v_mfma_f32_16x16x32_bf16 v[68:71], a[132:135], a[216:219], v[68:71]\n"
      "ds_read_b64_tr_b16 a[176:177], v214 offset:24576\n"
      "v_mfma_f32_16x16x32_bf16 v[100:103], a[132:135], a[220:223], v[100:103]\n"
      "ds_read_b64_tr_b16 a[178:179], v214 offset:25600\n"
      "v_mfma_f32_16x16x32_bf16 v[8:11], a[136:139], a[208:211], v[8:11]\n"
      "buffer_load_dwordx4 v221, s[52:55], s83 offen offset:1024 nt lds\n"
      "v_mfma_f32_16x16x32_bf16 v[40:43], a[136:139], a[212:215], v[40:43]\n"
      "ds_read_b64_tr_b16 a[180:181], v214 offset:24584\n"
      "v_mfma_f32_16x16x32_bf16 v[72:75], a[136:139], a[216:219], v[72:75]\n"
      "ds_read_b64_tr_b16 a[182:183], v214 offset:25608\n"
      "v_mfma_f32_16x16x32_bf16 v[104:107], a[136:139], a[220:223], v[104:107]\n"
      "buffer_load_dwordx4 v222, s[52:55], s83 offen offset:2048 nt lds\n"
      "v_mfma_f32_16x16x32_bf16 v[12:15], a[140:143], a[208:211], v[12:15]\n"
      "ds_read_b64_tr_b16 a[184:185], v215 offset:24576\n"
      "v_mfma_f32_16x16x32_bf16 v[44:47], a[140:143], a[212:215], v[44:47]\n"
      "ds_read_b64_tr_b16 a[186:187], v215 offset:25600\n"
      "v_mfma_f32_16x16x32_bf16 v[76:79], a[140:143], a[216:219], v[76:79]\n"
      "buffer_load_dwordx4 v223, s[52:55], s83 offen offset:3072 nt lds\n"
      "v_mfma_f32_16x16x32_bf16 v[108:111], a[140:143], a[220:223], v[108:111]\n"
      "ds_read_b64_tr_b16 a[188:189], v215 offset:24584\n"
      "v_mfma_f32_16x16x32_bf16 v[16:19], a[144:147], a[208:211], v[16:19]\n"
      "ds_read_b64_tr_b16 a[190:191], v215 offset:25608\n"
      "v_mfma_f32_16x16x32_bf16 v[48:51], a[144:147], a[212:215], v[48:51]\n"
      "v_mfma_f32_16x16x32_bf16 v[80:83], a[144:147], a[216:219], v[80:83]\n"
      "v_mfma_f32_16x16x32_bf16 v[112:115], a[144:147], a[220:223], v[112:115]\n"
      "v_mfma_f32_16x16x32_bf16 v[20:23], a[148:151], a[208:211], v[20:23]\n"
      "v_mfma_f32_16x16x32_bf16 v[52:55], a[148:151], a[212:215], v[52:55]\n"
      "v_mfma_f32_16x16x32_bf16 v[84:87], a[148:151], a[216:219], v[84:87]\n"
      "v_mfma_f32_16x16x32_bf16 v[116:119], a[148:151], a[220:223], v[116:119]\n"
      "v_mfma_f32_16x16x32_bf16 v[24:27], a[152:155], a[208:211], v[24:27]\n"
      "v_mfma_f32_16x16x32_bf16 v[56:59], a[152:155], a[212:215], v[56:59]\n"
      "v_mfma_f32_16x16x32_bf16 v[88:91], a[152:155], a[216:219], v[88:91]\n"
      "v_mfma_f32_16x16x32_bf16 v[120:123], a[152:155], a[220:223], v[120:123]\n"
      "v_mfma_f32_16x16x32_bf16 v[28:31], a[156:159], a[208:211], v[28:31]\n"
      "v_mfma_f32_16x16x32_bf16 v[60:63], a[156:159], a[212:215], v[60:63]\n"
      "v_mfma_f32_16x16x32_bf16 v[92:95], a[156:159], a[216:219], v[92:95]\n"
      "v_mfma_f32_16x16x32_bf16 v[124:127], a[156:159], a[220:223], v[124:127]\n"
      "s_waitcnt vmcnt(16) lgkmcnt(0)\n"
      "s_barrier\n"
      "v_mfma_f32_16x16x32_bf16 a[0:3], a[160:163], a[208:211], a[0:3]\n"
      "ds_read_b128 a[192:195], v216 offset:0\n"
      "ds_read_b128 a[196:199], v216 offset:2048\n"
      "v_mfma_f32_16x16x32_bf16 a[32:35], a[160:163], a[212:215], a[32:35]\n"
      "ds_read_b128 a[200:203], v216 offset:4096\n"
      "ds_read_b128 a[204:207], v216 offset:6144\n"
      "v_mfma_f32_16x16x32_bf16 a[64:67], a[160:163], a[216:219], a[64:67]\n"
      "s_add_u32 m0, s79, 131072\n"
      "s_mov_b32 s83, 768\n"
      "v_mfma_f32_16x16x32_bf16 a[96:99], a[160:163], a[220:223], a[96:99]\n"
      "ds_read_b64_tr_b16 a[128:129], v212 offset:32768\n"
      "ds_read_b64_tr_b16 a[130:131], v212 offset:33792\n"
      "v_mfma_f32_16x16x32_bf16 a[4:7], a[164:167], a[208:211], a[4:7]\n"
      "ds_read_b64_tr_b16 a[132:133], v212 offset:32776\n"
      "ds_read_b64_tr_b16 a[134:135], v212 offset:33800\n"
      "v_mfma_f32_16x16x32_bf16 a[36:39], a[164:167], a[212:215], a[36:39]\n"
      "buffer_load_dwordx4 v224, s[48:51], s83 offen offset:0 nt lds\n"
      "v_mfma_f32_16x16x32_bf16 a[68:71], a[164:167], a[216:219], a[68:71]\n"
      "ds_read_b64_tr_b16 a[136:137], v213 offset:32768\n"
      "v_mfma_f32_16x16x32_bf16 a[100:103], a[164:167], a[220:223], a[100:103]\n"
      "ds_read_b64_tr_b16 a[138:139], v213 offset:33792\n"
      "v_mfma_f32_16x16x32_bf16 a[8:11], a[168:171], a[208:211], a[8:11]\n"
      "buffer_load_dwordx4 v225, s[48:51], s83 offen offset:1024 nt lds\n"
      "v_mfma_f32_16x16x32_bf16 a[40:43], a[168:171], a[212:215], a[40:43]\n"
      "ds_read_b64_tr_b16 a[140:141], v213 offset:32776\n"
      "v_mfma_f32_16x16x32_bf16 a[72:75], a[168:171], a[216:219], a[72:75]\n"
      "ds_read_b64_tr_b16 a[142:143], v213 offset:33800\n"
      "v_mfma_f32_16x16x32_bf16 a[104:107], a[168:171], a[220:223], a[104:107]\n"
      "buffer_load_dwordx4 v226, s[48:51], s83 offen offset:2048 nt lds\n"
      "v_mfma_f32_16x16x32_bf16 a[12:15], a[172:175], a[208:211], a[12:15]\n"
      "ds_read_b64_tr_b16 a[144:145], v214 offset:32768\n"
      "v_mfma_f32_16x16x32_bf16 a[44:47], a[172:175], a[212:215], a[44:47]\n"
      "ds_read_b64_tr_b16 a[146:147], v214 offset:33792\n"
      "v_mfma_f32_16x16x32_bf16 a[76:79], a[172:175], a[216:219], a[76:79]\n"
      "buffer_load_dwordx4 v227, s[48:51], s83 offen offset:3072 nt lds\n"
      "v_mfma_f32_16x16x32_bf16 a[108:111], a[172:175], a[220:223], a[108:111]\n"
      "ds_read_b64_tr_b16 a[148:149], v214 offset:32776\n"
      "v_mfma_f32_16x16x32_bf16 a[16:19], a[176:179], a[208:211], a[16:19]\n"
      "ds_read_b64_tr_b16 a[150:151], v214 offset:33800\n"
      "v_mfma_f32_16x16x32_bf16 a[48:51], a[176:179], a[212:215], a[48:51]\n"
      "ds_read_b64_tr_b16 a[152:153], v215 offset:32768\n"
      "v_mfma_f32_16x16x32_bf16 a[80:83], a[176:179], a[216:219], a[80:83]\n"
      "ds_read_b64_tr_b16 a[154:155], v215 offset:33792\n"
      "v_mfma_f32_16x16x32_bf16 a[112:115], a[176:179], a[220:223], a[112:115]\n"
      "ds_read_b64_tr_b16 a[156:157], v215 offset:32776\n"
      "v_mfma_f32_16x16x32_bf16 a[20:23], a[180:183], a[208:211], a[20:23]\n"
      "ds_read_b64_tr_b16 a[158:159], v215 offset:33800\n"
      "v_mfma_f32_16x16x32_bf16 a[52:55], a[180:183], a[212:215], a[52:55]\n"
      "v_mfma_f32_16x16x32_bf16 a[84:87], a[180:183], a[216:219], a[84:87]\n"
      "v_mfma_f32_16x16x32_bf16 a[116:119], a[180:183], a[220:223], a[116:119]\n"
      "v_mfma_f32_16x16x32_bf16 a[24:27], a[184:187], a[208:211], a[24:27]\n"
      "v_mfma_f32_16x16x32_bf16 a[56:59], a[184:187], a[212:215], a[56:59]\n"
      "v_mfma_f32_16x16x32_bf16 a[88:91], a[184:187], a[216:219], a[88:91]\n"
      "v_mfma_f32_16x16x32_bf16 a[120:123], a[184:187], a[220:223], a[120:123]\n"
      "v_mfma_f32_16x16x32_bf16 a[28:31], a[188:191], a[208:211], a[28:31]\n"
      "v_mfma_f32_16x16x32_bf16 a[60:63], a[188:191], a[212:215], a[60:63]\n"
      "v_mfma_f32_16x16x32_bf16 a[92:95], a[188:191], a[216:219], a[92:95]\n"
      "v_mfma_f32_16x16x32_bf16 a[124:127], a[188:191], a[220:223], a[124:127]\n"
      "s_waitcnt lgkmcnt(0)\n"
      "v_mfma_f32_16x16x32_bf16 v[0:3], a[128:131], a[192:195], v[0:3]\n"
      "ds_read_b64_tr_b16 a[160:161], v212 offset:40960\n"
      "ds_read_b64_tr_b16 a[162:163], v212 offset:41984\n"
      "v_mfma_f32_16x16x32_bf16 v[32:35], a[128:131], a[196:199], v[32:35]\n"
      "ds_read_b64_tr_b16 a[164:165], v212 offset:40968\n"
      "ds_read_b64_tr_b16 a[166:167], v212 offset:41992\n"
      "v_mfma_f32_16x16x32_bf16 v[64:67], a[128:131], a[200:203], v[64:67]\n"
      "s_add_u32 m0, s78, 49152\n"
      "s_mov_b32 s83, 2883584\n"
      "v_mfma_f32_16x16x32_bf16 v[96:99], a[128:131], a[204:207], v[96:99]\n"
      "ds_read_b64_tr_b16 a[168:169], v213 offset:40960\n"
      "ds_read_b64_tr_b16 a[170:171], v213 offset:41984\n"
      "v_mfma_f32_16x16x32_bf16 v[4:7], a[132:135], a[192:195], v[4:7]\n"
      "ds_read_b64_tr_b16 a[172:173], v213 offset:40968\n"
      "ds_read_b64_tr_b16 a[174:175], v213 offset:41992\n"
      "v_mfma_f32_16x16x32_bf16 v[36:39], a[132:135], a[196:199], v[36:39]\n"
      "buffer_load_dwordx4 v220, s[52:55], s83 offen offset:0 nt lds\n"
      "v_mfma_f32_16x16x32_bf16 v[68:71], a[132:135], a[200:203], v[68:71]\n"
      "ds_read_b64_tr_b16 a[176:177], v214 offset:40960\n"
      "v_mfma_f32_16x16x32_bf16 v[100:103], a[132:135], a[204:207], v[100:103]\n"
      "ds_read_b64_tr_b16 a[178:179], v214 offset:41984\n"
      "v_mfma_f32_16x16x32_bf16 v[8:11], a[136:139], a[192:195], v[8:11]\n"
      "buffer_load_dwordx4 v221, s[52:55], s83 offen offset:1024 nt lds\n"
      "v_mfma_f32_16x16x32_bf16 v[40:43], a[136:139], a[196:199], v[40:43]\n"
      "ds_read_b64_tr_b16 a[180:181], v214 offset:40968\n"
      "v_mfma_f32_16x16x32_bf16 v[72:75], a[136:139], a[200:203], v[72:75]\n"
      "ds_read_b64_tr_b16 a[182:183], v214 offset:41992\n"
      "v_mfma_f32_16x16x32_bf16 v[104:107], a[136:139], a[204:207], v[104:107]\n"
      "buffer_load_dwordx4 v222, s[52:55], s83 offen offset:2048 nt lds\n"
      "v_mfma_f32_16x16x32_bf16 v[12:15], a[140:143], a[192:195], v[12:15]\n"
      "ds_read_b64_tr_b16 a[184:185], v215 offset:40960\n"
      "v_mfma_f32_16x16x32_bf16 v[44:47], a[140:143], a[196:199], v[44:47]\n"
      "ds_read_b64_tr_b16 a[186:187], v215 offset:41984\n"
      "v_mfma_f32_16x16x32_bf16 v[76:79], a[140:143], a[200:203], v[76:79]\n"
      "buffer_load_dwordx4 v223, s[52:55], s83 offen offset:3072 nt lds\n"
      "v_mfma_f32_16x16x32_bf16 v[108:111], a[140:143], a[204:207], v[108:111]\n"
      "ds_read_b64_tr_b16 a[188:189], v215 offset:40968\n"
      "v_mfma_f32_16x16x32_bf16 v[16:19], a[144:147], a[192:195], v[16:19]\n"
      "ds_read_b64_tr_b16 a[190:191], v215 offset:41992\n"
      "v_mfma_f32_16x16x32_bf16 v[48:51], a[144:147], a[196:199], v[48:51]\n"
      "v_mfma_f32_16x16x32_bf16 v[80:83], a[144:147], a[200:203], v[80:83]\n"
      "v_mfma_f32_16x16x32_bf16 v[112:115], a[144:147], a[204:207], v[112:115]\n"
      "v_mfma_f32_16x16x32_bf16 v[20:23], a[148:151], a[192:195], v[20:23]\n"
      "v_mfma_f32_16x16x32_bf16 v[52:55], a[148:151], a[196:199], v[52:55]\n"
      "v_mfma_f32_16x16x32_bf16 v[84:87], a[148:151], a[200:203], v[84:87]\n"
      "v_mfma_f32_16x16x32_bf16 v[116:119], a[148:151], a[204:207], v[116:119]\n"
      "v_mfma_f32_16x16x32_bf16 v[24:27], a[152:155], a[192:195], v[24:27]\n"
      "v_mfma_f32_16x16x32_bf16 v[56:59], a[152:155], a[196:199], v[56:59]\n"
      "v_mfma_f32_16x16x32_bf16 v[88:91], a[152:155], a[200:203], v[88:91]\n"
      "v_mfma_f32_16x16x32_bf16 v[120:123], a[152:155], a[204:207], v[120:123]\n"
      "v_mfma_f32_16x16x32_bf16 v[28:31], a[156:159], a[192:195], v[28:31]\n"
      "v_mfma_f32_16x16x32_bf16 v[60:63], a[156:159], a[196:199], v[60:63]\n"
      "v_mfma_f32_16x16x32_bf16 v[92:95], a[156:159], a[200:203], v[92:95]\n"
      "v_mfma_f32_16x16x32_bf16 v[124:127], a[156:159], a[204:207], v[124:127]\n"
      "s_waitcnt vmcnt(16) lgkmcnt(0)\n"
      "s_barrier\n"
      "v_mfma_f32_16x16x32_bf16 a[0:3], a[160:163], a[192:195], a[0:3]\n"
      "ds_read_b128 a[208:211], v217 offset:0\n"
      "ds_read_b128 a[212:215], v217 offset:2048\n"
      "v_mfma_f32_16x16x32_bf16 a[32:35], a[160:163], a[196:199], a[32:35]\n"
      "ds_read_b128 a[216:219], v217 offset:4096\n"
      "ds_read_b128 a[220:223], v217 offset:6144\n"
      "v_mfma_f32_16x16x32_bf16 a[64:67], a[160:163], a[200:203], a[64:67]\n"
      "s_add_u32 m0, s79, 135168\n"
      "s_mov_b32 s83, 41728\n"
      "v_mfma_f32_16x16x32_bf16 a[96:99], a[160:163], a[204:207], a[96:99]\n"
      "ds_read_b64_tr_b16 a[128:129], v212 offset:49152\n"
      "ds_read_b64_tr_b16 a[130:131], v212 offset:50176\n"
      "v_mfma_f32_16x16x32_bf16 a[4:7], a[164:167], a[192:195], a[4:7]\n"
      "ds_read_b64_tr_b16 a[132:133], v212 offset:49160\n"
      "ds_read_b64_tr_b16 a[134:135], v212 offset:50184\n"
      "v_mfma_f32_16x16x32_bf16 a[36:39], a[164:167], a[196:199], a[36:39]\n"
      "buffer_load_dwordx4 v224, s[48:51], s83 offen offset:0 nt lds\n"
      "v_mfma_f32_16x16x32_bf16 a[68:71], a[164:167], a[200:203], a[68:71]\n"
      "ds_read_b64_tr_b16 a[136:137], v213 offset:49152\n"
      "v_mfma_f32_16x16x32_bf16 a[100:103], a[164:167], a[204:207], a[100:103]\n"
      "ds_read_b64_tr_b16 a[138:139], v213 offset:50176\n"
      "v_mfma_f32_16x16x32_bf16 a[8:11], a[168:171], a[192:195], a[8:11]\n"
      "buffer_load_dwordx4 v225, s[48:51], s83 offen offset:1024 nt lds\n"
      "v_mfma_f32_16x16x32_bf16 a[40:43], a[168:171], a[196:199], a[40:43]\n"
      "ds_read_b64_tr_b16 a[140:141], v213 offset:49160\n"
      "v_mfma_f32_16x16x32_bf16 a[72:75], a[168:171], a[200:203], a[72:75]\n"
      "ds_read_b64_tr_b16 a[142:143], v213 offset:50184\n"
      "v_mfma_f32_16x16x32_bf16 a[104:107], a[168:171], a[204:207], a[104:107]\n"
      "buffer_load_dwordx4 v226, s[48:51], s83 offen offset:2048 nt lds\n"
      "v_mfma_f32_16x16x32_bf16 a[12:15], a[172:175], a[192:195], a[12:15]\n"
      "ds_read_b64_tr_b16 a[144:145], v214 offset:49152\n"
      "v_mfma_f32_16x16x32_bf16 a[44:47], a[172:175], a[196:199], a[44:47]\n"
      "ds_read_b64_tr_b16 a[146:147], v214 offset:50176\n"
      "v_mfma_f32_16x16x32_bf16 a[76:79], a[172:175], a[200:203], a[76:79]\n"
      "buffer_load_dwordx4 v227, s[48:51], s83 offen offset:3072 nt lds\n"
      "v_mfma_f32_16x16x32_bf16 a[108:111], a[172:175], a[204:207], a[108:111]\n"
      "ds_read_b64_tr_b16 a[148:149], v214 offset:49160\n"
      "v_mfma_f32_16x16x32_bf16 a[16:19], a[176:179], a[192:195], a[16:19]\n"
      "ds_read_b64_tr_b16 a[150:151], v214 offset:50184\n"
      "v_mfma_f32_16x16x32_bf16 a[48:51], a[176:179], a[196:199], a[48:51]\n"
      "ds_read_b64_tr_b16 a[152:153], v215 offset:49152\n"
      "v_mfma_f32_16x16x32_bf16 a[80:83], a[176:179], a[200:203], a[80:83]\n"
      "ds_read_b64_tr_b16 a[154:155], v215 offset:50176\n"
      "v_mfma_f32_16x16x32_bf16 a[112:115], a[176:179], a[204:207], a[112:115]\n"
      "ds_read_b64_tr_b16 a[156:157], v215 offset:49160\n"
      "v_mfma_f32_16x16x32_bf16 a[20:23], a[180:183], a[192:195], a[20:23]\n"
      "ds_read_b64_tr_b16 a[158:159], v215 offset:50184\n"
      "v_mfma_f32_16x16x32_bf16 a[52:55], a[180:183], a[196:199], a[52:55]\n"
      "v_mfma_f32_16x16x32_bf16 a[84:87], a[180:183], a[200:203], a[84:87]\n"
      "v_mfma_f32_16x16x32_bf16 a[116:119], a[180:183], a[204:207], a[116:119]\n"
      "v_mfma_f32_16x16x32_bf16 a[24:27], a[184:187], a[192:195], a[24:27]\n"
      "v_mfma_f32_16x16x32_bf16 a[56:59], a[184:187], a[196:199], a[56:59]\n"
      "v_mfma_f32_16x16x32_bf16 a[88:91], a[184:187], a[200:203], a[88:91]\n"
      "v_mfma_f32_16x16x32_bf16 a[120:123], a[184:187], a[204:207], a[120:123]\n"
      "v_mfma_f32_16x16x32_bf16 a[28:31], a[188:191], a[192:195], a[28:31]\n"
      "v_mfma_f32_16x16x32_bf16 a[60:63], a[188:191], a[196:199], a[60:63]\n"
      "v_mfma_f32_16x16x32_bf16 a[92:95], a[188:191], a[200:203], a[92:95]\n"
      "v_mfma_f32_16x16x32_bf16 a[124:127], a[188:191], a[204:207], a[124:127]\n"
      "s_waitcnt lgkmcnt(0)\n"
      "v_mfma_f32_16x16x32_bf16 v[0:3], a[128:131], a[208:211], v[0:3]\n"
      "ds_read_b64_tr_b16 a[160:161], v212 offset:57344\n"
      "ds_read_b64_tr_b16 a[162:163], v212 offset:58368\n"
      "v_mfma_f32_16x16x32_bf16 v[32:35], a[128:131], a[212:215], v[32:35]\n"
      "ds_read_b64_tr_b16 a[164:165], v212 offset:57352\n"
      "ds_read_b64_tr_b16 a[166:167], v212 offset:58376\n"
      "v_mfma_f32_16x16x32_bf16 v[64:67], a[128:131], a[216:219], v[64:67]\n"
      "s_add_u32 m0, s78, 65536\n"
      "s_mov_b32 s83, 3145728\n"
      "v_mfma_f32_16x16x32_bf16 v[96:99], a[128:131], a[220:223], v[96:99]\n"
      "ds_read_b64_tr_b16 a[168:169], v213 offset:57344\n"
      "ds_read_b64_tr_b16 a[170:171], v213 offset:58368\n"
      "v_mfma_f32_16x16x32_bf16 v[4:7], a[132:135], a[208:211], v[4:7]\n"
      "ds_read_b64_tr_b16 a[172:173], v213 offset:57352\n"
      "ds_read_b64_tr_b16 a[174:175], v213 offset:58376\n"
      "v_mfma_f32_16x16x32_bf16 v[36:39], a[132:135], a[212:215], v[36:39]\n"
      "buffer_load_dwordx4 v220, s[52:55], s83 offen offset:0 nt lds\n"
      "v_mfma_f32_16x16x32_bf16 v[68:71], a[132:135], a[216:219], v[68:71]\n"
      "ds_read_b64_tr_b16 a[176:177], v214 offset:57344\n"
      "v_mfma_f32_16x16x32_bf16 v[100:103], a[132:135], a[220:223], v[100:103]\n"
      "ds_read_b64_tr_b16 a[178:179], v214 offset:58368\n"
      "v_mfma_f32_16x16x32_bf16 v[8:11], a[136:139], a[208:211], v[8:11]\n"
      "buffer_load_dwordx4 v221, s[52:55], s83 offen offset:1024 nt lds\n"
      "v_mfma_f32_16x16x32_bf16 v[40:43], a[136:139], a[212:215], v[40:43]\n"
      "ds_read_b64_tr_b16 a[180:181], v214 offset:57352\n"
      "v_mfma_f32_16x16x32_bf16 v[72:75], a[136:139], a[216:219], v[72:75]\n"
      "ds_read_b64_tr_b16 a[182:183], v214 offset:58376\n"
      "v_mfma_f32_16x16x32_bf16 v[104:107], a[136:139], a[220:223], v[104:107]\n"
      "buffer_load_dwordx4 v222, s[52:55], s83 offen offset:2048 nt lds\n"
      "v_mfma_f32_16x16x32_bf16 v[12:15], a[140:143], a[208:211], v[12:15]\n"
      "ds_read_b64_tr_b16 a[184:185], v215 offset:57344\n"
      "v_mfma_f32_16x16x32_bf16 v[44:47], a[140:143], a[212:215], v[44:47]\n"
      "ds_read_b64_tr_b16 a[186:187], v215 offset:58368\n"
      "v_mfma_f32_16x16x32_bf16 v[76:79], a[140:143], a[216:219], v[76:79]\n"
      "buffer_load_dwordx4 v223, s[52:55], s83 offen offset:3072 nt lds\n"
      "v_mfma_f32_16x16x32_bf16 v[108:111], a[140:143], a[220:223], v[108:111]\n"
      "ds_read_b64_tr_b16 a[188:189], v215 offset:57352\n"
      "v_mfma_f32_16x16x32_bf16 v[16:19], a[144:147], a[208:211], v[16:19]\n"
      "ds_read_b64_tr_b16 a[190:191], v215 offset:58376\n"
      "v_mfma_f32_16x16x32_bf16 v[48:51], a[144:147], a[212:215], v[48:51]\n"
      "v_mfma_f32_16x16x32_bf16 v[80:83], a[144:147], a[216:219], v[80:83]\n"
      "v_mfma_f32_16x16x32_bf16 v[112:115], a[144:147], a[220:223], v[112:115]\n"
      "v_mfma_f32_16x16x32_bf16 v[20:23], a[148:151], a[208:211], v[20:23]\n"
      "v_mfma_f32_16x16x32_bf16 v[52:55], a[148:151], a[212:215], v[52:55]\n"
      "v_mfma_f32_16x16x32_bf16 v[84:87], a[148:151], a[216:219], v[84:87]\n"
      "v_mfma_f32_16x16x32_bf16 v[116:119], a[148:151], a[220:223], v[116:119]\n"
      "v_mfma_f32_16x16x32_bf16 v[24:27], a[152:155], a[208:211], v[24:27]\n"
      "v_mfma_f32_16x16x32_bf16 v[56:59], a[152:155], a[212:215], v[56:59]\n"
      "v_mfma_f32_16x16x32_bf16 v[88:91], a[152:155], a[216:219], v[88:91]\n"
      "v_mfma_f32_16x16x32_bf16 v[120:123], a[152:155], a[220:223], v[120:123]\n"
      "v_mfma_f32_16x16x32_bf16 v[28:31], a[156:159], a[208:211], v[28:31]\n"
      "v_mfma_f32_16x16x32_bf16 v[60:63], a[156:159], a[212:215], v[60:63]\n"
      "v_mfma_f32_16x16x32_bf16 v[92:95], a[156:159], a[216:219], v[92:95]\n"
      "v_mfma_f32_16x16x32_bf16 v[124:127], a[156:159], a[220:223], v[124:127]\n"
      "s_waitcnt vmcnt(16) lgkmcnt(0)\n"
      "s_barrier\n"
      "s_cmp_eq_u32 s80, 0\n"
      "s_cbranch_scc1 NONCE_%=\n"
      "NONCEB_%=:\n"
      "v_mfma_f32_16x16x32_bf16 a[0:3], a[160:163], a[208:211], a[0:3]\n"
      "ds_read_b128 a[192:195], v218 offset:0\n"
      "ds_read_b128 a[196:199], v218 offset:2048\n"
      "v_mfma_f32_16x16x32_bf16 a[32:35], a[160:163], a[212:215], a[32:35]\n"
      "ds_read_b128 a[200:203], v218 offset:4096\n"
      "ds_read_b128 a[204:207], v218 offset:6144\n"
      "v_mfma_f32_16x16x32_bf16 a[64:67], a[160:163], a[216:219], a[64:67]\n"
      "s_add_u32 m0, s79, 0\n"
      "s_mov_b32 s83, 896\n"
      "v_mfma_f32_16x16x32_bf16 a[96:99], a[160:163], a[220:223], a[96:99]\n"
      "ds_read_b64_tr_b16 a[128:129], v212 offset:0\n"
      "ds_read_b64_tr_b16 a[130:131], v212 offset:1024\n"
      "v_mfma_f32_16x16x32_bf16 a[4:7], a[164:167], a[208:211], a[4:7]\n"
      "ds_read_b64_tr_b16 a[132:133], v212 offset:8\n"
      "ds_read_b64_tr_b16 a[134:135], v212 offset:1032\n"
      "v_mfma_f32_16x16x32_bf16 a[36:39], a[164:167], a[212:215], a[36:39]\n"
      "buffer_load_dwordx4 v224, s[48:51], s83 offen offset:0 nt lds\n"
      "v_mfma_f32_16x16x32_bf16 a[68:71], a[164:167], a[216:219], a[68:71]\n"
      "ds_read_b64_tr_b16 a[136:137], v213 offset:0\n"
      "v_mfma_f32_16x16x32_bf16 a[100:103], a[164:167], a[220:223], a[100:103]\n"
      "ds_read_b64_tr_b16 a[138:139], v213 offset:1024\n"
      "v_mfma_f32_16x16x32_bf16 a[8:11], a[168:171], a[208:211], a[8:11]\n"
      "buffer_load_dwordx4 v225, s[48:51], s83 offen offset:1024 nt lds\n"
      "v_mfma_f32_16x16x32_bf16 a[40:43], a[168:171], a[212:215], a[40:43]\n"
      "ds_read_b64_tr_b16 a[140:141], v213 offset:8\n"
      "v_mfma_f32_16x16x32_bf16 a[72:75], a[168:171], a[216:219], a[72:75]\n"
      "ds_read_b64_tr_b16 a[142:143], v213 offset:1032\n"
      "v_mfma_f32_16x16x32_bf16 a[104:107], a[168:171], a[220:223], a[104:107]\n"
      "buffer_load_dwordx4 v226, s[48:51], s83 offen offset:2048 nt lds\n"
      "v_mfma_f32_16x16x32_bf16 a[12:15], a[172:175], a[208:211], a[12:15]\n"
      "ds_read_b64_tr_b16 a[144:145], v214 offset:0\n"
      "v_mfma_f32_16x16x32_bf16 a[44:47], a[172:175], a[212:215], a[44:47]\n"
      "ds_read_b64_tr_b16 a[146:147], v214 offset:1024\n"
      "v_mfma_f32_16x16x32_bf16 a[76:79], a[172:175], a[216:219], a[76:79]\n"
      "buffer_load_dwordx4 v227, s[48:51], s83 offen offset:3072 nt lds\n"
      "v_mfma_f32_16x16x32_bf16 a[108:111], a[172:175], a[220:223], a[108:111]\n"
      "ds_read_b64_tr_b16 a[148:149], v214 offset:8\n"
      "v_mfma_f32_16x16x32_bf16 a[16:19], a[176:179], a[208:211], a[16:19]\n"
      "ds_read_b64_tr_b16 a[150:151], v214 offset:1032\n"
      "v_mfma_f32_16x16x32_bf16 a[48:51], a[176:179], a[212:215], a[48:51]\n"
      "ds_read_b64_tr_b16 a[152:153], v215 offset:0\n"
      "v_mfma_f32_16x16x32_bf16 a[80:83], a[176:179], a[216:219], a[80:83]\n"
      "ds_read_b64_tr_b16 a[154:155], v215 offset:1024\n"
      "v_mfma_f32_16x16x32_bf16 a[112:115], a[176:179], a[220:223], a[112:115]\n"
      "ds_read_b64_tr_b16 a[156:157], v215 offset:8\n"
      "v_mfma_f32_16x16x32_bf16 a[20:23], a[180:183], a[208:211], a[20:23]\n"
      "ds_read_b64_tr_b16 a[158:159], v215 offset:1032\n"
      "v_mfma_f32_16x16x32_bf16 a[52:55], a[180:183], a[212:215], a[52:55]\n"
      "v_mfma_f32_16x16x32_bf16 a[84:87], a[180:183], a[216:219], a[84:87]\n"
      "v_mfma_f32_16x16x32_bf16 a[116:119], a[180:183], a[220:223], a[116:119]\n"
      "v_mfma_f32_16x16x32_bf16 a[24:27], a[184:187], a[208:211], a[24:27]\n"
      "v_mfma_f32_16x16x32_bf16 a[56:59], a[184:187], a[212:215], a[56:59]\n"
      "v_mfma_f32_16x16x32_bf16 a[88:91], a[184:187], a[216:219], a[88:91]\n"
      "v_mfma_f32_16x16x32_bf16 a[120:123], a[184:187], a[220:223], a[120:123]\n"
      "v_mfma_f32_16x16x32_bf16 a[28:31], a[188:191], a[208:211], a[28:31]\n"
      "v_mfma_f32_16x16x32_bf16 a[60:63], a[188:191], a[212:215], a[60:63]\n"
      "v_mfma_f32_16x16x32_bf16 a[92:95], a[188:191], a[216:219], a[92:95]\n"
      "v_mfma_f32_16x16x32_bf16 a[124:127], a[188:191], a[220:223], a[124:127]\n"
      "s_waitcnt lgkmcnt(0)\n"
      "v_mfma_f32_16x16x32_bf16 v[0:3], a[128:131], a[192:195], v[0:3]\n"
      "ds_read_b64_tr_b16 a[160:161], v212 offset:8192\n"
      "ds_read_b64_tr_b16 a[162:163], v212 offset:9216\n"
      "v_mfma_f32_16x16x32_bf16 v[32:35], a[128:131], a[196:199], v[32:35]\n"
      "ds_read_b64_tr_b16 a[164:165], v212 offset:8200\n"
      "ds_read_b64_tr_b16 a[166:167], v212 offset:9224\n"
      "v_mfma_f32_16x16x32_bf16 v[64:67], a[128:131], a[200:203], v[64:67]\n"
      "s_add_u32 m0, s78, 81920\n"
      "s_mov_b32 s83, 3407872\n"
      "v_mfma_f32_16x16x32_bf16 v[96:99], a[128:131], a[204:207], v[96:99]\n"
      "ds_read_b64_tr_b16 a[168:169], v213 offset:8192\n"
      "ds_read_b64_tr_b16 a[170:171], v213 offset:9216\n"
      "v_mfma_f32_16x16x32_bf16 v[4:7], a[132:135], a[192:195], v[4:7]\n"
      "ds_read_b64_tr_b16 a[172:173], v213 offset:8200\n"
      "ds_read_b64_tr_b16 a[174:175], v213 offset:9224\n"
      "v_mfma_f32_16x16x32_bf16 v[36:39], a[132:135], a[196:199], v[36:39]\n"
      "buffer_load_dwordx4 v220, s[52:55], s83 offen offset:0 nt lds\n"
      "v_mfma_f32_16x16x32_bf16 v[68:71], a[132:135], a[200:203], v[68:71]\n"
      "ds_read_b64_tr_b16 a[176:177], v214 offset:8192\n"
      "v_mfma_f32_16x16x32_bf16 v[100:103], a[132:135], a[204:207], v[100:103]\n"
      "ds_read_b64_tr_b16 a[178:179], v214 offset:9216\n"
      "v_mfma_f32_16x16x32_bf16 v[8:11], a[136:139], a[192:195], v[8:11]\n"
      "buffer_load_dwordx4 v221, s[52:55], s83 offen offset:1024 nt lds\n"
      "v_mfma_f32_16x16x32_bf16 v[40:43], a[136:139], a[196:199], v[40:43]\n"
      "ds_read_b64_tr_b16 a[180:181], v214 offset:8200\n"
      "v_mfma_f32_16x16x32_bf16 v[72:75], a[136:139], a[200:203], v[72:75]\n"
      "ds_read_b64_tr_b16 a[182:183], v214 offset:9224\n"
      "v_mfma_f32_16x16x32_bf16 v[104:107], a[136:139], a[204:207], v[104:107]\n"
      "buffer_load_dwordx4 v222, s[52:55], s83 offen offset:2048 nt lds\n"
      "v_mfma_f32_16x16x32_bf16 v[12:15], a[140:143], a[192:195], v[12:15]\n"
      "ds_read_b64_tr_b16 a[184:185], v215 offset:8192\n"
      "v_mfma_f32_16x16x32_bf16 v[44:47], a[140:143], a[196:199], v[44:47]\n"
      "ds_read_b64_tr_b16 a[186:187], v215 offset:9216\n"
      "v_mfma_f32_16x16x32_bf16 v[76:79], a[140:143], a[200:203], v[76:79]\n"
      "buffer_load_dwordx4 v223, s[52:55], s83 offen offset:3072 nt lds\n"
      "v_mfma_f32_16x16x32_bf16 v[108:111], a[140:143], a[204:207], v[108:111]\n"
      "ds_read_b64_tr_b16 a[188:189], v215 offset:8200\n"
      "v_mfma_f32_16x16x32_bf16 v[16:19], a[144:147], a[192:195], v[16:19]\n"
      "ds_read_b64_tr_b16 a[190:191], v215 offset:9224\n"
      "v_mfma_f32_16x16x32_bf16 v[48:51], a[144:147], a[196:199], v[48:51]\n"
      "v_mfma_f32_16x16x32_bf16 v[80:83], a[144:147], a[200:203], v[80:83]\n"
      "v_mfma_f32_16x16x32_bf16 v[112:115], a[144:147], a[204:207], v[112:115]\n"
      "v_mfma_f32_16x16x32_bf16 v[20:23], a[148:151], a[192:195], v[20:23]\n"
      "v_mfma_f32_16x16x32_bf16 v[52:55], a[148:151], a[196:199], v[52:55]\n"
      "v_mfma_f32_16x16x32_bf16 v[84:87], a[148:151], a[200:203], v[84:87]\n"
      "v_mfma_f32_16x16x32_bf16 v[116:119], a[148:151], a[204:207], v[116:119]\n"
      "v_mfma_f32_16x16x32_bf16 v[24:27], a[152:155], a[192:195], v[24:27]\n"
      "v_mfma_f32_16x16x32_bf16 v[56:59], a[152:155], a[196:199], v[56:59]\n"
      "v_mfma_f32_16x16x32_bf16 v[88:91], a[152:155], a[200:203], v[88:91]\n"
      "v_mfma_f32_16x16x32_bf16 v[120:123], a[152:155], a[204:207], v[120:123]\n"
      "v_mfma_f32_16x16x32_bf16 v[28:31], a[156:159], a[192:195], v[28:31]\n"
      "v_mfma_f32_16x16x32_bf16 v[60:63], a[156:159], a[196:199], v[60:63]\n"
      "v_mfma_f32_16x16x32_bf16 v[92:95], a[156:159], a[200:203], v[92:95]\n"
      "v_mfma_f32_16x16x32_bf16 v[124:127], a[156:159], a[204:207], v[124:127]\n"
      "s_waitcnt vmcnt(16) lgkmcnt(0)\n"
      "s_barrier\n"
      "v_mfma_f32_16x16x32_bf16 a[0:3], a[160:163], a[192:195], a[0:3]\n"
      "ds_read_b128 a[208:211], v219 offset:0\n"
      "ds_read_b128 a[212:215], v219 offset:2048\n"
      "v_mfma_f32_16x16x32_bf16 a[32:35], a[160:163], a[196:199], a[32:35]\n"
      "ds_read_b128 a[216:219], v219 offset:4096\n"
      "ds_read_b128 a[220:223], v219 offset:6144\n"
      "v_mfma_f32_16x16x32_bf16 a[64:67], a[160:163], a[200:203], a[64:67]\n"
      "s_add_u32 m0, s79, 4096\n"
      "s_mov_b32 s83, 41856\n"
      "v_mfma_f32_16x16x32_bf16 a[96:99], a[160:163], a[204:207], a[96:99]\n"
      "ds_read_b64_tr_b16 a[128:129], v212 offset:16384\n"
      "ds_read_b64_tr_b16 a[130:131], v212 offset:17408\n"
      "v_mfma_f32_16x16x32_bf16 a[4:7], a[164:167], a[192:195], a[4:7]\n"
      "ds_read_b64_tr_b16 a[132:133], v212 offset:16392\n"
      "ds_read_b64_tr_b16 a[134:135], v212 offset:17416\n"
      "v_mfma_f32_16x16x32_bf16 a[36:39], a[164:167], a[196:199], a[36:39]\n"
      "buffer_load_dwordx4 v224, s[48:51], s83 offen offset:0 nt lds\n"
      "v_mfma_f32_16x16x32_bf16 a[68:71], a[164:167], a[200:203], a[68:71]\n"
      "ds_read_b64_tr_b16 a[136:137], v213 offset:16384\n"
      "v_mfma_f32_16x16x32_bf16 a[100:103], a[164:167], a[204:207], a[100:103]\n"
      "ds_read_b64_tr_b16 a[138:139], v213 offset:17408\n"
      "v_mfma_f32_16x16x32_bf16 a[8:11], a[168:171], a[192:195], a[8:11]\n"
      "buffer_load_dwordx4 v225, s[48:51], s83 offen offset:1024 nt lds\n"
      "v_mfma_f32_16x16x32_bf16 a[40:43], a[168:171], a[196:199], a[40:43]\n"
      "ds_read_b64_tr_b16 a[140:141], v213 offset:16392\n"
      "v_mfma_f32_16x16x32_bf16 a[72:75], a[168:171], a[200:203], a[72:75]\n"
      "ds_read_b64_tr_b16 a[142:143], v213 offset:17416\n"
      "v_mfma_f32_16x16x32_bf16 a[104:107], a[168:171], a[204:207], a[104:107]\n"
      "buffer_load_dwordx4 v226, s[48:51], s83 offen offset:2048 nt lds\n"
      "v_mfma_f32_16x16x32_bf16 a[12:15], a[172:175], a[192:195], a[12:15]\n"
      "ds_read_b64_tr_b16 a[144:145], v214 offset:16384\n"
      "v_mfma_f32_16x16x32_bf16 a[44:47], a[172:175], a[196:199], a[44:47]\n"
      "ds_read_b64_tr_b16 a[146:147], v214 offset:17408\n"
      "v_mfma_f32_16x16x32_bf16 a[76:79], a[172:175], a[200:203], a[76:79]\n"
      "buffer_load_dwordx4 v227, s[48:51], s83 offen offset:3072 nt lds\n"
      "v_mfma_f32_16x16x32_bf16 a[108:111], a[172:175], a[204:207], a[108:111]\n"
      "ds_read_b64_tr_b16 a[148:149], v214 offset:16392\n"
      "v_mfma_f32_16x16x32_bf16 a[16:19], a[176:179], a[192:195], a[16:19]\n"
      "ds_read_b64_tr_b16 a[150:151], v214 offset:17416\n"
      "v_mfma_f32_16x16x32_bf16 a[48:51], a[176:179], a[196:199], a[48:51]\n"
      "ds_read_b64_tr_b16 a[152:153], v215 offset:16384\n"
      "v_mfma_f32_16x16x32_bf16 a[80:83], a[176:179], a[200:203], a[80:83]\n"
      "ds_read_b64_tr_b16 a[154:155], v215 offset:17408\n"
      "v_mfma_f32_16x16x32_bf16 a[112:115], a[176:179], a[204:207], a[112:115]\n"
      "ds_read_b64_tr_b16 a[156:157], v215 offset:16392\n"
      "v_mfma_f32_16x16x32_bf16 a[20:23], a[180:183], a[192:195], a[20:23]\n"
      "ds_read_b64_tr_b16 a[158:159], v215 offset:17416\n"
      "v_mfma_f32_16x16x32_bf16 a[52:55], a[180:183], a[196:199], a[52:55]\n"
      "v_mfma_f32_16x16x32_bf16 a[84:87], a[180:183], a[200:203], a[84:87]\n"
      "v_mfma_f32_16x16x32_bf16 a[116:119], a[180:183], a[204:207], a[116:119]\n"
      "v_mfma_f32_16x16x32_bf16 a[24:27], a[184:187], a[192:195], a[24:27]\n"
      "v_mfma_f32_16x16x32_bf16 a[56:59], a[184:187], a[196:199], a[56:59]\n"
      "v_mfma_f32_16x16x32_bf16 a[88:91], a[184:187], a[200:203], a[88:91]\n"
      "v_mfma_f32_16x16x32_bf16 a[120:123], a[184:187], a[204:207], a[120:123]\n"
      "v_mfma_f32_16x16x32_bf16 a[28:31], a[188:191], a[192:195], a[28:31]\n"
      "v_mfma_f32_16x16x32_bf16 a[60:63], a[188:191], a[196:199], a[60:63]\n"
      "v_mfma_f32_16x16x32_bf16 a[92:95], a[188:191], a[200:203], a[92:95]\n"
      "v_mfma_f32_16x16x32_bf16 a[124:127], a[188:191], a[204:207], a[124:127]\n"
      "s_waitcnt lgkmcnt(0)\n"
      "v_mfma_f32_16x16x32_bf16 v[0:3], a[128:131], a[208:211], v[0:3]\n"
      "ds_read_b64_tr_b16 a[160:161], v212 offset:24576\n"
      "ds_read_b64_tr_b16 a[162:163], v212 offset:25600\n"
      "v_mfma_f32_16x16x32_bf16 v[32:35], a[128:131], a[212:215], v[32:35]\n"
      "ds_read_b64_tr_b16 a[164:165], v212 offset:24584\n"
      "ds_read_b64_tr_b16 a[166:167], v212 offset:25608\n"
      "v_mfma_f32_16x16x32_bf16 v[64:67], a[128:131], a[216:219], v[64:67]\n"
      "s_add_u32 m0, s78, 32768\n"
      "s_mov_b32 s83, 3670016\n"
      "v_mfma_f32_16x16x32_bf16 v[96:99], a[128:131], a[220:223], v[96:99]\n"
      "ds_read_b64_tr_b16 a[168:169], v213 offset:24576\n"
      "ds_read_b64_tr_b16 a[170:171], v213 offset:25600\n"
      "v_mfma_f32_16x16x32_bf16 v[4:7], a[132:135], a[208:211], v[4:7]\n"
      "ds_read_b64_tr_b16 a[172:173], v213 offset:24584\n"
      "ds_read_b64_tr_b16 a[174:175], v213 offset:25608\n"
      "v_mfma_f32_16x16x32_bf16 v[36:39], a[132:135], a[212:215], v[36:39]\n"
      "buffer_load_dwordx4 v220, s[52:55], s83 offen offset:0 nt lds\n"
      "v_mfma_f32_16x16x32_bf16 v[68:71], a[132:135], a[216:219], v[68:71]\n"
      "ds_read_b64_tr_b16 a[176:177], v214 offset:24576\n"
      "v_mfma_f32_16x16x32_bf16 v[100:103], a[132:135], a[220:223], v[100:103]\n"
      "ds_read_b64_tr_b16 a[178:179], v214 offset:25600\n"
      "v_mfma_f32_16x16x32_bf16 v[8:11], a[136:139], a[208:211], v[8:11]\n"
      "buffer_load_dwordx4 v221, s[52:55], s83 offen offset:1024 nt lds\n"
      "v_mfma_f32_16x16x32_bf16 v[40:43], a[136:139], a[212:215], v[40:43]\n"
      "ds_read_b64_tr_b16 a[180:181], v214 offset:24584\n"
      "v_mfma_f32_16x16x32_bf16 v[72:75], a[136:139], a[216:219], v[72:75]\n"
      "ds_read_b64_tr_b16 a[182:183], v214 offset:25608\n"
      "v_mfma_f32_16x16x32_bf16 v[104:107], a[136:139], a[220:223], v[104:107]\n"
      "buffer_load_dwordx4 v222, s[52:55], s83 offen offset:2048 nt lds\n"
      "v_mfma_f32_16x16x32_bf16 v[12:15], a[140:143], a[208:211], v[12:15]\n"
      "ds_read_b64_tr_b16 a[184:185], v215 offset:24576\n"
      "v_mfma_f32_16x16x32_bf16 v[44:47], a[140:143], a[212:215], v[44:47]\n"
      "ds_read_b64_tr_b16 a[186:187], v215 offset:25600\n"
      "v_mfma_f32_16x16x32_bf16 v[76:79], a[140:143], a[216:219], v[76:79]\n"
      "buffer_load_dwordx4 v223, s[52:55], s83 offen offset:3072 nt lds\n"
      "v_mfma_f32_16x16x32_bf16 v[108:111], a[140:143], a[220:223], v[108:111]\n"
      "ds_read_b64_tr_b16 a[188:189], v215 offset:24584\n"
      "v_mfma_f32_16x16x32_bf16 v[16:19], a[144:147], a[208:211], v[16:19]\n"
      "ds_read_b64_tr_b16 a[190:191], v215 offset:25608\n"
      "v_mfma_f32_16x16x32_bf16 v[48:51], a[144:147], a[212:215], v[48:51]\n"
      "v_mfma_f32_16x16x32_bf16 v[80:83], a[144:147], a[216:219], v[80:83]\n"
      "v_mfma_f32_16x16x32_bf16 v[112:115], a[144:147], a[220:223], v[112:115]\n"
      "v_mfma_f32_16x16x32_bf16 v[20:23], a[148:151], a[208:211], v[20:23]\n"
      "v_mfma_f32_16x16x32_bf16 v[52:55], a[148:151], a[212:215], v[52:55]\n"
      "v_mfma_f32_16x16x32_bf16 v[84:87], a[148:151], a[216:219], v[84:87]\n"
      "v_mfma_f32_16x16x32_bf16 v[116:119], a[148:151], a[220:223], v[116:119]\n"
      "v_mfma_f32_16x16x32_bf16 v[24:27], a[152:155], a[208:211], v[24:27]\n"
      "v_mfma_f32_16x16x32_bf16 v[56:59], a[152:155], a[212:215], v[56:59]\n"
      "v_mfma_f32_16x16x32_bf16 v[88:91], a[152:155], a[216:219], v[88:91]\n"
      "v_mfma_f32_16x16x32_bf16 v[120:123], a[152:155], a[220:223], v[120:123]\n"
      "v_mfma_f32_16x16x32_bf16 v[28:31], a[156:159], a[208:211], v[28:31]\n"
      "v_mfma_f32_16x16x32_bf16 v[60:63], a[156:159], a[212:215], v[60:63]\n"
      "v_mfma_f32_16x16x32_bf16 v[92:95], a[156:159], a[216:219], v[92:95]\n"
      "v_mfma_f32_16x16x32_bf16 v[124:127], a[156:159], a[220:223], v[124:127]\n"
      "s_waitcnt vmcnt(16) lgkmcnt(0)\n"
      "s_barrier\n"
      "v_mfma_f32_16x16x32_bf16 a[0:3], a[160:163], a[208:211], a[0:3]\n"
      "ds_read_b128 a[192:195], v218 offset:32768\n"
      "ds_read_b128 a[196:199], v218 offset:34816\n"
      "v_mfma_f32_16x16x32_bf16 a[32:35], a[160:163], a[212:215], a[32:35]\n"
      "ds_read_b128 a[200:203], v218 offset:36864\n"
      "ds_read_b128 a[204:207], v218 offset:38912\n"
      "v_mfma_f32_16x16x32_bf16 a[64:67], a[160:163], a[216:219], a[64:67]\n"
      "s_add_u32 m0, s79, 98304\n"
      "s_mov_b32 s83, 1024\n"
      "v_mfma_f32_16x16x32_bf16 a[96:99], a[160:163], a[220:223], a[96:99]\n"
      "ds_read_b64_tr_b16 a[128:129], v212 offset:32768\n"
      "ds_read_b64_tr_b16 a[130:131], v212 offset:33792\n"
      "v_mfma_f32_16x16x32_bf16 a[4:7], a[164:167], a[208:211], a[4:7]\n"
      "ds_read_b64_tr_b16 a[132:133], v212 offset:32776\n"
      "ds_read_b64_tr_b16 a[134:135], v212 offset:33800\n"
      "v_mfma_f32_16x16x32_bf16 a[36:39], a[164:167], a[212:215], a[36:39]\n"
      "buffer_load_dwordx4 v224, s[48:51], s83 offen offset:0 nt lds\n"
      "v_mfma_f32_16x16x32_bf16 a[68:71], a[164:167], a[216:219], a[68:71]\n"
      "ds_read_b64_tr_b16 a[136:137], v213 offset:32768\n"
      "v_mfma_f32_16x16x32_bf16 a[100:103], a[164:167], a[220:223], a[100:103]\n"
      "ds_read_b64_tr_b16 a[138:139], v213 offset:33792\n"
      "v_mfma_f32_16x16x32_bf16 a[8:11], a[168:171], a[208:211], a[8:11]\n"
      "buffer_load_dwordx4 v225, s[48:51], s83 offen offset:1024 nt lds\n"
      "v_mfma_f32_16x16x32_bf16 a[40:43], a[168:171], a[212:215], a[40:43]\n"
      "ds_read_b64_tr_b16 a[140:141], v213 offset:32776\n"
      "v_mfma_f32_16x16x32_bf16 a[72:75], a[168:171], a[216:219], a[72:75]\n"
      "ds_read_b64_tr_b16 a[142:143], v213 offset:33800\n"
      "v_mfma_f32_16x16x32_bf16 a[104:107], a[168:171], a[220:223], a[104:107]\n"
      "buffer_load_dwordx4 v226, s[48:51], s83 offen offset:2048 nt lds\n"
      "v_mfma_f32_16x16x32_bf16 a[12:15], a[172:175], a[208:211], a[12:15]\n"
      "ds_read_b64_tr_b16 a[144:145], v214 offset:32768\n"
      "v_mfma_f32_16x16x32_bf16 a[44:47], a[172:175], a[212:215], a[44:47]\n"
      "ds_read_b64_tr_b16 a[146:147], v214 offset:33792\n"
      "v_mfma_f32_16x16x32_bf16 a[76:79], a[172:175], a[216:219], a[76:79]\n"
      "buffer_load_dwordx4 v227, s[48:51], s83 offen offset:3072 nt lds\n"
      "v_mfma_f32_16x16x32_bf16 a[108:111], a[172:175], a[220:223], a[108:111]\n"
      "ds_read_b64_tr_b16 a[148:149], v214 offset:32776\n"
      "v_mfma_f32_16x16x32_bf16 a[16:19], a[176:179], a[208:211], a[16:19]\n"
      "ds_read_b64_tr_b16 a[150:151], v214 offset:33800\n"
      "v_mfma_f32_16x16x32_bf16 a[48:51], a[176:179], a[212:215], a[48:51]\n"
      "ds_read_b64_tr_b16 a[152:153], v215 offset:32768\n"
      "v_mfma_f32_16x16x32_bf16 a[80:83], a[176:179], a[216:219], a[80:83]\n"
      "ds_read_b64_tr_b16 a[154:155], v215 offset:33792\n"
      "v_mfma_f32_16x16x32_bf16 a[112:115], a[176:179], a[220:223], a[112:115]\n"
      "ds_read_b64_tr_b16 a[156:157], v215 offset:32776\n"
      "v_mfma_f32_16x16x32_bf16 a[20:23], a[180:183], a[208:211], a[20:23]\n"
      "ds_read_b64_tr_b16 a[158:159], v215 offset:33800\n"
      "v_mfma_f32_16x16x32_bf16 a[52:55], a[180:183], a[212:215], a[52:55]\n"
      "v_mfma_f32_16x16x32_bf16 a[84:87], a[180:183], a[216:219], a[84:87]\n"
      "v_mfma_f32_16x16x32_bf16 a[116:119], a[180:183], a[220:223], a[116:119]\n"
      "v_mfma_f32_16x16x32_bf16 a[24:27], a[184:187], a[208:211], a[24:27]\n"
      "v_mfma_f32_16x16x32_bf16 a[56:59], a[184:187], a[212:215], a[56:59]\n"
      "v_mfma_f32_16x16x32_bf16 a[88:91], a[184:187], a[216:219], a[88:91]\n"
      "v_mfma_f32_16x16x32_bf16 a[120:123], a[184:187], a[220:223], a[120:123]\n"
      "v_mfma_f32_16x16x32_bf16 a[28:31], a[188:191], a[208:211], a[28:31]\n"
      "v_mfma_f32_16x16x32_bf16 a[60:63], a[188:191], a[212:215], a[60:63]\n"
      "v_mfma_f32_16x16x32_bf16 a[92:95], a[188:191], a[216:219], a[92:95]\n"
      "v_mfma_f32_16x16x32_bf16 a[124:127], a[188:191], a[220:223], a[124:127]\n"
      "s_waitcnt lgkmcnt(0)\n"
      "v_mfma_f32_16x16x32_bf16 v[0:3], a[128:131], a[192:195], v[0:3]\n"
      "ds_read_b64_tr_b16 a[160:161], v212 offset:40960\n"
      "ds_read_b64_tr_b16 a[162:163], v212 offset:41984\n"
      "v_mfma_f32_16x16x32_bf16 v[32:35], a[128:131], a[196:199], v[32:35]\n"
      "ds_read_b64_tr_b16 a[164:165], v212 offset:40968\n"
      "ds_read_b64_tr_b16 a[166:167], v212 offset:41992\n"
      "v_mfma_f32_16x16x32_bf16 v[64:67], a[128:131], a[200:203], v[64:67]\n"
      "s_add_u32 m0, s78, 49152\n"
      "s_mov_b32 s83, 3932160\n"
      "v_mfma_f32_16x16x32_bf16 v[96:99], a[128:131], a[204:207], v[96:99]\n"
      "ds_read_b64_tr_b16 a[168:169], v213 offset:40960\n"
      "ds_read_b64_tr_b16 a[170:171], v213 offset:41984\n"
      "v_mfma_f32_16x16x32_bf16 v[4:7], a[132:135], a[192:195], v[4:7]\n"
      "ds_read_b64_tr_b16 a[172:173], v213 offset:40968\n"
      "ds_read_b64_tr_b16 a[174:175], v213 offset:41992\n"
      "v_mfma_f32_16x16x32_bf16 v[36:39], a[132:135], a[196:199], v[36:39]\n"
      "buffer_load_dwordx4 v220, s[52:55], s83 offen offset:0 nt lds\n"
      "v_mfma_f32_16x16x32_bf16 v[68:71], a[132:135], a[200:203], v[68:71]\n"
      "ds_read_b64_tr_b16 a[176:177], v214 offset:40960\n"
      "v_mfma_f32_16x16x32_bf16 v[100:103], a[132:135], a[204:207], v[100:103]\n"
      "ds_read_b64_tr_b16 a[178:179], v214 offset:41984\n"
      "v_mfma_f32_16x16x32_bf16 v[8:11], a[136:139], a[192:195], v[8:11]\n"
      "buffer_load_dwordx4 v221, s[52:55], s83 offen offset:1024 nt lds\n"
      "v_mfma_f32_16x16x32_bf16 v[40:43], a[136:139], a[196:199], v[40:43]\n"
      "ds_read_b64_tr_b16 a[180:181], v214 offset:40968\n"
      "v_mfma_f32_16x16x32_bf16 v[72:75], a[136:139], a[200:203], v[72:75]\n"
      "ds_read_b64_tr_b16 a[182:183], v214 offset:41992\n"
      "v_mfma_f32_16x16x32_bf16 v[104:107], a[136:139], a[204:207], v[104:107]\n"
      "buffer_load_dwordx4 v222, s[52:55], s83 offen offset:2048 nt lds\n"
      "v_mfma_f32_16x16x32_bf16 v[12:15], a[140:143], a[192:195], v[12:15]\n"
      "ds_read_b64_tr_b16 a[184:185], v215 offset:40960\n"
      "v_mfma_f32_16x16x32_bf16 v[44:47], a[140:143], a[196:199], v[44:47]\n"
      "ds_read_b64_tr_b16 a[186:187], v215 offset:41984\n"
      "v_mfma_f32_16x16x32_bf16 v[76:79], a[140:143], a[200:203], v[76:79]\n"
      "buffer_load_dwordx4 v223, s[52:55], s83 offen offset:3072 nt lds\n"
      "v_mfma_f32_16x16x32_bf16 v[108:111], a[140:143], a[204:207], v[108:111]\n"
      "ds_read_b64_tr_b16 a[188:189], v215 offset:40968\n"
      "v_mfma_f32_16x16x32_bf16 v[16:19], a[144:147], a[192:195], v[16:19]\n"
      "ds_read_b64_tr_b16 a[190:191], v215 offset:41992\n"
      "v_mfma_f32_16x16x32_bf16 v[48:51], a[144:147], a[196:199], v[48:51]\n"
      "v_mfma_f32_16x16x32_bf16 v[80:83], a[144:147], a[200:203], v[80:83]\n"
      "v_mfma_f32_16x16x32_bf16 v[112:115], a[144:147], a[204:207], v[112:115]\n"
      "v_mfma_f32_16x16x32_bf16 v[20:23], a[148:151], a[192:195], v[20:23]\n"
      "v_mfma_f32_16x16x32_bf16 v[52:55], a[148:151], a[196:199], v[52:55]\n"
      "v_mfma_f32_16x16x32_bf16 v[84:87], a[148:151], a[200:203], v[84:87]\n"
      "v_mfma_f32_16x16x32_bf16 v[116:119], a[148:151], a[204:207], v[116:119]\n"
      "v_mfma_f32_16x16x32_bf16 v[24:27], a[152:155], a[192:195], v[24:27]\n"
      "v_mfma_f32_16x16x32_bf16 v[56:59], a[152:155], a[196:199], v[56:59]\n"
      "v_mfma_f32_16x16x32_bf16 v[88:91], a[152:155], a[200:203], v[88:91]\n"
      "v_mfma_f32_16x16x32_bf16 v[120:123], a[152:155], a[204:207], v[120:123]\n"
      "v_mfma_f32_16x16x32_bf16 v[28:31], a[156:159], a[192:195], v[28:31]\n"
      "v_mfma_f32_16x16x32_bf16 v[60:63], a[156:159], a[196:199], v[60:63]\n"
      "v_mfma_f32_16x16x32_bf16 v[92:95], a[156:159], a[200:203], v[92:95]\n"
      "v_mfma_f32_16x16x32_bf16 v[124:127], a[156:159], a[204:207], v[124:127]\n"
      "s_waitcnt vmcnt(16) lgkmcnt(0)\n"
      "s_barrier\n"
      "v_mfma_f32_16x16x32_bf16 a[0:3], a[160:163], a[192:195], a[0:3]\n"
      "ds_read_b128 a[208:211], v219 offset:32768\n"
      "ds_read_b128 a[212:215], v219 offset:34816\n"
      "v_mfma_f32_16x16x32_bf16 a[32:35], a[160:163], a[196:199], a[32:35]\n"
      "ds_read_b128 a[216:219], v219 offset:36864\n"
      "ds_read_b128 a[220:223], v219 offset:38912\n"
      "v_mfma_f32_16x16x32_bf16 a[64:67], a[160:163], a[200:203], a[64:67]\n"
      "s_add_u32 m0, s79, 102400\n"
      "s_mov_b32 s83, 41984\n"
      "v_mfma_f32_16x16x32_bf16 a[96:99], a[160:163], a[204:207], a[96:99]\n"
      "ds_read_b64_tr_b16 a[128:129], v212 offset:49152\n"
      "ds_read_b64_tr_b16 a[130:131], v212 offset:50176\n"
      "v_mfma_f32_16x16x32_bf16 a[4:7], a[164:167], a[192:195], a[4:7]\n"
      "ds_read_b64_tr_b16 a[132:133], v212 offset:49160\n"
      "ds_read_b64_tr_b16 a[134:135], v212 offset:50184\n"
      "v_mfma_f32_16x16x32_bf16 a[36:39], a[164:167], a[196:199], a[36:39]\n"
      "buffer_load_dwordx4 v224, s[48:51], s83 offen offset:0 nt lds\n"
      "v_mfma_f32_16x16x32_bf16 a[68:71], a[164:167], a[200:203], a[68:71]\n"
      "ds_read_b64_tr_b16 a[136:137], v213 offset:49152\n"
      "v_mfma_f32_16x16x32_bf16 a[100:103], a[164:167], a[204:207], a[100:103]\n"
      "ds_read_b64_tr_b16 a[138:139], v213 offset:50176\n"
      "v_mfma_f32_16x16x32_bf16 a[8:11], a[168:171], a[192:195], a[8:11]\n"
      "buffer_load_dwordx4 v225, s[48:51], s83 offen offset:1024 nt lds\n"
      "v_mfma_f32_16x16x32_bf16 a[40:43], a[168:171], a[196:199], a[40:43]\n"
      "ds_read_b64_tr_b16 a[140:141], v213 offset:49160\n"
      "v_mfma_f32_16x16x32_bf16 a[72:75], a[168:171], a[200:203], a[72:75]\n"
      "ds_read_b64_tr_b16 a[142:143], v213 offset:50184\n"
      "v_mfma_f32_16x16x32_bf16 a[104:107], a[168:171], a[204:207], a[104:107]\n"
      "buffer_load_dwordx4 v226, s[48:51], s83 offen offset:2048 nt lds\n"
      "v_mfma_f32_16x16x32_bf16 a[12:15], a[172:175], a[192:195], a[12:15]\n"
      "ds_read_b64_tr_b16 a[144:145], v214 offset:49152\n"
      "v_mfma_f32_16x16x32_bf16 a[44:47], a[172:175], a[196:199], a[44:47]\n"
      "ds_read_b64_tr_b16 a[146:147], v214 offset:50176\n"
      "v_mfma_f32_16x16x32_bf16 a[76:79], a[172:175], a[200:203], a[76:79]\n"
      "buffer_load_dwordx4 v227, s[48:51], s83 offen offset:3072 nt lds\n"
      "v_mfma_f32_16x16x32_bf16 a[108:111], a[172:175], a[204:207], a[108:111]\n"
      "ds_read_b64_tr_b16 a[148:149], v214 offset:49160\n"
      "v_mfma_f32_16x16x32_bf16 a[16:19], a[176:179], a[192:195], a[16:19]\n"
      "ds_read_b64_tr_b16 a[150:151], v214 offset:50184\n"
      "v_mfma_f32_16x16x32_bf16 a[48:51], a[176:179], a[196:199], a[48:51]\n"
      "ds_read_b64_tr_b16 a[152:153], v215 offset:49152\n"
      "v_mfma_f32_16x16x32_bf16 a[80:83], a[176:179], a[200:203], a[80:83]\n"
      "ds_read_b64_tr_b16 a[154:155], v215 offset:50176\n"
      "v_mfma_f32_16x16x32_bf16 a[112:115], a[176:179], a[204:207], a[112:115]\n"
      "ds_read_b64_tr_b16 a[156:157], v215 offset:49160\n"
      "v_mfma_f32_16x16x32_bf16 a[20:23], a[180:183], a[192:195], a[20:23]\n"
      "ds_read_b64_tr_b16 a[158:159], v215 offset:50184\n"
      "v_mfma_f32_16x16x32_bf16 a[52:55], a[180:183], a[196:199], a[52:55]\n"
      "v_mfma_f32_16x16x32_bf16 a[84:87], a[180:183], a[200:203], a[84:87]\n"
      "v_mfma_f32_16x16x32_bf16 a[116:119], a[180:183], a[204:207], a[116:119]\n"
      "v_mfma_f32_16x16x32_bf16 a[24:27], a[184:187], a[192:195], a[24:27]\n"
      "v_mfma_f32_16x16x32_bf16 a[56:59], a[184:187], a[196:199], a[56:59]\n"
      "v_mfma_f32_16x16x32_bf16 a[88:91], a[184:187], a[200:203], a[88:91]\n"
      "v_mfma_f32_16x16x32_bf16 a[120:123], a[184:187], a[204:207], a[120:123]\n"
      "v_mfma_f32_16x16x32_bf16 a[28:31], a[188:191], a[192:195], a[28:31]\n"
      "v_mfma_f32_16x16x32_bf16 a[60:63], a[188:191], a[196:199], a[60:63]\n"
      "v_mfma_f32_16x16x32_bf16 a[92:95], a[188:191], a[200:203], a[92:95]\n"
      "v_mfma_f32_16x16x32_bf16 a[124:127], a[188:191], a[204:207], a[124:127]\n"
      "s_waitcnt lgkmcnt(0)\n"
      "v_mfma_f32_16x16x32_bf16 v[0:3], a[128:131], a[208:211], v[0:3]\n"
      "ds_read_b64_tr_b16 a[160:161], v212 offset:57344\n"
      "ds_read_b64_tr_b16 a[162:163], v212 offset:58368\n"
      "v_mfma_f32_16x16x32_bf16 v[32:35], a[128:131], a[212:215], v[32:35]\n"
      "ds_read_b64_tr_b16 a[164:165], v212 offset:57352\n"
      "ds_read_b64_tr_b16 a[166:167], v212 offset:58376\n"
      "v_mfma_f32_16x16x32_bf16 v[64:67], a[128:131], a[216:219], v[64:67]\n"
      "s_add_u32 m0, s78, 65536\n"
      "s_mov_b32 s83, 4194304\n"
      "v_mfma_f32_16x16x32_bf16 v[96:99], a[128:131], a[220:223], v[96:99]\n"
      "ds_read_b64_tr_b16 a[168:169], v213 offset:57344\n"
      "ds_read_b64_tr_b16 a[170:171], v213 offset:58368\n"
      "v_mfma_f32_16x16x32_bf16 v[4:7], a[132:135], a[208:211], v[4:7]\n"
      "ds_read_b64_tr_b16 a[172:173], v213 offset:57352\n"
      "ds_read_b64_tr_b16 a[174:175], v213 offset:58376\n"
      "v_mfma_f32_16x16x32_bf16 v[36:39], a[132:135], a[212:215], v[36:39]\n"
      "buffer_load_dwordx4 v220, s[52:55], s83 offen offset:0 nt lds\n"
      "v_mfma_f32_16x16x32_bf16 v[68:71], a[132:135], a[216:219], v[68:71]\n"
      "ds_read_b64_tr_b16 a[176:177], v214 offset:57344\n"
      "v_mfma_f32_16x16x32_bf16 v[100:103], a[132:135], a[220:223], v[100:103]\n"
      "ds_read_b64_tr_b16 a[178:179], v214 offset:58368\n"
      "v_mfma_f32_16x16x32_bf16 v[8:11], a[136:139], a[208:211], v[8:11]\n"
      "buffer_load_dwordx4 v221, s[52:55], s83 offen offset:1024 nt lds\n"
      "v_mfma_f32_16x16x32_bf16 v[40:43], a[136:139], a[212:215], v[40:43]\n"
      "ds_read_b64_tr_b16 a[180:181], v214 offset:57352\n"
      "v_mfma_f32_16x16x32_bf16 v[72:75], a[136:139], a[216:219], v[72:75]\n"
      "ds_read_b64_tr_b16 a[182:183], v214 offset:58376\n"
      "v_mfma_f32_16x16x32_bf16 v[104:107], a[136:139], a[220:223], v[104:107]\n"
      "buffer_load_dwordx4 v222, s[52:55], s83 offen offset:2048 nt lds\n"
      "v_mfma_f32_16x16x32_bf16 v[12:15], a[140:143], a[208:211], v[12:15]\n"
      "ds_read_b64_tr_b16 a[184:185], v215 offset:57344\n"
      "v_mfma_f32_16x16x32_bf16 v[44:47], a[140:143], a[212:215], v[44:47]\n"
      "ds_read_b64_tr_b16 a[186:187], v215 offset:58368\n"
      "v_mfma_f32_16x16x32_bf16 v[76:79], a[140:143], a[216:219], v[76:79]\n"
      "buffer_load_dwordx4 v223, s[52:55], s83 offen offset:3072 nt lds\n"
      "v_mfma_f32_16x16x32_bf16 v[108:111], a[140:143], a[220:223], v[108:111]\n"
      "ds_read_b64_tr_b16 a[188:189], v215 offset:57352\n"
      "v_mfma_f32_16x16x32_bf16 v[16:19], a[144:147], a[208:211], v[16:19]\n"
      "ds_read_b64_tr_b16 a[190:191], v215 offset:58376\n"
      "v_mfma_f32_16x16x32_bf16 v[48:51], a[144:147], a[212:215], v[48:51]\n"
      "v_mfma_f32_16x16x32_bf16 v[80:83], a[144:147], a[216:219], v[80:83]\n"
      "v_mfma_f32_16x16x32_bf16 v[112:115], a[144:147], a[220:223], v[112:115]\n"
      "v_mfma_f32_16x16x32_bf16 v[20:23], a[148:151], a[208:211], v[20:23]\n"
      "v_mfma_f32_16x16x32_bf16 v[52:55], a[148:151], a[212:215], v[52:55]\n"
      "v_mfma_f32_16x16x32_bf16 v[84:87], a[148:151], a[216:219], v[84:87]\n"
      "v_mfma_f32_16x16x32_bf16 v[116:119], a[148:151], a[220:223], v[116:119]\n"
      "v_mfma_f32_16x16x32_bf16 v[24:27], a[152:155], a[208:211], v[24:27]\n"
      "v_mfma_f32_16x16x32_bf16 v[56:59], a[152:155], a[212:215], v[56:59]\n"
      "v_mfma_f32_16x16x32_bf16 v[88:91], a[152:155], a[216:219], v[88:91]\n"
      "v_mfma_f32_16x16x32_bf16 v[120:123], a[152:155], a[220:223], v[120:123]\n"
      "v_mfma_f32_16x16x32_bf16 v[28:31], a[156:159], a[208:211], v[28:31]\n"
      "v_mfma_f32_16x16x32_bf16 v[60:63], a[156:159], a[212:215], v[60:63]\n"
      "v_mfma_f32_16x16x32_bf16 v[92:95], a[156:159], a[216:219], v[92:95]\n"
      "v_mfma_f32_16x16x32_bf16 v[124:127], a[156:159], a[220:223], v[124:127]\n"
      "s_waitcnt vmcnt(16) lgkmcnt(0)\n"
      "s_barrier\n"
      "v_mfma_f32_16x16x32_bf16 a[0:3], a[160:163], a[208:211], a[0:3]\n"
      "ds_read_b128 a[192:195], v216 offset:0\n"
      "ds_read_b128 a[196:199], v216 offset:2048\n"
      "v_mfma_f32_16x16x32_bf16 a[32:35], a[160:163], a[212:215], a[32:35]\n"
      "ds_read_b128 a[200:203], v216 offset:4096\n"
      "ds_read_b128 a[204:207], v216 offset:6144\n"
      "v_mfma_f32_16x16x32_bf16 a[64:67], a[160:163], a[216:219], a[64:67]\n"
      "s_add_u32 m0, s79, 131072\n"
      "s_mov_b32 s83, 1152\n"
      "v_mfma_f32_16x16x32_bf16 a[96:99], a[160:163], a[220:223], a[96:99]\n"
      "ds_read_b64_tr_b16 a[128:129], v212 offset:0\n"
      "ds_read_b64_tr_b16 a[130:131], v212 offset:1024\n"
      "v_mfma_f32_16x16x32_bf16 a[4:7], a[164:167], a[208:211], a[4:7]\n"
      "ds_read_b64_tr_b16 a[132:133], v212 offset:8\n"
      "ds_read_b64_tr_b16 a[134:135], v212 offset:1032\n"
      "v_mfma_f32_16x16x32_bf16 a[36:39], a[164:167], a[212:215], a[36:39]\n"
      "buffer_load_dwordx4 v224, s[48:51], s83 offen offset:0 nt lds\n"
      "v_mfma_f32_16x16x32_bf16 a[68:71], a[164:167], a[216:219], a[68:71]\n"
      "ds_read_b64_tr_b16 a[136:137], v213 offset:0\n"
      "v_mfma_f32_16x16x32_bf16 a[100:103], a[164:167], a[220:223], a[100:103]\n"
      "ds_read_b64_tr_b16 a[138:139], v213 offset:1024\n"
      "v_mfma_f32_16x16x32_bf16 a[8:11], a[168:171], a[208:211], a[8:11]\n"
      "buffer_load_dwordx4 v225, s[48:51], s83 offen offset:1024 nt lds\n"
      "v_mfma_f32_16x16x32_bf16 a[40:43], a[168:171], a[212:215], a[40:43]\n"
      "ds_read_b64_tr_b16 a[140:141], v213 offset:8\n"
      "v_mfma_f32_16x16x32_bf16 a[72:75], a[168:171], a[216:219], a[72:75]\n"
      "ds_read_b64_tr_b16 a[142:143], v213 offset:1032\n"
      "v_mfma_f32_16x16x32_bf16 a[104:107], a[168:171], a[220:223], a[104:107]\n"
      "buffer_load_dwordx4 v226, s[48:51], s83 offen offset:2048 nt lds\n"
      "v_mfma_f32_16x16x32_bf16 a[12:15], a[172:175], a[208:211], a[12:15]\n"
      "ds_read_b64_tr_b16 a[144:145], v214 offset:0\n"
      "v_mfma_f32_16x16x32_bf16 a[44:47], a[172:175], a[212:215], a[44:47]\n"
      "ds_read_b64_tr_b16 a[146:147], v214 offset:1024\n"
      "v_mfma_f32_16x16x32_bf16 a[76:79], a[172:175], a[216:219], a[76:79]\n"
      "buffer_load_dwordx4 v227, s[48:51], s83 offen offset:3072 nt lds\n"
      "v_mfma_f32_16x16x32_bf16 a[108:111], a[172:175], a[220:223], a[108:111]\n"
      "ds_read_b64_tr_b16 a[148:149], v214 offset:8\n"
      "v_mfma_f32_16x16x32_bf16 a[16:19], a[176:179], a[208:211], a[16:19]\n"
      "ds_read_b64_tr_b16 a[150:151], v214 offset:1032\n"
      "v_mfma_f32_16x16x32_bf16 a[48:51], a[176:179], a[212:215], a[48:51]\n"
      "ds_read_b64_tr_b16 a[152:153], v215 offset:0\n"
      "v_mfma_f32_16x16x32_bf16 a[80:83], a[176:179], a[216:219], a[80:83]\n"
      "ds_read_b64_tr_b16 a[154:155], v215 offset:1024\n"
      "v_mfma_f32_16x16x32_bf16 a[112:115], a[176:179], a[220:223], a[112:115]\n"
      "ds_read_b64_tr_b16 a[156:157], v215 offset:8\n"
      "v_mfma_f32_16x16x32_bf16 a[20:23], a[180:183], a[208:211], a[20:23]\n"
      "ds_read_b64_tr_b16 a[158:159], v215 offset:1032\n"
      "v_mfma_f32_16x16x32_bf16 a[52:55], a[180:183], a[212:215], a[52:55]\n"
      "v_mfma_f32_16x16x32_bf16 a[84:87], a[180:183], a[216:219], a[84:87]\n"
      "v_mfma_f32_16x16x32_bf16 a[116:119], a[180:183], a[220:223], a[116:119]\n"
      "v_mfma_f32_16x16x32_bf16 a[24:27], a[184:187], a[208:211], a[24:27]\n"
      "v_mfma_f32_16x16x32_bf16 a[56:59], a[184:187], a[212:215], a[56:59]\n"
      "v_mfma_f32_16x16x32_bf16 a[88:91], a[184:187], a[216:219], a[88:91]\n"
      "v_mfma_f32_16x16x32_bf16 a[120:123], a[184:187], a[220:223], a[120:123]\n"
      "v_mfma_f32_16x16x32_bf16 a[28:31], a[188:191], a[208:211], a[28:31]\n"
      "v_mfma_f32_16x16x32_bf16 a[60:63], a[188:191], a[212:215], a[60:63]\n"
      "v_mfma_f32_16x16x32_bf16 a[92:95], a[188:191], a[216:219], a[92:95]\n"
      "v_mfma_f32_16x16x32_bf16 a[124:127], a[188:191], a[220:223], a[124:127]\n"
      "s_waitcnt lgkmcnt(0)\n"
      "v_mfma_f32_16x16x32_bf16 v[0:3], a[128:131], a[192:195], v[0:3]\n"
      "ds_read_b64_tr_b16 a[160:161], v212 offset:8192\n"
      "ds_read_b64_tr_b16 a[162:163], v212 offset:9216\n"
      "v_mfma_f32_16x16x32_bf16 v[32:35], a[128:131], a[196:199], v[32:35]\n"
      "ds_read_b64_tr_b16 a[164:165], v212 offset:8200\n"
      "ds_read_b64_tr_b16 a[166:167], v212 offset:9224\n"
      "v_mfma_f32_16x16x32_bf16 v[64:67], a[128:131], a[200:203], v[64:67]\n"
      "s_add_u32 m0, s78, 81920\n"
      "s_mov_b32 s83, 4456448\n"
      "v_mfma_f32_16x16x32_bf16 v[96:99], a[128:131], a[204:207], v[96:99]\n"
      "ds_read_b64_tr_b16 a[168:169], v213 offset:8192\n"
      "ds_read_b64_tr_b16 a[170:171], v213 offset:9216\n"
      "v_mfma_f32_16x16x32_bf16 v[4:7], a[132:135], a[192:195], v[4:7]\n"
      "ds_read_b64_tr_b16 a[172:173], v213 offset:8200\n"
      "ds_read_b64_tr_b16 a[174:175], v213 offset:9224\n"
      "v_mfma_f32_16x16x32_bf16 v[36:39], a[132:135], a[196:199], v[36:39]\n"
      "buffer_load_dwordx4 v220, s[52:55], s83 offen offset:0 nt lds\n"
      "v_mfma_f32_16x16x32_bf16 v[68:71], a[132:135], a[200:203], v[68:71]\n"
      "ds_read_b64_tr_b16 a[176:177], v214 offset:8192\n"
      "v_mfma_f32_16x16x32_bf16 v[100:103], a[132:135], a[204:207], v[100:103]\n"
      "ds_read_b64_tr_b16 a[178:179], v214 offset:9216\n"
      "v_mfma_f32_16x16x32_bf16 v[8:11], a[136:139], a[192:195], v[8:11]\n"
      "buffer_load_dwordx4 v221, s[52:55], s83 offen offset:1024 nt lds\n"
      "v_mfma_f32_16x16x32_bf16 v[40:43], a[136:139], a[196:199], v[40:43]\n"
      "ds_read_b64_tr_b16 a[180:181], v214 offset:8200\n"
      "v_mfma_f32_16x16x32_bf16 v[72:75], a[136:139], a[200:203], v[72:75]\n"
      "ds_read_b64_tr_b16 a[182:183], v214 offset:9224\n"
      "v_mfma_f32_16x16x32_bf16 v[104:107], a[136:139], a[204:207], v[104:107]\n"
      "buffer_load_dwordx4 v222, s[52:55], s83 offen offset:2048 nt lds\n"
      "v_mfma_f32_16x16x32_bf16 v[12:15], a[140:143], a[192:195], v[12:15]\n"
      "ds_read_b64_tr_b16 a[184:185], v215 offset:8192\n"
      "v_mfma_f32_16x16x32_bf16 v[44:47], a[140:143], a[196:199], v[44:47]\n"
      "ds_read_b64_tr_b16 a[186:187], v215 offset:9216\n"
      "v_mfma_f32_16x16x32_bf16 v[76:79], a[140:143], a[200:203], v[76:79]\n"
      "buffer_load_dwordx4 v223, s[52:55], s83 offen offset:3072 nt lds\n"
      "v_mfma_f32_16x16x32_bf16 v[108:111], a[140:143], a[204:207], v[108:111]\n"
      "ds_read_b64_tr_b16 a[188:189], v215 offset:8200\n"
      "v_mfma_f32_16x16x32_bf16 v[16:19], a[144:147], a[192:195], v[16:19]\n"
      "ds_read_b64_tr_b16 a[190:191], v215 offset:9224\n"
      "v_mfma_f32_16x16x32_bf16 v[48:51], a[144:147], a[196:199], v[48:51]\n"
      "v_mfma_f32_16x16x32_bf16 v[80:83], a[144:147], a[200:203], v[80:83]\n"
      "v_mfma_f32_16x16x32_bf16 v[112:115], a[144:147], a[204:207], v[112:115]\n"
      "v_mfma_f32_16x16x32_bf16 v[20:23], a[148:151], a[192:195], v[20:23]\n"
      "v_mfma_f32_16x16x32_bf16 v[52:55], a[148:151], a[196:199], v[52:55]\n"
      "v_mfma_f32_16x16x32_bf16 v[84:87], a[148:151], a[200:203], v[84:87]\n"
      "v_mfma_f32_16x16x32_bf16 v[116:119], a[148:151], a[204:207], v[116:119]\n"
      "v_mfma_f32_16x16x32_bf16 v[24:27], a[152:155], a[192:195], v[24:27]\n"
      "v_mfma_f32_16x16x32_bf16 v[56:59], a[152:155], a[196:199], v[56:59]\n"
      "v_mfma_f32_16x16x32_bf16 v[88:91], a[152:155], a[200:203], v[88:91]\n"
      "v_mfma_f32_16x16x32_bf16 v[120:123], a[152:155], a[204:207], v[120:123]\n"
      "v_mfma_f32_16x16x32_bf16 v[28:31], a[156:159], a[192:195], v[28:31]\n"
      "v_mfma_f32_16x16x32_bf16 v[60:63], a[156:159], a[196:199], v[60:63]\n"
      "v_mfma_f32_16x16x32_bf16 v[92:95], a[156:159], a[200:203], v[92:95]\n"
      "v_mfma_f32_16x16x32_bf16 v[124:127], a[156:159], a[204:207], v[124:127]\n"
      "s_waitcnt vmcnt(16) lgkmcnt(0)\n"
      "s_barrier\n"
      "v_mfma_f32_16x16x32_bf16 a[0:3], a[160:163], a[192:195], a[0:3]\n"
      "ds_read_b128 a[208:211], v217 offset:0\n"
      "ds_read_b128 a[212:215], v217 offset:2048\n"
      "v_mfma_f32_16x16x32_bf16 a[32:35], a[160:163], a[196:199], a[32:35]\n"
      "ds_read_b128 a[216:219], v217 offset:4096\n"
      "ds_read_b128 a[220:223], v217 offset:6144\n"
      "v_mfma_f32_16x16x32_bf16 a[64:67], a[160:163], a[200:203], a[64:67]\n"
      "s_add_u32 m0, s79, 135168\n"
      "s_mov_b32 s83, 42112\n"
      "v_mfma_f32_16x16x32_bf16 a[96:99], a[160:163], a[204:207], a[96:99]\n"
      "ds_read_b64_tr_b16 a[128:129], v212 offset:16384\n"
      "ds_read_b64_tr_b16 a[130:131], v212 offset:17408\n"
      "v_mfma_f32_16x16x32_bf16 a[4:7], a[164:167], a[192:195], a[4:7]\n"
      "ds_read_b64_tr_b16 a[132:133], v212 offset:16392\n"
      "ds_read_b64_tr_b16 a[134:135], v212 offset:17416\n"
      "v_mfma_f32_16x16x32_bf16 a[36:39], a[164:167], a[196:199], a[36:39]\n"
      "buffer_load_dwordx4 v224, s[48:51], s83 offen offset:0 nt lds\n"
      "v_mfma_f32_16x16x32_bf16 a[68:71], a[164:167], a[200:203], a[68:71]\n"
      "ds_read_b64_tr_b16 a[136:137], v213 offset:16384\n"
      "v_mfma_f32_16x16x32_bf16 a[100:103], a[164:167], a[204:207], a[100:103]\n"
      "ds_read_b64_tr_b16 a[138:139], v213 offset:17408\n"
      "v_mfma_f32_16x16x32_bf16 a[8:11], a[168:171], a[192:195], a[8:11]\n"
      "buffer_load_dwordx4 v225, s[48:51], s83 offen offset:1024 nt lds\n"
      "v_mfma_f32_16x16x32_bf16 a[40:43], a[168:171], a[196:199], a[40:43]\n"
      "ds_read_b64_tr_b16 a[140:141], v213 offset:16392\n"
      "v_mfma_f32_16x16x32_bf16 a[72:75], a[168:171], a[200:203], a[72:75]\n"
      "ds_read_b64_tr_b16 a[142:143], v213 offset:17416\n"
      "v_mfma_f32_16x16x32_bf16 a[104:107], a[168:171], a[204:207], a[104:107]\n"
      "buffer_load_dwordx4 v226, s[48:51], s83 offen offset:2048 nt lds\n"
      "v_mfma_f32_16x16x32_bf16 a[12:15], a[172:175], a[192:195], a[12:15]\n"
      "ds_read_b64_tr_b16 a[144:145], v214 offset:16384\n"
      "v_mfma_f32_16x16x32_bf16 a[44:47], a[172:175], a[196:199], a[44:47]\n"
      "ds_read_b64_tr_b16 a[146:147], v214 offset:17408\n"
      "v_mfma_f32_16x16x32_bf16 a[76:79], a[172:175], a[200:203], a[76:79]\n"
      "buffer_load_dwordx4 v227, s[48:51], s83 offen offset:3072 nt lds\n"
      "v_mfma_f32_16x16x32_bf16 a[108:111], a[172:175], a[204:207], a[108:111]\n"
      "ds_read_b64_tr_b16 a[148:149], v214 offset:16392\n"
      "v_mfma_f32_16x16x32_bf16 a[16:19], a[176:179], a[192:195], a[16:19]\n"
      "ds_read_b64_tr_b16 a[150:151], v214 offset:17416\n"
      "v_mfma_f32_16x16x32_bf16 a[48:51], a[176:179], a[196:199], a[48:51]\n"
      "ds_read_b64_tr_b16 a[152:153], v215 offset:16384\n"
      "v_mfma_f32_16x16x32_bf16 a[80:83], a[176:179], a[200:203], a[80:83]\n"
      "ds_read_b64_tr_b16 a[154:155], v215 offset:17408\n"
      "v_mfma_f32_16x16x32_bf16 a[112:115], a[176:179], a[204:207], a[112:115]\n"
      "ds_read_b64_tr_b16 a[156:157], v215 offset:16392\n"
      "v_mfma_f32_16x16x32_bf16 a[20:23], a[180:183], a[192:195], a[20:23]\n"
      "ds_read_b64_tr_b16 a[158:159], v215 offset:17416\n"
      "v_mfma_f32_16x16x32_bf16 a[52:55], a[180:183], a[196:199], a[52:55]\n"
      "v_mfma_f32_16x16x32_bf16 a[84:87], a[180:183], a[200:203], a[84:87]\n"
      "v_mfma_f32_16x16x32_bf16 a[116:119], a[180:183], a[204:207], a[116:119]\n"
      "v_mfma_f32_16x16x32_bf16 a[24:27], a[184:187], a[192:195], a[24:27]\n"
      "v_mfma_f32_16x16x32_bf16 a[56:59], a[184:187], a[196:199], a[56:59]\n"
      "v_mfma_f32_16x16x32_bf16 a[88:91], a[184:187], a[200:203], a[88:91]\n"
      "v_mfma_f32_16x16x32_bf16 a[120:123], a[184:187], a[204:207], a[120:123]\n"
      "v_mfma_f32_16x16x32_bf16 a[28:31], a[188:191], a[192:195], a[28:31]\n"
      "v_mfma_f32_16x16x32_bf16 a[60:63], a[188:191], a[196:199], a[60:63]\n"
      "v_mfma_f32_16x16x32_bf16 a[92:95], a[188:191], a[200:203], a[92:95]\n"
      "v_mfma_f32_16x16x32_bf16 a[124:127], a[188:191], a[204:207], a[124:127]\n"
      "s_waitcnt lgkmcnt(0)\n"
      "v_mfma_f32_16x16x32_bf16 v[0:3], a[128:131], a[208:211], v[0:3]\n"
      "ds_read_b64_tr_b16 a[160:161], v212 offset:24576\n"
      "ds_read_b64_tr_b16 a[162:163], v212 offset:25600\n"
      "v_mfma_f32_16x16x32_bf16 v[32:35], a[128:131], a[212:215], v[32:35]\n"
      "ds_read_b64_tr_b16 a[164:165], v212 offset:24584\n"
      "ds_read_b64_tr_b16 a[166:167], v212 offset:25608\n"
      "v_mfma_f32_16x16x32_bf16 v[64:67], a[128:131], a[216:219], v[64:67]\n"
      "s_add_u32 m0, s78, 32768\n"
      "s_mov_b32 s83, 4718592\n"
      "v_mfma_f32_16x16x32_bf16 v[96:99], a[128:131], a[220:223], v[96:99]\n"
      "ds_read_b64_tr_b16 a[168:169], v213 offset:24576\n"
      "ds_read_b64_tr_b16 a[170:171], v213 offset:25600\n"
      "v_mfma_f32_16x16x32_bf16 v[4:7], a[132:135], a[208:211], v[4:7]\n"
      "ds_read_b64_tr_b16 a[172:173], v213 offset:24584\n"
      "ds_read_b64_tr_b16 a[174:175], v213 offset:25608\n"
      "v_mfma_f32_16x16x32_bf16 v[36:39], a[132:135], a[212:215], v[36:39]\n"
      "buffer_load_dwordx4 v220, s[52:55], s83 offen offset:0 nt lds\n"
      "v_mfma_f32_16x16x32_bf16 v[68:71], a[132:135], a[216:219], v[68:71]\n"
      "ds_read_b64_tr_b16 a[176:177], v214 offset:24576\n"
      "v_mfma_f32_16x16x32_bf16 v[100:103], a[132:135], a[220:223], v[100:103]\n"
      "ds_read_b64_tr_b16 a[178:179], v214 offset:25600\n"
      "v_mfma_f32_16x16x32_bf16 v[8:11], a[136:139], a[208:211], v[8:11]\n"
      "buffer_load_dwordx4 v221, s[52:55], s83 offen offset:1024 nt lds\n"
      "v_mfma_f32_16x16x32_bf16 v[40:43], a[136:139], a[212:215], v[40:43]\n"
      "ds_read_b64_tr_b16 a[180:181], v214 offset:24584\n"
      "v_mfma_f32_16x16x32_bf16 v[72:75], a[136:139], a[216:219], v[72:75]\n"
      "ds_read_b64_tr_b16 a[182:183], v214 offset:25608\n"
      "v_mfma_f32_16x16x32_bf16 v[104:107], a[136:139], a[220:223], v[104:107]\n"
      "buffer_load_dwordx4 v222, s[52:55], s83 offen offset:2048 nt lds\n"
      "v_mfma_f32_16x16x32_bf16 v[12:15], a[140:143], a[208:211], v[12:15]\n"
      "ds_read_b64_tr_b16 a[184:185], v215 offset:24576\n"
      "v_mfma_f32_16x16x32_bf16 v[44:47], a[140:143], a[212:215], v[44:47]\n"
      "ds_read_b64_tr_b16 a[186:187], v215 offset:25600\n"
      "v_mfma_f32_16x16x32_bf16 v[76:79], a[140:143], a[216:219], v[76:79]\n"
      "buffer_load_dwordx4 v223, s[52:55], s83 offen offset:3072 nt lds\n"
      "v_mfma_f32_16x16x32_bf16 v[108:111], a[140:143], a[220:223], v[108:111]\n"
      "ds_read_b64_tr_b16 a[188:189], v215 offset:24584\n"
      "v_mfma_f32_16x16x32_bf16 v[16:19], a[144:147], a[208:211], v[16:19]\n"
      "ds_read_b64_tr_b16 a[190:191], v215 offset:25608\n"
      "v_mfma_f32_16x16x32_bf16 v[48:51], a[144:147], a[212:215], v[48:51]\n"
      "v_mfma_f32_16x16x32_bf16 v[80:83], a[144:147], a[216:219], v[80:83]\n"
      "v_mfma_f32_16x16x32_bf16 v[112:115], a[144:147], a[220:223], v[112:115]\n"
      "v_mfma_f32_16x16x32_bf16 v[20:23], a[148:151], a[208:211], v[20:23]\n"
      "v_mfma_f32_16x16x32_bf16 v[52:55], a[148:151], a[212:215], v[52:55]\n"
      "v_mfma_f32_16x16x32_bf16 v[84:87], a[148:151], a[216:219], v[84:87]\n"
      "v_mfma_f32_16x16x32_bf16 v[116:119], a[148:151], a[220:223], v[116:119]\n"
      "v_mfma_f32_16x16x32_bf16 v[24:27], a[152:155], a[208:211], v[24:27]\n"
      "v_mfma_f32_16x16x32_bf16 v[56:59], a[152:155], a[212:215], v[56:59]\n"
      "v_mfma_f32_16x16x32_bf16 v[88:91], a[152:155], a[216:219], v[88:91]\n"
      "v_mfma_f32_16x16x32_bf16 v[120:123], a[152:155], a[220:223], v[120:123]\n"
      "v_mfma_f32_16x16x32_bf16 v[28:31], a[156:159], a[208:211], v[28:31]\n"
      "v_mfma_f32_16x16x32_bf16 v[60:63], a[156:159], a[212:215], v[60:63]\n"
      "v_mfma_f32_16x16x32_bf16 v[92:95], a[156:159], a[216:219], v[92:95]\n"
      "v_mfma_f32_16x16x32_bf16 v[124:127], a[156:159], a[220:223], v[124:127]\n"
      "s_waitcnt vmcnt(16) lgkmcnt(0)\n"
      "s_barrier\n"
      "v_mfma_f32_16x16x32_bf16 a[0:3], a[160:163], a[208:211], a[0:3]\n"
      "ds_read_b128 a[192:195], v218 offset:0\n"
      "ds_read_b128 a[196:199], v218 offset:2048\n"
      "v_mfma_f32_16x16x32_bf16 a[32:35], a[160:163], a[212:215], a[32:35]\n"
      "ds_read_b128 a[200:203], v218 offset:4096\n"
      "ds_read_b128 a[204:207], v218 offset:6144\n"
      "v_mfma_f32_16x16x32_bf16 a[64:67], a[160:163], a[216:219], a[64:67]\n"
      "s_add_u32 m0, s78, 49152\n"
      "s_mov_b32 s83, 4980736\n"
      "v_mfma_f32_16x16x32_bf16 a[96:99], a[160:163], a[220:223], a[96:99]\n"
      "ds_read_b64_tr_b16 a[128:129], v212 offset:32768\n"
      "ds_read_b64_tr_b16 a[130:131], v212 offset:33792\n"
      "v_mfma_f32_16x16x32_bf16 a[4:7], a[164:167], a[208:211], a[4:7]\n"
      "ds_read_b64_tr_b16 a[132:133], v212 offset:32776\n"
      "ds_read_b64_tr_b16 a[134:135], v212 offset:33800\n"
      "v_mfma_f32_16x16x32_bf16 a[36:39], a[164:167], a[212:215], a[36:39]\n"
      "buffer_load_dwordx4 v220, s[52:55], s83 offen offset:0 nt lds\n"
      "v_mfma_f32_16x16x32_bf16 a[68:71], a[164:167], a[216:219], a[68:71]\n"
      "ds_read_b64_tr_b16 a[136:137], v213 offset:32768\n"
      "v_mfma_f32_16x16x32_bf16 a[100:103], a[164:167], a[220:223], a[100:103]\n"
      "ds_read_b64_tr_b16 a[138:139], v213 offset:33792\n"
      "v_mfma_f32_16x16x32_bf16 a[8:11], a[168:171], a[208:211], a[8:11]\n"
      "buffer_load_dwordx4 v221, s[52:55], s83 offen offset:1024 nt lds\n"
      "v_mfma_f32_16x16x32_bf16 a[40:43], a[168:171], a[212:215], a[40:43]\n"
      "ds_read_b64_tr_b16 a[140:141], v213 offset:32776\n"
      "v_mfma_f32_16x16x32_bf16 a[72:75], a[168:171], a[216:219], a[72:75]\n"
      "ds_read_b64_tr_b16 a[142:143], v213 offset:33800\n"
      "v_mfma_f32_16x16x32_bf16 a[104:107], a[168:171], a[220:223], a[104:107]\n"
      "buffer_load_dwordx4 v222, s[52:55], s83 offen offset:2048 nt lds\n"
      "v_mfma_f32_16x16x32_bf16 a[12:15], a[172:175], a[208:211], a[12:15]\n"
      "ds_read_b64_tr_b16 a[144:145], v214 offset:32768\n"
      "v_mfma_f32_16x16x32_bf16 a[44:47], a[172:175], a[212:215], a[44:47]\n"
      "ds_read_b64_tr_b16 a[146:147], v214 offset:33792\n"
      "v_mfma_f32_16x16x32_bf16 a[76:79], a[172:175], a[216:219], a[76:79]\n"
      "buffer_load_dwordx4 v223, s[52:55], s83 offen offset:3072 nt lds\n"
      "v_mfma_f32_16x16x32_bf16 a[108:111], a[172:175], a[220:223], a[108:111]\n"
      "ds_read_b64_tr_b16 a[148:149], v214 offset:32776\n"
      "v_mfma_f32_16x16x32_bf16 a[16:19], a[176:179], a[208:211], a[16:19]\n"
      "ds_read_b64_tr_b16 a[150:151], v214 offset:33800\n"
      "v_mfma_f32_16x16x32_bf16 a[48:51], a[176:179], a[212:215], a[48:51]\n"
      "ds_read_b64_tr_b16 a[152:153], v215 offset:32768\n"
      "v_mfma_f32_16x16x32_bf16 a[80:83], a[176:179], a[216:219], a[80:83]\n"
      "ds_read_b64_tr_b16 a[154:155], v215 offset:33792\n"
      "v_mfma_f32_16x16x32_bf16 a[112:115], a[176:179], a[220:223], a[112:115]\n"
      "ds_read_b64_tr_b16 a[156:157], v215 offset:32776\n"
      "v_mfma_f32_16x16x32_bf16 a[20:23], a[180:183], a[208:211], a[20:23]\n"
      "ds_read_b64_tr_b16 a[158:159], v215 offset:33800\n"
      "v_mfma_f32_16x16x32_bf16 a[52:55], a[180:183], a[212:215], a[52:55]\n"
      "v_mfma_f32_16x16x32_bf16 a[84:87], a[180:183], a[216:219], a[84:87]\n"
      "v_mfma_f32_16x16x32_bf16 a[116:119], a[180:183], a[220:223], a[116:119]\n"
      "v_mfma_f32_16x16x32_bf16 a[24:27], a[184:187], a[208:211], a[24:27]\n"
      "v_mfma_f32_16x16x32_bf16 a[56:59], a[184:187], a[212:215], a[56:59]\n"
      "v_mfma_f32_16x16x32_bf16 a[88:91], a[184:187], a[216:219], a[88:91]\n"
      "v_mfma_f32_16x16x32_bf16 a[120:123], a[184:187], a[220:223], a[120:123]\n"
      "v_mfma_f32_16x16x32_bf16 a[28:31], a[188:191], a[208:211], a[28:31]\n"
      "v_mfma_f32_16x16x32_bf16 a[60:63], a[188:191], a[212:215], a[60:63]\n"
      "v_mfma_f32_16x16x32_bf16 a[92:95], a[188:191], a[216:219], a[92:95]\n"
      "v_mfma_f32_16x16x32_bf16 a[124:127], a[188:191], a[220:223], a[124:127]\n"
      "s_waitcnt lgkmcnt(0)\n"
      "v_mfma_f32_16x16x32_bf16 v[0:3], a[128:131], a[192:195], v[0:3]\n"
      "ds_read_b64_tr_b16 a[160:161], v212 offset:40960\n"
      "ds_read_b64_tr_b16 a[162:163], v212 offset:41984\n"
      "v_mfma_f32_16x16x32_bf16 v[32:35], a[128:131], a[196:199], v[32:35]\n"
      "ds_read_b64_tr_b16 a[164:165], v212 offset:40968\n"
      "ds_read_b64_tr_b16 a[166:167], v212 offset:41992\n"
      "v_mfma_f32_16x16x32_bf16 v[64:67], a[128:131], a[200:203], v[64:67]\n"
      "s_add_u32 m0, s78, 0\n"
      "s_mov_b32 s83, 0\n"
      "v_mfma_f32_16x16x32_bf16 v[96:99], a[128:131], a[204:207], v[96:99]\n"
      "ds_read_b64_tr_b16 a[168:169], v213 offset:40960\n"
      "ds_read_b64_tr_b16 a[170:171], v213 offset:41984\n"
      "v_mfma_f32_16x16x32_bf16 v[4:7], a[132:135], a[192:195], v[4:7]\n"
      "ds_read_b64_tr_b16 a[172:173], v213 offset:40968\n"
      "ds_read_b64_tr_b16 a[174:175], v213 offset:41992\n"
      "v_mfma_f32_16x16x32_bf16 v[36:39], a[132:135], a[196:199], v[36:39]\n"
      "buffer_load_dwordx4 v196, s[40:43], s83 offen offset:0 nt lds\n"
      "v_mfma_f32_16x16x32_bf16 v[68:71], a[132:135], a[200:203], v[68:71]\n"
      "ds_read_b64_tr_b16 a[176:177], v214 offset:40960\n"
      "v_mfma_f32_16x16x32_bf16 v[100:103], a[132:135], a[204:207], v[100:103]\n"
      "ds_read_b64_tr_b16 a[178:179], v214 offset:41984\n"
      "v_mfma_f32_16x16x32_bf16 v[8:11], a[136:139], a[192:195], v[8:11]\n"
      "buffer_load_dwordx4 v197, s[40:43], s83 offen offset:1024 nt lds\n"
      "v_mfma_f32_16x16x32_bf16 v[40:43], a[136:139], a[196:199], v[40:43]\n"
      "ds_read_b64_tr_b16 a[180:181], v214 offset:40968\n"
      "v_mfma_f32_16x16x32_bf16 v[72:75], a[136:139], a[200:203], v[72:75]\n"
      "ds_read_b64_tr_b16 a[182:183], v214 offset:41992\n"
      "v_mfma_f32_16x16x32_bf16 v[104:107], a[136:139], a[204:207], v[104:107]\n"
      "buffer_load_dwordx4 v198, s[40:43], s83 offen offset:2048 nt lds\n"
      "v_mfma_f32_16x16x32_bf16 v[12:15], a[140:143], a[192:195], v[12:15]\n"
      "ds_read_b64_tr_b16 a[184:185], v215 offset:40960\n"
      "v_mfma_f32_16x16x32_bf16 v[44:47], a[140:143], a[196:199], v[44:47]\n"
      "ds_read_b64_tr_b16 a[186:187], v215 offset:41984\n"
      "v_mfma_f32_16x16x32_bf16 v[76:79], a[140:143], a[200:203], v[76:79]\n"
      "buffer_load_dwordx4 v199, s[40:43], s83 offen offset:3072 nt lds\n"
      "v_mfma_f32_16x16x32_bf16 v[108:111], a[140:143], a[204:207], v[108:111]\n"
      "ds_read_b64_tr_b16 a[188:189], v215 offset:40968\n"
      "v_mfma_f32_16x16x32_bf16 v[16:19], a[144:147], a[192:195], v[16:19]\n"
      "ds_read_b64_tr_b16 a[190:191], v215 offset:41992\n"
      "v_mfma_f32_16x16x32_bf16 v[48:51], a[144:147], a[196:199], v[48:51]\n"
      "v_mfma_f32_16x16x32_bf16 v[80:83], a[144:147], a[200:203], v[80:83]\n"
      "v_mfma_f32_16x16x32_bf16 v[112:115], a[144:147], a[204:207], v[112:115]\n"
      "v_mfma_f32_16x16x32_bf16 v[20:23], a[148:151], a[192:195], v[20:23]\n"
      "v_mfma_f32_16x16x32_bf16 v[52:55], a[148:151], a[196:199], v[52:55]\n"
      "v_mfma_f32_16x16x32_bf16 v[84:87], a[148:151], a[200:203], v[84:87]\n"
      "v_mfma_f32_16x16x32_bf16 v[116:119], a[148:151], a[204:207], v[116:119]\n"
      "v_mfma_f32_16x16x32_bf16 v[24:27], a[152:155], a[192:195], v[24:27]\n"
      "v_mfma_f32_16x16x32_bf16 v[56:59], a[152:155], a[196:199], v[56:59]\n"
      "v_mfma_f32_16x16x32_bf16 v[88:91], a[152:155], a[200:203], v[88:91]\n"
      "v_mfma_f32_16x16x32_bf16 v[120:123], a[152:155], a[204:207], v[120:123]\n"
      "v_mfma_f32_16x16x32_bf16 v[28:31], a[156:159], a[192:195], v[28:31]\n"
      "v_mfma_f32_16x16x32_bf16 v[60:63], a[156:159], a[196:199], v[60:63]\n"
      "v_mfma_f32_16x16x32_bf16 v[92:95], a[156:159], a[200:203], v[92:95]\n"
      "v_mfma_f32_16x16x32_bf16 v[124:127], a[156:159], a[204:207], v[124:127]\n"
      "s_waitcnt vmcnt(16) lgkmcnt(0)\n"
      "s_barrier\n"
      "v_mfma_f32_16x16x32_bf16 a[0:3], a[160:163], a[192:195], a[0:3]\n"
      "ds_read_b128 a[208:211], v219 offset:0\n"
      "ds_read_b128 a[212:215], v219 offset:2048\n"
      "v_mfma_f32_16x16x32_bf16 a[32:35], a[160:163], a[196:199], a[32:35]\n"
      "ds_read_b128 a[216:219], v219 offset:4096\n"
      "ds_read_b128 a[220:223], v219 offset:6144\n"
      "v_mfma_f32_16x16x32_bf16 a[64:67], a[160:163], a[200:203], a[64:67]\n"
      "s_add_u32 m0, s78, 16384\n"
      "s_mov_b32 s83, 16384\n"
      "v_mfma_f32_16x16x32_bf16 a[96:99], a[160:163], a[204:207], a[96:99]\n"
      "ds_read_b64_tr_b16 a[128:129], v212 offset:49152\n"
      "ds_read_b64_tr_b16 a[130:131], v212 offset:50176\n"
      "v_mfma_f32_16x16x32_bf16 a[4:7], a[164:167], a[192:195], a[4:7]\n"
      "ds_read_b64_tr_b16 a[132:133], v212 offset:49160\n"
      "ds_read_b64_tr_b16 a[134:135], v212 offset:50184\n"
      "v_mfma_f32_16x16x32_bf16 a[36:39], a[164:167], a[196:199], a[36:39]\n"
      "buffer_load_dwordx4 v196, s[40:43], s83 offen offset:0 nt lds\n"
      "v_mfma_f32_16x16x32_bf16 a[68:71], a[164:167], a[200:203], a[68:71]\n"
      "ds_read_b64_tr_b16 a[136:137], v213 offset:49152\n"
      "v_mfma_f32_16x16x32_bf16 a[100:103], a[164:167], a[204:207], a[100:103]\n"
      "ds_read_b64_tr_b16 a[138:139], v213 offset:50176\n"
      "v_mfma_f32_16x16x32_bf16 a[8:11], a[168:171], a[192:195], a[8:11]\n"
      "buffer_load_dwordx4 v197, s[40:43], s83 offen offset:1024 nt lds\n"
      "v_mfma_f32_16x16x32_bf16 a[40:43], a[168:171], a[196:199], a[40:43]\n"
      "ds_read_b64_tr_b16 a[140:141], v213 offset:49160\n"
      "v_mfma_f32_16x16x32_bf16 a[72:75], a[168:171], a[200:203], a[72:75]\n"
      "ds_read_b64_tr_b16 a[142:143], v213 offset:50184\n"
      "v_mfma_f32_16x16x32_bf16 a[104:107], a[168:171], a[204:207], a[104:107]\n"
      "ds_read_b64_tr_b16 a[144:145], v214 offset:49152\n"
      "v_mfma_f32_16x16x32_bf16 a[12:15], a[172:175], a[192:195], a[12:15]\n"
      "ds_read_b64_tr_b16 a[146:147], v214 offset:50176\n"
      "v_mfma_f32_16x16x32_bf16 a[44:47], a[172:175], a[196:199], a[44:47]\n"
      "ds_read_b64_tr_b16 a[148:149], v214 offset:49160\n"
      "v_mfma_f32_16x16x32_bf16 a[76:79], a[172:175], a[200:203], a[76:79]\n"
      "ds_read_b64_tr_b16 a[150:151], v214 offset:50184\n"
      "v_mfma_f32_16x16x32_bf16 a[108:111], a[172:175], a[204:207], a[108:111]\n"
      "ds_read_b64_tr_b16 a[152:153], v215 offset:49152\n"
      "v_mfma_f32_16x16x32_bf16 a[16:19], a[176:179], a[192:195], a[16:19]\n"
      "ds_read_b64_tr_b16 a[154:155], v215 offset:50176\n"
      "v_mfma_f32_16x16x32_bf16 a[48:51], a[176:179], a[196:199], a[48:51]\n"
      "ds_read_b64_tr_b16 a[156:157], v215 offset:49160\n"
      "v_mfma_f32_16x16x32_bf16 a[80:83], a[176:179], a[200:203], a[80:83]\n"
      "ds_read_b64_tr_b16 a[158:159], v215 offset:50184\n"
      "v_mfma_f32_16x16x32_bf16 a[112:115], a[176:179], a[204:207], a[112:115]\n"
      "v_mfma_f32_16x16x32_bf16 a[20:23], a[180:183], a[192:195], a[20:23]\n"
      "v_mfma_f32_16x16x32_bf16 a[52:55], a[180:183], a[196:199], a[52:55]\n"
      "v_mfma_f32_16x16x32_bf16 a[84:87], a[180:183], a[200:203], a[84:87]\n"
      "v_mfma_f32_16x16x32_bf16 a[116:119], a[180:183], a[204:207], a[116:119]\n"
      "v_mfma_f32_16x16x32_bf16 a[24:27], a[184:187], a[192:195], a[24:27]\n"
      "v_mfma_f32_16x16x32_bf16 a[56:59], a[184:187], a[196:199], a[56:59]\n"
      "v_mfma_f32_16x16x32_bf16 a[88:91], a[184:187], a[200:203], a[88:91]\n"
      "v_mfma_f32_16x16x32_bf16 a[120:123], a[184:187], a[204:207], a[120:123]\n"
      "v_mfma_f32_16x16x32_bf16 a[28:31], a[188:191], a[192:195], a[28:31]\n"
      "v_mfma_f32_16x16x32_bf16 a[60:63], a[188:191], a[196:199], a[60:63]\n"
      "v_mfma_f32_16x16x32_bf16 a[92:95], a[188:191], a[200:203], a[92:95]\n"
      "v_mfma_f32_16x16x32_bf16 a[124:127], a[188:191], a[204:207], a[124:127]\n"
      "s_waitcnt lgkmcnt(0)\n"
      "v_mfma_f32_16x16x32_bf16 v[0:3], a[128:131], a[208:211], v[0:3]\n"
      "ds_read_b64_tr_b16 a[160:161], v212 offset:57344\n"
      "ds_read_b64_tr_b16 a[162:163], v212 offset:58368\n"
      "v_mfma_f32_16x16x32_bf16 v[32:35], a[128:131], a[212:215], v[32:35]\n"
      "ds_read_b64_tr_b16 a[164:165], v212 offset:57352\n"
      "ds_read_b64_tr_b16 a[166:167], v212 offset:58376\n"
      "v_mfma_f32_16x16x32_bf16 v[64:67], a[128:131], a[216:219], v[64:67]\n"
      "buffer_load_dwordx4 v198, s[40:43], s83 offen offset:2048 nt lds\n"
      "v_mfma_f32_16x16x32_bf16 v[96:99], a[128:131], a[220:223], v[96:99]\n"
      "ds_read_b64_tr_b16 a[168:169], v213 offset:57344\n"
      "ds_read_b64_tr_b16 a[170:171], v213 offset:58368\n"
      "v_mfma_f32_16x16x32_bf16 v[4:7], a[132:135], a[208:211], v[4:7]\n"
      "ds_read_b64_tr_b16 a[172:173], v213 offset:57352\n"
      "ds_read_b64_tr_b16 a[174:175], v213 offset:58376\n"
      "v_mfma_f32_16x16x32_bf16 v[36:39], a[132:135], a[212:215], v[36:39]\n"
      "buffer_load_dwordx4 v199, s[40:43], s83 offen offset:3072 nt lds\n"
      "v_mfma_f32_16x16x32_bf16 v[68:71], a[132:135], a[216:219], v[68:71]\n"
      "ds_read_b64_tr_b16 a[176:177], v214 offset:57344\n"
      "v_mfma_f32_16x16x32_bf16 v[100:103], a[132:135], a[220:223], v[100:103]\n"
      "ds_read_b64_tr_b16 a[178:179], v214 offset:58368\n"
      "v_mfma_f32_16x16x32_bf16 v[8:11], a[136:139], a[208:211], v[8:11]\n"
      "ds_read_b64_tr_b16 a[180:181], v214 offset:57352\n"
      "v_mfma_f32_16x16x32_bf16 v[40:43], a[136:139], a[212:215], v[40:43]\n"
      "ds_read_b64_tr_b16 a[182:183], v214 offset:58376\n"
      "v_mfma_f32_16x16x32_bf16 v[72:75], a[136:139], a[216:219], v[72:75]\n"
      "ds_read_b64_tr_b16 a[184:185], v215 offset:57344\n"
      "v_mfma_f32_16x16x32_bf16 v[104:107], a[136:139], a[220:223], v[104:107]\n"
      "ds_read_b64_tr_b16 a[186:187], v215 offset:58368\n"
      "v_mfma_f32_16x16x32_bf16 v[12:15], a[140:143], a[208:211], v[12:15]\n"
      "ds_read_b64_tr_b16 a[188:189], v215 offset:57352\n"
      "v_mfma_f32_16x16x32_bf16 v[44:47], a[140:143], a[212:215], v[44:47]\n"
      "ds_read_b64_tr_b16 a[190:191], v215 offset:58376\n"
      "v_mfma_f32_16x16x32_bf16 v[76:79], a[140:143], a[216:219], v[76:79]\n"
      "v_mfma_f32_16x16x32_bf16 v[108:111], a[140:143], a[220:223], v[108:111]\n"
      "v_mfma_f32_16x16x32_bf16 v[16:19], a[144:147], a[208:211], v[16:19]\n"
      "v_mfma_f32_16x16x32_bf16 v[48:51], a[144:147], a[212:215], v[48:51]\n"
      "v_mfma_f32_16x16x32_bf16 v[80:83], a[144:147], a[216:219], v[80:83]\n"
      "v_mfma_f32_16x16x32_bf16 v[112:115], a[144:147], a[220:223], v[112:115]\n"
      "v_mfma_f32_16x16x32_bf16 v[20:23], a[148:151], a[208:211], v[20:23]\n"
      "v_mfma_f32_16x16x32_bf16 v[52:55], a[148:151], a[212:215], v[52:55]\n"
      "v_mfma_f32_16x16x32_bf16 v[84:87], a[148:151], a[216:219], v[84:87]\n"
      "v_mfma_f32_16x16x32_bf16 v[116:119], a[148:151], a[220:223], v[116:119]\n"
      "v_mfma_f32_16x16x32_bf16 v[24:27], a[152:155], a[208:211], v[24:27]\n"
      "v_mfma_f32_16x16x32_bf16 v[56:59], a[152:155], a[212:215], v[56:59]\n"
      "v_mfma_f32_16x16x32_bf16 v[88:91], a[152:155], a[216:219], v[88:91]\n"
      "v_mfma_f32_16x16x32_bf16 v[120:123], a[152:155], a[220:223], v[120:123]\n"
      "v_mfma_f32_16x16x32_bf16 v[28:31], a[156:159], a[208:211], v[28:31]\n"
      "v_mfma_f32_16x16x32_bf16 v[60:63], a[156:159], a[212:215], v[60:63]\n"
      "v_mfma_f32_16x16x32_bf16 v[92:95], a[156:159], a[216:219], v[92:95]\n"
      "v_mfma_f32_16x16x32_bf16 v[124:127], a[156:159], a[220:223], v[124:127]\n"
      "s_waitcnt vmcnt(12) lgkmcnt(0)\n"
      "s_barrier\n"
      "v_mfma_f32_16x16x32_bf16 a[0:3], a[160:163], a[208:211], a[0:3]\n"
      "ds_read_b128 a[192:195], v218 offset:32768\n"
      "ds_read_b128 a[196:199], v218 offset:34816\n"
      "v_mfma_f32_16x16x32_bf16 a[32:35], a[160:163], a[212:215], a[32:35]\n"
      "ds_read_b128 a[200:203], v218 offset:36864\n"
      "ds_read_b128 a[204:207], v218 offset:38912\n"
      "v_mfma_f32_16x16x32_bf16 a[64:67], a[160:163], a[216:219], a[64:67]\n"
      "s_add_u32 m0, s78, 65536\n"
      "s_mov_b32 s83, 0\n"
      "v_mfma_f32_16x16x32_bf16 a[96:99], a[160:163], a[220:223], a[96:99]\n"
      "ds_read_b64_tr_b16 a[128:129], v212 offset:0\n"
      "ds_read_b64_tr_b16 a[130:131], v212 offset:1024\n"
      "v_mfma_f32_16x16x32_bf16 a[4:7], a[164:167], a[208:211], a[4:7]\n"
      "ds_read_b64_tr_b16 a[132:133], v212 offset:8\n"
      "ds_read_b64_tr_b16 a[134:135], v212 offset:1032\n"
      "v_mfma_f32_16x16x32_bf16 a[36:39], a[164:167], a[212:215], a[36:39]\n"
      "buffer_load_dwordx4 v208, s[44:47], s83 offen offset:0 nt lds\n"
      "v_mfma_f32_16x16x32_bf16 a[68:71], a[164:167], a[216:219], a[68:71]\n"
      "ds_read_b64_tr_b16 a[136:137], v213 offset:0\n"
      "v_mfma_f32_16x16x32_bf16 a[100:103], a[164:167], a[220:223], a[100:103]\n"
      "ds_read_b64_tr_b16 a[138:139], v213 offset:1024\n"
      "v_mfma_f32_16x16x32_bf16 a[8:11], a[168:171], a[208:211], a[8:11]\n"
      "buffer_load_dwordx4 v209, s[44:47], s83 offen offset:1024 nt lds\n"
      "v_mfma_f32_16x16x32_bf16 a[40:43], a[168:171], a[212:215], a[40:43]\n"
      "ds_read_b64_tr_b16 a[140:141], v213 offset:8\n"
      "v_mfma_f32_16x16x32_bf16 a[72:75], a[168:171], a[216:219], a[72:75]\n"
      "ds_read_b64_tr_b16 a[142:143], v213 offset:1032\n"
      "v_mfma_f32_16x16x32_bf16 a[104:107], a[168:171], a[220:223], a[104:107]\n"
      "ds_read_b64_tr_b16 a[144:145], v214 offset:0\n"
      "v_mfma_f32_16x16x32_bf16 a[12:15], a[172:175], a[208:211], a[12:15]\n"
      "ds_read_b64_tr_b16 a[146:147], v214 offset:1024\n"
      "v_mfma_f32_16x16x32_bf16 a[44:47], a[172:175], a[212:215], a[44:47]\n"
      "ds_read_b64_tr_b16 a[148:149], v214 offset:8\n"
      "v_mfma_f32_16x16x32_bf16 a[76:79], a[172:175], a[216:219], a[76:79]\n"
      "ds_read_b64_tr_b16 a[150:151], v214 offset:1032\n"
      "v_mfma_f32_16x16x32_bf16 a[108:111], a[172:175], a[220:223], a[108:111]\n"
      "ds_read_b64_tr_b16 a[152:153], v215 offset:0\n"
      "v_mfma_f32_16x16x32_bf16 a[16:19], a[176:179], a[208:211], a[16:19]\n"
      "ds_read_b64_tr_b16 a[154:155], v215 offset:1024\n"
      "v_mfma_f32_16x16x32_bf16 a[48:51], a[176:179], a[212:215], a[48:51]\n"
      "ds_read_b64_tr_b16 a[156:157], v215 offset:8\n"
      "v_mfma_f32_16x16x32_bf16 a[80:83], a[176:179], a[216:219], a[80:83]\n"
      "ds_read_b64_tr_b16 a[158:159], v215 offset:1032\n"
      "v_mfma_f32_16x16x32_bf16 a[112:115], a[176:179], a[220:223], a[112:115]\n"
      "v_mfma_f32_16x16x32_bf16 a[20:23], a[180:183], a[208:211], a[20:23]\n"
      "v_mfma_f32_16x16x32_bf16 a[52:55], a[180:183], a[212:215], a[52:55]\n"
      "v_mfma_f32_16x16x32_bf16 a[84:87], a[180:183], a[216:219], a[84:87]\n"
      "v_mfma_f32_16x16x32_bf16 a[116:119], a[180:183], a[220:223], a[116:119]\n"
      "v_mfma_f32_16x16x32_bf16 a[24:27], a[184:187], a[208:211], a[24:27]\n"
      "v_mfma_f32_16x16x32_bf16 a[56:59], a[184:187], a[212:215], a[56:59]\n"
      "v_mfma_f32_16x16x32_bf16 a[88:91], a[184:187], a[216:219], a[88:91]\n"
      "v_mfma_f32_16x16x32_bf16 a[120:123], a[184:187], a[220:223], a[120:123]\n"
      "v_mfma_f32_16x16x32_bf16 a[28:31], a[188:191], a[208:211], a[28:31]\n"
      "v_mfma_f32_16x16x32_bf16 a[60:63], a[188:191], a[212:215], a[60:63]\n"
      "v_mfma_f32_16x16x32_bf16 a[92:95], a[188:191], a[216:219], a[92:95]\n"
      "v_mfma_f32_16x16x32_bf16 a[124:127], a[188:191], a[220:223], a[124:127]\n"
      "s_waitcnt lgkmcnt(0)\n"
      "v_mfma_f32_16x16x32_bf16 v[0:3], a[128:131], a[192:195], v[0:3]\n"
      "ds_read_b64_tr_b16 a[160:161], v212 offset:8192\n"
      "ds_read_b64_tr_b16 a[162:163], v212 offset:9216\n"
      "v_mfma_f32_16x16x32_bf16 v[32:35], a[128:131], a[196:199], v[32:35]\n"
      "ds_read_b64_tr_b16 a[164:165], v212 offset:8200\n"
      "ds_read_b64_tr_b16 a[166:167], v212 offset:9224\n"
      "v_mfma_f32_16x16x32_bf16 v[64:67], a[128:131], a[200:203], v[64:67]\n"
      "buffer_load_dwordx4 v208, s[44:47], s83 offen offset:2048 nt lds\n"
      "v_mfma_f32_16x16x32_bf16 v[96:99], a[128:131], a[204:207], v[96:99]\n"
      "ds_read_b64_tr_b16 a[168:169], v213 offset:8192\n"
      "ds_read_b64_tr_b16 a[170:171], v213 offset:9216\n"
      "v_mfma_f32_16x16x32_bf16 v[4:7], a[132:135], a[192:195], v[4:7]\n"
      "ds_read_b64_tr_b16 a[172:173], v213 offset:8200\n"
      "ds_read_b64_tr_b16 a[174:175], v213 offset:9224\n"
      "v_mfma_f32_16x16x32_bf16 v[36:39], a[132:135], a[196:199], v[36:39]\n"
      "buffer_load_dwordx4 v209, s[44:47], s83 offen offset:3072 nt lds\n"
      "v_mfma_f32_16x16x32_bf16 v[68:71], a[132:135], a[200:203], v[68:71]\n"
      "ds_read_b64_tr_b16 a[176:177], v214 offset:8192\n"
      "v_mfma_f32_16x16x32_bf16 v[100:103], a[132:135], a[204:207], v[100:103]\n"
      "ds_read_b64_tr_b16 a[178:179], v214 offset:9216\n"
      "v_mfma_f32_16x16x32_bf16 v[8:11], a[136:139], a[192:195], v[8:11]\n"
      "ds_read_b64_tr_b16 a[180:181], v214 offset:8200\n"
      "v_mfma_f32_16x16x32_bf16 v[40:43], a[136:139], a[196:199], v[40:43]\n"
      "ds_read_b64_tr_b16 a[182:183], v214 offset:9224\n"
      "v_mfma_f32_16x16x32_bf16 v[72:75], a[136:139], a[200:203], v[72:75]\n"
      "ds_read_b64_tr_b16 a[184:185], v215 offset:8192\n"
      "v_mfma_f32_16x16x32_bf16 v[104:107], a[136:139], a[204:207], v[104:107]\n"
      "ds_read_b64_tr_b16 a[186:187], v215 offset:9216\n"
      "v_mfma_f32_16x16x32_bf16 v[12:15], a[140:143], a[192:195], v[12:15]\n"
      "ds_read_b64_tr_b16 a[188:189], v215 offset:8200\n"
      "v_mfma_f32_16x16x32_bf16 v[44:47], a[140:143], a[196:199], v[44:47]\n"
      "ds_read_b64_tr_b16 a[190:191], v215 offset:9224\n"
      "v_mfma_f32_16x16x32_bf16 v[76:79], a[140:143], a[200:203], v[76:79]\n"
      "v_mfma_f32_16x16x32_bf16 v[108:111], a[140:143], a[204:207], v[108:111]\n"
      "v_mfma_f32_16x16x32_bf16 v[16:19], a[144:147], a[192:195], v[16:19]\n"
      "v_mfma_f32_16x16x32_bf16 v[48:51], a[144:147], a[196:199], v[48:51]\n"
      "v_mfma_f32_16x16x32_bf16 v[80:83], a[144:147], a[200:203], v[80:83]\n"
      "v_mfma_f32_16x16x32_bf16 v[112:115], a[144:147], a[204:207], v[112:115]\n"
      "v_mfma_f32_16x16x32_bf16 v[20:23], a[148:151], a[192:195], v[20:23]\n"
      "v_mfma_f32_16x16x32_bf16 v[52:55], a[148:151], a[196:199], v[52:55]\n"
      "v_mfma_f32_16x16x32_bf16 v[84:87], a[148:151], a[200:203], v[84:87]\n"
      "v_mfma_f32_16x16x32_bf16 v[116:119], a[148:151], a[204:207], v[116:119]\n"
      "v_mfma_f32_16x16x32_bf16 v[24:27], a[152:155], a[192:195], v[24:27]\n"
      "v_mfma_f32_16x16x32_bf16 v[56:59], a[152:155], a[196:199], v[56:59]\n"
      "v_mfma_f32_16x16x32_bf16 v[88:91], a[152:155], a[200:203], v[88:91]\n"
      "v_mfma_f32_16x16x32_bf16 v[120:123], a[152:155], a[204:207], v[120:123]\n"
      "v_mfma_f32_16x16x32_bf16 v[28:31], a[156:159], a[192:195], v[28:31]\n"
      "v_mfma_f32_16x16x32_bf16 v[60:63], a[156:159], a[196:199], v[60:63]\n"
      "v_mfma_f32_16x16x32_bf16 v[92:95], a[156:159], a[200:203], v[92:95]\n"
      "v_mfma_f32_16x16x32_bf16 v[124:127], a[156:159], a[204:207], v[124:127]\n"
      "s_waitcnt vmcnt(12) lgkmcnt(0)\n"
      "s_barrier\n"
      "v_mfma_f32_16x16x32_bf16 a[0:3], a[160:163], a[192:195], a[0:3]\n"
      "ds_read_b128 a[208:211], v219 offset:32768\n"
      "ds_read_b128 a[212:215], v219 offset:34816\n"
      "v_mfma_f32_16x16x32_bf16 a[32:35], a[160:163], a[196:199], a[32:35]\n"
      "ds_read_b128 a[216:219], v219 offset:36864\n"
      "ds_read_b128 a[220:223], v219 offset:38912\n"
      "v_mfma_f32_16x16x32_bf16 a[64:67], a[160:163], a[200:203], a[64:67]\n"
      "s_add_u32 m0, s78, 81920\n"
      "s_mov_b32 s83, 16384\n"
      "v_mfma_f32_16x16x32_bf16 a[96:99], a[160:163], a[204:207], a[96:99]\n"
      "ds_read_b64_tr_b16 a[128:129], v212 offset:16384\n"
      "ds_read_b64_tr_b16 a[130:131], v212 offset:17408\n"
      "v_mfma_f32_16x16x32_bf16 a[4:7], a[164:167], a[192:195], a[4:7]\n"
      "ds_read_b64_tr_b16 a[132:133], v212 offset:16392\n"
      "ds_read_b64_tr_b16 a[134:135], v212 offset:17416\n"
      "v_mfma_f32_16x16x32_bf16 a[36:39], a[164:167], a[196:199], a[36:39]\n"
      "buffer_load_dwordx4 v208, s[44:47], s83 offen offset:0 nt lds\n"
      "v_mfma_f32_16x16x32_bf16 a[68:71], a[164:167], a[200:203], a[68:71]\n"
      "ds_read_b64_tr_b16 a[136:137], v213 offset:16384\n"
      "v_mfma_f32_16x16x32_bf16 a[100:103], a[164:167], a[204:207], a[100:103]\n"
      "ds_read_b64_tr_b16 a[138:139], v213 offset:17408\n"
      "v_mfma_f32_16x16x32_bf16 a[8:11], a[168:171], a[192:195], a[8:11]\n"
      "buffer_load_dwordx4 v209, s[44:47], s83 offen offset:1024 nt lds\n"
      "v_mfma_f32_16x16x32_bf16 a[40:43], a[168:171], a[196:199], a[40:43]\n"
      "ds_read_b64_tr_b16 a[140:141], v213 offset:16392\n"
      "v_mfma_f32_16x16x32_bf16 a[72:75], a[168:171], a[200:203], a[72:75]\n"
      "ds_read_b64_tr_b16 a[142:143], v213 offset:17416\n"
      "v_mfma_f32_16x16x32_bf16 a[104:107], a[168:171], a[204:207], a[104:107]\n"
      "ds_read_b64_tr_b16 a[144:145], v214 offset:16384\n"
      "v_mfma_f32_16x16x32_bf16 a[12:15], a[172:175], a[192:195], a[12:15]\n"
      "ds_read_b64_tr_b16 a[146:147], v214 offset:17408\n"
      "v_mfma_f32_16x16x32_bf16 a[44:47], a[172:175], a[196:199], a[44:47]\n"
      "ds_read_b64_tr_b16 a[148:149], v214 offset:16392\n"
      "v_mfma_f32_16x16x32_bf16 a[76:79], a[172:175], a[200:203], a[76:79]\n"
      "ds_read_b64_tr_b16 a[150:151], v214 offset:17416\n"
      "v_mfma_f32_16x16x32_bf16 a[108:111], a[172:175], a[204:207], a[108:111]\n"
      "ds_read_b64_tr_b16 a[152:153], v215 offset:16384\n"
      "v_mfma_f32_16x16x32_bf16 a[16:19], a[176:179], a[192:195], a[16:19]\n"
      "ds_read_b64_tr_b16 a[154:155], v215 offset:17408\n"
      "v_mfma_f32_16x16x32_bf16 a[48:51], a[176:179], a[196:199], a[48:51]\n"
      "ds_read_b64_tr_b16 a[156:157], v215 offset:16392\n"
      "v_mfma_f32_16x16x32_bf16 a[80:83], a[176:179], a[200:203], a[80:83]\n"
      "ds_read_b64_tr_b16 a[158:159], v215 offset:17416\n"
      "v_mfma_f32_16x16x32_bf16 a[112:115], a[176:179], a[204:207], a[112:115]\n"
      "v_mfma_f32_16x16x32_bf16 a[20:23], a[180:183], a[192:195], a[20:23]\n"
      "v_mfma_f32_16x16x32_bf16 a[52:55], a[180:183], a[196:199], a[52:55]\n"
      "v_mfma_f32_16x16x32_bf16 a[84:87], a[180:183], a[200:203], a[84:87]\n"
      "v_mfma_f32_16x16x32_bf16 a[116:119], a[180:183], a[204:207], a[116:119]\n"
      "v_mfma_f32_16x16x32_bf16 a[24:27], a[184:187], a[192:195], a[24:27]\n"
      "v_mfma_f32_16x16x32_bf16 a[56:59], a[184:187], a[196:199], a[56:59]\n"
      "v_mfma_f32_16x16x32_bf16 a[88:91], a[184:187], a[200:203], a[88:91]\n"
      "v_mfma_f32_16x16x32_bf16 a[120:123], a[184:187], a[204:207], a[120:123]\n"
      "v_mfma_f32_16x16x32_bf16 a[28:31], a[188:191], a[192:195], a[28:31]\n"
      "v_mfma_f32_16x16x32_bf16 a[60:63], a[188:191], a[196:199], a[60:63]\n"
      "v_mfma_f32_16x16x32_bf16 a[92:95], a[188:191], a[200:203], a[92:95]\n"
      "v_mfma_f32_16x16x32_bf16 a[124:127], a[188:191], a[204:207], a[124:127]\n"
      "s_waitcnt lgkmcnt(0)\n"
      "v_mfma_f32_16x16x32_bf16 v[0:3], a[128:131], a[208:211], v[0:3]\n"
      "ds_read_b64_tr_b16 a[160:161], v212 offset:24576\n"
      "ds_read_b64_tr_b16 a[162:163], v212 offset:25600\n"
      "v_mfma_f32_16x16x32_bf16 v[32:35], a[128:131], a[212:215], v[32:35]\n"
      "ds_read_b64_tr_b16 a[164:165], v212 offset:24584\n"
      "ds_read_b64_tr_b16 a[166:167], v212 offset:25608\n"
      "v_mfma_f32_16x16x32_bf16 v[64:67], a[128:131], a[216:219], v[64:67]\n"
      "buffer_load_dwordx4 v208, s[44:47], s83 offen offset:2048 nt lds\n"
      "v_mfma_f32_16x16x32_bf16 v[96:99], a[128:131], a[220:223], v[96:99]\n"
      "ds_read_b64_tr_b16 a[168:169], v213 offset:24576\n"
      "ds_read_b64_tr_b16 a[170:171], v213 offset:25600\n"
      "v_mfma_f32_16x16x32_bf16 v[4:7], a[132:135], a[208:211], v[4:7]\n"
      "ds_read_b64_tr_b16 a[172:173], v213 offset:24584\n"
      "ds_read_b64_tr_b16 a[174:175], v213 offset:25608\n"
      "v_mfma_f32_16x16x32_bf16 v[36:39], a[132:135], a[212:215], v[36:39]\n"
      "buffer_load_dwordx4 v209, s[44:47], s83 offen offset:3072 nt lds\n"
      "v_mfma_f32_16x16x32_bf16 v[68:71], a[132:135], a[216:219], v[68:71]\n"
      "ds_read_b64_tr_b16 a[176:177], v214 offset:24576\n"
      "v_mfma_f32_16x16x32_bf16 v[100:103], a[132:135], a[220:223], v[100:103]\n"
      "ds_read_b64_tr_b16 a[178:179], v214 offset:25600\n"
      "v_mfma_f32_16x16x32_bf16 v[8:11], a[136:139], a[208:211], v[8:11]\n"
      "ds_read_b64_tr_b16 a[180:181], v214 offset:24584\n"
      "v_mfma_f32_16x16x32_bf16 v[40:43], a[136:139], a[212:215], v[40:43]\n"
      "ds_read_b64_tr_b16 a[182:183], v214 offset:25608\n"
      "v_mfma_f32_16x16x32_bf16 v[72:75], a[136:139], a[216:219], v[72:75]\n"
      "ds_read_b64_tr_b16 a[184:185], v215 offset:24576\n"
      "v_mfma_f32_16x16x32_bf16 v[104:107], a[136:139], a[220:223], v[104:107]\n"
      "ds_read_b64_tr_b16 a[186:187], v215 offset:25600\n"
      "v_mfma_f32_16x16x32_bf16 v[12:15], a[140:143], a[208:211], v[12:15]\n"
      "ds_read_b64_tr_b16 a[188:189], v215 offset:24584\n"
      "v_mfma_f32_16x16x32_bf16 v[44:47], a[140:143], a[212:215], v[44:47]\n"
      "ds_read_b64_tr_b16 a[190:191], v215 offset:25608\n"
      "v_mfma_f32_16x16x32_bf16 v[76:79], a[140:143], a[216:219], v[76:79]\n"
      "v_mfma_f32_16x16x32_bf16 v[108:111], a[140:143], a[220:223], v[108:111]\n"
      "v_mfma_f32_16x16x32_bf16 v[16:19], a[144:147], a[208:211], v[16:19]\n"
      "v_mfma_f32_16x16x32_bf16 v[48:51], a[144:147], a[212:215], v[48:51]\n"
      "v_mfma_f32_16x16x32_bf16 v[80:83], a[144:147], a[216:219], v[80:83]\n"
      "v_mfma_f32_16x16x32_bf16 v[112:115], a[144:147], a[220:223], v[112:115]\n"
      "v_mfma_f32_16x16x32_bf16 v[20:23], a[148:151], a[208:211], v[20:23]\n"
      "v_mfma_f32_16x16x32_bf16 v[52:55], a[148:151], a[212:215], v[52:55]\n"
      "v_mfma_f32_16x16x32_bf16 v[84:87], a[148:151], a[216:219], v[84:87]\n"
      "v_mfma_f32_16x16x32_bf16 v[116:119], a[148:151], a[220:223], v[116:119]\n"
      "v_mfma_f32_16x16x32_bf16 v[24:27], a[152:155], a[208:211], v[24:27]\n"
      "v_mfma_f32_16x16x32_bf16 v[56:59], a[152:155], a[212:215], v[56:59]\n"
      "v_mfma_f32_16x16x32_bf16 v[88:91], a[152:155], a[216:219], v[88:91]\n"
      "v_mfma_f32_16x16x32_bf16 v[120:123], a[152:155], a[220:223], v[120:123]\n"
      "v_mfma_f32_16x16x32_bf16 v[28:31], a[156:159], a[208:211], v[28:31]\n"
      "v_mfma_f32_16x16x32_bf16 v[60:63], a[156:159], a[212:215], v[60:63]\n"
      "v_mfma_f32_16x16x32_bf16 v[92:95], a[156:159], a[216:219], v[92:95]\n"
      "v_mfma_f32_16x16x32_bf16 v[124:127], a[156:159], a[220:223], v[124:127]\n"
      "s_waitcnt lgkmcnt(0)\n"
      "s_barrier\n"
      "v_mfma_f32_16x16x32_bf16 a[0:3], a[160:163], a[208:211], a[0:3]\n"
      "v_mfma_f32_16x16x32_bf16 a[32:35], a[160:163], a[212:215], a[32:35]\n"
      "v_mfma_f32_16x16x32_bf16 a[64:67], a[160:163], a[216:219], a[64:67]\n"
      "s_add_u32 m0, s78, 32768\n"
      "s_mov_b32 s83, 32768\n"
      "v_mfma_f32_16x16x32_bf16 a[96:99], a[160:163], a[220:223], a[96:99]\n"
      "v_mfma_f32_16x16x32_bf16 a[4:7], a[164:167], a[208:211], a[4:7]\n"
      "v_mfma_f32_16x16x32_bf16 a[36:39], a[164:167], a[212:215], a[36:39]\n"
      "buffer_load_dwordx4 v196, s[40:43], s83 offen offset:0 nt lds\n"
      "v_mfma_f32_16x16x32_bf16 a[68:71], a[164:167], a[216:219], a[68:71]\n"
      "v_mfma_f32_16x16x32_bf16 a[100:103], a[164:167], a[220:223], a[100:103]\n"
      "v_mfma_f32_16x16x32_bf16 a[8:11], a[168:171], a[208:211], a[8:11]\n"
      "buffer_load_dwordx4 v197, s[40:43], s83 offen offset:1024 nt lds\n"
      "v_mfma_f32_16x16x32_bf16 a[40:43], a[168:171], a[212:215], a[40:43]\n"
      "v_mfma_f32_16x16x32_bf16 a[72:75], a[168:171], a[216:219], a[72:75]\n"
      "v_mfma_f32_16x16x32_bf16 a[104:107], a[168:171], a[220:223], a[104:107]\n"
      "buffer_load_dwordx4 v198, s[40:43], s83 offen offset:2048 nt lds\n"
      "v_mfma_f32_16x16x32_bf16 a[12:15], a[172:175], a[208:211], a[12:15]\n"
      "v_mfma_f32_16x16x32_bf16 a[44:47], a[172:175], a[212:215], a[44:47]\n"
      "v_mfma_f32_16x16x32_bf16 a[76:79], a[172:175], a[216:219], a[76:79]\n"
      "buffer_load_dwordx4 v199, s[40:43], s83 offen offset:3072 nt lds\n"
      "v_mfma_f32_16x16x32_bf16 a[108:111], a[172:175], a[220:223], a[108:111]\n"
      "v_mfma_f32_16x16x32_bf16 a[16:19], a[176:179], a[208:211], a[16:19]\n"
      "v_mfma_f32_16x16x32_bf16 a[48:51], a[176:179], a[212:215], a[48:51]\n"
      "v_mfma_f32_16x16x32_bf16 a[80:83], a[176:179], a[216:219], a[80:83]\n"
      "v_mfma_f32_16x16x32_bf16 a[112:115], a[176:179], a[220:223], a[112:115]\n"
      "v_mfma_f32_16x16x32_bf16 a[20:23], a[180:183], a[208:211], a[20:23]\n"
      "v_mfma_f32_16x16x32_bf16 a[52:55], a[180:183], a[212:215], a[52:55]\n"
      "v_mfma_f32_16x16x32_bf16 a[84:87], a[180:183], a[216:219], a[84:87]\n"
      "v_mfma_f32_16x16x32_bf16 a[116:119], a[180:183], a[220:223], a[116:119]\n"
      "v_mfma_f32_16x16x32_bf16 a[24:27], a[184:187], a[208:211], a[24:27]\n"
      "v_mfma_f32_16x16x32_bf16 a[56:59], a[184:187], a[212:215], a[56:59]\n"
      "v_mfma_f32_16x16x32_bf16 a[88:91], a[184:187], a[216:219], a[88:91]\n"
      "v_mfma_f32_16x16x32_bf16 a[120:123], a[184:187], a[220:223], a[120:123]\n"
      "v_mfma_f32_16x16x32_bf16 a[28:31], a[188:191], a[208:211], a[28:31]\n"
      "v_mfma_f32_16x16x32_bf16 a[60:63], a[188:191], a[212:215], a[60:63]\n"
      "v_mfma_f32_16x16x32_bf16 a[92:95], a[188:191], a[216:219], a[92:95]\n"
      "v_mfma_f32_16x16x32_bf16 a[124:127], a[188:191], a[220:223], a[124:127]\n"
      "v_pk_mul_f32 v[0:1], v[0:1], s[72:73]\n"
      "v_pk_mul_f32 v[2:3], v[2:3], s[72:73]\n"
      "v_pk_mul_f32 v[4:5], v[4:5], s[72:73]\n"
      "v_pk_mul_f32 v[6:7], v[6:7], s[72:73]\n"
      "v_cvt_pk_bf16_f32 v128, v0, v1\n"
      "v_cvt_pk_bf16_f32 v129, v2, v3\n"
      "v_cvt_pk_bf16_f32 v130, v4, v5\n"
      "v_cvt_pk_bf16_f32 v131, v6, v7\n"
      "v_add_u32 v244, 0, v255\n"
      "v_mul_u32_u24 v245, 6554, v244\n"
      "v_lshrrev_b32 v245, 16, v245\n"
      "v_mul_u32_u24 v245, 1120, v245\n"
      "v_lshl_add_u32 v244, v244, 4, v245\n"
      "s_add_u32 m0, s79, 131072\n"
      "s_nop 0\n"
      "buffer_load_dwordx4 v244, s[60:63], s85 offen offset:0 nt lds\n"
      "v_pk_mul_f32 v[8:9], v[8:9], s[72:73]\n"
      "v_pk_mul_f32 v[10:11], v[10:11], s[72:73]\n"
      "v_pk_mul_f32 v[12:13], v[12:13], s[72:73]\n"
      "v_pk_mul_f32 v[14:15], v[14:15], s[72:73]\n"
      "v_cvt_pk_bf16_f32 v132, v8, v9\n"
      "v_cvt_pk_bf16_f32 v133, v10, v11\n"
      "v_cvt_pk_bf16_f32 v134, v12, v13\n"
      "v_cvt_pk_bf16_f32 v135, v14, v15\n"
      "v_pk_mul_f32 v[16:17], v[16:17], s[72:73]\n"
      "v_pk_mul_f32 v[18:19], v[18:19], s[72:73]\n"
      "v_pk_mul_f32 v[20:21], v[20:21], s[72:73]\n"
      "v_pk_mul_f32 v[22:23], v[22:23], s[72:73]\n"
      "v_cvt_pk_bf16_f32 v136, v16, v17\n"
      "v_cvt_pk_bf16_f32 v137, v18, v19\n"
      "v_cvt_pk_bf16_f32 v138, v20, v21\n"
      "v_cvt_pk_bf16_f32 v139, v22, v23\n"
      "v_pk_mul_f32 v[24:25], v[24:25], s[72:73]\n"
      "v_pk_mul_f32 v[26:27], v[26:27], s[72:73]\n"
      "v_pk_mul_f32 v[28:29], v[28:29], s[72:73]\n"
      "v_pk_mul_f32 v[30:31], v[30:31], s[72:73]\n"
      "v_cvt_pk_bf16_f32 v140, v24, v25\n"
      "v_cvt_pk_bf16_f32 v141, v26, v27\n"
      "v_cvt_pk_bf16_f32 v142, v28, v29\n"
      "v_cvt_pk_bf16_f32 v143, v30, v31\n"
      "v_add_u32 v244, 64, v255\n"
      "v_mul_u32_u24 v245, 6554, v244\n"
      "v_lshrrev_b32 v245, 16, v245\n"
      "v_mul_u32_u24 v245, 1120, v245\n"
      "v_lshl_add_u32 v244, v244, 4, v245\n"
      "s_add_u32 m0, s79, 132096\n"
      "s_nop 0\n"
      "buffer_load_dwordx4 v244, s[60:63], s85 offen offset:0 nt lds\n"
      "v_pk_mul_f32 v[32:33], v[32:33], s[72:73]\n"
      "v_pk_mul_f32 v[34:35], v[34:35], s[72:73]\n"
      "v_pk_mul_f32 v[36:37], v[36:37], s[72:73]\n"
      "v_pk_mul_f32 v[38:39], v[38:39], s[72:73]\n"
      "v_cvt_pk_bf16_f32 v144, v32, v33\n"
      "v_cvt_pk_bf16_f32 v145, v34, v35\n"
      "v_cvt_pk_bf16_f32 v146, v36, v37\n"
      "v_cvt_pk_bf16_f32 v147, v38, v39\n"
      "v_pk_mul_f32 v[40:41], v[40:41], s[72:73]\n"
      "v_pk_mul_f32 v[42:43], v[42:43], s[72:73]\n"
      "v_pk_mul_f32 v[44:45], v[44:45], s[72:73]\n"
      "v_pk_mul_f32 v[46:47], v[46:47], s[72:73]\n"
      "v_cvt_pk_bf16_f32 v148, v40, v41\n"
      "v_cvt_pk_bf16_f32 v149, v42, v43\n"
      "v_cvt_pk_bf16_f32 v150, v44, v45\n"
      "v_cvt_pk_bf16_f32 v151, v46, v47\n"
      "v_pk_mul_f32 v[48:49], v[48:49], s[72:73]\n"
      "v_pk_mul_f32 v[50:51], v[50:51], s[72:73]\n"
      "v_pk_mul_f32 v[52:53], v[52:53], s[72:73]\n"
      "v_pk_mul_f32 v[54:55], v[54:55], s[72:73]\n"
      "v_cvt_pk_bf16_f32 v152, v48, v49\n"
      "v_cvt_pk_bf16_f32 v153, v50, v51\n"
      "v_cvt_pk_bf16_f32 v154, v52, v53\n"
      "v_cvt_pk_bf16_f32 v155, v54, v55\n"
      "v_add_u32 v244, 128, v255\n"
      "v_mul_u32_u24 v245, 6554, v244\n"
      "v_lshrrev_b32 v245, 16, v245\n"
      "v_mul_u32_u24 v245, 1120, v245\n"
      "v_lshl_add_u32 v244, v244, 4, v245\n"
      "s_add_u32 m0, s79, 133120\n"
      "s_nop 0\n"
      "buffer_load_dwordx4 v244, s[60:63], s85 offen offset:0 nt lds\n"
      "v_pk_mul_f32 v[56:57], v[56:57], s[72:73]\n"
      "v_pk_mul_f32 v[58:59], v[58:59], s[72:73]\n"
      "v_pk_mul_f32 v[60:61], v[60:61], s[72:73]\n"
      "v_pk_mul_f32 v[62:63], v[62:63], s[72:73]\n"
      "v_cvt_pk_bf16_f32 v156, v56, v57\n"
      "v_cvt_pk_bf16_f32 v157, v58, v59\n"
      "v_cvt_pk_bf16_f32 v158, v60, v61\n"
      "v_cvt_pk_bf16_f32 v159, v62, v63\n"
      "v_pk_mul_f32 v[64:65], v[64:65], s[72:73]\n"
      "v_pk_mul_f32 v[66:67], v[66:67], s[72:73]\n"
      "v_pk_mul_f32 v[68:69], v[68:69], s[72:73]\n"
      "v_pk_mul_f32 v[70:71], v[70:71], s[72:73]\n"
      "v_cvt_pk_bf16_f32 v160, v64, v65\n"
      "v_cvt_pk_bf16_f32 v161, v66, v67\n"
      "v_cvt_pk_bf16_f32 v162, v68, v69\n"
      "v_cvt_pk_bf16_f32 v163, v70, v71\n"
      "v_pk_mul_f32 v[72:73], v[72:73], s[72:73]\n"
      "v_pk_mul_f32 v[74:75], v[74:75], s[72:73]\n"
      "v_pk_mul_f32 v[76:77], v[76:77], s[72:73]\n"
      "v_pk_mul_f32 v[78:79], v[78:79], s[72:73]\n"
      "v_cvt_pk_bf16_f32 v164, v72, v73\n"
      "v_cvt_pk_bf16_f32 v165, v74, v75\n"
      "v_cvt_pk_bf16_f32 v166, v76, v77\n"
      "v_cvt_pk_bf16_f32 v167, v78, v79\n"
      "v_add_u32 v244, 192, v255\n"
      "v_mul_u32_u24 v245, 6554, v244\n"
      "v_lshrrev_b32 v245, 16, v245\n"
      "v_mul_u32_u24 v245, 1120, v245\n"
      "v_lshl_add_u32 v244, v244, 4, v245\n"
      "s_add_u32 m0, s79, 134144\n"
      "s_nop 0\n"
      "buffer_load_dwordx4 v244, s[60:63], s85 offen offset:0 nt lds\n"
      "v_pk_mul_f32 v[80:81], v[80:81], s[72:73]\n"
      "v_pk_mul_f32 v[82:83], v[82:83], s[72:73]\n"
      "v_pk_mul_f32 v[84:85], v[84:85], s[72:73]\n"
      "v_pk_mul_f32 v[86:87], v[86:87], s[72:73]\n"
      "v_cvt_pk_bf16_f32 v168, v80, v81\n"
      "v_cvt_pk_bf16_f32 v169, v82, v83\n"
      "v_cvt_pk_bf16_f32 v170, v84, v85\n"
      "v_cvt_pk_bf16_f32 v171, v86, v87\n"
      "v_pk_mul_f32 v[88:89], v[88:89], s[72:73]\n"
      "v_pk_mul_f32 v[90:91], v[90:91], s[72:73]\n"
      "v_pk_mul_f32 v[92:93], v[92:93], s[72:73]\n"
      "v_pk_mul_f32 v[94:95], v[94:95], s[72:73]\n"
      "v_cvt_pk_bf16_f32 v172, v88, v89\n"
      "v_cvt_pk_bf16_f32 v173, v90, v91\n"
      "v_cvt_pk_bf16_f32 v174, v92, v93\n"
      "v_cvt_pk_bf16_f32 v175, v94, v95\n"
      "v_pk_mul_f32 v[96:97], v[96:97], s[72:73]\n"
      "v_pk_mul_f32 v[98:99], v[98:99], s[72:73]\n"
      "v_pk_mul_f32 v[100:101], v[100:101], s[72:73]\n"
      "v_pk_mul_f32 v[102:103], v[102:103], s[72:73]\n"
      "v_cvt_pk_bf16_f32 v176, v96, v97\n"
      "v_cvt_pk_bf16_f32 v177, v98, v99\n"
      "v_cvt_pk_bf16_f32 v178, v100, v101\n"
      "v_cvt_pk_bf16_f32 v179, v102, v103\n"
      "v_add_u32 v244, 256, v255\n"
      "v_mul_u32_u24 v245, 6554, v244\n"
      "v_lshrrev_b32 v245, 16, v245\n"
      "v_mul_u32_u24 v245, 1120, v245\n"
      "v_lshl_add_u32 v244, v244, 4, v245\n"
      "s_add_u32 m0, s79, 135168\n"
      "s_nop 0\n"
      "buffer_load_dwordx4 v244, s[60:63], s85 offen offset:0 nt lds\n"
      "v_pk_mul_f32 v[104:105], v[104:105], s[72:73]\n"
      "v_pk_mul_f32 v[106:107], v[106:107], s[72:73]\n"
      "v_pk_mul_f32 v[108:109], v[108:109], s[72:73]\n"
      "v_pk_mul_f32 v[110:111], v[110:111], s[72:73]\n"
      "v_cvt_pk_bf16_f32 v180, v104, v105\n"
      "v_cvt_pk_bf16_f32 v181, v106, v107\n"
      "v_cvt_pk_bf16_f32 v182, v108, v109\n"
      "v_cvt_pk_bf16_f32 v183, v110, v111\n"
      "v_pk_mul_f32 v[112:113], v[112:113], s[72:73]\n"
      "v_pk_mul_f32 v[114:115], v[114:115], s[72:73]\n"
      "v_pk_mul_f32 v[116:117], v[116:117], s[72:73]\n"
      "v_pk_mul_f32 v[118:119], v[118:119], s[72:73]\n"
      "v_cvt_pk_bf16_f32 v184, v112, v113\n"
      "v_cvt_pk_bf16_f32 v185, v114, v115\n"
      "v_cvt_pk_bf16_f32 v186, v116, v117\n"
      "v_cvt_pk_bf16_f32 v187, v118, v119\n"
      "v_pk_mul_f32 v[120:121], v[120:121], s[72:73]\n"
      "v_pk_mul_f32 v[122:123], v[122:123], s[72:73]\n"
      "v_pk_mul_f32 v[124:125], v[124:125], s[72:73]\n"
      "v_pk_mul_f32 v[126:127], v[126:127], s[72:73]\n"
      "v_cvt_pk_bf16_f32 v188, v120, v121\n"
      "v_cvt_pk_bf16_f32 v189, v122, v123\n"
      "v_cvt_pk_bf16_f32 v190, v124, v125\n"
      "v_cvt_pk_bf16_f32 v191, v126, v127\n"
      "s_waitcnt vmcnt(13)\n"
      "s_barrier\n"
      "ds_read_b128 a[192:195], v192 offset:0\n"
      "ds_read_b128 a[196:199], v192 offset:4096\n"
      "ds_read_b128 a[200:203], v193 offset:0\n"
      "ds_read_b128 a[204:207], v193 offset:4096\n"
      "ds_read_b128 a[208:211], v194 offset:0\n"
      "ds_read_b128 a[212:215], v194 offset:4096\n"
      "ds_read_b128 a[216:219], v195 offset:0\n"
      "ds_read_b128 a[220:223], v195 offset:4096\n"
      "s_add_u32 m0, s78, 98304\n"
      "s_mov_b32 s83, 32768\n"
      "v_accvgpr_read_b32 v0, a0\n"
      "v_accvgpr_read_b32 v1, a1\n"
      "v_accvgpr_read_b32 v2, a2\n"
      "v_accvgpr_read_b32 v3, a3\n"
      "v_accvgpr_read_b32 v4, a4\n"
      "v_accvgpr_read_b32 v5, a5\n"
      "v_accvgpr_read_b32 v6, a6\n"
      "v_accvgpr_read_b32 v7, a7\n"
      "v_pk_mul_f32 v[0:1], v[0:1], s[72:73]\n"
      "v_pk_mul_f32 v[2:3], v[2:3], s[72:73]\n"
      "v_pk_mul_f32 v[4:5], v[4:5], s[72:73]\n"
      "v_pk_mul_f32 v[6:7], v[6:7], s[72:73]\n"
      "v_cvt_pk_bf16_f32 v0, v0, v1\n"
      "v_cvt_pk_bf16_f32 v1, v2, v3\n"
      "v_cvt_pk_bf16_f32 v2, v4, v5\n"
      "v_cvt_pk_bf16_f32 v3, v6, v7\n"
      "v_accvgpr_write_b32 a128, v0\n"
      "v_accvgpr_write_b32 a129, v1\n"
      "v_accvgpr_write_b32 a130, v2\n"
      "v_accvgpr_write_b32 a131, v3\n"
      "buffer_load_dwordx4 v208, s[44:47], s83 offen offset:0 nt lds\n"
      "v_accvgpr_read_b32 v8, a8\n"
      "v_accvgpr_read_b32 v9, a9\n"
      "v_accvgpr_read_b32 v10, a10\n"
      "v_accvgpr_read_b32 v11, a11\n"
      "v_accvgpr_read_b32 v12, a12\n"
      "v_accvgpr_read_b32 v13, a13\n"
      "v_accvgpr_read_b32 v14, a14\n"
      "v_accvgpr_read_b32 v15, a15\n"
      "v_pk_mul_f32 v[8:9], v[8:9], s[72:73]\n"
      "v_pk_mul_f32 v[10:11], v[10:11], s[72:73]\n"
      "v_pk_mul_f32 v[12:13], v[12:13], s[72:73]\n"
      "v_pk_mul_f32 v[14:15], v[14:15], s[72:73]\n"
      "v_cvt_pk_bf16_f32 v8, v8, v9\n"
      "v_cvt_pk_bf16_f32 v9, v10, v11\n"
      "v_cvt_pk_bf16_f32 v10, v12, v13\n"
      "v_cvt_pk_bf16_f32 v11, v14, v15\n"
      "v_accvgpr_write_b32 a132, v8\n"
      "v_accvgpr_write_b32 a133, v9\n"
      "v_accvgpr_write_b32 a134, v10\n"
      "v_accvgpr_write_b32 a135, v11\n"
      "v_accvgpr_read_b32 v16, a16\n"
      "v_accvgpr_read_b32 v17, a17\n"
      "v_accvgpr_read_b32 v18, a18\n"
      "v_accvgpr_read_b32 v19, a19\n"
      "v_accvgpr_read_b32 v20, a20\n"
      "v_accvgpr_read_b32 v21, a21\n"
      "v_accvgpr_read_b32 v22, a22\n"
      "v_accvgpr_read_b32 v23, a23\n"
      "v_pk_mul_f32 v[16:17], v[16:17], s[72:73]\n"
      "v_pk_mul_f32 v[18:19], v[18:19], s[72:73]\n"
      "v_pk_mul_f32 v[20:21], v[20:21], s[72:73]\n"
      "v_pk_mul_f32 v[22:23], v[22:23], s[72:73]\n"
      "v_cvt_pk_bf16_f32 v16, v16, v17\n"
      "v_cvt_pk_bf16_f32 v17, v18, v19\n"
      "v_cvt_pk_bf16_f32 v18, v20, v21\n"
      "v_cvt_pk_bf16_f32 v19, v22, v23\n"
      "v_accvgpr_write_b32 a136, v16\n"
      "v_accvgpr_write_b32 a137, v17\n"
      "v_accvgpr_write_b32 a138, v18\n"
      "v_accvgpr_write_b32 a139, v19\n"
      "buffer_load_dwordx4 v209, s[44:47], s83 offen offset:1024 nt lds\n"
      "v_accvgpr_read_b32 v24, a24\n"
      "v_accvgpr_read_b32 v25, a25\n"
      "v_accvgpr_read_b32 v26, a26\n"
      "v_accvgpr_read_b32 v27, a27\n"
      "v_accvgpr_read_b32 v28, a28\n"
      "v_accvgpr_read_b32 v29, a29\n"
      "v_accvgpr_read_b32 v30, a30\n"
      "v_accvgpr_read_b32 v31, a31\n"
      "v_pk_mul_f32 v[24:25], v[24:25], s[72:73]\n"
      "v_pk_mul_f32 v[26:27], v[26:27], s[72:73]\n"
      "v_pk_mul_f32 v[28:29], v[28:29], s[72:73]\n"
      "v_pk_mul_f32 v[30:31], v[30:31], s[72:73]\n"
      "v_cvt_pk_bf16_f32 v24, v24, v25\n"
      "v_cvt_pk_bf16_f32 v25, v26, v27\n"
      "v_cvt_pk_bf16_f32 v26, v28, v29\n"
      "v_cvt_pk_bf16_f32 v27, v30, v31\n"
      "v_accvgpr_write_b32 a140, v24\n"
      "v_accvgpr_write_b32 a141, v25\n"
      "v_accvgpr_write_b32 a142, v26\n"
      "v_accvgpr_write_b32 a143, v27\n"
      "buffer_load_dwordx4 v208, s[44:47], s83 offen offset:2048 nt lds\n"
      "v_accvgpr_read_b32 v32, a32\n"
      "v_accvgpr_read_b32 v33, a33\n"
      "v_accvgpr_read_b32 v34, a34\n"
      "v_accvgpr_read_b32 v35, a35\n"
      "v_accvgpr_read_b32 v36, a36\n"
      "v_accvgpr_read_b32 v37, a37\n"
      "v_accvgpr_read_b32 v38, a38\n"
      "v_accvgpr_read_b32 v39, a39\n"
      "v_pk_mul_f32 v[32:33], v[32:33], s[72:73]\n"
      "v_pk_mul_f32 v[34:35], v[34:35], s[72:73]\n"
      "v_pk_mul_f32 v[36:37], v[36:37], s[72:73]\n"
      "v_pk_mul_f32 v[38:39], v[38:39], s[72:73]\n"
      "v_cvt_pk_bf16_f32 v32, v32, v33\n"
      "v_cvt_pk_bf16_f32 v33, v34, v35\n"
      "v_cvt_pk_bf16_f32 v34, v36, v37\n"
      "v_cvt_pk_bf16_f32 v35, v38, v39\n"
      "v_accvgpr_write_b32 a144, v32\n"
      "v_accvgpr_write_b32 a145, v33\n"
      "v_accvgpr_write_b32 a146, v34\n"
      "v_accvgpr_write_b32 a147, v35\n"
      "v_accvgpr_read_b32 v40, a40\n"
      "v_accvgpr_read_b32 v41, a41\n"
      "v_accvgpr_read_b32 v42, a42\n"
      "v_accvgpr_read_b32 v43, a43\n"
      "v_accvgpr_read_b32 v44, a44\n"
      "v_accvgpr_read_b32 v45, a45\n"
      "v_accvgpr_read_b32 v46, a46\n"
      "v_accvgpr_read_b32 v47, a47\n"
      "v_pk_mul_f32 v[40:41], v[40:41], s[72:73]\n"
      "v_pk_mul_f32 v[42:43], v[42:43], s[72:73]\n"
      "v_pk_mul_f32 v[44:45], v[44:45], s[72:73]\n"
      "v_pk_mul_f32 v[46:47], v[46:47], s[72:73]\n"
      "v_cvt_pk_bf16_f32 v40, v40, v41\n"
      "v_cvt_pk_bf16_f32 v41, v42, v43\n"
      "v_cvt_pk_bf16_f32 v42, v44, v45\n"
      "v_cvt_pk_bf16_f32 v43, v46, v47\n"
      "v_accvgpr_write_b32 a148, v40\n"
      "v_accvgpr_write_b32 a149, v41\n"
      "v_accvgpr_write_b32 a150, v42\n"
      "v_accvgpr_write_b32 a151, v43\n"
      "buffer_load_dwordx4 v209, s[44:47], s83 offen offset:3072 nt lds\n"
      "v_accvgpr_read_b32 v48, a48\n"
      "v_accvgpr_read_b32 v49, a49\n"
      "v_accvgpr_read_b32 v50, a50\n"
      "v_accvgpr_read_b32 v51, a51\n"
      "v_accvgpr_read_b32 v52, a52\n"
      "v_accvgpr_read_b32 v53, a53\n"
      "v_accvgpr_read_b32 v54, a54\n"
      "v_accvgpr_read_b32 v55, a55\n"
      "v_pk_mul_f32 v[48:49], v[48:49], s[72:73]\n"
      "v_pk_mul_f32 v[50:51], v[50:51], s[72:73]\n"
      "v_pk_mul_f32 v[52:53], v[52:53], s[72:73]\n"
      "v_pk_mul_f32 v[54:55], v[54:55], s[72:73]\n"
      "v_cvt_pk_bf16_f32 v48, v48, v49\n"
      "v_cvt_pk_bf16_f32 v49, v50, v51\n"
      "v_cvt_pk_bf16_f32 v50, v52, v53\n"
      "v_cvt_pk_bf16_f32 v51, v54, v55\n"
      "v_accvgpr_write_b32 a152, v48\n"
      "v_accvgpr_write_b32 a153, v49\n"
      "v_accvgpr_write_b32 a154, v50\n"
      "v_accvgpr_write_b32 a155, v51\n"
      "s_add_u32 m0, s78, 49152\n"
      "s_mov_b32 s83, 49152\n"
      "v_accvgpr_read_b32 v56, a56\n"
      "v_accvgpr_read_b32 v57, a57\n"
      "v_accvgpr_read_b32 v58, a58\n"
      "v_accvgpr_read_b32 v59, a59\n"
      "v_accvgpr_read_b32 v60, a60\n"
      "v_accvgpr_read_b32 v61, a61\n"
      "v_accvgpr_read_b32 v62, a62\n"
      "v_accvgpr_read_b32 v63, a63\n"
      "v_pk_mul_f32 v[56:57], v[56:57], s[72:73]\n"
      "v_pk_mul_f32 v[58:59], v[58:59], s[72:73]\n"
      "v_pk_mul_f32 v[60:61], v[60:61], s[72:73]\n"
      "v_pk_mul_f32 v[62:63], v[62:63], s[72:73]\n"
      "v_cvt_pk_bf16_f32 v56, v56, v57\n"
      "v_cvt_pk_bf16_f32 v57, v58, v59\n"
      "v_cvt_pk_bf16_f32 v58, v60, v61\n"
      "v_cvt_pk_bf16_f32 v59, v62, v63\n"
      "v_accvgpr_write_b32 a156, v56\n"
      "v_accvgpr_write_b32 a157, v57\n"
      "v_accvgpr_write_b32 a158, v58\n"
      "v_accvgpr_write_b32 a159, v59\n"
      "v_accvgpr_read_b32 v64, a64\n"
      "v_accvgpr_read_b32 v65, a65\n"
      "v_accvgpr_read_b32 v66, a66\n"
      "v_accvgpr_read_b32 v67, a67\n"
      "v_accvgpr_read_b32 v68, a68\n"
      "v_accvgpr_read_b32 v69, a69\n"
      "v_accvgpr_read_b32 v70, a70\n"
      "v_accvgpr_read_b32 v71, a71\n"
      "v_pk_mul_f32 v[64:65], v[64:65], s[72:73]\n"
      "v_pk_mul_f32 v[66:67], v[66:67], s[72:73]\n"
      "v_pk_mul_f32 v[68:69], v[68:69], s[72:73]\n"
      "v_pk_mul_f32 v[70:71], v[70:71], s[72:73]\n"
      "v_cvt_pk_bf16_f32 v64, v64, v65\n"
      "v_cvt_pk_bf16_f32 v65, v66, v67\n"
      "v_cvt_pk_bf16_f32 v66, v68, v69\n"
      "v_cvt_pk_bf16_f32 v67, v70, v71\n"
      "v_accvgpr_write_b32 a160, v64\n"
      "v_accvgpr_write_b32 a161, v65\n"
      "v_accvgpr_write_b32 a162, v66\n"
      "v_accvgpr_write_b32 a163, v67\n"
      "buffer_load_dwordx4 v196, s[40:43], s83 offen offset:0 nt lds\n"
      "v_accvgpr_read_b32 v72, a72\n"
      "v_accvgpr_read_b32 v73, a73\n"
      "v_accvgpr_read_b32 v74, a74\n"
      "v_accvgpr_read_b32 v75, a75\n"
      "v_accvgpr_read_b32 v76, a76\n"
      "v_accvgpr_read_b32 v77, a77\n"
      "v_accvgpr_read_b32 v78, a78\n"
      "v_accvgpr_read_b32 v79, a79\n"
      "v_pk_mul_f32 v[72:73], v[72:73], s[72:73]\n"
      "v_pk_mul_f32 v[74:75], v[74:75], s[72:73]\n"
      "v_pk_mul_f32 v[76:77], v[76:77], s[72:73]\n"
      "v_pk_mul_f32 v[78:79], v[78:79], s[72:73]\n"
      "v_cvt_pk_bf16_f32 v72, v72, v73\n"
      "v_cvt_pk_bf16_f32 v73, v74, v75\n"
      "v_cvt_pk_bf16_f32 v74, v76, v77\n"
      "v_cvt_pk_bf16_f32 v75, v78, v79\n"
      "v_accvgpr_write_b32 a164, v72\n"
      "v_accvgpr_write_b32 a165, v73\n"
      "v_accvgpr_write_b32 a166, v74\n"
      "v_accvgpr_write_b32 a167, v75\n"
      "buffer_load_dwordx4 v197, s[40:43], s83 offen offset:1024 nt lds\n"
      "v_accvgpr_read_b32 v80, a80\n"
      "v_accvgpr_read_b32 v81, a81\n"
      "v_accvgpr_read_b32 v82, a82\n"
      "v_accvgpr_read_b32 v83, a83\n"
      "v_accvgpr_read_b32 v84, a84\n"
      "v_accvgpr_read_b32 v85, a85\n"
      "v_accvgpr_read_b32 v86, a86\n"
      "v_accvgpr_read_b32 v87, a87\n"
      "v_pk_mul_f32 v[80:81], v[80:81], s[72:73]\n"
      "v_pk_mul_f32 v[82:83], v[82:83], s[72:73]\n"
      "v_pk_mul_f32 v[84:85], v[84:85], s[72:73]\n"
      "v_pk_mul_f32 v[86:87], v[86:87], s[72:73]\n"
      "v_cvt_pk_bf16_f32 v80, v80, v81\n"
      "v_cvt_pk_bf16_f32 v81, v82, v83\n"
      "v_cvt_pk_bf16_f32 v82, v84, v85\n"
      "v_cvt_pk_bf16_f32 v83, v86, v87\n"
      "v_accvgpr_write_b32 a168, v80\n"
      "v_accvgpr_write_b32 a169, v81\n"
      "v_accvgpr_write_b32 a170, v82\n"
      "v_accvgpr_write_b32 a171, v83\n"
      "v_accvgpr_read_b32 v88, a88\n"
      "v_accvgpr_read_b32 v89, a89\n"
      "v_accvgpr_read_b32 v90, a90\n"
      "v_accvgpr_read_b32 v91, a91\n"
      "v_accvgpr_read_b32 v92, a92\n"
      "v_accvgpr_read_b32 v93, a93\n"
      "v_accvgpr_read_b32 v94, a94\n"
      "v_accvgpr_read_b32 v95, a95\n"
      "v_pk_mul_f32 v[88:89], v[88:89], s[72:73]\n"
      "v_pk_mul_f32 v[90:91], v[90:91], s[72:73]\n"
      "v_pk_mul_f32 v[92:93], v[92:93], s[72:73]\n"
      "v_pk_mul_f32 v[94:95], v[94:95], s[72:73]\n"
      "v_cvt_pk_bf16_f32 v88, v88, v89\n"
      "v_cvt_pk_bf16_f32 v89, v90, v91\n"
      "v_cvt_pk_bf16_f32 v90, v92, v93\n"
      "v_cvt_pk_bf16_f32 v91, v94, v95\n"
      "v_accvgpr_write_b32 a172, v88\n"
      "v_accvgpr_write_b32 a173, v89\n"
      "v_accvgpr_write_b32 a174, v90\n"
      "v_accvgpr_write_b32 a175, v91\n"
      "buffer_load_dwordx4 v198, s[40:43], s83 offen offset:2048 nt lds\n"
      "v_accvgpr_read_b32 v96, a96\n"
      "v_accvgpr_read_b32 v97, a97\n"
      "v_accvgpr_read_b32 v98, a98\n"
      "v_accvgpr_read_b32 v99, a99\n"
      "v_accvgpr_read_b32 v100, a100\n"
      "v_accvgpr_read_b32 v101, a101\n"
      "v_accvgpr_read_b32 v102, a102\n"
      "v_accvgpr_read_b32 v103, a103\n"
      "v_pk_mul_f32 v[96:97], v[96:97], s[72:73]\n"
      "v_pk_mul_f32 v[98:99], v[98:99], s[72:73]\n"
      "v_pk_mul_f32 v[100:101], v[100:101], s[72:73]\n"
      "v_pk_mul_f32 v[102:103], v[102:103], s[72:73]\n"
      "v_cvt_pk_bf16_f32 v96, v96, v97\n"
      "v_cvt_pk_bf16_f32 v97, v98, v99\n"
      "v_cvt_pk_bf16_f32 v98, v100, v101\n"
      "v_cvt_pk_bf16_f32 v99, v102, v103\n"
      "v_accvgpr_write_b32 a176, v96\n"
      "v_accvgpr_write_b32 a177, v97\n"
      "v_accvgpr_write_b32 a178, v98\n"
      "v_accvgpr_write_b32 a179, v99\n"
      "buffer_load_dwordx4 v199, s[40:43], s83 offen offset:3072 nt lds\n"
      "v_accvgpr_read_b32 v104, a104\n"
      "v_accvgpr_read_b32 v105, a105\n"
      "v_accvgpr_read_b32 v106, a106\n"
      "v_accvgpr_read_b32 v107, a107\n"
      "v_accvgpr_read_b32 v108, a108\n"
      "v_accvgpr_read_b32 v109, a109\n"
      "v_accvgpr_read_b32 v110, a110\n"
      "v_accvgpr_read_b32 v111, a111\n"
      "v_pk_mul_f32 v[104:105], v[104:105], s[72:73]\n"
      "v_pk_mul_f32 v[106:107], v[106:107], s[72:73]\n"
      "v_pk_mul_f32 v[108:109], v[108:109], s[72:73]\n"
      "v_pk_mul_f32 v[110:111], v[110:111], s[72:73]\n"
      "v_cvt_pk_bf16_f32 v104, v104, v105\n"
      "v_cvt_pk_bf16_f32 v105, v106, v107\n"
      "v_cvt_pk_bf16_f32 v106, v108, v109\n"
      "v_cvt_pk_bf16_f32 v107, v110, v111\n"
      "v_accvgpr_write_b32 a180, v104\n"
      "v_accvgpr_write_b32 a181, v105\n"
      "v_accvgpr_write_b32 a182, v106\n"
      "v_accvgpr_write_b32 a183, v107\n"
      "v_accvgpr_read_b32 v112, a112\n"
      "v_accvgpr_read_b32 v113, a113\n"
      "v_accvgpr_read_b32 v114, a114\n"
      "v_accvgpr_read_b32 v115, a115\n"
      "v_accvgpr_read_b32 v116, a116\n"
      "v_accvgpr_read_b32 v117, a117\n"
      "v_accvgpr_read_b32 v118, a118\n"
      "v_accvgpr_read_b32 v119, a119\n"
      "v_pk_mul_f32 v[112:113], v[112:113], s[72:73]\n"
      "v_pk_mul_f32 v[114:115], v[114:115], s[72:73]\n"
      "v_pk_mul_f32 v[116:117], v[116:117], s[72:73]\n"
      "v_pk_mul_f32 v[118:119], v[118:119], s[72:73]\n"
      "v_cvt_pk_bf16_f32 v112, v112, v113\n"
      "v_cvt_pk_bf16_f32 v113, v114, v115\n"
      "v_cvt_pk_bf16_f32 v114, v116, v117\n"
      "v_cvt_pk_bf16_f32 v115, v118, v119\n"
      "v_accvgpr_write_b32 a184, v112\n"
      "v_accvgpr_write_b32 a185, v113\n"
      "v_accvgpr_write_b32 a186, v114\n"
      "v_accvgpr_write_b32 a187, v115\n"
      "v_accvgpr_read_b32 v120, a120\n"
      "v_accvgpr_read_b32 v121, a121\n"
      "v_accvgpr_read_b32 v122, a122\n"
      "v_accvgpr_read_b32 v123, a123\n"
      "v_accvgpr_read_b32 v124, a124\n"
      "v_accvgpr_read_b32 v125, a125\n"
      "v_accvgpr_read_b32 v126, a126\n"
      "v_accvgpr_read_b32 v127, a127\n"
      "v_pk_mul_f32 v[120:121], v[120:121], s[72:73]\n"
      "v_pk_mul_f32 v[122:123], v[122:123], s[72:73]\n"
      "v_pk_mul_f32 v[124:125], v[124:125], s[72:73]\n"
      "v_pk_mul_f32 v[126:127], v[126:127], s[72:73]\n"
      "v_cvt_pk_bf16_f32 v120, v120, v121\n"
      "v_cvt_pk_bf16_f32 v121, v122, v123\n"
      "v_cvt_pk_bf16_f32 v122, v124, v125\n"
      "v_cvt_pk_bf16_f32 v123, v126, v127\n"
      "v_accvgpr_write_b32 a188, v120\n"
      "v_accvgpr_write_b32 a189, v121\n"
      "v_accvgpr_write_b32 a190, v122\n"
      "v_accvgpr_write_b32 a191, v123\n"
      "s_mov_b32 s68, 65536\n"
      "s_mov_b32 s69, 49152\n"
      "s_memrealtime s[82:83]\n"
      "s_waitcnt lgkmcnt(0)\n"
      "s_mov_b32 s95, s82\n"
      "v_mfma_f32_16x16x32_bf16 v[0:3], a[192:195], v[128:131], 0\n"
      "v_mfma_f32_16x16x32_bf16 v[4:7], a[196:199], v[128:131], 0\n"
      "v_mfma_f32_16x16x32_bf16 v[0:3], a[200:203], v[132:135], v[0:3]\n"
      "v_mfma_f32_16x16x32_bf16 v[4:7], a[204:207], v[132:135], v[4:7]\n"
      "v_mfma_f32_16x16x32_bf16 v[0:3], a[208:211], v[136:139], v[0:3]\n"
      "v_mfma_f32_16x16x32_bf16 v[4:7], a[212:215], v[136:139], v[4:7]\n"
      "v_mfma_f32_16x16x32_bf16 v[0:3], a[216:219], v[140:143], v[0:3]\n"
      "v_mfma_f32_16x16x32_bf16 v[4:7], a[220:223], v[140:143], v[4:7]\n"
      "v_mfma_f32_16x16x32_bf16 v[8:11], a[192:195], v[144:147], 0\n"
      "v_mfma_f32_16x16x32_bf16 v[12:15], a[196:199], v[144:147], 0\n"
      "v_mfma_f32_16x16x32_bf16 v[8:11], a[200:203], v[148:151], v[8:11]\n"
      "v_mfma_f32_16x16x32_bf16 v[12:15], a[204:207], v[148:151], v[12:15]\n"
      "v_mfma_f32_16x16x32_bf16 v[8:11], a[208:211], v[152:155], v[8:11]\n"
      "v_mfma_f32_16x16x32_bf16 v[12:15], a[212:215], v[152:155], v[12:15]\n"
      "v_mfma_f32_16x16x32_bf16 v[8:11], a[216:219], v[156:159], v[8:11]\n"
      "v_mfma_f32_16x16x32_bf16 v[12:15], a[220:223], v[156:159], v[12:15]\n"
      "v_max3_f32 v241, v0, v1, v2\n"
      "v_max3_f32 v242, v3, v4, v5\n"
      "v_max3_f32 v241, v241, v6, v7\n"
      "v_mfma_f32_16x16x32_bf16 v[16:19], a[192:195], v[160:163], 0\n"
      "v_max_f32 v241, v241, v242\n"
      "v_mov_b32 v242, v241\n"
      "s_nop 1\n"
      "v_mfma_f32_16x16x32_bf16 v[20:23], a[196:199], v[160:163], 0\n"
      "v_permlane16_swap_b32 v242, v241\n"
      "s_nop 0\n"
      "v_max_f32 v241, v241, v242\n"
      "v_mfma_f32_16x16x32_bf16 v[16:19], a[200:203], v[164:167], v[16:19]\n"
      "v_mov_b32 v242, v241\n"
      "s_nop 1\n"
      "v_permlane32_swap_b32 v242, v241\n"
      "v_mfma_f32_16x16x32_bf16 v[20:23], a[204:207], v[164:167], v[20:23]\n"
      "s_nop 0\n"
      "v_max_f32 v241, v241, v242\n"
      "v_sub_f32 v0, v0, v241\n"
      "v_mfma_f32_16x16x32_bf16 v[16:19], a[208:211], v[168:171], v[16:19]\n"
      "v_sub_f32 v1, v1, v241\n"
      "v_sub_f32 v2, v2, v241\n"
      "v_sub_f32 v3, v3, v241\n"
      "v_mfma_f32_16x16x32_bf16 v[20:23], a[212:215], v[168:171], v[20:23]\n"
      "v_sub_f32 v4, v4, v241\n"
      "v_sub_f32 v5, v5, v241\n"
      "v_sub_f32 v6, v6, v241\n"
      "v_mfma_f32_16x16x32_bf16 v[16:19], a[216:219], v[172:175], v[16:19]\n"
      "v_sub_f32 v7, v7, v241\n"
      "v_sub_f32 v64, 0, v241\n"
      "v_sub_f32 v65, 0, v241\n"
      "v_mfma_f32_16x16x32_bf16 v[20:23], a[220:223], v[172:175], v[20:23]\n"
      "v_sub_f32 v66, 0, v241\n"
      "v_sub_f32 v67, 0, v241\n"
      "v_max3_f32 v241, v8, v9, v10\n"
      "v_mfma_f32_16x16x32_bf16 v[24:27], a[192:195], v[176:179], 0\n"
      "v_max3_f32 v242, v11, v12, v13\n"
      "v_max3_f32 v241, v241, v14, v15\n"
      "v_max_f32 v241, v241, v242\n"
      "v_mfma_f32_16x16x32_bf16 v[28:31], a[196:199], v[176:179], 0\n"
      "v_mov_b32 v242, v241\n"
      "s_nop 1\n"
      "v_permlane16_swap_b32 v242, v241\n"
      "v_mfma_f32_16x16x32_bf16 v[24:27], a[200:203], v[180:183], v[24:27]\n"
      "s_nop 0\n"
      "v_max_f32 v241, v241, v242\n"
      "v_mov_b32 v242, v241\n"
      "v_mfma_f32_16x16x32_bf16 v[28:31], a[204:207], v[180:183], v[28:31]\n"
      "s_nop 1\n"
      "v_permlane32_swap_b32 v242, v241\n"
      "s_nop 0\n"
      "v_mfma_f32_16x16x32_bf16 v[24:27], a[208:211], v[184:187], v[24:27]\n"
      "v_max_f32 v241, v241, v242\n"
      "v_sub_f32 v8, v8, v241\n"
      "v_sub_f32 v9, v9, v241\n"
      "v_mfma_f32_16x16x32_bf16 v[28:31], a[212:215], v[184:187], v[28:31]\n"
      "v_sub_f32 v10, v10, v241\n"
      "v_sub_f32 v11, v11, v241\n"
      "v_sub_f32 v12, v12, v241\n"
      "v_mfma_f32_16x16x32_bf16 v[24:27], a[216:219], v[188:191], v[24:27]\n"
      "v_sub_f32 v13, v13, v241\n"
      "v_sub_f32 v14, v14, v241\n"
      "v_sub_f32 v15, v15, v241\n"
      "v_mfma_f32_16x16x32_bf16 v[28:31], a[220:223], v[188:191], v[28:31]\n"
      "v_sub_f32 v68, 0, v241\n"
      "v_sub_f32 v69, 0, v241\n"
      "v_sub_f32 v70, 0, v241\n"
      "ds_read_b128 a[192:195], v192 offset:8192\n"
      "ds_read_b128 a[196:199], v192 offset:12288\n"
      "ds_read_b128 a[200:203], v193 offset:8192\n"
      "ds_read_b128 a[204:207], v193 offset:12288\n"
      "ds_read_b128 a[208:211], v194 offset:8192\n"
      "ds_read_b128 a[212:215], v194 offset:12288\n"
      "ds_read_b128 a[216:219], v195 offset:8192\n"
      "ds_read_b128 a[220:223], v195 offset:12288\n"
      "s_waitcnt lgkmcnt(0)\n"
      "v_sub_f32 v71, 0, v241\n"
      "v_max3_f32 v241, v16, v17, v18\n"
      "v_max3_f32 v242, v19, v20, v21\n"
      "v_max3_f32 v241, v241, v22, v23\n"
      "v_max_f32 v241, v241, v242\n"
      "v_mov_b32 v242, v241\n"
      "s_nop 1\n"
      "v_permlane16_swap_b32 v242, v241\n"
      "s_nop 0\n"
      "v_max_f32 v241, v241, v242\n"
      "v_mov_b32 v242, v241\n"
      "s_nop 1\n"
      "v_permlane32_swap_b32 v242, v241\n"
      "s_nop 0\n"
      "v_max_f32 v241, v241, v242\n"
      "v_sub_f32 v16, v16, v241\n"
      "v_sub_f32 v17, v17, v241\n"
      "v_sub_f32 v18, v18, v241\n"
      "v_sub_f32 v19, v19, v241\n"
      "v_sub_f32 v20, v20, v241\n"
      "v_sub_f32 v21, v21, v241\n"
      "v_sub_f32 v22, v22, v241\n"
      "v_sub_f32 v23, v23, v241\n"
      "v_sub_f32 v72, 0, v241\n"
      "v_sub_f32 v73, 0, v241\n"
      "v_sub_f32 v74, 0, v241\n"
      "v_sub_f32 v75, 0, v241\n"
      "s_nop 7\n"
      "v_max3_f32 v241, v24, v25, v26\n"
      "v_max3_f32 v242, v27, v28, v29\n"
      "v_max3_f32 v241, v241, v30, v31\n"
      "v_max_f32 v241, v241, v242\n"
      "v_mov_b32 v242, v241\n"
      "s_nop 1\n"
      "v_permlane16_swap_b32 v242, v241\n"
      "s_nop 0\n"
      "v_max_f32 v241, v241, v242\n"
      "v_mov_b32 v242, v241\n"
      "s_nop 1\n"
      "v_permlane32_swap_b32 v242, v241\n"
      "s_nop 0\n"
      "v_max_f32 v241, v241, v242\n"
      "v_sub_f32 v24, v24, v241\n"
      "v_sub_f32 v25, v25, v241\n"
      "v_sub_f32 v26, v26, v241\n"
      "v_sub_f32 v27, v27, v241\n"
      "v_sub_f32 v28, v28, v241\n"
      "v_sub_f32 v29, v29, v241\n"
      "v_sub_f32 v30, v30, v241\n"
      "v_sub_f32 v31, v31, v241\n"
      "v_sub_f32 v76, 0, v241\n"
      "v_sub_f32 v77, 0, v241\n"
      "v_sub_f32 v78, 0, v241\n"
      "v_sub_f32 v79, 0, v241\n"
      "v_exp_f32 v232, v0\n"
      "v_exp_f32 v233, v1\n"
      "s_nop 0\n"
      "v_cvt_pk_bf16_f32 v96, v232, v233\n"
      "v_exp_f32 v234, v2\n"
      "v_exp_f32 v235, v3\n"
      "s_nop 0\n"
      "v_cvt_pk_bf16_f32 v97, v234, v235\n"
      "v_exp_f32 v236, v4\n"
      "v_exp_f32 v237, v5\n"
      "s_nop 0\n"
      "v_cvt_pk_bf16_f32 v98, v236, v237\n"
      "v_exp_f32 v238, v6\n"
      "v_exp_f32 v239, v7\n"
      "s_nop 0\n"
      "v_cvt_pk_bf16_f32 v99, v238, v239\n"
      "v_exp_f32 v232, v8\n"
      "v_exp_f32 v233, v9\n"
      "v_mfma_f32_16x16x32_bf16 v[80:83], v[228:231], v[96:99], 0\n"
      "s_nop 0\n"
      "v_cvt_pk_bf16_f32 v100, v232, v233\n"
      "v_exp_f32 v234, v10\n"
      "v_exp_f32 v235, v11\n"
      "s_nop 0\n"
      "v_cvt_pk_bf16_f32 v101, v234, v235\n"
      "v_exp_f32 v236, v12\n"
      "v_exp_f32 v237, v13\n"
      "s_nop 0\n"
      "v_cvt_pk_bf16_f32 v102, v236, v237\n"
      "v_exp_f32 v238, v14\n"
      "v_exp_f32 v239, v15\n"
      "s_nop 0\n"
      "v_cvt_pk_bf16_f32 v103, v238, v239\n"
      "v_exp_f32 v232, v16\n"
      "v_exp_f32 v233, v17\n"
      "v_mfma_f32_16x16x32_bf16 v[84:87], v[228:231], v[100:103], 0\n"
      "s_nop 0\n"
      "v_cvt_pk_bf16_f32 v104, v232, v233\n"
      "v_exp_f32 v234, v18\n"
      "v_exp_f32 v235, v19\n"
      "s_nop 0\n"
      "v_cvt_pk_bf16_f32 v105, v234, v235\n"
      "v_exp_f32 v236, v20\n"
      "v_exp_f32 v237, v21\n"
      "s_nop 0\n"
      "v_cvt_pk_bf16_f32 v106, v236, v237\n"
      "v_exp_f32 v238, v22\n"
      "v_exp_f32 v239, v23\n"
      "s_nop 0\n"
      "v_cvt_pk_bf16_f32 v107, v238, v239\n"
      "v_exp_f32 v232, v24\n"
      "v_exp_f32 v233, v25\n"
      "v_mfma_f32_16x16x32_bf16 v[88:91], v[228:231], v[104:107], 0\n"
      "s_nop 0\n"
      "v_cvt_pk_bf16_f32 v108, v232, v233\n"
      "v_exp_f32 v234, v26\n"
      "s_cmp_eq_u32 s80, 0\n"
      "s_cselect_b32 s71, 2, -1\n"
      "v_mfma_f32_16x16x32_bf16 v[32:35], a[192:195], v[128:131], v[64:67]\n"
      "ds_read_b64_tr_b16 a[224:225], v200 offset:0\n"
      "ds_read_b64_tr_b16 a[226:227], v200 offset:4096\n"
      "v_mfma_f32_16x16x32_bf16 v[36:39], a[196:199], v[128:131], v[64:67]\n"
      "s_add_u32 s95, s82, 80\n"
      "v_exp_f32 v235, v27\n"
      "v_mfma_f32_16x16x32_bf16 v[32:35], a[200:203], v[132:135], v[32:35]\n"
      "ds_read_b64_tr_b16 a[228:229], v201 offset:0\n"
      "v_cvt_pk_bf16_f32 v109, v234, v235\n"
      "v_mfma_f32_16x16x32_bf16 v[36:39], a[204:207], v[132:135], v[36:39]\n"
      "v_exp_f32 v236, v28\n"
      "v_mfma_f32_16x16x32_bf16 v[32:35], a[208:211], v[136:139], v[32:35]\n"
      "ds_read_b64_tr_b16 a[230:231], v201 offset:4096\n"
      "ds_read_b64_tr_b16 a[232:233], v202 offset:0\n"
      "v_mfma_f32_16x16x32_bf16 v[36:39], a[212:215], v[136:139], v[36:39]\n"
      "v_exp_f32 v237, v29\n"
      "v_mfma_f32_16x16x32_bf16 v[32:35], a[216:219], v[140:143], v[32:35]\n"
      "ds_read_b64_tr_b16 a[234:235], v202 offset:4096\n"
      "v_cvt_pk_bf16_f32 v110, v236, v237\n"
      "v_mfma_f32_16x16x32_bf16 v[36:39], a[220:223], v[140:143], v[36:39]\n"
      "v_exp_f32 v238, v30\n"
      "v_mfma_f32_16x16x32_bf16 v[40:43], a[192:195], v[144:147], v[68:71]\n"
      "ds_read_b64_tr_b16 a[236:237], v203 offset:0\n"
      "ds_read_b64_tr_b16 a[238:239], v203 offset:4096\n"
      "v_mfma_f32_16x16x32_bf16 v[44:47], a[196:199], v[144:147], v[68:71]\n"
      "v_exp_f32 v239, v31\n"
      "v_mfma_f32_16x16x32_bf16 v[40:43], a[200:203], v[148:151], v[40:43]\n"
      "ds_read_b64_tr_b16 a[240:241], v204 offset:0\n"
      "v_cvt_pk_bf16_f32 v111, v238, v239\n"
      "v_mfma_f32_16x16x32_bf16 v[44:47], a[204:207], v[148:151], v[44:47]\n"
      "ds_read_b64_tr_b16 a[242:243], v204 offset:4096\n"
      "ds_read_b64_tr_b16 a[244:245], v205 offset:0\n"
      "v_mfma_f32_16x16x32_bf16 v[40:43], a[208:211], v[152:155], v[40:43]\n"
      "v_exp_f32 v232, v32\n"
      "v_mfma_f32_16x16x32_bf16 v[92:95], v[228:231], v[108:111], 0\n"
      "ds_read_b64_tr_b16 a[246:247], v205 offset:4096\n"
      "ds_read_b64_tr_b16 a[248:249], v206 offset:0\n"
      "v_mfma_f32_16x16x32_bf16 v[44:47], a[212:215], v[152:155], v[44:47]\n"
      "v_exp_f32 v233, v33\n"
      "v_mfma_f32_16x16x32_bf16 v[40:43], a[216:219], v[156:159], v[40:43]\n"
      "ds_read_b64_tr_b16 a[250:251], v206 offset:4096\n"
      "v_cvt_pk_bf16_f32 v112, v232, v233\n"
      "v_mfma_f32_16x16x32_bf16 v[44:47], a[220:223], v[156:159], v[44:47]\n"
      "v_exp_f32 v234, v34\n"
      "v_mfma_f32_16x16x32_bf16 v[48:51], a[192:195], v[160:163], v[72:75]\n"
      "ds_read_b64_tr_b16 a[252:253], v207 offset:0\n"
      "ds_read_b64_tr_b16 a[254:255], v207 offset:4096\n"
      "v_mfma_f32_16x16x32_bf16 v[52:55], a[196:199], v[160:163], v[72:75]\n"
      "v_exp_f32 v235, v35\n"
      "v_mfma_f32_16x16x32_bf16 v[48:51], a[200:203], v[164:167], v[48:51]\n"
      "v_cvt_pk_bf16_f32 v113, v234, v235\n"
      "v_mfma_f32_16x16x32_bf16 v[52:55], a[204:207], v[164:167], v[52:55]\n"
      "v_exp_f32 v236, v36\n"
      "v_mfma_f32_16x16x32_bf16 v[48:51], a[208:211], v[168:171], v[48:51]\n"
      "v_max3_f32 v240, v80, v84, v88\n"
      "v_max_f32 v240, v240, v92\n"
      "v_mfma_f32_16x16x32_bf16 v[52:55], a[212:215], v[168:171], v[52:55]\n"
      "v_exp_f32 v237, v37\n"
      "v_mfma_f32_16x16x32_bf16 v[48:51], a[216:219], v[172:175], v[48:51]\n"
      "v_cvt_pk_bf16_f32 v114, v236, v237\n"
      "v_mfma_f32_16x16x32_bf16 v[52:55], a[220:223], v[172:175], v[52:55]\n"
      "v_exp_f32 v238, v38\n"
      "v_mfma_f32_16x16x32_bf16 v[56:59], a[192:195], v[176:179], v[76:79]\n"
      "v_cmp_nge_f32 vcc, s74, v240\n"
      "v_mfma_f32_16x16x32_bf16 v[60:63], a[196:199], v[176:179], v[76:79]\n"
      "v_exp_f32 v239, v39\n"
      "v_mfma_f32_16x16x32_bf16 v[56:59], a[200:203], v[180:183], v[56:59]\n"
      "v_cvt_pk_bf16_f32 v115, v238, v239\n"
      "v_mfma_f32_16x16x32_bf16 v[60:63], a[204:207], v[180:183], v[60:63]\n"
      "v_mfma_f32_16x16x32_bf16 v[56:59], a[208:211], v[184:187], v[56:59]\n"
      "v_exp_f32 v232, v40\n"
      "v_mfma_f32_16x16x32_bf16 v[60:63], a[212:215], v[184:187], v[60:63]\n"
      "s_waitcnt vmcnt(8) lgkmcnt(0)\n"
      "s_barrier\n"
      "v_mfma_f32_16x16x32_bf16 v[56:59], a[216:219], v[188:191], v[56:59]\n"
      "v_exp_f32 v233, v41\n"
      "v_mfma_f32_16x16x32_bf16 v[60:63], a[220:223], v[188:191], v[60:63]\n"
      "v_cvt_pk_bf16_f32 v116, v232, v233\n"
      "s_cbranch_vccnz STUB0_%=\n"
      "BACK0_%=:\n"
      "v_mfma_f32_16x16x32_bf16 a[0:3], a[224:227], v[96:99], 0\n"
      "v_exp_f32 v234, v42\n"
      "v_mfma_f32_16x16x32_bf16 a[4:7], a[228:231], v[96:99], 0\n"
      "ds_read_b128 a[192:195], v192 offset:16384\n"
      "v_mfma_f32_16x16x32_bf16 v[212:215], v[228:231], v[112:115], v[80:83]\n"
      "v_exp_f32 v235, v43\n"
      "v_mfma_f32_16x16x32_bf16 a[8:11], a[232:235], v[96:99], 0\n"
      "ds_read_b128 a[196:199], v192 offset:20480\n"
      "v_cvt_pk_bf16_f32 v117, v234, v235\n"
      "v_mfma_f32_16x16x32_bf16 a[12:15], a[236:239], v[96:99], 0\n"
      "v_exp_f32 v236, v44\n"
      "v_mfma_f32_16x16x32_bf16 a[16:19], a[240:243], v[96:99], 0\n"
      "ds_read_b128 a[200:203], v193 offset:16384\n"
      "v_mfma_f32_16x16x32_bf16 a[20:23], a[244:247], v[96:99], 0\n"
      "v_exp_f32 v237, v45\n"
      "v_mfma_f32_16x16x32_bf16 a[24:27], a[248:251], v[96:99], 0\n"
      "ds_read_b128 a[204:207], v193 offset:20480\n"
      "v_cvt_pk_bf16_f32 v118, v236, v237\n"
      "v_mfma_f32_16x16x32_bf16 a[28:31], a[252:255], v[96:99], 0\n"
      "v_exp_f32 v238, v46\n"
      "v_mfma_f32_16x16x32_bf16 a[32:35], a[224:227], v[100:103], 0\n"
      "ds_read_b128 a[208:211], v194 offset:16384\n"
      "v_mfma_f32_16x16x32_bf16 a[36:39], a[228:231], v[100:103], 0\n"
      "v_exp_f32 v239, v47\n"
      "v_mfma_f32_16x16x32_bf16 a[40:43], a[232:235], v[100:103], 0\n"
      "ds_read_b128 a[212:215], v194 offset:20480\n"
      "v_cvt_pk_bf16_f32 v119, v238, v239\n"
      "v_mfma_f32_16x16x32_bf16 a[44:47], a[236:239], v[100:103], 0\n"
      "ds_read_b128 a[216:219], v195 offset:16384\n"
      "v_mfma_f32_16x16x32_bf16 a[48:51], a[240:243], v[100:103], 0\n"
      "v_exp_f32 v232, v48\n"
      "v_mfma_f32_16x16x32_bf16 v[216:219], v[228:231], v[116:119], v[84:87]\n"
      "ds_read_b128 a[220:223], v195 offset:20480\n"
      "v_mfma_f32_16x16x32_bf16 a[52:55], a[244:247], v[100:103], 0\n"
      "v_exp_f32 v233, v49\n"
      "v_mfma_f32_16x16x32_bf16 a[56:59], a[248:251], v[100:103], 0\n"
      "v_cvt_pk_bf16_f32 v120, v232, v233\n"
      "v_mfma_f32_16x16x32_bf16 a[60:63], a[252:255], v[100:103], 0\n"
      "s_add_u32 m0, s78, 114688\n"
      "v_exp_f32 v234, v50\n"
      "v_mfma_f32_16x16x32_bf16 a[64:67], a[224:227], v[104:107], 0\n"
      "buffer_load_dwordx4 v208, s[44:47], s69 offen offset:0 nt lds\n"
      "v_mfma_f32_16x16x32_bf16 a[68:71], a[228:231], v[104:107], 0\n"
      "v_exp_f32 v235, v51\n"
      "v_mfma_f32_16x16x32_bf16 a[72:75], a[232:235], v[104:107], 0\n"
      "v_cvt_pk_bf16_f32 v121, v234, v235\n"
      "v_mfma_f32_16x16x32_bf16 a[76:79], a[236:239], v[104:107], 0\n"
      "v_exp_f32 v236, v52\n"
      "v_mfma_f32_16x16x32_bf16 a[80:83], a[240:243], v[104:107], 0\n"
      "buffer_load_dwordx4 v209, s[44:47], s69 offen offset:1024 nt lds\n"
      "v_mfma_f32_16x16x32_bf16 a[84:87], a[244:247], v[104:107], 0\n"
      "v_exp_f32 v237, v53\n"
      "v_mfma_f32_16x16x32_bf16 a[88:91], a[248:251], v[104:107], 0\n"
      "v_cvt_pk_bf16_f32 v122, v236, v237\n"
      "v_mfma_f32_16x16x32_bf16 a[92:95], a[252:255], v[104:107], 0\n"
      "v_exp_f32 v238, v54\n"
      "v_mfma_f32_16x16x32_bf16 a[96:99], a[224:227], v[108:111], 0\n"
      "buffer_load_dwordx4 v208, s[44:47], s69 offen offset:2048 nt lds\n"
      "v_mfma_f32_16x16x32_bf16 a[100:103], a[228:231], v[108:111], 0\n"
      "v_exp_f32 v239, v55\n"
      "v_mfma_f32_16x16x32_bf16 a[104:107], a[232:235], v[108:111], 0\n"
      "v_cvt_pk_bf16_f32 v123, v238, v239\n"
      "v_mfma_f32_16x16x32_bf16 a[108:111], a[236:239], v[108:111], 0\n"
      "buffer_load_dwordx4 v209, s[44:47], s69 offen offset:3072 nt lds\n"
      "v_mfma_f32_16x16x32_bf16 a[112:115], a[240:243], v[108:111], 0\n"
      "s_add_u32 s69, s69, 16384\n"
      "v_exp_f32 v232, v56\n"
      "v_mfma_f32_16x16x32_bf16 v[220:223], v[228:231], v[120:123], v[88:91]\n"
      "v_mfma_f32_16x16x32_bf16 a[116:119], a[244:247], v[108:111], 0\n"
      "v_exp_f32 v233, v57\n"
      "v_mfma_f32_16x16x32_bf16 a[120:123], a[248:251], v[108:111], 0\n"
      "s_waitcnt lgkmcnt(0)\n"
      "v_cvt_pk_bf16_f32 v124, v232, v233\n"
      "v_mfma_f32_16x16x32_bf16 a[124:127], a[252:255], v[108:111], 0\n"
      "v_exp_f32 v234, v58\n"
      "s_mov_b32 s92, -1\n"
      "s_mov_b32 s70, 3\n"
      "s_branch L10_%=\n"
      ".p2align 3\n"
      "L00_%=:\n"
      "v_mfma_f32_16x16x32_bf16 v[0:3], a[192:195], v[128:131], v[64:67]\n"
      "ds_read_b64_tr_b16 a[224:225], v200 offset:57344\n"
      "ds_read_b64_tr_b16 a[226:227], v200 offset:61440\n"
      "v_mfma_f32_16x16x32_bf16 v[4:7], a[196:199], v[128:131], v[64:67]\n"
      "s_add_u32 s95, s82, 80\n"
      "v_exp_f32 v235, v59\n"
      "v_mfma_f32_16x16x32_bf16 v[0:3], a[200:203], v[132:135], v[0:3]\n"
      "ds_read_b64_tr_b16 a[228:229], v201 offset:57344\n"
      "v_cvt_pk_bf16_f32 v125, v234, v235\n"
      "v_mfma_f32_16x16x32_bf16 v[4:7], a[204:207], v[132:135], v[4:7]\n"
      "v_exp_f32 v236, v60\n"
      "v_mfma_f32_16x16x32_bf16 v[0:3], a[208:211], v[136:139], v[0:3]\n"
      "ds_read_b64_tr_b16 a[230:231], v201 offset:61440\n"
      "ds_read_b64_tr_b16 a[232:233], v202 offset:57344\n"
      "v_mfma_f32_16x16x32_bf16 v[4:7], a[212:215], v[136:139], v[4:7]\n"
      "v_exp_f32 v237, v61\n"
      "v_mfma_f32_16x16x32_bf16 v[0:3], a[216:219], v[140:143], v[0:3]\n"
      "ds_read_b64_tr_b16 a[234:235], v202 offset:61440\n"
      "v_cvt_pk_bf16_f32 v126, v236, v237\n"
      "v_mfma_f32_16x16x32_bf16 v[4:7], a[220:223], v[140:143], v[4:7]\n"
      "v_exp_f32 v238, v62\n"
      "v_mfma_f32_16x16x32_bf16 v[8:11], a[192:195], v[144:147], v[68:71]\n"
      "ds_read_b64_tr_b16 a[236:237], v203 offset:57344\n"
      "ds_read_b64_tr_b16 a[238:239], v203 offset:61440\n"
      "v_mfma_f32_16x16x32_bf16 v[12:15], a[196:199], v[144:147], v[68:71]\n"
      "v_exp_f32 v239, v63\n"
      "v_mfma_f32_16x16x32_bf16 v[8:11], a[200:203], v[148:151], v[8:11]\n"
      "ds_read_b64_tr_b16 a[240:241], v204 offset:57344\n"
      "v_cvt_pk_bf16_f32 v127, v238, v239\n"
      "v_mfma_f32_16x16x32_bf16 v[12:15], a[204:207], v[148:151], v[12:15]\n"
      "ds_read_b64_tr_b16 a[242:243], v204 offset:61440\n"
      "ds_read_b64_tr_b16 a[244:245], v205 offset:57344\n"
      "v_mfma_f32_16x16x32_bf16 v[8:11], a[208:211], v[152:155], v[8:11]\n"
      "v_exp_f32 v232, v0\n"
      "v_mfma_f32_16x16x32_bf16 v[224:227], v[228:231], v[124:127], v[92:95]\n"
      "ds_read_b64_tr_b16 a[246:247], v205 offset:61440\n"
      "ds_read_b64_tr_b16 a[248:249], v206 offset:57344\n"
      "v_mfma_f32_16x16x32_bf16 v[12:15], a[212:215], v[152:155], v[12:15]\n"
      "v_exp_f32 v233, v1\n"
      "v_mfma_f32_16x16x32_bf16 v[8:11], a[216:219], v[156:159], v[8:11]\n"
      "ds_read_b64_tr_b16 a[250:251], v206 offset:61440\n"
      "v_cvt_pk_bf16_f32 v96, v232, v233\n"
      "v_mfma_f32_16x16x32_bf16 v[12:15], a[220:223], v[156:159], v[12:15]\n"
      "v_exp_f32 v234, v2\n"
      "v_mfma_f32_16x16x32_bf16 v[16:19], a[192:195], v[160:163], v[72:75]\n"
      "ds_read_b64_tr_b16 a[252:253], v207 offset:57344\n"
      "ds_read_b64_tr_b16 a[254:255], v207 offset:61440\n"
      "v_mfma_f32_16x16x32_bf16 v[20:23], a[196:199], v[160:163], v[72:75]\n"
      "v_exp_f32 v235, v3\n"
      "v_mfma_f32_16x16x32_bf16 v[16:19], a[200:203], v[164:167], v[16:19]\n"
      "v_cvt_pk_bf16_f32 v97, v234, v235\n"
      "v_mfma_f32_16x16x32_bf16 v[20:23], a[204:207], v[164:167], v[20:23]\n"
      "v_exp_f32 v236, v4\n"
      "v_mfma_f32_16x16x32_bf16 v[16:19], a[208:211], v[168:171], v[16:19]\n"
      "v_max3_f32 v240, v212, v216, v220\n"
      "v_max_f32 v240, v240, v224\n"
      "v_mfma_f32_16x16x32_bf16 v[20:23], a[212:215], v[168:171], v[20:23]\n"
      "v_exp_f32 v237, v5\n"
      "v_mfma_f32_16x16x32_bf16 v[16:19], a[216:219], v[172:175], v[16:19]\n"
      "v_cvt_pk_bf16_f32 v98, v236, v237\n"
      "v_mfma_f32_16x16x32_bf16 v[20:23], a[220:223], v[172:175], v[20:23]\n"
      "v_exp_f32 v238, v6\n"
      "v_mfma_f32_16x16x32_bf16 v[24:27], a[192:195], v[176:179], v[76:79]\n"
      "v_cmp_nge_f32 vcc, s74, v240\n"
      "v_mfma_f32_16x16x32_bf16 v[28:31], a[196:199], v[176:179], v[76:79]\n"
      "v_exp_f32 v239, v7\n"
      "v_mfma_f32_16x16x32_bf16 v[24:27], a[200:203], v[180:183], v[24:27]\n"
      "v_cvt_pk_bf16_f32 v99, v238, v239\n"
      "v_mfma_f32_16x16x32_bf16 v[28:31], a[204:207], v[180:183], v[28:31]\n"
      "v_mfma_f32_16x16x32_bf16 v[24:27], a[208:211], v[184:187], v[24:27]\n"
      "v_exp_f32 v232, v8\n"
      "v_mfma_f32_16x16x32_bf16 v[28:31], a[212:215], v[184:187], v[28:31]\n"
      "s_waitcnt lgkmcnt(0)\n"
      "v_mfma_f32_16x16x32_bf16 v[24:27], a[216:219], v[188:191], v[24:27]\n"
      "v_exp_f32 v233, v9\n"
      "v_mfma_f32_16x16x32_bf16 v[28:31], a[220:223], v[188:191], v[28:31]\n"
      "v_cvt_pk_bf16_f32 v100, v232, v233\n"
      "s_cbranch_vccnz STUB1_%=\n"
      "BACK1_%=:\n"
      "v_mfma_f32_16x16x32_bf16 a[0:3], a[224:227], v[112:115], a[0:3]\n"
      "v_exp_f32 v234, v10\n"
      "v_mfma_f32_16x16x32_bf16 a[4:7], a[228:231], v[112:115], a[4:7]\n"
      "ds_read_b128 a[192:195], v192 offset:8192\n"
      "v_mfma_f32_16x16x32_bf16 v[80:83], v[228:231], v[96:99], v[212:215]\n"
      "v_exp_f32 v235, v11\n"
      "v_mfma_f32_16x16x32_bf16 a[8:11], a[232:235], v[112:115], a[8:11]\n"
      "ds_read_b128 a[196:199], v192 offset:12288\n"
      "v_cvt_pk_bf16_f32 v101, v234, v235\n"
      "v_mfma_f32_16x16x32_bf16 a[12:15], a[236:239], v[112:115], a[12:15]\n"
      "v_exp_f32 v236, v12\n"
      "v_mfma_f32_16x16x32_bf16 a[16:19], a[240:243], v[112:115], a[16:19]\n"
      "ds_read_b128 a[200:203], v193 offset:8192\n"
      "v_mfma_f32_16x16x32_bf16 a[20:23], a[244:247], v[112:115], a[20:23]\n"
      "v_exp_f32 v237, v13\n"
      "v_mfma_f32_16x16x32_bf16 a[24:27], a[248:251], v[112:115], a[24:27]\n"
      "ds_read_b128 a[204:207], v193 offset:12288\n"
      "v_cvt_pk_bf16_f32 v102, v236, v237\n"
      "v_mfma_f32_16x16x32_bf16 a[28:31], a[252:255], v[112:115], a[28:31]\n"
      "v_exp_f32 v238, v14\n"
      "v_mfma_f32_16x16x32_bf16 a[32:35], a[224:227], v[116:119], a[32:35]\n"
      "ds_read_b128 a[208:211], v194 offset:8192\n"
      "v_mfma_f32_16x16x32_bf16 a[36:39], a[228:231], v[116:119], a[36:39]\n"
      "v_exp_f32 v239, v15\n"
      "v_mfma_f32_16x16x32_bf16 a[40:43], a[232:235], v[116:119], a[40:43]\n"
      "ds_read_b128 a[212:215], v194 offset:12288\n"
      "v_cvt_pk_bf16_f32 v103, v238, v239\n"
      "v_mfma_f32_16x16x32_bf16 a[44:47], a[236:239], v[116:119], a[44:47]\n"
      "ds_read_b128 a[216:219], v195 offset:8192\n"
      "v_mfma_f32_16x16x32_bf16 a[48:51], a[240:243], v[116:119], a[48:51]\n"
      "v_exp_f32 v232, v16\n"
      "v_mfma_f32_16x16x32_bf16 v[84:87], v[228:231], v[100:103], v[216:219]\n"
      "ds_read_b128 a[220:223], v195 offset:12288\n"
      "v_mfma_f32_16x16x32_bf16 a[52:55], a[244:247], v[116:119], a[52:55]\n"
      "v_exp_f32 v233, v17\n"
      "v_mfma_f32_16x16x32_bf16 a[56:59], a[248:251], v[116:119], a[56:59]\n"
      "s_memrealtime s[82:83]\n"
      "v_cvt_pk_bf16_f32 v104, v232, v233\n"
      "v_mfma_f32_16x16x32_bf16 a[60:63], a[252:255], v[116:119], a[60:63]\n"
      "s_add_u32 m0, s78, 49152\n"
      "v_exp_f32 v234, v18\n"
      "v_mfma_f32_16x16x32_bf16 a[64:67], a[224:227], v[120:123], a[64:67]\n"
      "buffer_load_dwordx4 v196, s[40:43], s68 offen offset:0 nt lds\n"
      "v_mfma_f32_16x16x32_bf16 a[68:71], a[228:231], v[120:123], a[68:71]\n"
      "v_exp_f32 v235, v19\n"
      "v_mfma_f32_16x16x32_bf16 a[72:75], a[232:235], v[120:123], a[72:75]\n"
      "v_cvt_pk_bf16_f32 v105, v234, v235\n"
      "v_mfma_f32_16x16x32_bf16 a[76:79], a[236:239], v[120:123], a[76:79]\n"
      "v_exp_f32 v236, v20\n"
      "v_mfma_f32_16x16x32_bf16 a[80:83], a[240:243], v[120:123], a[80:83]\n"
      "buffer_load_dwordx4 v197, s[40:43], s68 offen offset:1024 nt lds\n"
      "v_mfma_f32_16x16x32_bf16 a[84:87], a[244:247], v[120:123], a[84:87]\n"
      "v_exp_f32 v237, v21\n"
      "v_mfma_f32_16x16x32_bf16 a[88:91], a[248:251], v[120:123], a[88:91]\n"
      "v_cvt_pk_bf16_f32 v106, v236, v237\n"
      "v_mfma_f32_16x16x32_bf16 a[92:95], a[252:255], v[120:123], a[92:95]\n"
      "v_exp_f32 v238, v22\n"
      "v_mfma_f32_16x16x32_bf16 a[96:99], a[224:227], v[124:127], a[96:99]\n"
      "buffer_load_dwordx4 v198, s[40:43], s68 offen offset:2048 nt lds\n"
      "v_mfma_f32_16x16x32_bf16 a[100:103], a[228:231], v[124:127], a[100:103]\n"
      "v_exp_f32 v239, v23\n"
      "v_mfma_f32_16x16x32_bf16 a[104:107], a[232:235], v[124:127], a[104:107]\n"
      "v_cvt_pk_bf16_f32 v107, v238, v239\n"
      "v_mfma_f32_16x16x32_bf16 a[108:111], a[236:239], v[124:127], a[108:111]\n"
      "buffer_load_dwordx4 v199, s[40:43], s68 offen offset:3072 nt lds\n"
      "v_mfma_f32_16x16x32_bf16 a[112:115], a[240:243], v[124:127], a[112:115]\n"
      "s_add_u32 s68, s68, 16384\n"
      "v_exp_f32 v232, v24\n"
      "v_mfma_f32_16x16x32_bf16 v[88:91], v[228:231], v[104:107], v[220:223]\n"
      "v_mfma_f32_16x16x32_bf16 a[116:119], a[244:247], v[124:127], a[116:119]\n"
      "v_exp_f32 v233, v25\n"
      "v_mfma_f32_16x16x32_bf16 a[120:123], a[248:251], v[124:127], a[120:123]\n"
      "s_waitcnt lgkmcnt(0)\n"
      "v_cvt_pk_bf16_f32 v108, v232, v233\n"
      "v_mfma_f32_16x16x32_bf16 a[124:127], a[252:255], v[124:127], a[124:127]\n"
      "v_exp_f32 v234, v26\n"
      "s_cmp_lt_u32 s82, s95\n"
      "s_cbranch_scc1 STUB2_%=\n"
      "BACK2_%=:\n"
      "v_mfma_f32_16x16x32_bf16 v[32:35], a[192:195], v[128:131], v[64:67]\n"
      "ds_read_b64_tr_b16 a[224:225], v200 offset:0\n"
      "ds_read_b64_tr_b16 a[226:227], v200 offset:4096\n"
      "v_mfma_f32_16x16x32_bf16 v[36:39], a[196:199], v[128:131], v[64:67]\n"
      "s_add_u32 s95, s82, 80\n"
      "v_exp_f32 v235, v27\n"
      "v_mfma_f32_16x16x32_bf16 v[32:35], a[200:203], v[132:135], v[32:35]\n"
      "ds_read_b64_tr_b16 a[228:229], v201 offset:0\n"
      "v_cvt_pk_bf16_f32 v109, v234, v235\n"
      "v_mfma_f32_16x16x32_bf16 v[36:39], a[204:207], v[132:135], v[36:39]\n"
      "v_exp_f32 v236, v28\n"
      "v_mfma_f32_16x16x32_bf16 v[32:35], a[208:211], v[136:139], v[32:35]\n"
      "ds_read_b64_tr_b16 a[230:231], v201 offset:4096\n"
      "ds_read_b64_tr_b16 a[232:233], v202 offset:0\n"
      "v_mfma_f32_16x16x32_bf16 v[36:39], a[212:215], v[136:139], v[36:39]\n"
      "v_exp_f32 v237, v29\n"
      "v_mfma_f32_16x16x32_bf16 v[32:35], a[216:219], v[140:143], v[32:35]\n"
      "ds_read_b64_tr_b16 a[234:235], v202 offset:4096\n"
      "v_cvt_pk_bf16_f32 v110, v236, v237\n"
      "v_mfma_f32_16x16x32_bf16 v[36:39], a[220:223], v[140:143], v[36:39]\n"
      "v_exp_f32 v238, v30\n"
      "v_mfma_f32_16x16x32_bf16 v[40:43], a[192:195], v[144:147], v[68:71]\n"
      "ds_read_b64_tr_b16 a[236:237], v203 offset:0\n"
      "ds_read_b64_tr_b16 a[238:239], v203 offset:4096\n"
      "v_mfma_f32_16x16x32_bf16 v[44:47], a[196:199], v[144:147], v[68:71]\n"
      "v_exp_f32 v239, v31\n"
      "v_mfma_f32_16x16x32_bf16 v[40:43], a[200:203], v[148:151], v[40:43]\n"
      "ds_read_b64_tr_b16 a[240:241], v204 offset:0\n"
      "v_cvt_pk_bf16_f32 v111, v238, v239\n"
      "v_mfma_f32_16x16x32_bf16 v[44:47], a[204:207], v[148:151], v[44:47]\n"
      "ds_read_b64_tr_b16 a[242:243], v204 offset:4096\n"
      "ds_read_b64_tr_b16 a[244:245], v205 offset:0\n"
      "v_mfma_f32_16x16x32_bf16 v[40:43], a[208:211], v[152:155], v[40:43]\n"
      "v_exp_f32 v232, v32\n"
      "v_mfma_f32_16x16x32_bf16 v[92:95], v[228:231], v[108:111], v[224:227]\n"
      "ds_read_b64_tr_b16 a[246:247], v205 offset:4096\n"
      "ds_read_b64_tr_b16 a[248:249], v206 offset:0\n"
      "v_mfma_f32_16x16x32_bf16 v[44:47], a[212:215], v[152:155], v[44:47]\n"
      "v_exp_f32 v233, v33\n"
      "v_mfma_f32_16x16x32_bf16 v[40:43], a[216:219], v[156:159], v[40:43]\n"
      "ds_read_b64_tr_b16 a[250:251], v206 offset:4096\n"
      "v_cvt_pk_bf16_f32 v112, v232, v233\n"
      "v_mfma_f32_16x16x32_bf16 v[44:47], a[220:223], v[156:159], v[44:47]\n"
      "v_exp_f32 v234, v34\n"
      "v_mfma_f32_16x16x32_bf16 v[48:51], a[192:195], v[160:163], v[72:75]\n"
      "ds_read_b64_tr_b16 a[252:253], v207 offset:0\n"
      "ds_read_b64_tr_b16 a[254:255], v207 offset:4096\n"
      "v_mfma_f32_16x16x32_bf16 v[52:55], a[196:199], v[160:163], v[72:75]\n"
      "v_exp_f32 v235, v35\n"
      "v_mfma_f32_16x16x32_bf16 v[48:51], a[200:203], v[164:167], v[48:51]\n"
      "v_cvt_pk_bf16_f32 v113, v234, v235\n"
      "v_mfma_f32_16x16x32_bf16 v[52:55], a[204:207], v[164:167], v[52:55]\n"
      "v_exp_f32 v236, v36\n"
      "v_mfma_f32_16x16x32_bf16 v[48:51], a[208:211], v[168:171], v[48:51]\n"
      "v_max3_f32 v240, v80, v84, v88\n"
      "v_max_f32 v240, v240, v92\n"
      "v_mfma_f32_16x16x32_bf16 v[52:55], a[212:215], v[168:171], v[52:55]\n"
      "v_exp_f32 v237, v37\n"
      "v_mfma_f32_16x16x32_bf16 v[48:51], a[216:219], v[172:175], v[48:51]\n"
      "v_cvt_pk_bf16_f32 v114, v236, v237\n"
      "v_mfma_f32_16x16x32_bf16 v[52:55], a[220:223], v[172:175], v[52:55]\n"
      "v_exp_f32 v238, v38\n"
      "v_mfma_f32_16x16x32_bf16 v[56:59], a[192:195], v[176:179], v[76:79]\n"
      "v_cmp_nge_f32 vcc, s74, v240\n"
      "v_mfma_f32_16x16x32_bf16 v[60:63], a[196:199], v[176:179], v[76:79]\n"
      "v_exp_f32 v239, v39\n"
      "v_mfma_f32_16x16x32_bf16 v[56:59], a[200:203], v[180:183], v[56:59]\n"
      "v_cvt_pk_bf16_f32 v115, v238, v239\n"
      "v_mfma_f32_16x16x32_bf16 v[60:63], a[204:207], v[180:183], v[60:63]\n"
      "v_mfma_f32_16x16x32_bf16 v[56:59], a[208:211], v[184:187], v[56:59]\n"
      "v_exp_f32 v232, v40\n"
      "v_mfma_f32_16x16x32_bf16 v[60:63], a[212:215], v[184:187], v[60:63]\n"
      "s_waitcnt vmcnt(14) lgkmcnt(0)\n"
      "s_barrier\n"
      "v_mfma_f32_16x16x32_bf16 v[56:59], a[216:219], v[188:191], v[56:59]\n"
      "v_exp_f32 v233, v41\n"
      "v_mfma_f32_16x16x32_bf16 v[60:63], a[220:223], v[188:191], v[60:63]\n"
      "v_cvt_pk_bf16_f32 v116, v232, v233\n"
      "s_cbranch_vccnz STUB3_%=\n"
      "BACK3_%=:\n"
      "v_mfma_f32_16x16x32_bf16 a[0:3], a[224:227], v[96:99], a[0:3]\n"
      "v_exp_f32 v234, v42\n"
      "v_mfma_f32_16x16x32_bf16 a[4:7], a[228:231], v[96:99], a[4:7]\n"
      "ds_read_b128 a[192:195], v192 offset:16384\n"
      "v_mfma_f32_16x16x32_bf16 v[212:215], v[228:231], v[112:115], v[80:83]\n"
      "v_exp_f32 v235, v43\n"
      "v_mfma_f32_16x16x32_bf16 a[8:11], a[232:235], v[96:99], a[8:11]\n"
      "ds_read_b128 a[196:199], v192 offset:20480\n"
      "v_cvt_pk_bf16_f32 v117, v234, v235\n"
      "v_mfma_f32_16x16x32_bf16 a[12:15], a[236:239], v[96:99], a[12:15]\n"
      "v_exp_f32 v236, v44\n"
      "v_mfma_f32_16x16x32_bf16 a[16:19], a[240:243], v[96:99], a[16:19]\n"
      "ds_read_b128 a[200:203], v193 offset:16384\n"
      "v_mfma_f32_16x16x32_bf16 a[20:23], a[244:247], v[96:99], a[20:23]\n"
      "v_exp_f32 v237, v45\n"
      "v_mfma_f32_16x16x32_bf16 a[24:27], a[248:251], v[96:99], a[24:27]\n"
      "ds_read_b128 a[204:207], v193 offset:20480\n"
      "v_cvt_pk_bf16_f32 v118, v236, v237\n"
      "v_mfma_f32_16x16x32_bf16 a[28:31], a[252:255], v[96:99], a[28:31]\n"
      "v_exp_f32 v238, v46\n"
      "v_mfma_f32_16x16x32_bf16 a[32:35], a[224:227], v[100:103], a[32:35]\n"
      "ds_read_b128 a[208:211], v194 offset:16384\n"
      "v_mfma_f32_16x16x32_bf16 a[36:39], a[228:231], v[100:103], a[36:39]\n"
      "v_exp_f32 v239, v47\n"
      "v_mfma_f32_16x16x32_bf16 a[40:43], a[232:235], v[100:103], a[40:43]\n"
      "ds_read_b128 a[212:215], v194 offset:20480\n"
      "v_cvt_pk_bf16_f32 v119, v238, v239\n"
      "v_mfma_f32_16x16x32_bf16 a[44:47], a[236:239], v[100:103], a[44:47]\n"
      "ds_read_b128 a[216:219], v195 offset:16384\n"
      "v_mfma_f32_16x16x32_bf16 a[48:51], a[240:243], v[100:103], a[48:51]\n"
      "v_exp_f32 v232, v48\n"
      "v_mfma_f32_16x16x32_bf16 v[216:219], v[228:231], v[116:119], v[84:87]\n"
      "ds_read_b128 a[220:223], v195 offset:20480\n"
      "v_mfma_f32_16x16x32_bf16 a[52:55], a[244:247], v[100:103], a[52:55]\n"
      "v_exp_f32 v233, v49\n"
      "v_mfma_f32_16x16x32_bf16 a[56:59], a[248:251], v[100:103], a[56:59]\n"
      "s_memrealtime s[82:83]\n"
      "v_cvt_pk_bf16_f32 v120, v232, v233\n"
      "v_mfma_f32_16x16x32_bf16 a[60:63], a[252:255], v[100:103], a[60:63]\n"
      "s_add_u32 m0, s78, 114688\n"
      "v_exp_f32 v234, v50\n"
      "v_mfma_f32_16x16x32_bf16 a[64:67], a[224:227], v[104:107], a[64:67]\n"
      "buffer_load_dwordx4 v208, s[44:47], s69 offen offset:0 nt lds\n"
      "v_mfma_f32_16x16x32_bf16 a[68:71], a[228:231], v[104:107], a[68:71]\n"
      "v_exp_f32 v235, v51\n"
      "v_mfma_f32_16x16x32_bf16 a[72:75], a[232:235], v[104:107], a[72:75]\n"
      "v_cvt_pk_bf16_f32 v121, v234, v235\n"
      "v_mfma_f32_16x16x32_bf16 a[76:79], a[236:239], v[104:107], a[76:79]\n"
      "v_exp_f32 v236, v52\n"
      "v_mfma_f32_16x16x32_bf16 a[80:83], a[240:243], v[104:107], a[80:83]\n"
      "buffer_load_dwordx4 v209, s[44:47], s69 offen offset:1024 nt lds\n"
      "v_mfma_f32_16x16x32_bf16 a[84:87], a[244:247], v[104:107], a[84:87]\n"
      "v_exp_f32 v237, v53\n"
      "v_mfma_f32_16x16x32_bf16 a[88:91], a[248:251], v[104:107], a[88:91]\n"
      "v_cvt_pk_bf16_f32 v122, v236, v237\n"
      "v_mfma_f32_16x16x32_bf16 a[92:95], a[252:255], v[104:107], a[92:95]\n"
      "v_exp_f32 v238, v54\n"
      "v_mfma_f32_16x16x32_bf16 a[96:99], a[224:227], v[108:111], a[96:99]\n"
      "buffer_load_dwordx4 v208, s[44:47], s69 offen offset:2048 nt lds\n"
      "v_mfma_f32_16x16x32_bf16 a[100:103], a[228:231], v[108:111], a[100:103]\n"
      "v_exp_f32 v239, v55\n"
      "v_mfma_f32_16x16x32_bf16 a[104:107], a[232:235], v[108:111], a[104:107]\n"
      "v_cvt_pk_bf16_f32 v123, v238, v239\n"
      "v_mfma_f32_16x16x32_bf16 a[108:111], a[236:239], v[108:111], a[108:111]\n"
      "buffer_load_dwordx4 v209, s[44:47], s69 offen offset:3072 nt lds\n"
      "v_mfma_f32_16x16x32_bf16 a[112:115], a[240:243], v[108:111], a[112:115]\n"
      "s_add_u32 s69, s69, 16384\n"
      "v_exp_f32 v232, v56\n"
      "v_mfma_f32_16x16x32_bf16 v[220:223], v[228:231], v[120:123], v[88:91]\n"
      "v_mfma_f32_16x16x32_bf16 a[116:119], a[244:247], v[108:111], a[116:119]\n"
      "v_exp_f32 v233, v57\n"
      "v_mfma_f32_16x16x32_bf16 a[120:123], a[248:251], v[108:111], a[120:123]\n"
      "s_waitcnt lgkmcnt(0)\n"
      "v_cvt_pk_bf16_f32 v124, v232, v233\n"
      "v_mfma_f32_16x16x32_bf16 a[124:127], a[252:255], v[108:111], a[124:127]\n"
      "v_exp_f32 v234, v58\n"
      "s_cmp_lt_u32 s82, s95\n"
      "s_cbranch_scc1 STUB4_%=\n"
      "BACK4_%=:\n"
      "L10_%=:\n"
      "v_mfma_f32_16x16x32_bf16 v[0:3], a[192:195], v[128:131], v[64:67]\n"
      "ds_read_b64_tr_b16 a[224:225], v200 offset:8192\n"
      "ds_read_b64_tr_b16 a[226:227], v200 offset:12288\n"
      "v_mfma_f32_16x16x32_bf16 v[4:7], a[196:199], v[128:131], v[64:67]\n"
      "s_add_u32 s95, s82, 80\n"
      "v_exp_f32 v235, v59\n"
      "v_mfma_f32_16x16x32_bf16 v[0:3], a[200:203], v[132:135], v[0:3]\n"
      "ds_read_b64_tr_b16 a[228:229], v201 offset:8192\n"
      "v_cvt_pk_bf16_f32 v125, v234, v235\n"
      "v_mfma_f32_16x16x32_bf16 v[4:7], a[204:207], v[132:135], v[4:7]\n"
      "v_exp_f32 v236, v60\n"
      "v_mfma_f32_16x16x32_bf16 v[0:3], a[208:211], v[136:139], v[0:3]\n"
      "ds_read_b64_tr_b16 a[230:231], v201 offset:12288\n"
      "ds_read_b64_tr_b16 a[232:233], v202 offset:8192\n"
      "v_mfma_f32_16x16x32_bf16 v[4:7], a[212:215], v[136:139], v[4:7]\n"
      "v_exp_f32 v237, v61\n"
      "v_mfma_f32_16x16x32_bf16 v[0:3], a[216:219], v[140:143], v[0:3]\n"
      "ds_read_b64_tr_b16 a[234:235], v202 offset:12288\n"
      "v_cvt_pk_bf16_f32 v126, v236, v237\n"
      "v_mfma_f32_16x16x32_bf16 v[4:7], a[220:223], v[140:143], v[4:7]\n"
      "v_exp_f32 v238, v62\n"
      "v_mfma_f32_16x16x32_bf16 v[8:11], a[192:195], v[144:147], v[68:71]\n"
      "ds_read_b64_tr_b16 a[236:237], v203 offset:8192\n"
      "ds_read_b64_tr_b16 a[238:239], v203 offset:12288\n"
      "v_mfma_f32_16x16x32_bf16 v[12:15], a[196:199], v[144:147], v[68:71]\n"
      "v_exp_f32 v239, v63\n"
      "v_mfma_f32_16x16x32_bf16 v[8:11], a[200:203], v[148:151], v[8:11]\n"
      "ds_read_b64_tr_b16 a[240:241], v204 offset:8192\n"
      "v_cvt_pk_bf16_f32 v127, v238, v239\n"
      "v_mfma_f32_16x16x32_bf16 v[12:15], a[204:207], v[148:151], v[12:15]\n"
      "ds_read_b64_tr_b16 a[242:243], v204 offset:12288\n"
      "ds_read_b64_tr_b16 a[244:245], v205 offset:8192\n"
      "v_mfma_f32_16x16x32_bf16 v[8:11], a[208:211], v[152:155], v[8:11]\n"
      "v_exp_f32 v232, v0\n"
      "v_mfma_f32_16x16x32_bf16 v[224:227], v[228:231], v[124:127], v[92:95]\n"
      "ds_read_b64_tr_b16 a[246:247], v205 offset:12288\n"
      "ds_read_b64_tr_b16 a[248:249], v206 offset:8192\n"
      "v_mfma_f32_16x16x32_bf16 v[12:15], a[212:215], v[152:155], v[12:15]\n"
      "v_exp_f32 v233, v1\n"
      "v_mfma_f32_16x16x32_bf16 v[8:11], a[216:219], v[156:159], v[8:11]\n"
      "ds_read_b64_tr_b16 a[250:251], v206 offset:12288\n"
      "v_cvt_pk_bf16_f32 v96, v232, v233\n"
      "v_mfma_f32_16x16x32_bf16 v[12:15], a[220:223], v[156:159], v[12:15]\n"
      "v_exp_f32 v234, v2\n"
      "v_mfma_f32_16x16x32_bf16 v[16:19], a[192:195], v[160:163], v[72:75]\n"
      "ds_read_b64_tr_b16 a[252:253], v207 offset:8192\n"
      "ds_read_b64_tr_b16 a[254:255], v207 offset:12288\n"
      "v_mfma_f32_16x16x32_bf16 v[20:23], a[196:199], v[160:163], v[72:75]\n"
      "v_exp_f32 v235, v3\n"
      "v_mfma_f32_16x16x32_bf16 v[16:19], a[200:203], v[164:167], v[16:19]\n"
      "v_cvt_pk_bf16_f32 v97, v234, v235\n"
      "v_mfma_f32_16x16x32_bf16 v[20:23], a[204:207], v[164:167], v[20:23]\n"
      "v_exp_f32 v236, v4\n"
      "v_mfma_f32_16x16x32_bf16 v[16:19], a[208:211], v[168:171], v[16:19]\n"
      "v_max3_f32 v240, v212, v216, v220\n"
      "v_max_f32 v240, v240, v224\n"
      "v_mfma_f32_16x16x32_bf16 v[20:23], a[212:215], v[168:171], v[20:23]\n"
      "v_exp_f32 v237, v5\n"
      "v_mfma_f32_16x16x32_bf16 v[16:19], a[216:219], v[172:175], v[16:19]\n"
      "v_cvt_pk_bf16_f32 v98, v236, v237\n"
      "v_mfma_f32_16x16x32_bf16 v[20:23], a[220:223], v[172:175], v[20:23]\n"
      "v_exp_f32 v238, v6\n"
      "v_mfma_f32_16x16x32_bf16 v[24:27], a[192:195], v[176:179], v[76:79]\n"
      "v_cmp_nge_f32 vcc, s74, v240\n"
      "v_mfma_f32_16x16x32_bf16 v[28:31], a[196:199], v[176:179], v[76:79]\n"
      "v_exp_f32 v239, v7\n"
      "v_mfma_f32_16x16x32_bf16 v[24:27], a[200:203], v[180:183], v[24:27]\n"
      "v_cvt_pk_bf16_f32 v99, v238, v239\n"
      "v_mfma_f32_16x16x32_bf16 v[28:31], a[204:207], v[180:183], v[28:31]\n"
      "v_mfma_f32_16x16x32_bf16 v[24:27], a[208:211], v[184:187], v[24:27]\n"
      "v_exp_f32 v232, v8\n"
      "v_mfma_f32_16x16x32_bf16 v[28:31], a[212:215], v[184:187], v[28:31]\n"
      "s_waitcnt lgkmcnt(0)\n"
      "v_mfma_f32_16x16x32_bf16 v[24:27], a[216:219], v[188:191], v[24:27]\n"
      "v_exp_f32 v233, v9\n"
      "v_mfma_f32_16x16x32_bf16 v[28:31], a[220:223], v[188:191], v[28:31]\n"
      "v_cvt_pk_bf16_f32 v100, v232, v233\n"
      "s_cbranch_vccnz STUB5_%=\n"
      "BACK5_%=:\n"
      "v_mfma_f32_16x16x32_bf16 a[0:3], a[224:227], v[112:115], a[0:3]\n"
      "v_exp_f32 v234, v10\n"
      "v_mfma_f32_16x16x32_bf16 a[4:7], a[228:231], v[112:115], a[4:7]\n"
      "ds_read_b128 a[192:195], v192 offset:24576\n"
      "v_mfma_f32_16x16x32_bf16 v[80:83], v[228:231], v[96:99], v[212:215]\n"
      "v_exp_f32 v235, v11\n"
      "v_mfma_f32_16x16x32_bf16 a[8:11], a[232:235], v[112:115], a[8:11]\n"
      "ds_read_b128 a[196:199], v192 offset:28672\n"
      "v_cvt_pk_bf16_f32 v101, v234, v235\n"
      "v_mfma_f32_16x16x32_bf16 a[12:15], a[236:239], v[112:115], a[12:15]\n"
      "v_exp_f32 v236, v12\n"
      "v_mfma_f32_16x16x32_bf16 a[16:19], a[240:243], v[112:115], a[16:19]\n"
      "ds_read_b128 a[200:203], v193 offset:24576\n"
      "v_mfma_f32_16x16x32_bf16 a[20:23], a[244:247], v[112:115], a[20:23]\n"
      "v_exp_f32 v237, v13\n"
      "v_mfma_f32_16x16x32_bf16 a[24:27], a[248:251], v[112:115], a[24:27]\n"
      "ds_read_b128 a[204:207], v193 offset:28672\n"
      "v_cvt_pk_bf16_f32 v102, v236, v237\n"
      "v_mfma_f32_16x16x32_bf16 a[28:31], a[252:255], v[112:115], a[28:31]\n"
      "v_exp_f32 v238, v14\n"
      "v_mfma_f32_16x16x32_bf16 a[32:35], a[224:227], v[116:119], a[32:35]\n"
      "ds_read_b128 a[208:211], v194 offset:24576\n"
      "v_mfma_f32_16x16x32_bf16 a[36:39], a[228:231], v[116:119], a[36:39]\n"
      "v_exp_f32 v239, v15\n"
      "v_mfma_f32_16x16x32_bf16 a[40:43], a[232:235], v[116:119], a[40:43]\n"
      "ds_read_b128 a[212:215], v194 offset:28672\n"
      "v_cvt_pk_bf16_f32 v103, v238, v239\n"
      "v_mfma_f32_16x16x32_bf16 a[44:47], a[236:239], v[116:119], a[44:47]\n"
      "ds_read_b128 a[216:219], v195 offset:24576\n"
      "v_mfma_f32_16x16x32_bf16 a[48:51], a[240:243], v[116:119], a[48:51]\n"
      "v_exp_f32 v232, v16\n"
      "v_mfma_f32_16x16x32_bf16 v[84:87], v[228:231], v[100:103], v[216:219]\n"
      "ds_read_b128 a[220:223], v195 offset:28672\n"
      "v_mfma_f32_16x16x32_bf16 a[52:55], a[244:247], v[116:119], a[52:55]\n"
      "v_exp_f32 v233, v17\n"
      "v_mfma_f32_16x16x32_bf16 a[56:59], a[248:251], v[116:119], a[56:59]\n"
      "s_memrealtime s[82:83]\n"
      "v_cvt_pk_bf16_f32 v104, v232, v233\n"
      "v_mfma_f32_16x16x32_bf16 a[60:63], a[252:255], v[116:119], a[60:63]\n"
      "s_add_u32 m0, s78, 0\n"
      "v_exp_f32 v234, v18\n"
      "v_mfma_f32_16x16x32_bf16 a[64:67], a[224:227], v[120:123], a[64:67]\n"
      "buffer_load_dwordx4 v196, s[40:43], s68 offen offset:0 nt lds\n"
      "v_mfma_f32_16x16x32_bf16 a[68:71], a[228:231], v[120:123], a[68:71]\n"
      "v_exp_f32 v235, v19\n"
      "v_mfma_f32_16x16x32_bf16 a[72:75], a[232:235], v[120:123], a[72:75]\n"
      "v_cvt_pk_bf16_f32 v105, v234, v235\n"
      "v_mfma_f32_16x16x32_bf16 a[76:79], a[236:239], v[120:123], a[76:79]\n"
      "v_exp_f32 v236, v20\n"
      "v_mfma_f32_16x16x32_bf16 a[80:83], a[240:243], v[120:123], a[80:83]\n"
      "buffer_load_dwordx4 v197, s[40:43], s68 offen offset:1024 nt lds\n"
      "v_mfma_f32_16x16x32_bf16 a[84:87], a[244:247], v[120:123], a[84:87]\n"
      "v_exp_f32 v237, v21\n"
      "v_mfma_f32_16x16x32_bf16 a[88:91], a[248:251], v[120:123], a[88:91]\n"
      "v_cvt_pk_bf16_f32 v106, v236, v237\n"
      "v_mfma_f32_16x16x32_bf16 a[92:95], a[252:255], v[120:123], a[92:95]\n"
      "v_exp_f32 v238, v22\n"
      "v_mfma_f32_16x16x32_bf16 a[96:99], a[224:227], v[124:127], a[96:99]\n"
      "buffer_load_dwordx4 v198, s[40:43], s68 offen offset:2048 nt lds\n"
      "v_mfma_f32_16x16x32_bf16 a[100:103], a[228:231], v[124:127], a[100:103]\n"
      "v_exp_f32 v239, v23\n"
      "v_mfma_f32_16x16x32_bf16 a[104:107], a[232:235], v[124:127], a[104:107]\n"
      "v_cvt_pk_bf16_f32 v107, v238, v239\n"
      "v_mfma_f32_16x16x32_bf16 a[108:111], a[236:239], v[124:127], a[108:111]\n"
      "buffer_load_dwordx4 v199, s[40:43], s68 offen offset:3072 nt lds\n"
      "v_mfma_f32_16x16x32_bf16 a[112:115], a[240:243], v[124:127], a[112:115]\n"
      "s_add_u32 s68, s68, 16384\n"
      "v_exp_f32 v232, v24\n"
      "v_mfma_f32_16x16x32_bf16 v[88:91], v[228:231], v[104:107], v[220:223]\n"
      "v_mfma_f32_16x16x32_bf16 a[116:119], a[244:247], v[124:127], a[116:119]\n"
      "v_exp_f32 v233, v25\n"
      "v_mfma_f32_16x16x32_bf16 a[120:123], a[248:251], v[124:127], a[120:123]\n"
      "s_waitcnt lgkmcnt(0)\n"
      "v_cvt_pk_bf16_f32 v108, v232, v233\n"
      "v_mfma_f32_16x16x32_bf16 a[124:127], a[252:255], v[124:127], a[124:127]\n"
      "v_exp_f32 v234, v26\n"
      "s_cmp_lt_u32 s82, s95\n"
      "s_cbranch_scc1 STUB6_%=\n"
      "BACK6_%=:\n"
      "v_mfma_f32_16x16x32_bf16 v[32:35], a[192:195], v[128:131], v[64:67]\n"
      "ds_read_b64_tr_b16 a[224:225], v200 offset:16384\n"
      "ds_read_b64_tr_b16 a[226:227], v200 offset:20480\n"
      "v_mfma_f32_16x16x32_bf16 v[36:39], a[196:199], v[128:131], v[64:67]\n"
      "s_add_u32 s95, s82, 80\n"
      "v_exp_f32 v235, v27\n"
      "v_mfma_f32_16x16x32_bf16 v[32:35], a[200:203], v[132:135], v[32:35]\n"
      "ds_read_b64_tr_b16 a[228:229], v201 offset:16384\n"
      "v_cvt_pk_bf16_f32 v109, v234, v235\n"
      "v_mfma_f32_16x16x32_bf16 v[36:39], a[204:207], v[132:135], v[36:39]\n"
      "v_exp_f32 v236, v28\n"
      "v_mfma_f32_16x16x32_bf16 v[32:35], a[208:211], v[136:139], v[32:35]\n"
      "ds_read_b64_tr_b16 a[230:231], v201 offset:20480\n"
      "ds_read_b64_tr_b16 a[232:233], v202 offset:16384\n"
      "v_mfma_f32_16x16x32_bf16 v[36:39], a[212:215], v[136:139], v[36:39]\n"
      "v_exp_f32 v237, v29\n"
      "v_mfma_f32_16x16x32_bf16 v[32:35], a[216:219], v[140:143], v[32:35]\n"
      "ds_read_b64_tr_b16 a[234:235], v202 offset:20480\n"
      "v_cvt_pk_bf16_f32 v110, v236, v237\n"
      "v_mfma_f32_16x16x32_bf16 v[36:39], a[220:223], v[140:143], v[36:39]\n"
      "v_exp_f32 v238, v30\n"
      "v_mfma_f32_16x16x32_bf16 v[40:43], a[192:195], v[144:147], v[68:71]\n"
      "ds_read_b64_tr_b16 a[236:237], v203 offset:16384\n"
      "ds_read_b64_tr_b16 a[238:239], v203 offset:20480\n"
      "v_mfma_f32_16x16x32_bf16 v[44:47], a[196:199], v[144:147], v[68:71]\n"
      "v_exp_f32 v239, v31\n"
      "v_mfma_f32_16x16x32_bf16 v[40:43], a[200:203], v[148:151], v[40:43]\n"
      "ds_read_b64_tr_b16 a[240:241], v204 offset:16384\n"
      "v_cvt_pk_bf16_f32 v111, v238, v239\n"
      "v_mfma_f32_16x16x32_bf16 v[44:47], a[204:207], v[148:151], v[44:47]\n"
      "ds_read_b64_tr_b16 a[242:243], v204 offset:20480\n"
      "ds_read_b64_tr_b16 a[244:245], v205 offset:16384\n"
      "v_mfma_f32_16x16x32_bf16 v[40:43], a[208:211], v[152:155], v[40:43]\n"
      "v_exp_f32 v232, v32\n"
      "v_mfma_f32_16x16x32_bf16 v[92:95], v[228:231], v[108:111], v[224:227]\n"
      "ds_read_b64_tr_b16 a[246:247], v205 offset:20480\n"
      "ds_read_b64_tr_b16 a[248:249], v206 offset:16384\n"
      "v_mfma_f32_16x16x32_bf16 v[44:47], a[212:215], v[152:155], v[44:47]\n"
      "v_exp_f32 v233, v33\n"
      "v_mfma_f32_16x16x32_bf16 v[40:43], a[216:219], v[156:159], v[40:43]\n"
      "ds_read_b64_tr_b16 a[250:251], v206 offset:20480\n"
      "v_cvt_pk_bf16_f32 v112, v232, v233\n"
      "v_mfma_f32_16x16x32_bf16 v[44:47], a[220:223], v[156:159], v[44:47]\n"
      "v_exp_f32 v234, v34\n"
      "v_mfma_f32_16x16x32_bf16 v[48:51], a[192:195], v[160:163], v[72:75]\n"
      "ds_read_b64_tr_b16 a[252:253], v207 offset:16384\n"
      "ds_read_b64_tr_b16 a[254:255], v207 offset:20480\n"
      "v_mfma_f32_16x16x32_bf16 v[52:55], a[196:199], v[160:163], v[72:75]\n"
      "v_exp_f32 v235, v35\n"
      "v_mfma_f32_16x16x32_bf16 v[48:51], a[200:203], v[164:167], v[48:51]\n"
      "v_cvt_pk_bf16_f32 v113, v234, v235\n"
      "v_mfma_f32_16x16x32_bf16 v[52:55], a[204:207], v[164:167], v[52:55]\n"
      "s_add_u32 s92, s92, 1\n"
      "v_exp_f32 v236, v36\n"
      "v_mfma_f32_16x16x32_bf16 v[48:51], a[208:211], v[168:171], v[48:51]\n"
      "v_max3_f32 v240, v80, v84, v88\n"
      "v_max_f32 v240, v240, v92\n"
      "v_mfma_f32_16x16x32_bf16 v[52:55], a[212:215], v[168:171], v[52:55]\n"
      "s_min_u32 s93, s92, 4\n"
      "v_exp_f32 v237, v37\n"
      "v_mfma_f32_16x16x32_bf16 v[48:51], a[216:219], v[172:175], v[48:51]\n"
      "v_cvt_pk_bf16_f32 v114, v236, v237\n"
      "v_mfma_f32_16x16x32_bf16 v[52:55], a[220:223], v[172:175], v[52:55]\n"
      "s_lshl_b32 s94, s93, 5\n"
      "v_exp_f32 v238, v38\n"
      "v_mfma_f32_16x16x32_bf16 v[56:59], a[192:195], v[176:179], v[76:79]\n"
      "v_cmp_nge_f32 vcc, s74, v240\n"
      "v_mfma_f32_16x16x32_bf16 v[60:63], a[196:199], v[176:179], v[76:79]\n"
      "s_lshl_b32 s93, s93, 10\n"
      "v_exp_f32 v239, v39\n"
      "v_mfma_f32_16x16x32_bf16 v[56:59], a[200:203], v[180:183], v[56:59]\n"
      "v_cvt_pk_bf16_f32 v115, v238, v239\n"
      "v_mfma_f32_16x16x32_bf16 v[60:63], a[204:207], v[180:183], v[60:63]\n"
      "s_add_u32 s93, s93, s86\n"
      "v_mfma_f32_16x16x32_bf16 v[56:59], a[208:211], v[184:187], v[56:59]\n"
      "v_exp_f32 v232, v40\n"
      "v_mfma_f32_16x16x32_bf16 v[60:63], a[212:215], v[184:187], v[60:63]\n"
      "s_waitcnt vmcnt(12) lgkmcnt(0)\n"
      "s_barrier\n"
      "v_mfma_f32_16x16x32_bf16 v[56:59], a[216:219], v[188:191], v[56:59]\n"
      "v_exp_f32 v233, v41\n"
      "v_mfma_f32_16x16x32_bf16 v[60:63], a[220:223], v[188:191], v[60:63]\n"
      "v_cvt_pk_bf16_f32 v116, v232, v233\n"
      "s_cbranch_vccnz STUB7_%=\n"
      "BACK7_%=:\n"
      "v_mfma_f32_16x16x32_bf16 a[0:3], a[224:227], v[96:99], a[0:3]\n"
      "v_exp_f32 v234, v42\n"
      "v_add_u32 v4, s94, v253\n"
      "v_mfma_f32_16x16x32_bf16 a[4:7], a[228:231], v[96:99], a[4:7]\n"
      "ds_read_b128 a[192:195], v192 offset:32768\n"
      "v_mfma_f32_16x16x32_bf16 v[212:215], v[228:231], v[112:115], v[80:83]\n"
      "v_exp_f32 v235, v43\n"
      "v_mfma_f32_16x16x32_bf16 a[8:11], a[232:235], v[96:99], a[8:11]\n"
      "ds_read_b128 a[196:199], v192 offset:36864\n"
      "v_cvt_pk_bf16_f32 v117, v234, v235\n"
      "v_mfma_f32_16x16x32_bf16 a[12:15], a[236:239], v[96:99], a[12:15]\n"
      "ds_read_b64_tr_b16 v[0:1], v4 offset:0\n"
      "v_exp_f32 v236, v44\n"
      "v_mfma_f32_16x16x32_bf16 a[16:19], a[240:243], v[96:99], a[16:19]\n"
      "ds_read_b128 a[200:203], v193 offset:32768\n"
      "v_mfma_f32_16x16x32_bf16 a[20:23], a[244:247], v[96:99], a[20:23]\n"
      "v_exp_f32 v237, v45\n"
      "v_mfma_f32_16x16x32_bf16 a[24:27], a[248:251], v[96:99], a[24:27]\n"
      "ds_read_b128 a[204:207], v193 offset:36864\n"
      "v_cvt_pk_bf16_f32 v118, v236, v237\n"
      "v_mfma_f32_16x16x32_bf16 a[28:31], a[252:255], v[96:99], a[28:31]\n"
      "ds_read_b64_tr_b16 v[2:3], v4 offset:2560\n"
      "v_exp_f32 v238, v46\n"
      "v_mfma_f32_16x16x32_bf16 a[32:35], a[224:227], v[100:103], a[32:35]\n"
      "ds_read_b128 a[208:211], v194 offset:32768\n"
      "v_mfma_f32_16x16x32_bf16 a[36:39], a[228:231], v[100:103], a[36:39]\n"
      "v_exp_f32 v239, v47\n"
      "v_mfma_f32_16x16x32_bf16 a[40:43], a[232:235], v[100:103], a[40:43]\n"
      "ds_read_b128 a[212:215], v194 offset:36864\n"
      "v_cvt_pk_bf16_f32 v119, v238, v239\n"
      "v_mfma_f32_16x16x32_bf16 a[44:47], a[236:239], v[100:103], a[44:47]\n"
      "ds_read_b128 a[216:219], v195 offset:32768\n"
      "v_mfma_f32_16x16x32_bf16 a[48:51], a[240:243], v[100:103], a[48:51]\n"
      "v_exp_f32 v232, v48\n"
      "v_mfma_f32_16x16x32_bf16 v[216:219], v[228:231], v[116:119], v[84:87]\n"
      "ds_read_b128 a[220:223], v195 offset:36864\n"
      "v_mfma_f32_16x16x32_bf16 a[52:55], a[244:247], v[100:103], a[52:55]\n"
      "v_exp_f32 v233, v49\n"
      "v_mfma_f32_16x16x32_bf16 a[56:59], a[248:251], v[100:103], a[56:59]\n"
      "s_memrealtime s[82:83]\n"
      "v_cvt_pk_bf16_f32 v120, v232, v233\n"
      "v_mfma_f32_16x16x32_bf16 a[60:63], a[252:255], v[100:103], a[60:63]\n"
      "s_add_u32 m0, s78, 65536\n"
      "v_exp_f32 v234, v50\n"
      "v_mfma_f32_16x16x32_bf16 a[64:67], a[224:227], v[104:107], a[64:67]\n"
      "buffer_load_dwordx4 v208, s[44:47], s69 offen offset:0 nt lds\n"
      "v_mfma_f32_16x16x32_bf16 a[68:71], a[228:231], v[104:107], a[68:71]\n"
      "v_exp_f32 v235, v51\n"
      "v_mfma_f32_16x16x32_bf16 a[72:75], a[232:235], v[104:107], a[72:75]\n"
      "v_cvt_pk_bf16_f32 v121, v234, v235\n"
      "v_mfma_f32_16x16x32_bf16 a[76:79], a[236:239], v[104:107], a[76:79]\n"
      "v_exp_f32 v236, v52\n"
      "v_mfma_f32_16x16x32_bf16 a[80:83], a[240:243], v[104:107], a[80:83]\n"
      "buffer_load_dwordx4 v209, s[44:47], s69 offen offset:1024 nt lds\n"
      "v_mfma_f32_16x16x32_bf16 a[84:87], a[244:247], v[104:107], a[84:87]\n"
      "v_exp_f32 v237, v53\n"
      "v_mfma_f32_16x16x32_bf16 a[88:91], a[248:251], v[104:107], a[88:91]\n"
      "v_cvt_pk_bf16_f32 v122, v236, v237\n"
      "v_mfma_f32_16x16x32_bf16 a[92:95], a[252:255], v[104:107], a[92:95]\n"
      "v_exp_f32 v238, v54\n"
      "v_mfma_f32_16x16x32_bf16 a[96:99], a[224:227], v[108:111], a[96:99]\n"
      "buffer_load_dwordx4 v208, s[44:47], s69 offen offset:2048 nt lds\n"
      "v_mfma_f32_16x16x32_bf16 a[100:103], a[228:231], v[108:111], a[100:103]\n"
      "v_exp_f32 v239, v55\n"
      "v_mfma_f32_16x16x32_bf16 a[104:107], a[232:235], v[108:111], a[104:107]\n"
      "v_cvt_pk_bf16_f32 v123, v238, v239\n"
      "v_mfma_f32_16x16x32_bf16 a[108:111], a[236:239], v[108:111], a[108:111]\n"
      "buffer_load_dwordx4 v209, s[44:47], s69 offen offset:3072 nt lds\n"
      "v_mfma_f32_16x16x32_bf16 a[112:115], a[240:243], v[108:111], a[112:115]\n"
      "s_add_u32 s69, s69, 16384\n"
      "v_exp_f32 v232, v56\n"
      "v_mfma_f32_16x16x32_bf16 v[220:223], v[228:231], v[120:123], v[88:91]\n"
      "s_waitcnt lgkmcnt(4)\n"
      "buffer_store_dwordx4 v[0:3], v252, s[64:67], s93 offen sc1\n"
      "v_mfma_f32_16x16x32_bf16 a[116:119], a[244:247], v[108:111], a[116:119]\n"
      "v_exp_f32 v233, v57\n"
      "v_mfma_f32_16x16x32_bf16 a[120:123], a[248:251], v[108:111], a[120:123]\n"
      "s_waitcnt lgkmcnt(0)\n"
      "v_cvt_pk_bf16_f32 v124, v232, v233\n"
      "v_mfma_f32_16x16x32_bf16 a[124:127], a[252:255], v[108:111], a[124:127]\n"
      "v_exp_f32 v234, v58\n"
      "s_cmp_lt_u32 s82, s95\n"
      "s_cbranch_scc1 STUB8_%=\n"
      "BACK8_%=:\n"
      "v_mfma_f32_16x16x32_bf16 v[0:3], a[192:195], v[128:131], v[64:67]\n"
      "ds_read_b64_tr_b16 a[224:225], v200 offset:24576\n"
      "ds_read_b64_tr_b16 a[226:227], v200 offset:28672\n"
      "v_mfma_f32_16x16x32_bf16 v[4:7], a[196:199], v[128:131], v[64:67]\n"
      "s_add_u32 s95, s82, 80\n"
      "v_exp_f32 v235, v59\n"
      "v_mfma_f32_16x16x32_bf16 v[0:3], a[200:203], v[132:135], v[0:3]\n"
      "ds_read_b64_tr_b16 a[228:229], v201 offset:24576\n"
      "v_cvt_pk_bf16_f32 v125, v234, v235\n"
      "v_mfma_f32_16x16x32_bf16 v[4:7], a[204:207], v[132:135], v[4:7]\n"
      "v_exp_f32 v236, v60\n"
      "v_mfma_f32_16x16x32_bf16 v[0:3], a[208:211], v[136:139], v[0:3]\n"
      "ds_read_b64_tr_b16 a[230:231], v201 offset:28672\n"
      "ds_read_b64_tr_b16 a[232:233], v202 offset:24576\n"
      "v_mfma_f32_16x16x32_bf16 v[4:7], a[212:215], v[136:139], v[4:7]\n"
      "v_exp_f32 v237, v61\n"
      "v_mfma_f32_16x16x32_bf16 v[0:3], a[216:219], v[140:143], v[0:3]\n"
      "ds_read_b64_tr_b16 a[234:235], v202 offset:28672\n"
      "v_cvt_pk_bf16_f32 v126, v236, v237\n"
      "v_mfma_f32_16x16x32_bf16 v[4:7], a[220:223], v[140:143], v[4:7]\n"
      "v_exp_f32 v238, v62\n"
      "v_mfma_f32_16x16x32_bf16 v[8:11], a[192:195], v[144:147], v[68:71]\n"
      "ds_read_b64_tr_b16 a[236:237], v203 offset:24576\n"
      "ds_read_b64_tr_b16 a[238:239], v203 offset:28672\n"
      "v_mfma_f32_16x16x32_bf16 v[12:15], a[196:199], v[144:147], v[68:71]\n"
      "v_exp_f32 v239, v63\n"
      "v_mfma_f32_16x16x32_bf16 v[8:11], a[200:203], v[148:151], v[8:11]\n"
      "ds_read_b64_tr_b16 a[240:241], v204 offset:24576\n"
      "v_cvt_pk_bf16_f32 v127, v238, v239\n"
      "v_mfma_f32_16x16x32_bf16 v[12:15], a[204:207], v[148:151], v[12:15]\n"
      "ds_read_b64_tr_b16 a[242:243], v204 offset:28672\n"
      "ds_read_b64_tr_b16 a[244:245], v205 offset:24576\n"
      "v_mfma_f32_16x16x32_bf16 v[8:11], a[208:211], v[152:155], v[8:11]\n"
      "v_exp_f32 v232, v0\n"
      "v_mfma_f32_16x16x32_bf16 v[224:227], v[228:231], v[124:127], v[92:95]\n"
      "ds_read_b64_tr_b16 a[246:247], v205 offset:28672\n"
      "ds_read_b64_tr_b16 a[248:249], v206 offset:24576\n"
      "v_mfma_f32_16x16x32_bf16 v[12:15], a[212:215], v[152:155], v[12:15]\n"
      "v_exp_f32 v233, v1\n"
      "v_mfma_f32_16x16x32_bf16 v[8:11], a[216:219], v[156:159], v[8:11]\n"
      "ds_read_b64_tr_b16 a[250:251], v206 offset:28672\n"
      "v_cvt_pk_bf16_f32 v96, v232, v233\n"
      "v_mfma_f32_16x16x32_bf16 v[12:15], a[220:223], v[156:159], v[12:15]\n"
      "v_exp_f32 v234, v2\n"
      "v_mfma_f32_16x16x32_bf16 v[16:19], a[192:195], v[160:163], v[72:75]\n"
      "ds_read_b64_tr_b16 a[252:253], v207 offset:24576\n"
      "ds_read_b64_tr_b16 a[254:255], v207 offset:28672\n"
      "v_mfma_f32_16x16x32_bf16 v[20:23], a[196:199], v[160:163], v[72:75]\n"
      "v_exp_f32 v235, v3\n"
      "v_mfma_f32_16x16x32_bf16 v[16:19], a[200:203], v[164:167], v[16:19]\n"
      "v_cvt_pk_bf16_f32 v97, v234, v235\n"
      "v_mfma_f32_16x16x32_bf16 v[20:23], a[204:207], v[164:167], v[20:23]\n"
      "v_exp_f32 v236, v4\n"
      "v_mfma_f32_16x16x32_bf16 v[16:19], a[208:211], v[168:171], v[16:19]\n"
      "v_max3_f32 v240, v212, v216, v220\n"
      "v_max_f32 v240, v240, v224\n"
      "v_mfma_f32_16x16x32_bf16 v[20:23], a[212:215], v[168:171], v[20:23]\n"
      "v_exp_f32 v237, v5\n"
      "v_mfma_f32_16x16x32_bf16 v[16:19], a[216:219], v[172:175], v[16:19]\n"
      "v_cvt_pk_bf16_f32 v98, v236, v237\n"
      "v_mfma_f32_16x16x32_bf16 v[20:23], a[220:223], v[172:175], v[20:23]\n"
      "v_exp_f32 v238, v6\n"
      "v_mfma_f32_16x16x32_bf16 v[24:27], a[192:195], v[176:179], v[76:79]\n"
      "v_cmp_nge_f32 vcc, s74, v240\n"
      "v_mfma_f32_16x16x32_bf16 v[28:31], a[196:199], v[176:179], v[76:79]\n"
      "v_exp_f32 v239, v7\n"
      "v_mfma_f32_16x16x32_bf16 v[24:27], a[200:203], v[180:183], v[24:27]\n"
      "v_cvt_pk_bf16_f32 v99, v238, v239\n"
      "v_mfma_f32_16x16x32_bf16 v[28:31], a[204:207], v[180:183], v[28:31]\n"
      "v_mfma_f32_16x16x32_bf16 v[24:27], a[208:211], v[184:187], v[24:27]\n"
      "v_exp_f32 v232, v8\n"
      "v_mfma_f32_16x16x32_bf16 v[28:31], a[212:215], v[184:187], v[28:31]\n"
      "s_waitcnt lgkmcnt(0)\n"
      "v_mfma_f32_16x16x32_bf16 v[24:27], a[216:219], v[188:191], v[24:27]\n"
      "v_exp_f32 v233, v9\n"
      "v_mfma_f32_16x16x32_bf16 v[28:31], a[220:223], v[188:191], v[28:31]\n"
      "v_cvt_pk_bf16_f32 v100, v232, v233\n"
      "s_cbranch_vccnz STUB9_%=\n"
      "BACK9_%=:\n"
      "v_mfma_f32_16x16x32_bf16 a[0:3], a[224:227], v[112:115], a[0:3]\n"
      "v_exp_f32 v234, v10\n"
      "v_mfma_f32_16x16x32_bf16 a[4:7], a[228:231], v[112:115], a[4:7]\n"
      "ds_read_b128 a[192:195], v192 offset:40960\n"
      "v_mfma_f32_16x16x32_bf16 v[80:83], v[228:231], v[96:99], v[212:215]\n"
      "v_exp_f32 v235, v11\n"
      "v_mfma_f32_16x16x32_bf16 a[8:11], a[232:235], v[112:115], a[8:11]\n"
      "ds_read_b128 a[196:199], v192 offset:45056\n"
      "v_cvt_pk_bf16_f32 v101, v234, v235\n"
      "v_mfma_f32_16x16x32_bf16 a[12:15], a[236:239], v[112:115], a[12:15]\n"
      "v_exp_f32 v236, v12\n"
      "v_mfma_f32_16x16x32_bf16 a[16:19], a[240:243], v[112:115], a[16:19]\n"
      "ds_read_b128 a[200:203], v193 offset:40960\n"
      "v_mfma_f32_16x16x32_bf16 a[20:23], a[244:247], v[112:115], a[20:23]\n"
      "v_exp_f32 v237, v13\n"
      "v_mfma_f32_16x16x32_bf16 a[24:27], a[248:251], v[112:115], a[24:27]\n"
      "ds_read_b128 a[204:207], v193 offset:45056\n"
      "v_cvt_pk_bf16_f32 v102, v236, v237\n"
      "v_mfma_f32_16x16x32_bf16 a[28:31], a[252:255], v[112:115], a[28:31]\n"
      "v_exp_f32 v238, v14\n"
      "v_mfma_f32_16x16x32_bf16 a[32:35], a[224:227], v[116:119], a[32:35]\n"
      "ds_read_b128 a[208:211], v194 offset:40960\n"
      "v_mfma_f32_16x16x32_bf16 a[36:39], a[228:231], v[116:119], a[36:39]\n"
      "v_exp_f32 v239, v15\n"
      "v_mfma_f32_16x16x32_bf16 a[40:43], a[232:235], v[116:119], a[40:43]\n"
      "ds_read_b128 a[212:215], v194 offset:45056\n"
      "v_cvt_pk_bf16_f32 v103, v238, v239\n"
      "v_mfma_f32_16x16x32_bf16 a[44:47], a[236:239], v[116:119], a[44:47]\n"
      "ds_read_b128 a[216:219], v195 offset:40960\n"
      "v_mfma_f32_16x16x32_bf16 a[48:51], a[240:243], v[116:119], a[48:51]\n"
      "v_exp_f32 v232, v16\n"
      "v_mfma_f32_16x16x32_bf16 v[84:87], v[228:231], v[100:103], v[216:219]\n"
      "ds_read_b128 a[220:223], v195 offset:45056\n"
      "v_mfma_f32_16x16x32_bf16 a[52:55], a[244:247], v[116:119], a[52:55]\n"
      "v_exp_f32 v233, v17\n"
      "v_mfma_f32_16x16x32_bf16 a[56:59], a[248:251], v[116:119], a[56:59]\n"
      "s_memrealtime s[82:83]\n"
      "v_cvt_pk_bf16_f32 v104, v232, v233\n"
      "v_mfma_f32_16x16x32_bf16 a[60:63], a[252:255], v[116:119], a[60:63]\n"
      "s_add_u32 m0, s78, 16384\n"
      "v_exp_f32 v234, v18\n"
      "v_mfma_f32_16x16x32_bf16 a[64:67], a[224:227], v[120:123], a[64:67]\n"
      "buffer_load_dwordx4 v196, s[40:43], s68 offen offset:0 nt lds\n"
      "v_mfma_f32_16x16x32_bf16 a[68:71], a[228:231], v[120:123], a[68:71]\n"
      "v_exp_f32 v235, v19\n"
      "v_mfma_f32_16x16x32_bf16 a[72:75], a[232:235], v[120:123], a[72:75]\n"
      "v_cvt_pk_bf16_f32 v105, v234, v235\n"
      "v_mfma_f32_16x16x32_bf16 a[76:79], a[236:239], v[120:123], a[76:79]\n"
      "v_exp_f32 v236, v20\n"
      "v_mfma_f32_16x16x32_bf16 a[80:83], a[240:243], v[120:123], a[80:83]\n"
      "buffer_load_dwordx4 v197, s[40:43], s68 offen offset:1024 nt lds\n"
      "v_mfma_f32_16x16x32_bf16 a[84:87], a[244:247], v[120:123], a[84:87]\n"
      "v_exp_f32 v237, v21\n"
      "v_mfma_f32_16x16x32_bf16 a[88:91], a[248:251], v[120:123], a[88:91]\n"
      "v_cvt_pk_bf16_f32 v106, v236, v237\n"
      "v_mfma_f32_16x16x32_bf16 a[92:95], a[252:255], v[120:123], a[92:95]\n"
      "v_exp_f32 v238, v22\n"
      "v_mfma_f32_16x16x32_bf16 a[96:99], a[224:227], v[124:127], a[96:99]\n"
      "buffer_load_dwordx4 v198, s[40:43], s68 offen offset:2048 nt lds\n"
      "v_mfma_f32_16x16x32_bf16 a[100:103], a[228:231], v[124:127], a[100:103]\n"
      "v_exp_f32 v239, v23\n"
      "v_mfma_f32_16x16x32_bf16 a[104:107], a[232:235], v[124:127], a[104:107]\n"
      "v_cvt_pk_bf16_f32 v107, v238, v239\n"
      "v_mfma_f32_16x16x32_bf16 a[108:111], a[236:239], v[124:127], a[108:111]\n"
      "buffer_load_dwordx4 v199, s[40:43], s68 offen offset:3072 nt lds\n"
      "v_mfma_f32_16x16x32_bf16 a[112:115], a[240:243], v[124:127], a[112:115]\n"
      "s_add_u32 s68, s68, 16384\n"
      "v_exp_f32 v232, v24\n"
      "v_mfma_f32_16x16x32_bf16 v[88:91], v[228:231], v[104:107], v[220:223]\n"
      "v_mfma_f32_16x16x32_bf16 a[116:119], a[244:247], v[124:127], a[116:119]\n"
      "v_exp_f32 v233, v25\n"
      "v_mfma_f32_16x16x32_bf16 a[120:123], a[248:251], v[124:127], a[120:123]\n"
      "s_waitcnt lgkmcnt(0)\n"
      "v_cvt_pk_bf16_f32 v108, v232, v233\n"
      "v_mfma_f32_16x16x32_bf16 a[124:127], a[252:255], v[124:127], a[124:127]\n"
      "v_exp_f32 v234, v26\n"
      "s_cmp_lt_u32 s82, s95\n"
      "s_cbranch_scc1 STUB10_%=\n"
      "BACK10_%=:\n"
      "v_mfma_f32_16x16x32_bf16 v[32:35], a[192:195], v[128:131], v[64:67]\n"
      "ds_read_b64_tr_b16 a[224:225], v200 offset:32768\n"
      "ds_read_b64_tr_b16 a[226:227], v200 offset:36864\n"
      "v_mfma_f32_16x16x32_bf16 v[36:39], a[196:199], v[128:131], v[64:67]\n"
      "s_add_u32 s95, s82, 80\n"
      "v_exp_f32 v235, v27\n"
      "v_mfma_f32_16x16x32_bf16 v[32:35], a[200:203], v[132:135], v[32:35]\n"
      "ds_read_b64_tr_b16 a[228:229], v201 offset:32768\n"
      "v_cvt_pk_bf16_f32 v109, v234, v235\n"
      "v_mfma_f32_16x16x32_bf16 v[36:39], a[204:207], v[132:135], v[36:39]\n"
      "v_exp_f32 v236, v28\n"
      "v_mfma_f32_16x16x32_bf16 v[32:35], a[208:211], v[136:139], v[32:35]\n"
      "ds_read_b64_tr_b16 a[230:231], v201 offset:36864\n"
      "ds_read_b64_tr_b16 a[232:233], v202 offset:32768\n"
      "v_mfma_f32_16x16x32_bf16 v[36:39], a[212:215], v[136:139], v[36:39]\n"
      "v_exp_f32 v237, v29\n"
      "v_mfma_f32_16x16x32_bf16 v[32:35], a[216:219], v[140:143], v[32:35]\n"
      "ds_read_b64_tr_b16 a[234:235], v202 offset:36864\n"
      "v_cvt_pk_bf16_f32 v110, v236, v237\n"
      "v_mfma_f32_16x16x32_bf16 v[36:39], a[220:223], v[140:143], v[36:39]\n"
      "v_exp_f32 v238, v30\n"
      "v_mfma_f32_16x16x32_bf16 v[40:43], a[192:195], v[144:147], v[68:71]\n"
      "ds_read_b64_tr_b16 a[236:237], v203 offset:32768\n"
      "ds_read_b64_tr_b16 a[238:239], v203 offset:36864\n"
      "v_mfma_f32_16x16x32_bf16 v[44:47], a[196:199], v[144:147], v[68:71]\n"
      "v_exp_f32 v239, v31\n"
      "v_mfma_f32_16x16x32_bf16 v[40:43], a[200:203], v[148:151], v[40:43]\n"
      "ds_read_b64_tr_b16 a[240:241], v204 offset:32768\n"
      "v_cvt_pk_bf16_f32 v111, v238, v239\n"
      "v_mfma_f32_16x16x32_bf16 v[44:47], a[204:207], v[148:151], v[44:47]\n"
      "ds_read_b64_tr_b16 a[242:243], v204 offset:36864\n"
      "ds_read_b64_tr_b16 a[244:245], v205 offset:32768\n"
      "v_mfma_f32_16x16x32_bf16 v[40:43], a[208:211], v[152:155], v[40:43]\n"
      "v_exp_f32 v232, v32\n"
      "v_mfma_f32_16x16x32_bf16 v[92:95], v[228:231], v[108:111], v[224:227]\n"
      "ds_read_b64_tr_b16 a[246:247], v205 offset:36864\n"
      "ds_read_b64_tr_b16 a[248:249], v206 offset:32768\n"
      "v_mfma_f32_16x16x32_bf16 v[44:47], a[212:215], v[152:155], v[44:47]\n"
      "v_exp_f32 v233, v33\n"
      "v_mfma_f32_16x16x32_bf16 v[40:43], a[216:219], v[156:159], v[40:43]\n"
      "ds_read_b64_tr_b16 a[250:251], v206 offset:36864\n"
      "v_cvt_pk_bf16_f32 v112, v232, v233\n"
      "v_mfma_f32_16x16x32_bf16 v[44:47], a[220:223], v[156:159], v[44:47]\n"
      "v_exp_f32 v234, v34\n"
      "v_mfma_f32_16x16x32_bf16 v[48:51], a[192:195], v[160:163], v[72:75]\n"
      "ds_read_b64_tr_b16 a[252:253], v207 offset:32768\n"
      "ds_read_b64_tr_b16 a[254:255], v207 offset:36864\n"
      "v_mfma_f32_16x16x32_bf16 v[52:55], a[196:199], v[160:163], v[72:75]\n"
      "v_exp_f32 v235, v35\n"
      "v_mfma_f32_16x16x32_bf16 v[48:51], a[200:203], v[164:167], v[48:51]\n"
      "v_cvt_pk_bf16_f32 v113, v234, v235\n"
      "v_mfma_f32_16x16x32_bf16 v[52:55], a[204:207], v[164:167], v[52:55]\n"
      "s_add_u32 s92, s92, 1\n"
      "v_exp_f32 v236, v36\n"
      "v_mfma_f32_16x16x32_bf16 v[48:51], a[208:211], v[168:171], v[48:51]\n"
      "v_max3_f32 v240, v80, v84, v88\n"
      "v_max_f32 v240, v240, v92\n"
      "v_mfma_f32_16x16x32_bf16 v[52:55], a[212:215], v[168:171], v[52:55]\n"
      "s_min_u32 s93, s92, 4\n"
      "v_exp_f32 v237, v37\n"
      "v_mfma_f32_16x16x32_bf16 v[48:51], a[216:219], v[172:175], v[48:51]\n"
      "v_cvt_pk_bf16_f32 v114, v236, v237\n"
      "v_mfma_f32_16x16x32_bf16 v[52:55], a[220:223], v[172:175], v[52:55]\n"
      "s_lshl_b32 s94, s93, 5\n"
      "v_exp_f32 v238, v38\n"
      "v_mfma_f32_16x16x32_bf16 v[56:59], a[192:195], v[176:179], v[76:79]\n"
      "v_cmp_nge_f32 vcc, s74, v240\n"
      "v_mfma_f32_16x16x32_bf16 v[60:63], a[196:199], v[176:179], v[76:79]\n"
      "s_lshl_b32 s93, s93, 10\n"
      "v_exp_f32 v239, v39\n"
      "v_mfma_f32_16x16x32_bf16 v[56:59], a[200:203], v[180:183], v[56:59]\n"
      "v_cvt_pk_bf16_f32 v115, v238, v239\n"
      "v_mfma_f32_16x16x32_bf16 v[60:63], a[204:207], v[180:183], v[60:63]\n"
      "s_add_u32 s93, s93, s86\n"
      "v_mfma_f32_16x16x32_bf16 v[56:59], a[208:211], v[184:187], v[56:59]\n"
      "v_exp_f32 v232, v40\n"
      "v_mfma_f32_16x16x32_bf16 v[60:63], a[212:215], v[184:187], v[60:63]\n"
      "s_waitcnt vmcnt(13) lgkmcnt(0)\n"
      "s_barrier\n"
      "v_mfma_f32_16x16x32_bf16 v[56:59], a[216:219], v[188:191], v[56:59]\n"
      "v_exp_f32 v233, v41\n"
      "v_mfma_f32_16x16x32_bf16 v[60:63], a[220:223], v[188:191], v[60:63]\n"
      "v_cvt_pk_bf16_f32 v116, v232, v233\n"
      "s_cbranch_vccnz STUB11_%=\n"
      "BACK11_%=:\n"
      "v_mfma_f32_16x16x32_bf16 a[0:3], a[224:227], v[96:99], a[0:3]\n"
      "v_exp_f32 v234, v42\n"
      "v_add_u32 v4, s94, v253\n"
      "v_mfma_f32_16x16x32_bf16 a[4:7], a[228:231], v[96:99], a[4:7]\n"
      "ds_read_b128 a[192:195], v192 offset:49152\n"
      "v_mfma_f32_16x16x32_bf16 v[212:215], v[228:231], v[112:115], v[80:83]\n"
      "v_exp_f32 v235, v43\n"
      "v_mfma_f32_16x16x32_bf16 a[8:11], a[232:235], v[96:99], a[8:11]\n"
      "ds_read_b128 a[196:199], v192 offset:53248\n"
      "v_cvt_pk_bf16_f32 v117, v234, v235\n"
      "v_mfma_f32_16x16x32_bf16 a[12:15], a[236:239], v[96:99], a[12:15]\n"
      "ds_read_b64_tr_b16 v[0:1], v4 offset:0\n"
      "v_exp_f32 v236, v44\n"
      "v_mfma_f32_16x16x32_bf16 a[16:19], a[240:243], v[96:99], a[16:19]\n"
      "ds_read_b128 a[200:203], v193 offset:49152\n"
      "v_mfma_f32_16x16x32_bf16 a[20:23], a[244:247], v[96:99], a[20:23]\n"
      "v_exp_f32 v237, v45\n"
      "v_mfma_f32_16x16x32_bf16 a[24:27], a[248:251], v[96:99], a[24:27]\n"
      "ds_read_b128 a[204:207], v193 offset:53248\n"
      "v_cvt_pk_bf16_f32 v118, v236, v237\n"
      "v_mfma_f32_16x16x32_bf16 a[28:31], a[252:255], v[96:99], a[28:31]\n"
      "ds_read_b64_tr_b16 v[2:3], v4 offset:2560\n"
      "v_exp_f32 v238, v46\n"
      "v_mfma_f32_16x16x32_bf16 a[32:35], a[224:227], v[100:103], a[32:35]\n"
      "ds_read_b128 a[208:211], v194 offset:49152\n"
      "v_mfma_f32_16x16x32_bf16 a[36:39], a[228:231], v[100:103], a[36:39]\n"
      "v_exp_f32 v239, v47\n"
      "v_mfma_f32_16x16x32_bf16 a[40:43], a[232:235], v[100:103], a[40:43]\n"
      "ds_read_b128 a[212:215], v194 offset:53248\n"
      "v_cvt_pk_bf16_f32 v119, v238, v239\n"
      "v_mfma_f32_16x16x32_bf16 a[44:47], a[236:239], v[100:103], a[44:47]\n"
      "ds_read_b128 a[216:219], v195 offset:49152\n"
      "v_mfma_f32_16x16x32_bf16 a[48:51], a[240:243], v[100:103], a[48:51]\n"
      "v_exp_f32 v232, v48\n"
      "v_mfma_f32_16x16x32_bf16 v[216:219], v[228:231], v[116:119], v[84:87]\n"
      "ds_read_b128 a[220:223], v195 offset:53248\n"
      "v_mfma_f32_16x16x32_bf16 a[52:55], a[244:247], v[100:103], a[52:55]\n"
      "v_exp_f32 v233, v49\n"
      "v_mfma_f32_16x16x32_bf16 a[56:59], a[248:251], v[100:103], a[56:59]\n"
      "s_memrealtime s[82:83]\n"
      "v_cvt_pk_bf16_f32 v120, v232, v233\n"
      "v_mfma_f32_16x16x32_bf16 a[60:63], a[252:255], v[100:103], a[60:63]\n"
      "s_add_u32 m0, s78, 81920\n"
      "v_exp_f32 v234, v50\n"
      "v_mfma_f32_16x16x32_bf16 a[64:67], a[224:227], v[104:107], a[64:67]\n"
      "buffer_load_dwordx4 v208, s[44:47], s69 offen offset:0 nt lds\n"
      "v_mfma_f32_16x16x32_bf16 a[68:71], a[228:231], v[104:107], a[68:71]\n"
      "v_exp_f32 v235, v51\n"
      "v_mfma_f32_16x16x32_bf16 a[72:75], a[232:235], v[104:107], a[72:75]\n"
      "v_cvt_pk_bf16_f32 v121, v234, v235\n"
      "v_mfma_f32_16x16x32_bf16 a[76:79], a[236:239], v[104:107], a[76:79]\n"
      "v_exp_f32 v236, v52\n"
      "v_mfma_f32_16x16x32_bf16 a[80:83], a[240:243], v[104:107], a[80:83]\n"
      "buffer_load_dwordx4 v209, s[44:47], s69 offen offset:1024 nt lds\n"
      "v_mfma_f32_16x16x32_bf16 a[84:87], a[244:247], v[104:107], a[84:87]\n"
      "v_exp_f32 v237, v53\n"
      "v_mfma_f32_16x16x32_bf16 a[88:91], a[248:251], v[104:107], a[88:91]\n"
      "v_cvt_pk_bf16_f32 v122, v236, v237\n"
      "v_mfma_f32_16x16x32_bf16 a[92:95], a[252:255], v[104:107], a[92:95]\n"
      "v_exp_f32 v238, v54\n"
      "v_mfma_f32_16x16x32_bf16 a[96:99], a[224:227], v[108:111], a[96:99]\n"
      "buffer_load_dwordx4 v208, s[44:47], s69 offen offset:2048 nt lds\n"
      "v_mfma_f32_16x16x32_bf16 a[100:103], a[228:231], v[108:111], a[100:103]\n"
      "v_exp_f32 v239, v55\n"
      "v_mfma_f32_16x16x32_bf16 a[104:107], a[232:235], v[108:111], a[104:107]\n"
      "v_cvt_pk_bf16_f32 v123, v238, v239\n"
      "v_mfma_f32_16x16x32_bf16 a[108:111], a[236:239], v[108:111], a[108:111]\n"
      "buffer_load_dwordx4 v209, s[44:47], s69 offen offset:3072 nt lds\n"
      "v_mfma_f32_16x16x32_bf16 a[112:115], a[240:243], v[108:111], a[112:115]\n"
      "s_add_u32 s69, s69, 16384\n"
      "v_exp_f32 v232, v56\n"
      "v_mfma_f32_16x16x32_bf16 v[220:223], v[228:231], v[120:123], v[88:91]\n"
      "s_waitcnt lgkmcnt(4)\n"
      "buffer_store_dwordx4 v[0:3], v252, s[64:67], s93 offen sc1\n"
      "v_mfma_f32_16x16x32_bf16 a[116:119], a[244:247], v[108:111], a[116:119]\n"
      "v_exp_f32 v233, v57\n"
      "v_mfma_f32_16x16x32_bf16 a[120:123], a[248:251], v[108:111], a[120:123]\n"
      "s_waitcnt lgkmcnt(0)\n"
      "v_cvt_pk_bf16_f32 v124, v232, v233\n"
      "v_mfma_f32_16x16x32_bf16 a[124:127], a[252:255], v[108:111], a[124:127]\n"
      "v_exp_f32 v234, v58\n"
      "s_cmp_lt_u32 s82, s95\n"
      "s_cbranch_scc1 STUB12_%=\n"
      "BACK12_%=:\n"
      "v_mfma_f32_16x16x32_bf16 v[0:3], a[192:195], v[128:131], v[64:67]\n"
      "ds_read_b64_tr_b16 a[224:225], v200 offset:40960\n"
      "ds_read_b64_tr_b16 a[226:227], v200 offset:45056\n"
      "v_mfma_f32_16x16x32_bf16 v[4:7], a[196:199], v[128:131], v[64:67]\n"
      "s_add_u32 s95, s82, 80\n"
      "v_exp_f32 v235, v59\n"
      "v_mfma_f32_16x16x32_bf16 v[0:3], a[200:203], v[132:135], v[0:3]\n"
      "ds_read_b64_tr_b16 a[228:229], v201 offset:40960\n"
      "v_cvt_pk_bf16_f32 v125, v234, v235\n"
      "v_mfma_f32_16x16x32_bf16 v[4:7], a[204:207], v[132:135], v[4:7]\n"
      "v_exp_f32 v236, v60\n"
      "v_mfma_f32_16x16x32_bf16 v[0:3], a[208:211], v[136:139], v[0:3]\n"
      "ds_read_b64_tr_b16 a[230:231], v201 offset:45056\n"
      "ds_read_b64_tr_b16 a[232:233], v202 offset:40960\n"
      "v_mfma_f32_16x16x32_bf16 v[4:7], a[212:215], v[136:139], v[4:7]\n"
      "v_exp_f32 v237, v61\n"
      "v_mfma_f32_16x16x32_bf16 v[0:3], a[216:219], v[140:143], v[0:3]\n"
      "ds_read_b64_tr_b16 a[234:235], v202 offset:45056\n"
      "v_cvt_pk_bf16_f32 v126, v236, v237\n"
      "v_mfma_f32_16x16x32_bf16 v[4:7], a[220:223], v[140:143], v[4:7]\n"
      "v_exp_f32 v238, v62\n"
      "v_mfma_f32_16x16x32_bf16 v[8:11], a[192:195], v[144:147], v[68:71]\n"
      "ds_read_b64_tr_b16 a[236:237], v203 offset:40960\n"
      "ds_read_b64_tr_b16 a[238:239], v203 offset:45056\n"
      "v_mfma_f32_16x16x32_bf16 v[12:15], a[196:199], v[144:147], v[68:71]\n"
      "v_exp_f32 v239, v63\n"
      "v_mfma_f32_16x16x32_bf16 v[8:11], a[200:203], v[148:151], v[8:11]\n"
      "ds_read_b64_tr_b16 a[240:241], v204 offset:40960\n"
      "v_cvt_pk_bf16_f32 v127, v238, v239\n"
      "v_mfma_f32_16x16x32_bf16 v[12:15], a[204:207], v[148:151], v[12:15]\n"
      "ds_read_b64_tr_b16 a[242:243], v204 offset:45056\n"
      "ds_read_b64_tr_b16 a[244:245], v205 offset:40960\n"
      "v_mfma_f32_16x16x32_bf16 v[8:11], a[208:211], v[152:155], v[8:11]\n"
      "v_exp_f32 v232, v0\n"
      "v_mfma_f32_16x16x32_bf16 v[224:227], v[228:231], v[124:127], v[92:95]\n"
      "ds_read_b64_tr_b16 a[246:247], v205 offset:45056\n"
      "ds_read_b64_tr_b16 a[248:249], v206 offset:40960\n"
      "v_mfma_f32_16x16x32_bf16 v[12:15], a[212:215], v[152:155], v[12:15]\n"
      "v_exp_f32 v233, v1\n"
      "v_mfma_f32_16x16x32_bf16 v[8:11], a[216:219], v[156:159], v[8:11]\n"
      "ds_read_b64_tr_b16 a[250:251], v206 offset:45056\n"
      "v_cvt_pk_bf16_f32 v96, v232, v233\n"
      "v_mfma_f32_16x16x32_bf16 v[12:15], a[220:223], v[156:159], v[12:15]\n"
      "v_exp_f32 v234, v2\n"
      "v_mfma_f32_16x16x32_bf16 v[16:19], a[192:195], v[160:163], v[72:75]\n"
      "ds_read_b64_tr_b16 a[252:253], v207 offset:40960\n"
      "ds_read_b64_tr_b16 a[254:255], v207 offset:45056\n"
      "v_mfma_f32_16x16x32_bf16 v[20:23], a[196:199], v[160:163], v[72:75]\n"
      "v_exp_f32 v235, v3\n"
      "v_mfma_f32_16x16x32_bf16 v[16:19], a[200:203], v[164:167], v[16:19]\n"
      "v_cvt_pk_bf16_f32 v97, v234, v235\n"
      "v_mfma_f32_16x16x32_bf16 v[20:23], a[204:207], v[164:167], v[20:23]\n"
      "v_exp_f32 v236, v4\n"
      "v_mfma_f32_16x16x32_bf16 v[16:19], a[208:211], v[168:171], v[16:19]\n"
      "v_max3_f32 v240, v212, v216, v220\n"
      "v_max_f32 v240, v240, v224\n"
      "v_mfma_f32_16x16x32_bf16 v[20:23], a[212:215], v[168:171], v[20:23]\n"
      "v_exp_f32 v237, v5\n"
      "v_mfma_f32_16x16x32_bf16 v[16:19], a[216:219], v[172:175], v[16:19]\n"
      "v_cvt_pk_bf16_f32 v98, v236, v237\n"
      "v_mfma_f32_16x16x32_bf16 v[20:23], a[220:223], v[172:175], v[20:23]\n"
      "v_exp_f32 v238, v6\n"
      "v_mfma_f32_16x16x32_bf16 v[24:27], a[192:195], v[176:179], v[76:79]\n"
      "v_cmp_nge_f32 vcc, s74, v240\n"
      "v_mfma_f32_16x16x32_bf16 v[28:31], a[196:199], v[176:179], v[76:79]\n"
      "v_exp_f32 v239, v7\n"
      "v_mfma_f32_16x16x32_bf16 v[24:27], a[200:203], v[180:183], v[24:27]\n"
      "v_cvt_pk_bf16_f32 v99, v238, v239\n"
      "v_mfma_f32_16x16x32_bf16 v[28:31], a[204:207], v[180:183], v[28:31]\n"
      "v_mfma_f32_16x16x32_bf16 v[24:27], a[208:211], v[184:187], v[24:27]\n"
      "v_exp_f32 v232, v8\n"
      "v_mfma_f32_16x16x32_bf16 v[28:31], a[212:215], v[184:187], v[28:31]\n"
      "s_waitcnt lgkmcnt(0)\n"
      "v_mfma_f32_16x16x32_bf16 v[24:27], a[216:219], v[188:191], v[24:27]\n"
      "v_exp_f32 v233, v9\n"
      "v_mfma_f32_16x16x32_bf16 v[28:31], a[220:223], v[188:191], v[28:31]\n"
      "v_cvt_pk_bf16_f32 v100, v232, v233\n"
      "s_cbranch_vccnz STUB13_%=\n"
      "BACK13_%=:\n"
      "v_mfma_f32_16x16x32_bf16 a[0:3], a[224:227], v[112:115], a[0:3]\n"
      "v_exp_f32 v234, v10\n"
      "v_mfma_f32_16x16x32_bf16 a[4:7], a[228:231], v[112:115], a[4:7]\n"
      "ds_read_b128 a[192:195], v192 offset:57344\n"
      "v_mfma_f32_16x16x32_bf16 v[80:83], v[228:231], v[96:99], v[212:215]\n"
      "v_exp_f32 v235, v11\n"
      "v_mfma_f32_16x16x32_bf16 a[8:11], a[232:235], v[112:115], a[8:11]\n"
      "ds_read_b128 a[196:199], v192 offset:61440\n"
      "v_cvt_pk_bf16_f32 v101, v234, v235\n"
      "v_mfma_f32_16x16x32_bf16 a[12:15], a[236:239], v[112:115], a[12:15]\n"
      "v_exp_f32 v236, v12\n"
      "v_mfma_f32_16x16x32_bf16 a[16:19], a[240:243], v[112:115], a[16:19]\n"
      "ds_read_b128 a[200:203], v193 offset:57344\n"
      "v_mfma_f32_16x16x32_bf16 a[20:23], a[244:247], v[112:115], a[20:23]\n"
      "v_exp_f32 v237, v13\n"
      "v_mfma_f32_16x16x32_bf16 a[24:27], a[248:251], v[112:115], a[24:27]\n"
      "ds_read_b128 a[204:207], v193 offset:61440\n"
      "v_cvt_pk_bf16_f32 v102, v236, v237\n"
      "v_mfma_f32_16x16x32_bf16 a[28:31], a[252:255], v[112:115], a[28:31]\n"
      "v_exp_f32 v238, v14\n"
      "v_mfma_f32_16x16x32_bf16 a[32:35], a[224:227], v[116:119], a[32:35]\n"
      "ds_read_b128 a[208:211], v194 offset:57344\n"
      "v_mfma_f32_16x16x32_bf16 a[36:39], a[228:231], v[116:119], a[36:39]\n"
      "v_exp_f32 v239, v15\n"
      "v_mfma_f32_16x16x32_bf16 a[40:43], a[232:235], v[116:119], a[40:43]\n"
      "ds_read_b128 a[212:215], v194 offset:61440\n"
      "v_cvt_pk_bf16_f32 v103, v238, v239\n"
      "v_mfma_f32_16x16x32_bf16 a[44:47], a[236:239], v[116:119], a[44:47]\n"
      "ds_read_b128 a[216:219], v195 offset:57344\n"
      "v_mfma_f32_16x16x32_bf16 a[48:51], a[240:243], v[116:119], a[48:51]\n"
      "v_exp_f32 v232, v16\n"
      "v_mfma_f32_16x16x32_bf16 v[84:87], v[228:231], v[100:103], v[216:219]\n"
      "ds_read_b128 a[220:223], v195 offset:61440\n"
      "v_mfma_f32_16x16x32_bf16 a[52:55], a[244:247], v[116:119], a[52:55]\n"
      "v_exp_f32 v233, v17\n"
      "v_mfma_f32_16x16x32_bf16 a[56:59], a[248:251], v[116:119], a[56:59]\n"
      "s_memrealtime s[82:83]\n"
      "v_cvt_pk_bf16_f32 v104, v232, v233\n"
      "v_mfma_f32_16x16x32_bf16 a[60:63], a[252:255], v[116:119], a[60:63]\n"
      "s_add_u32 m0, s78, 32768\n"
      "v_exp_f32 v234, v18\n"
      "v_mfma_f32_16x16x32_bf16 a[64:67], a[224:227], v[120:123], a[64:67]\n"
      "buffer_load_dwordx4 v196, s[40:43], s68 offen offset:0 nt lds\n"
      "v_mfma_f32_16x16x32_bf16 a[68:71], a[228:231], v[120:123], a[68:71]\n"
      "v_exp_f32 v235, v19\n"
      "v_mfma_f32_16x16x32_bf16 a[72:75], a[232:235], v[120:123], a[72:75]\n"
      "v_cvt_pk_bf16_f32 v105, v234, v235\n"
      "v_mfma_f32_16x16x32_bf16 a[76:79], a[236:239], v[120:123], a[76:79]\n"
      "v_exp_f32 v236, v20\n"
      "v_mfma_f32_16x16x32_bf16 a[80:83], a[240:243], v[120:123], a[80:83]\n"
      "buffer_load_dwordx4 v197, s[40:43], s68 offen offset:1024 nt lds\n"
      "v_mfma_f32_16x16x32_bf16 a[84:87], a[244:247], v[120:123], a[84:87]\n"
      "v_exp_f32 v237, v21\n"
      "v_mfma_f32_16x16x32_bf16 a[88:91], a[248:251], v[120:123], a[88:91]\n"
      "v_cvt_pk_bf16_f32 v106, v236, v237\n"
      "v_mfma_f32_16x16x32_bf16 a[92:95], a[252:255], v[120:123], a[92:95]\n"
      "v_exp_f32 v238, v22\n"
      "v_mfma_f32_16x16x32_bf16 a[96:99], a[224:227], v[124:127], a[96:99]\n"
      "buffer_load_dwordx4 v198, s[40:43], s68 offen offset:2048 nt lds\n"
      "v_mfma_f32_16x16x32_bf16 a[100:103], a[228:231], v[124:127], a[100:103]\n"
      "v_exp_f32 v239, v23\n"
      "v_mfma_f32_16x16x32_bf16 a[104:107], a[232:235], v[124:127], a[104:107]\n"
      "v_cvt_pk_bf16_f32 v107, v238, v239\n"
      "v_mfma_f32_16x16x32_bf16 a[108:111], a[236:239], v[124:127], a[108:111]\n"
      "buffer_load_dwordx4 v199, s[40:43], s68 offen offset:3072 nt lds\n"
      "v_mfma_f32_16x16x32_bf16 a[112:115], a[240:243], v[124:127], a[112:115]\n"
      "s_add_u32 s68, s68, 16384\n"
      "v_exp_f32 v232, v24\n"
      "v_mfma_f32_16x16x32_bf16 v[88:91], v[228:231], v[104:107], v[220:223]\n"
      "v_mfma_f32_16x16x32_bf16 a[116:119], a[244:247], v[124:127], a[116:119]\n"
      "v_exp_f32 v233, v25\n"
      "v_mfma_f32_16x16x32_bf16 a[120:123], a[248:251], v[124:127], a[120:123]\n"
      "s_waitcnt lgkmcnt(0)\n"
      "v_cvt_pk_bf16_f32 v108, v232, v233\n"
      "v_mfma_f32_16x16x32_bf16 a[124:127], a[252:255], v[124:127], a[124:127]\n"
      "v_exp_f32 v234, v26\n"
      "s_cmp_lt_u32 s82, s95\n"
      "s_cbranch_scc1 STUB14_%=\n"
      "BACK14_%=:\n"
      "v_mfma_f32_16x16x32_bf16 v[32:35], a[192:195], v[128:131], v[64:67]\n"
      "ds_read_b64_tr_b16 a[224:225], v200 offset:49152\n"
      "ds_read_b64_tr_b16 a[226:227], v200 offset:53248\n"
      "v_mfma_f32_16x16x32_bf16 v[36:39], a[196:199], v[128:131], v[64:67]\n"
      "s_add_u32 s95, s82, 80\n"
      "v_exp_f32 v235, v27\n"
      "v_mfma_f32_16x16x32_bf16 v[32:35], a[200:203], v[132:135], v[32:35]\n"
      "ds_read_b64_tr_b16 a[228:229], v201 offset:49152\n"
      "v_cvt_pk_bf16_f32 v109, v234, v235\n"
      "v_mfma_f32_16x16x32_bf16 v[36:39], a[204:207], v[132:135], v[36:39]\n"
      "v_exp_f32 v236, v28\n"
      "v_mfma_f32_16x16x32_bf16 v[32:35], a[208:211], v[136:139], v[32:35]\n"
      "ds_read_b64_tr_b16 a[230:231], v201 offset:53248\n"
      "ds_read_b64_tr_b16 a[232:233], v202 offset:49152\n"
      "v_mfma_f32_16x16x32_bf16 v[36:39], a[212:215], v[136:139], v[36:39]\n"
      "v_exp_f32 v237, v29\n"
      "v_mfma_f32_16x16x32_bf16 v[32:35], a[216:219], v[140:143], v[32:35]\n"
      "ds_read_b64_tr_b16 a[234:235], v202 offset:53248\n"
      "v_cvt_pk_bf16_f32 v110, v236, v237\n"
      "v_mfma_f32_16x16x32_bf16 v[36:39], a[220:223], v[140:143], v[36:39]\n"
      "v_exp_f32 v238, v30\n"
      "v_mfma_f32_16x16x32_bf16 v[40:43], a[192:195], v[144:147], v[68:71]\n"
      "ds_read_b64_tr_b16 a[236:237], v203 offset:49152\n"
      "ds_read_b64_tr_b16 a[238:239], v203 offset:53248\n"
      "v_mfma_f32_16x16x32_bf16 v[44:47], a[196:199], v[144:147], v[68:71]\n"
      "v_exp_f32 v239, v31\n"
      "v_mfma_f32_16x16x32_bf16 v[40:43], a[200:203], v[148:151], v[40:43]\n"
      "ds_read_b64_tr_b16 a[240:241], v204 offset:49152\n"
      "v_cvt_pk_bf16_f32 v111, v238, v239\n"
      "v_mfma_f32_16x16x32_bf16 v[44:47], a[204:207], v[148:151], v[44:47]\n"
      "ds_read_b64_tr_b16 a[242:243], v204 offset:53248\n"
      "ds_read_b64_tr_b16 a[244:245], v205 offset:49152\n"
      "v_mfma_f32_16x16x32_bf16 v[40:43], a[208:211], v[152:155], v[40:43]\n"
      "v_exp_f32 v232, v32\n"
      "v_mfma_f32_16x16x32_bf16 v[92:95], v[228:231], v[108:111], v[224:227]\n"
      "ds_read_b64_tr_b16 a[246:247], v205 offset:53248\n"
      "ds_read_b64_tr_b16 a[248:249], v206 offset:49152\n"
      "v_mfma_f32_16x16x32_bf16 v[44:47], a[212:215], v[152:155], v[44:47]\n"
      "v_exp_f32 v233, v33\n"
      "v_mfma_f32_16x16x32_bf16 v[40:43], a[216:219], v[156:159], v[40:43]\n"
      "ds_read_b64_tr_b16 a[250:251], v206 offset:53248\n"
      "v_cvt_pk_bf16_f32 v112, v232, v233\n"
      "v_mfma_f32_16x16x32_bf16 v[44:47], a[220:223], v[156:159], v[44:47]\n"
      "v_exp_f32 v234, v34\n"
      "v_mfma_f32_16x16x32_bf16 v[48:51], a[192:195], v[160:163], v[72:75]\n"
      "ds_read_b64_tr_b16 a[252:253], v207 offset:49152\n"
      "ds_read_b64_tr_b16 a[254:255], v207 offset:53248\n"
      "v_mfma_f32_16x16x32_bf16 v[52:55], a[196:199], v[160:163], v[72:75]\n"
      "v_exp_f32 v235, v35\n"
      "v_mfma_f32_16x16x32_bf16 v[48:51], a[200:203], v[164:167], v[48:51]\n"
      "v_cvt_pk_bf16_f32 v113, v234, v235\n"
      "v_mfma_f32_16x16x32_bf16 v[52:55], a[204:207], v[164:167], v[52:55]\n"
      "s_add_u32 s92, s92, 1\n"
      "v_exp_f32 v236, v36\n"
      "v_mfma_f32_16x16x32_bf16 v[48:51], a[208:211], v[168:171], v[48:51]\n"
      "v_max3_f32 v240, v80, v84, v88\n"
      "v_max_f32 v240, v240, v92\n"
      "v_mfma_f32_16x16x32_bf16 v[52:55], a[212:215], v[168:171], v[52:55]\n"
      "s_min_u32 s93, s92, 4\n"
      "v_exp_f32 v237, v37\n"
      "v_mfma_f32_16x16x32_bf16 v[48:51], a[216:219], v[172:175], v[48:51]\n"
      "v_cvt_pk_bf16_f32 v114, v236, v237\n"
      "v_mfma_f32_16x16x32_bf16 v[52:55], a[220:223], v[172:175], v[52:55]\n"
      "s_lshl_b32 s94, s93, 5\n"
      "v_exp_f32 v238, v38\n"
      "v_mfma_f32_16x16x32_bf16 v[56:59], a[192:195], v[176:179], v[76:79]\n"
      "v_cmp_nge_f32 vcc, s74, v240\n"
      "v_mfma_f32_16x16x32_bf16 v[60:63], a[196:199], v[176:179], v[76:79]\n"
      "s_lshl_b32 s93, s93, 10\n"
      "v_exp_f32 v239, v39\n"
      "v_mfma_f32_16x16x32_bf16 v[56:59], a[200:203], v[180:183], v[56:59]\n"
      "v_cvt_pk_bf16_f32 v115, v238, v239\n"
      "v_mfma_f32_16x16x32_bf16 v[60:63], a[204:207], v[180:183], v[60:63]\n"
      "s_add_u32 s93, s93, s86\n"
      "v_mfma_f32_16x16x32_bf16 v[56:59], a[208:211], v[184:187], v[56:59]\n"
      "v_exp_f32 v232, v40\n"
      "v_mfma_f32_16x16x32_bf16 v[60:63], a[212:215], v[184:187], v[60:63]\n"
      "s_waitcnt vmcnt(14) lgkmcnt(0)\n"
      "s_barrier\n"
      "v_mfma_f32_16x16x32_bf16 v[56:59], a[216:219], v[188:191], v[56:59]\n"
      "v_exp_f32 v233, v41\n"
      "v_mfma_f32_16x16x32_bf16 v[60:63], a[220:223], v[188:191], v[60:63]\n"
      "v_cvt_pk_bf16_f32 v116, v232, v233\n"
      "s_cbranch_vccnz STUB15_%=\n"
      "BACK15_%=:\n"
      "v_mfma_f32_16x16x32_bf16 a[0:3], a[224:227], v[96:99], a[0:3]\n"
      "v_exp_f32 v234, v42\n"
      "v_add_u32 v4, s94, v253\n"
      "v_mfma_f32_16x16x32_bf16 a[4:7], a[228:231], v[96:99], a[4:7]\n"
      "ds_read_b128 a[192:195], v192 offset:0\n"
      "v_mfma_f32_16x16x32_bf16 v[212:215], v[228:231], v[112:115], v[80:83]\n"
      "v_exp_f32 v235, v43\n"
      "v_mfma_f32_16x16x32_bf16 a[8:11], a[232:235], v[96:99], a[8:11]\n"
      "ds_read_b128 a[196:199], v192 offset:4096\n"
      "v_cvt_pk_bf16_f32 v117, v234, v235\n"
      "v_mfma_f32_16x16x32_bf16 a[12:15], a[236:239], v[96:99], a[12:15]\n"
      "ds_read_b64_tr_b16 v[0:1], v4 offset:0\n"
      "v_exp_f32 v236, v44\n"
      "v_mfma_f32_16x16x32_bf16 a[16:19], a[240:243], v[96:99], a[16:19]\n"
      "ds_read_b128 a[200:203], v193 offset:0\n"
      "v_mfma_f32_16x16x32_bf16 a[20:23], a[244:247], v[96:99], a[20:23]\n"
      "v_exp_f32 v237, v45\n"
      "v_mfma_f32_16x16x32_bf16 a[24:27], a[248:251], v[96:99], a[24:27]\n"
      "ds_read_b128 a[204:207], v193 offset:4096\n"
      "v_cvt_pk_bf16_f32 v118, v236, v237\n"
      "v_mfma_f32_16x16x32_bf16 a[28:31], a[252:255], v[96:99], a[28:31]\n"
      "ds_read_b64_tr_b16 v[2:3], v4 offset:2560\n"
      "v_exp_f32 v238, v46\n"
      "v_mfma_f32_16x16x32_bf16 a[32:35], a[224:227], v[100:103], a[32:35]\n"
      "ds_read_b128 a[208:211], v194 offset:0\n"
      "v_mfma_f32_16x16x32_bf16 a[36:39], a[228:231], v[100:103], a[36:39]\n"
      "v_exp_f32 v239, v47\n"
      "v_mfma_f32_16x16x32_bf16 a[40:43], a[232:235], v[100:103], a[40:43]\n"
      "ds_read_b128 a[212:215], v194 offset:4096\n"
      "v_cvt_pk_bf16_f32 v119, v238, v239\n"
      "v_mfma_f32_16x16x32_bf16 a[44:47], a[236:239], v[100:103], a[44:47]\n"
      "ds_read_b128 a[216:219], v195 offset:0\n"
      "v_mfma_f32_16x16x32_bf16 a[48:51], a[240:243], v[100:103], a[48:51]\n"
      "v_exp_f32 v232, v48\n"
      "v_mfma_f32_16x16x32_bf16 v[216:219], v[228:231], v[116:119], v[84:87]\n"
      "ds_read_b128 a[220:223], v195 offset:4096\n"
      "v_mfma_f32_16x16x32_bf16 a[52:55], a[244:247], v[100:103], a[52:55]\n"
      "v_exp_f32 v233, v49\n"
      "v_mfma_f32_16x16x32_bf16 a[56:59], a[248:251], v[100:103], a[56:59]\n"
      "s_memrealtime s[82:83]\n"
      "v_cvt_pk_bf16_f32 v120, v232, v233\n"
      "v_mfma_f32_16x16x32_bf16 a[60:63], a[252:255], v[100:103], a[60:63]\n"
      "s_add_u32 m0, s78, 98304\n"
      "v_exp_f32 v234, v50\n"
      "v_mfma_f32_16x16x32_bf16 a[64:67], a[224:227], v[104:107], a[64:67]\n"
      "buffer_load_dwordx4 v208, s[44:47], s69 offen offset:0 nt lds\n"
      "v_mfma_f32_16x16x32_bf16 a[68:71], a[228:231], v[104:107], a[68:71]\n"
      "v_exp_f32 v235, v51\n"
      "v_mfma_f32_16x16x32_bf16 a[72:75], a[232:235], v[104:107], a[72:75]\n"
      "v_cvt_pk_bf16_f32 v121, v234, v235\n"
      "v_mfma_f32_16x16x32_bf16 a[76:79], a[236:239], v[104:107], a[76:79]\n"
      "v_exp_f32 v236, v52\n"
      "v_mfma_f32_16x16x32_bf16 a[80:83], a[240:243], v[104:107], a[80:83]\n"
      "buffer_load_dwordx4 v209, s[44:47], s69 offen offset:1024 nt lds\n"
      "v_mfma_f32_16x16x32_bf16 a[84:87], a[244:247], v[104:107], a[84:87]\n"
      "v_exp_f32 v237, v53\n"
      "v_mfma_f32_16x16x32_bf16 a[88:91], a[248:251], v[104:107], a[88:91]\n"
      "v_cvt_pk_bf16_f32 v122, v236, v237\n"
      "v_mfma_f32_16x16x32_bf16 a[92:95], a[252:255], v[104:107], a[92:95]\n"
      "v_exp_f32 v238, v54\n"
      "v_mfma_f32_16x16x32_bf16 a[96:99], a[224:227], v[108:111], a[96:99]\n"
      "buffer_load_dwordx4 v208, s[44:47], s69 offen offset:2048 nt lds\n"
      "v_mfma_f32_16x16x32_bf16 a[100:103], a[228:231], v[108:111], a[100:103]\n"
      "v_exp_f32 v239, v55\n"
      "v_mfma_f32_16x16x32_bf16 a[104:107], a[232:235], v[108:111], a[104:107]\n"
      "v_cvt_pk_bf16_f32 v123, v238, v239\n"
      "v_mfma_f32_16x16x32_bf16 a[108:111], a[236:239], v[108:111], a[108:111]\n"
      "buffer_load_dwordx4 v209, s[44:47], s69 offen offset:3072 nt lds\n"
      "v_mfma_f32_16x16x32_bf16 a[112:115], a[240:243], v[108:111], a[112:115]\n"
      "s_add_u32 s69, s69, 16384\n"
      "v_exp_f32 v232, v56\n"
      "v_mfma_f32_16x16x32_bf16 v[220:223], v[228:231], v[120:123], v[88:91]\n"
      "s_waitcnt lgkmcnt(4)\n"
      "buffer_store_dwordx4 v[0:3], v252, s[64:67], s93 offen sc1\n"
      "v_mfma_f32_16x16x32_bf16 a[116:119], a[244:247], v[108:111], a[116:119]\n"
      "v_exp_f32 v233, v57\n"
      "v_mfma_f32_16x16x32_bf16 a[120:123], a[248:251], v[108:111], a[120:123]\n"
      "s_waitcnt lgkmcnt(0)\n"
      "v_cvt_pk_bf16_f32 v124, v232, v233\n"
      "v_mfma_f32_16x16x32_bf16 a[124:127], a[252:255], v[108:111], a[124:127]\n"
      "v_exp_f32 v234, v58\n"
      "s_cmp_lt_u32 s82, s95\n"
      "s_cbranch_scc1 STUB16_%=\n"
      "BACK16_%=:\n"
      "s_cmp_eq_u32 s70, 2\n"
      "s_cbranch_scc1 WOD_%=\n"
      "WODB_%=:\n"
      "s_sub_u32 s70, s70, 1\n"
      "s_cbranch_scc0 L00_%=\n"
      "s_mov_b32 s75, 1\n"
      "v_mfma_f32_16x16x32_bf16 v[0:3], a[192:195], a[128:131], 0\n"
      "ds_read_b64_tr_b16 a[224:225], v200 offset:57344\n"
      "ds_read_b64_tr_b16 a[226:227], v200 offset:61440\n"
      "v_mfma_f32_16x16x32_bf16 v[4:7], a[196:199], a[128:131], 0\n"
      "s_add_u32 s95, s82, 80\n"
      "v_exp_f32 v235, v59\n"
      "v_mfma_f32_16x16x32_bf16 v[0:3], a[200:203], a[132:135], v[0:3]\n"
      "ds_read_b64_tr_b16 a[228:229], v201 offset:57344\n"
      "v_cvt_pk_bf16_f32 v125, v234, v235\n"
      "v_mfma_f32_16x16x32_bf16 v[4:7], a[204:207], a[132:135], v[4:7]\n"
      "v_exp_f32 v236, v60\n"
      "v_mfma_f32_16x16x32_bf16 v[0:3], a[208:211], a[136:139], v[0:3]\n"
      "ds_read_b64_tr_b16 a[230:231], v201 offset:61440\n"
      "ds_read_b64_tr_b16 a[232:233], v202 offset:57344\n"
      "v_mfma_f32_16x16x32_bf16 v[4:7], a[212:215], a[136:139], v[4:7]\n"
      "v_exp_f32 v237, v61\n"
      "v_mfma_f32_16x16x32_bf16 v[0:3], a[216:219], a[140:143], v[0:3]\n"
      "ds_read_b64_tr_b16 a[234:235], v202 offset:61440\n"
      "v_cvt_pk_bf16_f32 v126, v236, v237\n"
      "v_mfma_f32_16x16x32_bf16 v[4:7], a[220:223], a[140:143], v[4:7]\n"
      "v_exp_f32 v238, v62\n"
      "v_mfma_f32_16x16x32_bf16 v[8:11], a[192:195], a[144:147], 0\n"
      "ds_read_b64_tr_b16 a[236:237], v203 offset:57344\n"
      "ds_read_b64_tr_b16 a[238:239], v203 offset:61440\n"
      "v_mfma_f32_16x16x32_bf16 v[12:15], a[196:199], a[144:147], 0\n"
      "v_exp_f32 v239, v63\n"
      "v_mfma_f32_16x16x32_bf16 v[8:11], a[200:203], a[148:151], v[8:11]\n"
      "ds_read_b64_tr_b16 a[240:241], v204 offset:57344\n"
      "v_cvt_pk_bf16_f32 v127, v238, v239\n"
      "v_mfma_f32_16x16x32_bf16 v[12:15], a[204:207], a[148:151], v[12:15]\n"
      "ds_read_b64_tr_b16 a[242:243], v204 offset:61440\n"
      "ds_read_b64_tr_b16 a[244:245], v205 offset:57344\n"
      "v_mfma_f32_16x16x32_bf16 v[8:11], a[208:211], a[152:155], v[8:11]\n"
      "v_mfma_f32_16x16x32_bf16 v[224:227], v[228:231], v[124:127], v[92:95]\n"
      "ds_read_b64_tr_b16 a[246:247], v205 offset:61440\n"
      "ds_read_b64_tr_b16 a[248:249], v206 offset:57344\n"
      "v_mfma_f32_16x16x32_bf16 v[12:15], a[212:215], a[152:155], v[12:15]\n"
      "v_mfma_f32_16x16x32_bf16 v[8:11], a[216:219], a[156:159], v[8:11]\n"
      "ds_read_b64_tr_b16 a[250:251], v206 offset:61440\n"
      "v_mfma_f32_16x16x32_bf16 v[12:15], a[220:223], a[156:159], v[12:15]\n"
      "v_mfma_f32_16x16x32_bf16 v[16:19], a[192:195], a[160:163], 0\n"
      "ds_read_b64_tr_b16 a[252:253], v207 offset:57344\n"
      "ds_read_b64_tr_b16 a[254:255], v207 offset:61440\n"
      "v_mfma_f32_16x16x32_bf16 v[20:23], a[196:199], a[160:163], 0\n"
      "v_mfma_f32_16x16x32_bf16 v[16:19], a[200:203], a[164:167], v[16:19]\n"
      "v_max3_f32 v240, v212, v216, v220\n"
      "v_max_f32 v240, v240, v224\n"
      "v_mfma_f32_16x16x32_bf16 v[20:23], a[204:207], a[164:167], v[20:23]\n"
      "v_cmp_nge_f32 vcc, s74, v240\n"
      "v_mfma_f32_16x16x32_bf16 v[16:19], a[208:211], a[168:171], v[16:19]\n"
      "v_mfma_f32_16x16x32_bf16 v[20:23], a[212:215], a[168:171], v[20:23]\n"
      "s_cbranch_vccnz STUB17_%=\n"
      "BACK17_%=:\n"
      "v_mfma_f32_16x16x32_bf16 v[16:19], a[216:219], a[172:175], v[16:19]\n"
      "v_max3_f32 v241, v0, v1, v2\n"
      "v_max3_f32 v242, v3, v4, v5\n"
      "v_max3_f32 v241, v241, v6, v7\n"
      "v_max_f32 v241, v241, v242\n"
      "v_mov_b32 v242, v241\n"
      "v_mfma_f32_16x16x32_bf16 v[20:23], a[220:223], a[172:175], v[20:23]\n"
      "s_nop 1\n"
      "v_permlane16_swap_b32 v242, v241\n"
      "s_nop 0\n"
      "v_max_f32 v241, v241, v242\n"
      "v_mov_b32 v242, v241\n"
      "v_mfma_f32_16x16x32_bf16 v[24:27], a[192:195], a[176:179], 0\n"
      "s_nop 1\n"
      "v_permlane32_swap_b32 v242, v241\n"
      "s_nop 0\n"
      "v_max_f32 v241, v241, v242\n"
      "v_sub_f32 v0, v0, v241\n"
      "v_mfma_f32_16x16x32_bf16 v[28:31], a[196:199], a[176:179], 0\n"
      "v_sub_f32 v1, v1, v241\n"
      "v_sub_f32 v2, v2, v241\n"
      "v_sub_f32 v3, v3, v241\n"
      "v_sub_f32 v4, v4, v241\n"
      "v_sub_f32 v5, v5, v241\n"
      "v_mfma_f32_16x16x32_bf16 v[24:27], a[200:203], a[180:183], v[24:27]\n"
      "v_sub_f32 v6, v6, v241\n"
      "v_sub_f32 v7, v7, v241\n"
      "v_sub_f32 v64, 0, v241\n"
      "v_sub_f32 v65, 0, v241\n"
      "v_sub_f32 v66, 0, v241\n"
      "v_mfma_f32_16x16x32_bf16 v[28:31], a[204:207], a[180:183], v[28:31]\n"
      "v_sub_f32 v67, 0, v241\n"
      "v_max3_f32 v241, v8, v9, v10\n"
      "v_max3_f32 v242, v11, v12, v13\n"
      "v_max3_f32 v241, v241, v14, v15\n"
      "v_max_f32 v241, v241, v242\n"
      "v_mfma_f32_16x16x32_bf16 v[24:27], a[208:211], a[184:187], v[24:27]\n"
      "v_mov_b32 v242, v241\n"
      "s_nop 1\n"
      "v_permlane16_swap_b32 v242, v241\n"
      "s_nop 0\n"
      "v_max_f32 v241, v241, v242\n"
      "v_mfma_f32_16x16x32_bf16 v[28:31], a[212:215], a[184:187], v[28:31]\n"
      "s_waitcnt lgkmcnt(0)\n"
      "v_mov_b32 v242, v241\n"
      "s_nop 1\n"
      "v_permlane32_swap_b32 v242, v241\n"
      "s_nop 0\n"
      "v_max_f32 v241, v241, v242\n"
      "v_mfma_f32_16x16x32_bf16 v[24:27], a[216:219], a[188:191], v[24:27]\n"
      "v_sub_f32 v8, v8, v241\n"
      "v_sub_f32 v9, v9, v241\n"
      "v_sub_f32 v10, v10, v241\n"
      "v_sub_f32 v11, v11, v241\n"
      "v_sub_f32 v12, v12, v241\n"
      "v_mfma_f32_16x16x32_bf16 v[28:31], a[220:223], a[188:191], v[28:31]\n"
      "v_sub_f32 v13, v13, v241\n"
      "v_sub_f32 v14, v14, v241\n"
      "v_sub_f32 v15, v15, v241\n"
      "v_sub_f32 v68, 0, v241\n"
      "v_sub_f32 v69, 0, v241\n"
      "v_mfma_f32_16x16x32_bf16 a[0:3], a[224:227], v[112:115], a[0:3]\n"
      "v_sub_f32 v70, 0, v241\n"
      "v_sub_f32 v71, 0, v241\n"
      "v_max3_f32 v241, v16, v17, v18\n"
      "v_max3_f32 v242, v19, v20, v21\n"
      "v_max3_f32 v241, v241, v22, v23\n"
      "v_mfma_f32_16x16x32_bf16 a[4:7], a[228:231], v[112:115], a[4:7]\n"
      "ds_read_b128 a[192:195], v192 offset:8192\n"
      "v_max_f32 v241, v241, v242\n"
      "v_mov_b32 v242, v241\n"
      "s_nop 1\n"
      "v_permlane16_swap_b32 v242, v241\n"
      "v_mfma_f32_16x16x32_bf16 a[8:11], a[232:235], v[112:115], a[8:11]\n"
      "ds_read_b128 a[196:199], v192 offset:12288\n"
      "s_nop 0\n"
      "v_max_f32 v241, v241, v242\n"
      "v_mov_b32 v242, v241\n"
      "s_nop 1\n"
      "v_mfma_f32_16x16x32_bf16 a[12:15], a[236:239], v[112:115], a[12:15]\n"
      "v_permlane32_swap_b32 v242, v241\n"
      "s_nop 0\n"
      "v_max_f32 v241, v241, v242\n"
      "v_sub_f32 v16, v16, v241\n"
      "v_sub_f32 v17, v17, v241\n"
      "v_mfma_f32_16x16x32_bf16 a[16:19], a[240:243], v[112:115], a[16:19]\n"
      "ds_read_b128 a[200:203], v193 offset:8192\n"
      "v_sub_f32 v18, v18, v241\n"
      "v_sub_f32 v19, v19, v241\n"
      "v_sub_f32 v20, v20, v241\n"
      "v_sub_f32 v21, v21, v241\n"
      "v_mfma_f32_16x16x32_bf16 a[20:23], a[244:247], v[112:115], a[20:23]\n"
      "v_sub_f32 v22, v22, v241\n"
      "v_sub_f32 v23, v23, v241\n"
      "v_sub_f32 v72, 0, v241\n"
      "v_sub_f32 v73, 0, v241\n"
      "v_sub_f32 v74, 0, v241\n"
      "v_mfma_f32_16x16x32_bf16 a[24:27], a[248:251], v[112:115], a[24:27]\n"
      "ds_read_b128 a[204:207], v193 offset:12288\n"
      "v_sub_f32 v75, 0, v241\n"
      "s_nop 7\n"
      "v_max3_f32 v241, v24, v25, v26\n"
      "v_max3_f32 v242, v27, v28, v29\n"
      "v_mfma_f32_16x16x32_bf16 a[28:31], a[252:255], v[112:115], a[28:31]\n"
      "v_max3_f32 v241, v241, v30, v31\n"
      "v_max_f32 v241, v241, v242\n"
      "v_mov_b32 v242, v241\n"
      "s_nop 1\n"
      "v_permlane16_swap_b32 v242, v241\n"
      "v_mfma_f32_16x16x32_bf16 a[32:35], a[224:227], v[116:119], a[32:35]\n"
      "ds_read_b128 a[208:211], v194 offset:8192\n"
      "s_nop 0\n"
      "v_max_f32 v241, v241, v242\n"
      "v_mov_b32 v242, v241\n"
      "s_nop 1\n"
      "v_mfma_f32_16x16x32_bf16 a[36:39], a[228:231], v[116:119], a[36:39]\n"
      "v_permlane32_swap_b32 v242, v241\n"
      "s_nop 0\n"
      "v_max_f32 v241, v241, v242\n"
      "v_sub_f32 v24, v24, v241\n"
      "v_sub_f32 v25, v25, v241\n"
      "v_mfma_f32_16x16x32_bf16 a[40:43], a[232:235], v[116:119], a[40:43]\n"
      "ds_read_b128 a[212:215], v194 offset:12288\n"
      "v_sub_f32 v26, v26, v241\n"
      "v_sub_f32 v27, v27, v241\n"
      "v_sub_f32 v28, v28, v241\n"
      "v_sub_f32 v29, v29, v241\n"
      "v_mfma_f32_16x16x32_bf16 a[44:47], a[236:239], v[116:119], a[44:47]\n"
      "ds_read_b128 a[216:219], v195 offset:8192\n"
      "v_sub_f32 v30, v30, v241\n"
      "v_sub_f32 v31, v31, v241\n"
      "v_sub_f32 v76, 0, v241\n"
      "v_sub_f32 v77, 0, v241\n"
      "v_mfma_f32_16x16x32_bf16 a[48:51], a[240:243], v[116:119], a[48:51]\n"
      "v_sub_f32 v78, 0, v241\n"
      "v_sub_f32 v79, 0, v241\n"
      "v_exp_f32 v232, v0\n"
      "ds_read_b128 a[220:223], v195 offset:12288\n"
      "v_mfma_f32_16x16x32_bf16 a[52:55], a[244:247], v[116:119], a[52:55]\n"
      "v_exp_f32 v233, v1\n"
      "s_nop 0\n"
      "v_cvt_pk_bf16_f32 v96, v232, v233\n"
      "v_mfma_f32_16x16x32_bf16 a[56:59], a[248:251], v[116:119], a[56:59]\n"
      "v_exp_f32 v234, v2\n"
      "v_exp_f32 v235, v3\n"
      "v_mfma_f32_16x16x32_bf16 a[60:63], a[252:255], v[116:119], a[60:63]\n"
      "s_add_u32 m0, s78, 49152\n"
      "s_nop 0\n"
      "v_cvt_pk_bf16_f32 v97, v234, v235\n"
      "v_exp_f32 v236, v4\n"
      "v_mfma_f32_16x16x32_bf16 a[64:67], a[224:227], v[120:123], a[64:67]\n"
      "buffer_load_dwordx4 v196, s[40:43], s68 offen offset:0 nt lds\n"
      "v_exp_f32 v237, v5\n"
      "s_nop 0\n"
      "v_cvt_pk_bf16_f32 v98, v236, v237\n"
      "v_mfma_f32_16x16x32_bf16 a[68:71], a[228:231], v[120:123], a[68:71]\n"
      "v_exp_f32 v238, v6\n"
      "v_exp_f32 v239, v7\n"
      "v_mfma_f32_16x16x32_bf16 a[72:75], a[232:235], v[120:123], a[72:75]\n"
      "s_nop 0\n"
      "v_cvt_pk_bf16_f32 v99, v238, v239\n"
      "v_exp_f32 v232, v8\n"
      "v_mfma_f32_16x16x32_bf16 a[76:79], a[236:239], v[120:123], a[76:79]\n"
      "v_exp_f32 v233, v9\n"
      "v_mfma_f32_16x16x32_bf16 v[80:83], v[228:231], v[96:99], 0\n"
      "s_nop 0\n"
      "v_cvt_pk_bf16_f32 v100, v232, v233\n"
      "v_mfma_f32_16x16x32_bf16 a[80:83], a[240:243], v[120:123], a[80:83]\n"
      "buffer_load_dwordx4 v197, s[40:43], s68 offen offset:1024 nt lds\n"
      "v_exp_f32 v234, v10\n"
      "v_exp_f32 v235, v11\n"
      "v_mfma_f32_16x16x32_bf16 a[84:87], a[244:247], v[120:123], a[84:87]\n"
      "s_nop 0\n"
      "v_cvt_pk_bf16_f32 v101, v234, v235\n"
      "v_exp_f32 v236, v12\n"
      "v_mfma_f32_16x16x32_bf16 a[88:91], a[248:251], v[120:123], a[88:91]\n"
      "v_exp_f32 v237, v13\n"
      "s_nop 0\n"
      "v_cvt_pk_bf16_f32 v102, v236, v237\n"
      "v_mfma_f32_16x16x32_bf16 a[92:95], a[252:255], v[120:123], a[92:95]\n"
      "v_exp_f32 v238, v14\n"
      "v_exp_f32 v239, v15\n"
      "v_mfma_f32_16x16x32_bf16 a[96:99], a[224:227], v[124:127], a[96:99]\n"
      "buffer_load_dwordx4 v198, s[40:43], s68 offen offset:2048 nt lds\n"
      "s_nop 0\n"
      "v_cvt_pk_bf16_f32 v103, v238, v239\n"
      "v_exp_f32 v232, v16\n"
      "v_mfma_f32_16x16x32_bf16 a[100:103], a[228:231], v[124:127], a[100:103]\n"
      "v_exp_f32 v233, v17\n"
      "v_mfma_f32_16x16x32_bf16 v[84:87], v[228:231], v[100:103], 0\n"
      "s_nop 0\n"
      "v_cvt_pk_bf16_f32 v104, v232, v233\n"
      "v_mfma_f32_16x16x32_bf16 a[104:107], a[232:235], v[124:127], a[104:107]\n"
      "v_exp_f32 v234, v18\n"
      "v_exp_f32 v235, v19\n"
      "v_mfma_f32_16x16x32_bf16 a[108:111], a[236:239], v[124:127], a[108:111]\n"
      "buffer_load_dwordx4 v199, s[40:43], s68 offen offset:3072 nt lds\n"
      "s_nop 0\n"
      "v_cvt_pk_bf16_f32 v105, v234, v235\n"
      "v_exp_f32 v236, v20\n"
      "v_mfma_f32_16x16x32_bf16 a[112:115], a[240:243], v[124:127], a[112:115]\n"
      "s_add_u32 s68, s68, 16384\n"
      "v_exp_f32 v237, v21\n"
      "s_nop 0\n"
      "v_cvt_pk_bf16_f32 v106, v236, v237\n"
      "v_mfma_f32_16x16x32_bf16 a[116:119], a[244:247], v[124:127], a[116:119]\n"
      "v_exp_f32 v238, v22\n"
      "v_exp_f32 v239, v23\n"
      "v_mfma_f32_16x16x32_bf16 a[120:123], a[248:251], v[124:127], a[120:123]\n"
      "s_waitcnt lgkmcnt(0)\n"
      "s_nop 0\n"
      "v_cvt_pk_bf16_f32 v107, v238, v239\n"
      "v_exp_f32 v232, v24\n"
      "v_mfma_f32_16x16x32_bf16 a[124:127], a[252:255], v[124:127], a[124:127]\n"
      "v_exp_f32 v233, v25\n"
      "v_mfma_f32_16x16x32_bf16 v[88:91], v[228:231], v[104:107], 0\n"
      "s_nop 0\n"
      "v_cvt_pk_bf16_f32 v108, v232, v233\n"
      "v_exp_f32 v234, v26\n"
      "s_mov_b32 s75, 0\n"
      "v_mfma_f32_16x16x32_bf16 v[32:35], a[192:195], a[128:131], v[64:67]\n"
      "ds_read_b64_tr_b16 a[224:225], v200 offset:0\n"
      "ds_read_b64_tr_b16 a[226:227], v200 offset:4096\n"
      "v_rcp_f32 v214, v212\n"
      "v_rcp_f32 v218, v216\n"
      "v_rcp_f32 v222, v220\n"
      "v_rcp_f32 v226, v224\n"
      "s_nop 0\n"
      "v_mov_b32 v215, v214\n"
      "v_mfma_f32_16x16x32_bf16 v[36:39], a[196:199], a[128:131], v[64:67]\n"
      "s_add_u32 s95, s82, 80\n"
      "v_exp_f32 v235, v27\n"
      "v_mov_b32 v219, v218\n"
      "v_mov_b32 v223, v222\n"
      "v_mov_b32 v227, v226\n"
      "v_accvgpr_read_b32 v132, a0\n"
      "v_accvgpr_read_b32 v133, a1\n"
      "v_accvgpr_read_b32 v134, a2\n"
      "v_accvgpr_read_b32 v135, a3\n"
      "v_accvgpr_read_b32 v136, a4\n"
      "v_accvgpr_read_b32 v137, a5\n"
      "v_accvgpr_read_b32 v138, a6\n"
      "v_mfma_f32_16x16x32_bf16 v[32:35], a[200:203], a[132:135], v[32:35]\n"
      "ds_read_b64_tr_b16 a[228:229], v201 offset:0\n"
      "v_cvt_pk_bf16_f32 v109, v234, v235\n"
      "v_accvgpr_read_b32 v139, a7\n"
      "v_pk_mul_f32 v[132:133], v[132:133], v[214:215]\n"
      "v_pk_mul_f32 v[134:135], v[134:135], v[214:215]\n"
      "v_pk_mul_f32 v[136:137], v[136:137], v[214:215]\n"
      "v_pk_mul_f32 v[138:139], v[138:139], v[214:215]\n"
      "v_cvt_pk_bf16_f32 v128, v132, v133\n"
      "v_mfma_f32_16x16x32_bf16 v[36:39], a[204:207], a[132:135], v[36:39]\n"
      "v_exp_f32 v236, v28\n"
      "v_cvt_pk_bf16_f32 v129, v134, v135\n"
      "v_cvt_pk_bf16_f32 v130, v136, v137\n"
      "v_cvt_pk_bf16_f32 v131, v138, v139\n"
      "v_accvgpr_read_b32 v136, a8\n"
      "v_accvgpr_read_b32 v137, a9\n"
      "v_accvgpr_read_b32 v138, a10\n"
      "v_accvgpr_read_b32 v139, a11\n"
      "v_accvgpr_read_b32 v140, a12\n"
      "v_accvgpr_read_b32 v141, a13\n"
      "v_accvgpr_read_b32 v142, a14\n"
      "v_mfma_f32_16x16x32_bf16 v[32:35], a[208:211], a[136:139], v[32:35]\n"
      "ds_read_b64_tr_b16 a[230:231], v201 offset:4096\n"
      "ds_read_b64_tr_b16 a[232:233], v202 offset:0\n"
      "v_accvgpr_read_b32 v143, a15\n"
      "v_pk_mul_f32 v[136:137], v[136:137], v[214:215]\n"
      "v_pk_mul_f32 v[138:139], v[138:139], v[214:215]\n"
      "v_pk_mul_f32 v[140:141], v[140:141], v[214:215]\n"
      "v_pk_mul_f32 v[142:143], v[142:143], v[214:215]\n"
      "v_cvt_pk_bf16_f32 v132, v136, v137\n"
      "v_mfma_f32_16x16x32_bf16 v[36:39], a[212:215], a[136:139], v[36:39]\n"
      "v_exp_f32 v237, v29\n"
      "v_cvt_pk_bf16_f32 v133, v138, v139\n"
      "v_cvt_pk_bf16_f32 v134, v140, v141\n"
      "v_cvt_pk_bf16_f32 v135, v142, v143\n"
      "v_accvgpr_read_b32 v140, a16\n"
      "v_accvgpr_read_b32 v141, a17\n"
      "v_accvgpr_read_b32 v142, a18\n"
      "v_accvgpr_read_b32 v143, a19\n"
      "v_accvgpr_read_b32 v144, a20\n"
      "v_accvgpr_read_b32 v145, a21\n"
      "v_accvgpr_read_b32 v146, a22\n"
      "v_mfma_f32_16x16x32_bf16 v[32:35], a[216:219], a[140:143], v[32:35]\n"
      "ds_read_b64_tr_b16 a[234:235], v202 offset:4096\n"
      "v_cvt_pk_bf16_f32 v110, v236, v237\n"
      "v_accvgpr_read_b32 v147, a23\n"
      "v_pk_mul_f32 v[140:141], v[140:141], v[214:215]\n"
      "v_pk_mul_f32 v[142:143], v[142:143], v[214:215]\n"
      "v_pk_mul_f32 v[144:145], v[144:145], v[214:215]\n"
      "v_pk_mul_f32 v[146:147], v[146:147], v[214:215]\n"
      "v_cvt_pk_bf16_f32 v136, v140, v141\n"
      "v_mfma_f32_16x16x32_bf16 v[36:39], a[220:223], a[140:143], v[36:39]\n"
      "v_exp_f32 v238, v30\n"
      "v_cvt_pk_bf16_f32 v137, v142, v143\n"
      "v_cvt_pk_bf16_f32 v138, v144, v145\n"
      "v_cvt_pk_bf16_f32 v139, v146, v147\n"
      "v_accvgpr_read_b32 v144, a24\n"
      "v_accvgpr_read_b32 v145, a25\n"
      "v_accvgpr_read_b32 v146, a26\n"
      "v_accvgpr_read_b32 v147, a27\n"
      "v_accvgpr_read_b32 v148, a28\n"
      "v_accvgpr_read_b32 v149, a29\n"
      "v_accvgpr_read_b32 v150, a30\n"
      "v_mfma_f32_16x16x32_bf16 v[40:43], a[192:195], a[144:147], v[68:71]\n"
      "ds_read_b64_tr_b16 a[236:237], v203 offset:0\n"
      "ds_read_b64_tr_b16 a[238:239], v203 offset:4096\n"
      "v_accvgpr_read_b32 v151, a31\n"
      "v_pk_mul_f32 v[144:145], v[144:145], v[214:215]\n"
      "v_pk_mul_f32 v[146:147], v[146:147], v[214:215]\n"
      "v_pk_mul_f32 v[148:149], v[148:149], v[214:215]\n"
      "v_pk_mul_f32 v[150:151], v[150:151], v[214:215]\n"
      "v_cvt_pk_bf16_f32 v140, v144, v145\n"
      "v_mfma_f32_16x16x32_bf16 v[44:47], a[196:199], a[144:147], v[68:71]\n"
      "v_exp_f32 v239, v31\n"
      "v_cvt_pk_bf16_f32 v141, v146, v147\n"
      "v_cvt_pk_bf16_f32 v142, v148, v149\n"
      "v_cvt_pk_bf16_f32 v143, v150, v151\n"
      "v_accvgpr_read_b32 v148, a32\n"
      "v_accvgpr_read_b32 v149, a33\n"
      "v_accvgpr_read_b32 v150, a34\n"
      "v_accvgpr_read_b32 v151, a35\n"
      "v_accvgpr_read_b32 v152, a36\n"
      "v_accvgpr_read_b32 v153, a37\n"
      "v_accvgpr_read_b32 v154, a38\n"
      "v_mfma_f32_16x16x32_bf16 v[40:43], a[200:203], a[148:151], v[40:43]\n"
      "ds_read_b64_tr_b16 a[240:241], v204 offset:0\n"
      "v_cvt_pk_bf16_f32 v111, v238, v239\n"
      "v_accvgpr_read_b32 v155, a39\n"
      "v_pk_mul_f32 v[148:149], v[148:149], v[218:219]\n"
      "v_pk_mul_f32 v[150:151], v[150:151], v[218:219]\n"
      "v_pk_mul_f32 v[152:153], v[152:153], v[218:219]\n"
      "v_pk_mul_f32 v[154:155], v[154:155], v[218:219]\n"
      "v_cvt_pk_bf16_f32 v144, v148, v149\n"
      "v_mfma_f32_16x16x32_bf16 v[44:47], a[204:207], a[148:151], v[44:47]\n"
      "ds_read_b64_tr_b16 a[242:243], v204 offset:4096\n"
      "ds_read_b64_tr_b16 a[244:245], v205 offset:0\n"
      "v_cvt_pk_bf16_f32 v145, v150, v151\n"
      "v_cvt_pk_bf16_f32 v146, v152, v153\n"
      "v_cvt_pk_bf16_f32 v147, v154, v155\n"
      "v_accvgpr_read_b32 v152, a40\n"
      "v_accvgpr_read_b32 v153, a41\n"
      "v_accvgpr_read_b32 v154, a42\n"
      "v_accvgpr_read_b32 v155, a43\n"
      "v_accvgpr_read_b32 v156, a44\n"
      "v_accvgpr_read_b32 v157, a45\n"
      "v_accvgpr_read_b32 v158, a46\n"
      "v_mfma_f32_16x16x32_bf16 v[40:43], a[208:211], a[152:155], v[40:43]\n"
      "v_exp_f32 v232, v32\n"
      "v_accvgpr_read_b32 v159, a47\n"
      "v_pk_mul_f32 v[152:153], v[152:153], v[218:219]\n"
      "v_pk_mul_f32 v[154:155], v[154:155], v[218:219]\n"
      "v_pk_mul_f32 v[156:157], v[156:157], v[218:219]\n"
      "v_pk_mul_f32 v[158:159], v[158:159], v[218:219]\n"
      "v_cvt_pk_bf16_f32 v148, v152, v153\n"
      "v_mfma_f32_16x16x32_bf16 v[92:95], v[228:231], v[108:111], 0\n"
      "ds_read_b64_tr_b16 a[246:247], v205 offset:4096\n"
      "ds_read_b64_tr_b16 a[248:249], v206 offset:0\n"
      "v_cvt_pk_bf16_f32 v149, v154, v155\n"
      "v_cvt_pk_bf16_f32 v150, v156, v157\n"
      "v_cvt_pk_bf16_f32 v151, v158, v159\n"
      "v_accvgpr_read_b32 v156, a48\n"
      "v_accvgpr_read_b32 v157, a49\n"
      "v_accvgpr_read_b32 v158, a50\n"
      "v_accvgpr_read_b32 v159, a51\n"
      "v_accvgpr_read_b32 v160, a52\n"
      "v_accvgpr_read_b32 v161, a53\n"
      "v_accvgpr_read_b32 v162, a54\n"
      "v_mfma_f32_16x16x32_bf16 v[44:47], a[212:215], a[152:155], v[44:47]\n"
      "v_exp_f32 v233, v33\n"
      "v_accvgpr_read_b32 v163, a55\n"
      "v_pk_mul_f32 v[156:157], v[156:157], v[218:219]\n"
      "v_pk_mul_f32 v[158:159], v[158:159], v[218:219]\n"
      "v_pk_mul_f32 v[160:161], v[160:161], v[218:219]\n"
      "v_pk_mul_f32 v[162:163], v[162:163], v[218:219]\n"
      "v_cvt_pk_bf16_f32 v152, v156, v157\n"
      "v_mfma_f32_16x16x32_bf16 v[40:43], a[216:219], a[156:159], v[40:43]\n"
      "ds_read_b64_tr_b16 a[250:251], v206 offset:4096\n"
      "v_cvt_pk_bf16_f32 v112, v232, v233\n"
      "v_cvt_pk_bf16_f32 v153, v158, v159\n"
      "v_cvt_pk_bf16_f32 v154, v160, v161\n"
      "v_cvt_pk_bf16_f32 v155, v162, v163\n"
      "v_accvgpr_read_b32 v160, a56\n"
      "v_accvgpr_read_b32 v161, a57\n"
      "v_accvgpr_read_b32 v162, a58\n"
      "v_accvgpr_read_b32 v163, a59\n"
      "v_accvgpr_read_b32 v164, a60\n"
      "v_accvgpr_read_b32 v165, a61\n"
      "v_accvgpr_read_b32 v166, a62\n"
      "v_mfma_f32_16x16x32_bf16 v[44:47], a[220:223], a[156:159], v[44:47]\n"
      "v_exp_f32 v234, v34\n"
      "v_accvgpr_read_b32 v167, a63\n"
      "v_pk_mul_f32 v[160:161], v[160:161], v[218:219]\n"
      "v_pk_mul_f32 v[162:163], v[162:163], v[218:219]\n"
      "v_pk_mul_f32 v[164:165], v[164:165], v[218:219]\n"
      "v_pk_mul_f32 v[166:167], v[166:167], v[218:219]\n"
      "v_cvt_pk_bf16_f32 v156, v160, v161\n"
      "v_mfma_f32_16x16x32_bf16 v[48:51], a[192:195], a[160:163], v[72:75]\n"
      "ds_read_b64_tr_b16 a[252:253], v207 offset:0\n"
      "ds_read_b64_tr_b16 a[254:255], v207 offset:4096\n"
      "v_cvt_pk_bf16_f32 v157, v162, v163\n"
      "v_cvt_pk_bf16_f32 v158, v164, v165\n"
      "v_cvt_pk_bf16_f32 v159, v166, v167\n"
      "v_accvgpr_read_b32 v164, a64\n"
      "v_accvgpr_read_b32 v165, a65\n"
      "v_accvgpr_read_b32 v166, a66\n"
      "v_accvgpr_read_b32 v167, a67\n"
      "v_accvgpr_read_b32 v168, a68\n"
      "v_accvgpr_read_b32 v169, a69\n"
      "v_accvgpr_read_b32 v170, a70\n"
      "v_mfma_f32_16x16x32_bf16 v[52:55], a[196:199], a[160:163], v[72:75]\n"
      "v_exp_f32 v235, v35\n"
      "v_accvgpr_read_b32 v171, a71\n"
      "v_pk_mul_f32 v[164:165], v[164:165], v[222:223]\n"
      "v_pk_mul_f32 v[166:167], v[166:167], v[222:223]\n"
      "v_pk_mul_f32 v[168:169], v[168:169], v[222:223]\n"
      "v_pk_mul_f32 v[170:171], v[170:171], v[222:223]\n"
      "v_cvt_pk_bf16_f32 v160, v164, v165\n"
      "v_mfma_f32_16x16x32_bf16 v[48:51], a[200:203], a[164:167], v[48:51]\n"
      "v_cvt_pk_bf16_f32 v113, v234, v235\n"
      "v_cvt_pk_bf16_f32 v161, v166, v167\n"
      "v_cvt_pk_bf16_f32 v162, v168, v169\n"
      "v_cvt_pk_bf16_f32 v163, v170, v171\n"
      "v_accvgpr_read_b32 v168, a72\n"
      "v_accvgpr_read_b32 v169, a73\n"
      "v_accvgpr_read_b32 v170, a74\n"
      "v_accvgpr_read_b32 v171, a75\n"
      "v_accvgpr_read_b32 v172, a76\n"
      "v_accvgpr_read_b32 v173, a77\n"
      "v_accvgpr_read_b32 v174, a78\n"
      "v_accvgpr_read_b32 v175, a79\n"
      "v_mfma_f32_16x16x32_bf16 v[52:55], a[204:207], a[164:167], v[52:55]\n"
      "v_exp_f32 v236, v36\n"
      "v_pk_mul_f32 v[168:169], v[168:169], v[222:223]\n"
      "v_pk_mul_f32 v[170:171], v[170:171], v[222:223]\n"
      "v_pk_mul_f32 v[172:173], v[172:173], v[222:223]\n"
      "v_pk_mul_f32 v[174:175], v[174:175], v[222:223]\n"
      "v_cvt_pk_bf16_f32 v164, v168, v169\n"
      "v_cvt_pk_bf16_f32 v165, v170, v171\n"
      "v_mfma_f32_16x16x32_bf16 v[48:51], a[208:211], a[168:171], v[48:51]\n"
      "v_max3_f32 v240, v80, v84, v88\n"
      "v_max_f32 v240, v240, v92\n"
      "v_cvt_pk_bf16_f32 v166, v172, v173\n"
      "v_cvt_pk_bf16_f32 v167, v174, v175\n"
      "v_accvgpr_read_b32 v172, a80\n"
      "v_accvgpr_read_b32 v173, a81\n"
      "v_accvgpr_read_b32 v174, a82\n"
      "v_accvgpr_read_b32 v175, a83\n"
      "v_accvgpr_read_b32 v176, a84\n"
      "v_accvgpr_read_b32 v177, a85\n"
      "v_accvgpr_read_b32 v178, a86\n"
      "v_accvgpr_read_b32 v179, a87\n"
      "v_mfma_f32_16x16x32_bf16 v[52:55], a[212:215], a[168:171], v[52:55]\n"
      "v_exp_f32 v237, v37\n"
      "v_pk_mul_f32 v[172:173], v[172:173], v[222:223]\n"
      "v_pk_mul_f32 v[174:175], v[174:175], v[222:223]\n"
      "v_pk_mul_f32 v[176:177], v[176:177], v[222:223]\n"
      "v_pk_mul_f32 v[178:179], v[178:179], v[222:223]\n"
      "v_cvt_pk_bf16_f32 v168, v172, v173\n"
      "v_cvt_pk_bf16_f32 v169, v174, v175\n"
      "v_mfma_f32_16x16x32_bf16 v[48:51], a[216:219], a[172:175], v[48:51]\n"
      "v_cvt_pk_bf16_f32 v114, v236, v237\n"
      "v_cvt_pk_bf16_f32 v170, v176, v177\n"
      "v_cvt_pk_bf16_f32 v171, v178, v179\n"
      "v_accvgpr_read_b32 v176, a88\n"
      "v_accvgpr_read_b32 v177, a89\n"
      "v_accvgpr_read_b32 v178, a90\n"
      "v_accvgpr_read_b32 v179, a91\n"
      "v_accvgpr_read_b32 v180, a92\n"
      "v_accvgpr_read_b32 v181, a93\n"
      "v_accvgpr_read_b32 v182, a94\n"
      "v_accvgpr_read_b32 v183, a95\n"
      "v_mfma_f32_16x16x32_bf16 v[52:55], a[220:223], a[172:175], v[52:55]\n"
      "v_exp_f32 v238, v38\n"
      "v_pk_mul_f32 v[176:177], v[176:177], v[222:223]\n"
      "v_pk_mul_f32 v[178:179], v[178:179], v[222:223]\n"
      "v_pk_mul_f32 v[180:181], v[180:181], v[222:223]\n"
      "v_pk_mul_f32 v[182:183], v[182:183], v[222:223]\n"
      "v_cvt_pk_bf16_f32 v172, v176, v177\n"
      "v_cvt_pk_bf16_f32 v173, v178, v179\n"
      "v_mfma_f32_16x16x32_bf16 v[56:59], a[192:195], a[176:179], v[76:79]\n"
      "v_cmp_nge_f32 vcc, s74, v240\n"
      "v_cvt_pk_bf16_f32 v174, v180, v181\n"
      "v_cvt_pk_bf16_f32 v175, v182, v183\n"
      "s_nop 7\n"
      "v_accvgpr_read_b32 v180, a96\n"
      "v_accvgpr_read_b32 v181, a97\n"
      "v_accvgpr_read_b32 v182, a98\n"
      "v_accvgpr_read_b32 v183, a99\n"
      "v_accvgpr_read_b32 v184, a100\n"
      "v_accvgpr_read_b32 v185, a101\n"
      "v_accvgpr_read_b32 v186, a102\n"
      "v_accvgpr_read_b32 v187, a103\n"
      "v_mfma_f32_16x16x32_bf16 v[60:63], a[196:199], a[176:179], v[76:79]\n"
      "v_exp_f32 v239, v39\n"
      "v_pk_mul_f32 v[180:181], v[180:181], v[226:227]\n"
      "v_pk_mul_f32 v[182:183], v[182:183], v[226:227]\n"
      "v_pk_mul_f32 v[184:185], v[184:185], v[226:227]\n"
      "v_pk_mul_f32 v[186:187], v[186:187], v[226:227]\n"
      "v_cvt_pk_bf16_f32 v176, v180, v181\n"
      "v_cvt_pk_bf16_f32 v177, v182, v183\n"
      "v_mfma_f32_16x16x32_bf16 v[56:59], a[200:203], a[180:183], v[56:59]\n"
      "v_cvt_pk_bf16_f32 v115, v238, v239\n"
      "v_cvt_pk_bf16_f32 v178, v184, v185\n"
      "v_cvt_pk_bf16_f32 v179, v186, v187\n"
      "v_accvgpr_read_b32 v184, a104\n"
      "v_accvgpr_read_b32 v185, a105\n"
      "v_accvgpr_read_b32 v186, a106\n"
      "v_accvgpr_read_b32 v187, a107\n"
      "v_accvgpr_read_b32 v188, a108\n"
      "v_accvgpr_read_b32 v189, a109\n"
      "v_accvgpr_read_b32 v190, a110\n"
      "v_accvgpr_read_b32 v191, a111\n"
      "v_mfma_f32_16x16x32_bf16 v[60:63], a[204:207], a[180:183], v[60:63]\n"
      "v_pk_mul_f32 v[184:185], v[184:185], v[226:227]\n"
      "v_pk_mul_f32 v[186:187], v[186:187], v[226:227]\n"
      "v_pk_mul_f32 v[188:189], v[188:189], v[226:227]\n"
      "v_pk_mul_f32 v[190:191], v[190:191], v[226:227]\n"
      "v_cvt_pk_bf16_f32 v180, v184, v185\n"
      "v_cvt_pk_bf16_f32 v181, v186, v187\n"
      "v_cvt_pk_bf16_f32 v182, v188, v189\n"
      "v_cvt_pk_bf16_f32 v183, v190, v191\n"
      "v_mfma_f32_16x16x32_bf16 v[56:59], a[208:211], a[184:187], v[56:59]\n"
      "v_exp_f32 v232, v40\n"
      "v_accvgpr_read_b32 v242, a112\n"
      "v_accvgpr_read_b32 v243, a113\n"
      "v_accvgpr_read_b32 v244, a114\n"
      "v_accvgpr_read_b32 v245, a115\n"
      "v_accvgpr_read_b32 v246, a116\n"
      "v_accvgpr_read_b32 v247, a117\n"
      "v_accvgpr_read_b32 v248, a118\n"
      "v_accvgpr_read_b32 v249, a119\n"
      "v_pk_mul_f32 v[242:243], v[242:243], v[226:227]\n"
      "v_mfma_f32_16x16x32_bf16 v[60:63], a[212:215], a[184:187], v[60:63]\n"
      "s_waitcnt vmcnt(12) lgkmcnt(0)\n"
      "s_barrier\n"
      "v_pk_mul_f32 v[244:245], v[244:245], v[226:227]\n"
      "v_pk_mul_f32 v[246:247], v[246:247], v[226:227]\n"
      "v_pk_mul_f32 v[248:249], v[248:249], v[226:227]\n"
      "v_cvt_pk_bf16_f32 v184, v242, v243\n"
      "v_cvt_pk_bf16_f32 v185, v244, v245\n"
      "v_cvt_pk_bf16_f32 v186, v246, v247\n"
      "v_cvt_pk_bf16_f32 v187, v248, v249\n"
      "v_accvgpr_read_b32 v242, a120\n"
      "v_accvgpr_read_b32 v243, a121\n"
      "v_mfma_f32_16x16x32_bf16 v[56:59], a[216:219], a[188:191], v[56:59]\n"
      "v_exp_f32 v233, v41\n"
      "v_accvgpr_read_b32 v244, a122\n"
      "v_accvgpr_read_b32 v245, a123\n"
      "v_accvgpr_read_b32 v246, a124\n"
      "v_accvgpr_read_b32 v247, a125\n"
      "v_accvgpr_read_b32 v248, a126\n"
      "v_accvgpr_read_b32 v249, a127\n"
      "v_pk_mul_f32 v[242:243], v[242:243], v[226:227]\n"
      "v_pk_mul_f32 v[244:245], v[244:245], v[226:227]\n"
      "v_pk_mul_f32 v[246:247], v[246:247], v[226:227]\n"
      "v_pk_mul_f32 v[248:249], v[248:249], v[226:227]\n"
      "v_cvt_pk_bf16_f32 v188, v242, v243\n"
      "v_cvt_pk_bf16_f32 v189, v244, v245\n"
      "v_cvt_pk_bf16_f32 v190, v246, v247\n"
      "v_cvt_pk_bf16_f32 v191, v248, v249\n"
      "v_and_b32 v249, 15, v255\n"
      "v_lshlrev_b32 v249, 2, v249\n"
      "v_mfma_f32_16x16x32_bf16 v[60:63], a[220:223], a[188:191], v[60:63]\n"
      "v_cvt_pk_bf16_f32 v116, v232, v233\n"
      "s_cbranch_vccnz STUB18_%=\n"
      "BACK18_%=:\n"
      "v_mfma_f32_16x16x32_bf16 a[0:3], a[224:227], v[96:99], 0\n"
      "v_exp_f32 v234, v42\n"
      "v_mfma_f32_16x16x32_bf16 a[4:7], a[228:231], v[96:99], 0\n"
      "ds_read_b128 a[192:195], v192 offset:16384\n"
      "v_mfma_f32_16x16x32_bf16 v[212:215], v[228:231], v[112:115], v[80:83]\n"
      "v_exp_f32 v235, v43\n"
      "v_mfma_f32_16x16x32_bf16 a[8:11], a[232:235], v[96:99], 0\n"
      "ds_read_b128 a[196:199], v192 offset:20480\n"
      "v_cvt_pk_bf16_f32 v117, v234, v235\n"
      "v_mfma_f32_16x16x32_bf16 a[12:15], a[236:239], v[96:99], 0\n"
      "v_exp_f32 v236, v44\n"
      "v_mfma_f32_16x16x32_bf16 a[16:19], a[240:243], v[96:99], 0\n"
      "ds_read_b128 a[200:203], v193 offset:16384\n"
      "v_mfma_f32_16x16x32_bf16 a[20:23], a[244:247], v[96:99], 0\n"
      "v_exp_f32 v237, v45\n"
      "v_mfma_f32_16x16x32_bf16 a[24:27], a[248:251], v[96:99], 0\n"
      "ds_read_b128 a[204:207], v193 offset:20480\n"
      "v_cvt_pk_bf16_f32 v118, v236, v237\n"
      "v_mfma_f32_16x16x32_bf16 a[28:31], a[252:255], v[96:99], 0\n"
      "v_exp_f32 v238, v46\n"
      "v_mfma_f32_16x16x32_bf16 a[32:35], a[224:227], v[100:103], 0\n"
      "ds_read_b128 a[208:211], v194 offset:16384\n"
      "s_mov_b32 s87, 0\n"
      "buffer_store_dwordx4 v[128:131], v254, s[56:59], s87 offen offset:0 sc1\n"
      "v_mfma_f32_16x16x32_bf16 a[36:39], a[228:231], v[100:103], 0\n"
      "v_exp_f32 v239, v47\n"
      "v_mfma_f32_16x16x32_bf16 a[40:43], a[232:235], v[100:103], 0\n"
      "ds_read_b128 a[212:215], v194 offset:20480\n"
      "v_cvt_pk_bf16_f32 v119, v238, v239\n"
      "v_mfma_f32_16x16x32_bf16 a[44:47], a[236:239], v[100:103], 0\n"
      "ds_read_b128 a[216:219], v195 offset:16384\n"
      "v_mfma_f32_16x16x32_bf16 a[48:51], a[240:243], v[100:103], 0\n"
      "v_exp_f32 v232, v48\n"
      "v_mfma_f32_16x16x32_bf16 v[216:219], v[228:231], v[116:119], v[84:87]\n"
      "ds_read_b128 a[220:223], v195 offset:20480\n"
      "v_mfma_f32_16x16x32_bf16 a[52:55], a[244:247], v[100:103], 0\n"
      "v_exp_f32 v233, v49\n"
      "v_mfma_f32_16x16x32_bf16 a[56:59], a[248:251], v[100:103], 0\n"
      "v_cvt_pk_bf16_f32 v120, v232, v233\n"
      "v_mfma_f32_16x16x32_bf16 a[60:63], a[252:255], v[100:103], 0\n"
      "s_add_u32 m0, s78, 114688\n"
      "v_exp_f32 v234, v50\n"
      "v_mfma_f32_16x16x32_bf16 a[64:67], a[224:227], v[104:107], 0\n"
      "buffer_load_dwordx4 v208, s[44:47], s69 offen offset:0 nt lds\n"
      "v_mfma_f32_16x16x32_bf16 a[68:71], a[228:231], v[104:107], 0\n"
      "v_exp_f32 v235, v51\n"
      "v_mfma_f32_16x16x32_bf16 a[72:75], a[232:235], v[104:107], 0\n"
      "v_cvt_pk_bf16_f32 v121, v234, v235\n"
      "v_mfma_f32_16x16x32_bf16 a[76:79], a[236:239], v[104:107], 0\n"
      "v_exp_f32 v236, v52\n"
      "v_mfma_f32_16x16x32_bf16 a[80:83], a[240:243], v[104:107], 0\n"
      "buffer_load_dwordx4 v209, s[44:47], s69 offen offset:1024 nt lds\n"
      "v_mfma_f32_16x16x32_bf16 a[84:87], a[244:247], v[104:107], 0\n"
      "v_exp_f32 v237, v53\n"
      "v_mfma_f32_16x16x32_bf16 a[88:91], a[248:251], v[104:107], 0\n"
      "v_cvt_pk_bf16_f32 v122, v236, v237\n"
      "v_mfma_f32_16x16x32_bf16 a[92:95], a[252:255], v[104:107], 0\n"
      "v_exp_f32 v238, v54\n"
      "v_mfma_f32_16x16x32_bf16 a[96:99], a[224:227], v[108:111], 0\n"
      "buffer_load_dwordx4 v208, s[44:47], s69 offen offset:2048 nt lds\n"
      "v_mfma_f32_16x16x32_bf16 a[100:103], a[228:231], v[108:111], 0\n"
      "v_exp_f32 v239, v55\n"
      "v_mfma_f32_16x16x32_bf16 a[104:107], a[232:235], v[108:111], 0\n"
      "v_cvt_pk_bf16_f32 v123, v238, v239\n"
      "v_mfma_f32_16x16x32_bf16 a[108:111], a[236:239], v[108:111], 0\n"
      "buffer_load_dwordx4 v209, s[44:47], s69 offen offset:3072 nt lds\n"
      "v_mfma_f32_16x16x32_bf16 a[112:115], a[240:243], v[108:111], 0\n"
      "s_add_u32 s69, s69, 16384\n"
      "v_exp_f32 v232, v56\n"
      "v_mfma_f32_16x16x32_bf16 v[220:223], v[228:231], v[120:123], v[88:91]\n"
      "v_mfma_f32_16x16x32_bf16 a[116:119], a[244:247], v[108:111], 0\n"
      "v_exp_f32 v233, v57\n"
      "v_mfma_f32_16x16x32_bf16 a[120:123], a[248:251], v[108:111], 0\n"
      "s_waitcnt lgkmcnt(0)\n"
      "v_cvt_pk_bf16_f32 v124, v232, v233\n"
      "v_mfma_f32_16x16x32_bf16 a[124:127], a[252:255], v[108:111], 0\n"
      "v_exp_f32 v234, v58\n"
      "v_mfma_f32_16x16x32_bf16 v[0:3], a[192:195], a[128:131], v[64:67]\n"
      "ds_read_b64_tr_b16 a[224:225], v200 offset:8192\n"
      "ds_read_b64_tr_b16 a[226:227], v200 offset:12288\n"
      "v_mfma_f32_16x16x32_bf16 v[4:7], a[196:199], a[128:131], v[64:67]\n"
      "s_add_u32 s95, s82, 80\n"
      "v_exp_f32 v235, v59\n"
      "v_mfma_f32_16x16x32_bf16 v[0:3], a[200:203], a[132:135], v[0:3]\n"
      "ds_read_b64_tr_b16 a[228:229], v201 offset:8192\n"
      "v_cvt_pk_bf16_f32 v125, v234, v235\n"
      "v_mfma_f32_16x16x32_bf16 v[4:7], a[204:207], a[132:135], v[4:7]\n"
      "v_exp_f32 v236, v60\n"
      "v_mfma_f32_16x16x32_bf16 v[0:3], a[208:211], a[136:139], v[0:3]\n"
      "ds_read_b64_tr_b16 a[230:231], v201 offset:12288\n"
      "ds_read_b64_tr_b16 a[232:233], v202 offset:8192\n"
      "v_mfma_f32_16x16x32_bf16 v[4:7], a[212:215], a[136:139], v[4:7]\n"
      "v_exp_f32 v237, v61\n"
      "v_mfma_f32_16x16x32_bf16 v[0:3], a[216:219], a[140:143], v[0:3]\n"
      "ds_read_b64_tr_b16 a[234:235], v202 offset:12288\n"
      "v_cvt_pk_bf16_f32 v126, v236, v237\n"
      "v_mfma_f32_16x16x32_bf16 v[4:7], a[220:223], a[140:143], v[4:7]\n"
      "v_exp_f32 v238, v62\n"
      "v_mfma_f32_16x16x32_bf16 v[8:11], a[192:195], a[144:147], v[68:71]\n"
      "ds_read_b64_tr_b16 a[236:237], v203 offset:8192\n"
      "ds_read_b64_tr_b16 a[238:239], v203 offset:12288\n"
      "v_mfma_f32_16x16x32_bf16 v[12:15], a[196:199], a[144:147], v[68:71]\n"
      "v_exp_f32 v239, v63\n"
      "v_mfma_f32_16x16x32_bf16 v[8:11], a[200:203], a[148:151], v[8:11]\n"
      "ds_read_b64_tr_b16 a[240:241], v204 offset:8192\n"
      "v_cvt_pk_bf16_f32 v127, v238, v239\n"
      "v_mfma_f32_16x16x32_bf16 v[12:15], a[204:207], a[148:151], v[12:15]\n"
      "ds_read_b64_tr_b16 a[242:243], v204 offset:12288\n"
      "ds_read_b64_tr_b16 a[244:245], v205 offset:8192\n"
      "v_mfma_f32_16x16x32_bf16 v[8:11], a[208:211], a[152:155], v[8:11]\n"
      "v_exp_f32 v232, v0\n"
      "v_mfma_f32_16x16x32_bf16 v[224:227], v[228:231], v[124:127], v[92:95]\n"
      "ds_read_b64_tr_b16 a[246:247], v205 offset:12288\n"
      "ds_read_b64_tr_b16 a[248:249], v206 offset:8192\n"
      "v_mfma_f32_16x16x32_bf16 v[12:15], a[212:215], a[152:155], v[12:15]\n"
      "v_exp_f32 v233, v1\n"
      "v_mfma_f32_16x16x32_bf16 v[8:11], a[216:219], a[156:159], v[8:11]\n"
      "ds_read_b64_tr_b16 a[250:251], v206 offset:12288\n"
      "v_cvt_pk_bf16_f32 v96, v232, v233\n"
      "v_mfma_f32_16x16x32_bf16 v[12:15], a[220:223], a[156:159], v[12:15]\n"
      "v_exp_f32 v234, v2\n"
      "v_mfma_f32_16x16x32_bf16 v[16:19], a[192:195], a[160:163], v[72:75]\n"
      "ds_read_b64_tr_b16 a[252:253], v207 offset:8192\n"
      "ds_read_b64_tr_b16 a[254:255], v207 offset:12288\n"
      "v_mfma_f32_16x16x32_bf16 v[20:23], a[196:199], a[160:163], v[72:75]\n"
      "v_exp_f32 v235, v3\n"
      "v_mfma_f32_16x16x32_bf16 v[16:19], a[200:203], a[164:167], v[16:19]\n"
      "s_mov_b32 s87, 8192\n"
      "buffer_store_dwordx4 v[132:135], v254, s[56:59], s87 offen offset:0 sc1\n"
      "v_cvt_pk_bf16_f32 v97, v234, v235\n"
      "v_mfma_f32_16x16x32_bf16 v[20:23], a[204:207], a[164:167], v[20:23]\n"
      "v_exp_f32 v236, v4\n"
      "v_mfma_f32_16x16x32_bf16 v[16:19], a[208:211], a[168:171], v[16:19]\n"
      "v_max3_f32 v240, v212, v216, v220\n"
      "v_max_f32 v240, v240, v224\n"
      "v_mfma_f32_16x16x32_bf16 v[20:23], a[212:215], a[168:171], v[20:23]\n"
      "v_exp_f32 v237, v5\n"
      "v_mfma_f32_16x16x32_bf16 v[16:19], a[216:219], a[172:175], v[16:19]\n"
      "v_cvt_pk_bf16_f32 v98, v236, v237\n"
      "v_mfma_f32_16x16x32_bf16 v[20:23], a[220:223], a[172:175], v[20:23]\n"
      "v_exp_f32 v238, v6\n"
      "v_mfma_f32_16x16x32_bf16 v[24:27], a[192:195], a[176:179], v[76:79]\n"
      "v_cmp_nge_f32 vcc, s74, v240\n"
      "v_mfma_f32_16x16x32_bf16 v[28:31], a[196:199], a[176:179], v[76:79]\n"
      "v_exp_f32 v239, v7\n"
      "v_mfma_f32_16x16x32_bf16 v[24:27], a[200:203], a[180:183], v[24:27]\n"
      "v_cvt_pk_bf16_f32 v99, v238, v239\n"
      "v_mfma_f32_16x16x32_bf16 v[28:31], a[204:207], a[180:183], v[28:31]\n"
      "s_mov_b32 s87, 16384\n"
      "buffer_store_dwordx4 v[136:139], v254, s[56:59], s87 offen offset:0 sc1\n"
      "v_mfma_f32_16x16x32_bf16 v[24:27], a[208:211], a[184:187], v[24:27]\n"
      "v_exp_f32 v232, v8\n"
      "v_mfma_f32_16x16x32_bf16 v[28:31], a[212:215], a[184:187], v[28:31]\n"
      "s_waitcnt lgkmcnt(0)\n"
      "v_mfma_f32_16x16x32_bf16 v[24:27], a[216:219], a[188:191], v[24:27]\n"
      "v_exp_f32 v233, v9\n"
      "v_mfma_f32_16x16x32_bf16 v[28:31], a[220:223], a[188:191], v[28:31]\n"
      "v_cvt_pk_bf16_f32 v100, v232, v233\n"
      "s_cbranch_vccnz STUB19_%=\n"
      "BACK19_%=:\n"
      "v_mfma_f32_16x16x32_bf16 a[0:3], a[224:227], v[112:115], a[0:3]\n"
      "v_exp_f32 v234, v10\n"
      "v_mfma_f32_16x16x32_bf16 a[4:7], a[228:231], v[112:115], a[4:7]\n"
      "ds_read_b128 a[192:195], v192 offset:24576\n"
      "v_mfma_f32_16x16x32_bf16 v[80:83], v[228:231], v[96:99], v[212:215]\n"
      "v_exp_f32 v235, v11\n"
      "v_mfma_f32_16x16x32_bf16 a[8:11], a[232:235], v[112:115], a[8:11]\n"
      "ds_read_b128 a[196:199], v192 offset:28672\n"
      "v_cvt_pk_bf16_f32 v101, v234, v235\n"
      "v_mfma_f32_16x16x32_bf16 a[12:15], a[236:239], v[112:115], a[12:15]\n"
      "v_exp_f32 v236, v12\n"
      "v_mfma_f32_16x16x32_bf16 a[16:19], a[240:243], v[112:115], a[16:19]\n"
      "ds_read_b128 a[200:203], v193 offset:24576\n"
      "v_mfma_f32_16x16x32_bf16 a[20:23], a[244:247], v[112:115], a[20:23]\n"
      "v_exp_f32 v237, v13\n"
      "v_mfma_f32_16x16x32_bf16 a[24:27], a[248:251], v[112:115], a[24:27]\n"
      "ds_read_b128 a[204:207], v193 offset:28672\n"
      "v_cvt_pk_bf16_f32 v102, v236, v237\n"
      "v_mfma_f32_16x16x32_bf16 a[28:31], a[252:255], v[112:115], a[28:31]\n"
      "v_exp_f32 v238, v14\n"
      "v_mfma_f32_16x16x32_bf16 a[32:35], a[224:227], v[116:119], a[32:35]\n"
      "ds_read_b128 a[208:211], v194 offset:24576\n"
      "v_mfma_f32_16x16x32_bf16 a[36:39], a[228:231], v[116:119], a[36:39]\n"
      "v_exp_f32 v239, v15\n"
      "v_mfma_f32_16x16x32_bf16 a[40:43], a[232:235], v[116:119], a[40:43]\n"
      "ds_read_b128 a[212:215], v194 offset:28672\n"
      "v_cvt_pk_bf16_f32 v103, v238, v239\n"
      "v_mfma_f32_16x16x32_bf16 a[44:47], a[236:239], v[116:119], a[44:47]\n"
      "ds_read_b128 a[216:219], v195 offset:24576\n"
      "v_mfma_f32_16x16x32_bf16 a[48:51], a[240:243], v[116:119], a[48:51]\n"
      "v_exp_f32 v232, v16\n"
      "v_mfma_f32_16x16x32_bf16 v[84:87], v[228:231], v[100:103], v[216:219]\n"
      "ds_read_b128 a[220:223], v195 offset:28672\n"
      "v_mfma_f32_16x16x32_bf16 a[52:55], a[244:247], v[116:119], a[52:55]\n"
      "v_exp_f32 v233, v17\n"
      "v_mfma_f32_16x16x32_bf16 a[56:59], a[248:251], v[116:119], a[56:59]\n"
      "s_memrealtime s[82:83]\n"
      "v_cvt_pk_bf16_f32 v104, v232, v233\n"
      "v_mfma_f32_16x16x32_bf16 a[60:63], a[252:255], v[116:119], a[60:63]\n"
      "s_add_u32 m0, s78, 0\n"
      "v_exp_f32 v234, v18\n"
      "v_mfma_f32_16x16x32_bf16 a[64:67], a[224:227], v[120:123], a[64:67]\n"
      "buffer_load_dwordx4 v196, s[40:43], s68 offen offset:0 nt lds\n"
      "v_mfma_f32_16x16x32_bf16 a[68:71], a[228:231], v[120:123], a[68:71]\n"
      "v_exp_f32 v235, v19\n"
      "v_mfma_f32_16x16x32_bf16 a[72:75], a[232:235], v[120:123], a[72:75]\n"
      "s_mov_b32 s87, 24576\n"
      "buffer_store_dwordx4 v[140:143], v254, s[56:59], s87 offen offset:0 sc1\n"
      "v_cvt_pk_bf16_f32 v105, v234, v235\n"
      "v_mfma_f32_16x16x32_bf16 a[76:79], a[236:239], v[120:123], a[76:79]\n"
      "v_exp_f32 v236, v20\n"
      "v_mfma_f32_16x16x32_bf16 a[80:83], a[240:243], v[120:123], a[80:83]\n"
      "buffer_load_dwordx4 v197, s[40:43], s68 offen offset:1024 nt lds\n"
      "v_mfma_f32_16x16x32_bf16 a[84:87], a[244:247], v[120:123], a[84:87]\n"
      "v_exp_f32 v237, v21\n"
      "v_mfma_f32_16x16x32_bf16 a[88:91], a[248:251], v[120:123], a[88:91]\n"
      "v_cvt_pk_bf16_f32 v106, v236, v237\n"
      "v_mfma_f32_16x16x32_bf16 a[92:95], a[252:255], v[120:123], a[92:95]\n"
      "v_exp_f32 v238, v22\n"
      "v_mfma_f32_16x16x32_bf16 a[96:99], a[224:227], v[124:127], a[96:99]\n"
      "buffer_load_dwordx4 v198, s[40:43], s68 offen offset:2048 nt lds\n"
      "v_mfma_f32_16x16x32_bf16 a[100:103], a[228:231], v[124:127], a[100:103]\n"
      "v_exp_f32 v239, v23\n"
      "v_mfma_f32_16x16x32_bf16 a[104:107], a[232:235], v[124:127], a[104:107]\n"
      "v_cvt_pk_bf16_f32 v107, v238, v239\n"
      "v_mfma_f32_16x16x32_bf16 a[108:111], a[236:239], v[124:127], a[108:111]\n"
      "buffer_load_dwordx4 v199, s[40:43], s68 offen offset:3072 nt lds\n"
      "v_mfma_f32_16x16x32_bf16 a[112:115], a[240:243], v[124:127], a[112:115]\n"
      "s_add_u32 s68, s68, 16384\n"
      "v_exp_f32 v232, v24\n"
      "v_mfma_f32_16x16x32_bf16 v[88:91], v[228:231], v[104:107], v[220:223]\n"
      "v_mfma_f32_16x16x32_bf16 a[116:119], a[244:247], v[124:127], a[116:119]\n"
      "v_exp_f32 v233, v25\n"
      "v_mfma_f32_16x16x32_bf16 a[120:123], a[248:251], v[124:127], a[120:123]\n"
      "s_waitcnt lgkmcnt(0)\n"
      "v_cvt_pk_bf16_f32 v108, v232, v233\n"
      "v_mfma_f32_16x16x32_bf16 a[124:127], a[252:255], v[124:127], a[124:127]\n"
      "v_exp_f32 v234, v26\n"
      "s_cmp_lt_u32 s82, s95\n"
      "s_cbranch_scc1 STUB20_%=\n"
      "BACK20_%=:\n"
      "v_mfma_f32_16x16x32_bf16 v[32:35], a[192:195], a[128:131], v[64:67]\n"
      "ds_read_b64_tr_b16 a[224:225], v200 offset:16384\n"
      "ds_read_b64_tr_b16 a[226:227], v200 offset:20480\n"
      "v_mfma_f32_16x16x32_bf16 v[36:39], a[196:199], a[128:131], v[64:67]\n"
      "s_add_u32 s95, s82, 80\n"
      "v_exp_f32 v235, v27\n"
      "v_mfma_f32_16x16x32_bf16 v[32:35], a[200:203], a[132:135], v[32:35]\n"
      "ds_read_b64_tr_b16 a[228:229], v201 offset:16384\n"
      "v_cvt_pk_bf16_f32 v109, v234, v235\n"
      "v_mfma_f32_16x16x32_bf16 v[36:39], a[204:207], a[132:135], v[36:39]\n"
      "v_exp_f32 v236, v28\n"
      "v_mfma_f32_16x16x32_bf16 v[32:35], a[208:211], a[136:139], v[32:35]\n"
      "ds_read_b64_tr_b16 a[230:231], v201 offset:20480\n"
      "ds_read_b64_tr_b16 a[232:233], v202 offset:16384\n"
      "v_mfma_f32_16x16x32_bf16 v[36:39], a[212:215], a[136:139], v[36:39]\n"
      "v_exp_f32 v237, v29\n"
      "v_mfma_f32_16x16x32_bf16 v[32:35], a[216:219], a[140:143], v[32:35]\n"
      "ds_read_b64_tr_b16 a[234:235], v202 offset:20480\n"
      "v_cvt_pk_bf16_f32 v110, v236, v237\n"
      "v_mfma_f32_16x16x32_bf16 v[36:39], a[220:223], a[140:143], v[36:39]\n"
      "v_exp_f32 v238, v30\n"
      "v_mfma_f32_16x16x32_bf16 v[40:43], a[192:195], a[144:147], v[68:71]\n"
      "ds_read_b64_tr_b16 a[236:237], v203 offset:16384\n"
      "ds_read_b64_tr_b16 a[238:239], v203 offset:20480\n"
      "v_mfma_f32_16x16x32_bf16 v[44:47], a[196:199], a[144:147], v[68:71]\n"
      "v_exp_f32 v239, v31\n"
      "v_mfma_f32_16x16x32_bf16 v[40:43], a[200:203], a[148:151], v[40:43]\n"
      "ds_read_b64_tr_b16 a[240:241], v204 offset:16384\n"
      "v_cvt_pk_bf16_f32 v111, v238, v239\n"
      "v_mfma_f32_16x16x32_bf16 v[44:47], a[204:207], a[148:151], v[44:47]\n"
      "ds_read_b64_tr_b16 a[242:243], v204 offset:20480\n"
      "ds_read_b64_tr_b16 a[244:245], v205 offset:16384\n"
      "v_mfma_f32_16x16x32_bf16 v[40:43], a[208:211], a[152:155], v[40:43]\n"
      "v_exp_f32 v232, v32\n"
      "v_mfma_f32_16x16x32_bf16 v[92:95], v[228:231], v[108:111], v[224:227]\n"
      "ds_read_b64_tr_b16 a[246:247], v205 offset:20480\n"
      "ds_read_b64_tr_b16 a[248:249], v206 offset:16384\n"
      "v_mfma_f32_16x16x32_bf16 v[44:47], a[212:215], a[152:155], v[44:47]\n"
      "v_exp_f32 v233, v33\n"
      "v_mfma_f32_16x16x32_bf16 v[40:43], a[216:219], a[156:159], v[40:43]\n"
      "ds_read_b64_tr_b16 a[250:251], v206 offset:20480\n"
      "v_cvt_pk_bf16_f32 v112, v232, v233\n"
      "v_mfma_f32_16x16x32_bf16 v[44:47], a[220:223], a[156:159], v[44:47]\n"
      "v_exp_f32 v234, v34\n"
      "v_mfma_f32_16x16x32_bf16 v[48:51], a[192:195], a[160:163], v[72:75]\n"
      "ds_read_b64_tr_b16 a[252:253], v207 offset:16384\n"
      "ds_read_b64_tr_b16 a[254:255], v207 offset:20480\n"
      "v_mfma_f32_16x16x32_bf16 v[52:55], a[196:199], a[160:163], v[72:75]\n"
      "v_exp_f32 v235, v35\n"
      "v_mfma_f32_16x16x32_bf16 v[48:51], a[200:203], a[164:167], v[48:51]\n"
      "s_mov_b32 s87, 0\n"
      "buffer_store_dwordx4 v[144:147], v254, s[56:59], s87 offen offset:1024 sc1\n"
      "v_cvt_pk_bf16_f32 v113, v234, v235\n"
      "v_mfma_f32_16x16x32_bf16 v[52:55], a[204:207], a[164:167], v[52:55]\n"
      "v_exp_f32 v236, v36\n"
      "v_mfma_f32_16x16x32_bf16 v[48:51], a[208:211], a[168:171], v[48:51]\n"
      "v_max3_f32 v240, v80, v84, v88\n"
      "v_max_f32 v240, v240, v92\n"
      "v_mfma_f32_16x16x32_bf16 v[52:55], a[212:215], a[168:171], v[52:55]\n"
      "v_exp_f32 v237, v37\n"
      "v_mfma_f32_16x16x32_bf16 v[48:51], a[216:219], a[172:175], v[48:51]\n"
      "v_cvt_pk_bf16_f32 v114, v236, v237\n"
      "v_mfma_f32_16x16x32_bf16 v[52:55], a[220:223], a[172:175], v[52:55]\n"
      "v_exp_f32 v238, v38\n"
      "v_mfma_f32_16x16x32_bf16 v[56:59], a[192:195], a[176:179], v[76:79]\n"
      "v_cmp_nge_f32 vcc, s74, v240\n"
      "v_mfma_f32_16x16x32_bf16 v[60:63], a[196:199], a[176:179], v[76:79]\n"
      "v_exp_f32 v239, v39\n"
      "v_mfma_f32_16x16x32_bf16 v[56:59], a[200:203], a[180:183], v[56:59]\n"
      "v_cvt_pk_bf16_f32 v115, v238, v239\n"
      "v_mfma_f32_16x16x32_bf16 v[60:63], a[204:207], a[180:183], v[60:63]\n"
      "v_mfma_f32_16x16x32_bf16 v[56:59], a[208:211], a[184:187], v[56:59]\n"
      "v_exp_f32 v232, v40\n"
      "v_mfma_f32_16x16x32_bf16 v[60:63], a[212:215], a[184:187], v[60:63]\n"
      "s_waitcnt vmcnt(18) lgkmcnt(0)\n"
      "s_barrier\n"
      "v_mfma_f32_16x16x32_bf16 v[56:59], a[216:219], a[188:191], v[56:59]\n"
      "v_exp_f32 v233, v41\n"
      "v_mfma_f32_16x16x32_bf16 v[60:63], a[220:223], a[188:191], v[60:63]\n"
      "v_cvt_pk_bf16_f32 v116, v232, v233\n"
      "s_cbranch_vccnz STUB21_%=\n"
      "BACK21_%=:\n"
      "v_mfma_f32_16x16x32_bf16 a[0:3], a[224:227], v[96:99], a[0:3]\n"
      "v_exp_f32 v234, v42\n"
      "v_mfma_f32_16x16x32_bf16 a[4:7], a[228:231], v[96:99], a[4:7]\n"
      "ds_read_b128 a[192:195], v192 offset:32768\n"
      "v_mfma_f32_16x16x32_bf16 v[212:215], v[228:231], v[112:115], v[80:83]\n"
      "v_exp_f32 v235, v43\n"
      "v_mfma_f32_16x16x32_bf16 a[8:11], a[232:235], v[96:99], a[8:11]\n"
      "ds_read_b128 a[196:199], v192 offset:36864\n"
      "v_cvt_pk_bf16_f32 v117, v234, v235\n"
      "v_mfma_f32_16x16x32_bf16 a[12:15], a[236:239], v[96:99], a[12:15]\n"
      "v_exp_f32 v236, v44\n"
      "v_mfma_f32_16x16x32_bf16 a[16:19], a[240:243], v[96:99], a[16:19]\n"
      "ds_read_b128 a[200:203], v193 offset:32768\n"
      "s_mov_b32 s87, 8192\n"
      "buffer_store_dwordx4 v[148:151], v254, s[56:59], s87 offen offset:1024 sc1\n"
      "v_mfma_f32_16x16x32_bf16 a[20:23], a[244:247], v[96:99], a[20:23]\n"
      "v_exp_f32 v237, v45\n"
      "v_mfma_f32_16x16x32_bf16 a[24:27], a[248:251], v[96:99], a[24:27]\n"
      "ds_read_b128 a[204:207], v193 offset:36864\n"
      "v_cvt_pk_bf16_f32 v118, v236, v237\n"
      "v_mfma_f32_16x16x32_bf16 a[28:31], a[252:255], v[96:99], a[28:31]\n"
      "v_exp_f32 v238, v46\n"
      "v_mfma_f32_16x16x32_bf16 a[32:35], a[224:227], v[100:103], a[32:35]\n"
      "ds_read_b128 a[208:211], v194 offset:32768\n"
      "v_mfma_f32_16x16x32_bf16 a[36:39], a[228:231], v[100:103], a[36:39]\n"
      "v_exp_f32 v239, v47\n"
      "v_mfma_f32_16x16x32_bf16 a[40:43], a[232:235], v[100:103], a[40:43]\n"
      "ds_read_b128 a[212:215], v194 offset:36864\n"
      "v_cvt_pk_bf16_f32 v119, v238, v239\n"
      "v_mfma_f32_16x16x32_bf16 a[44:47], a[236:239], v[100:103], a[44:47]\n"
      "ds_read_b128 a[216:219], v195 offset:32768\n"
      "v_mfma_f32_16x16x32_bf16 a[48:51], a[240:243], v[100:103], a[48:51]\n"
      "v_exp_f32 v232, v48\n"
      "v_mfma_f32_16x16x32_bf16 v[216:219], v[228:231], v[116:119], v[84:87]\n"
      "ds_read_b128 a[220:223], v195 offset:36864\n"
      "v_mfma_f32_16x16x32_bf16 a[52:55], a[244:247], v[100:103], a[52:55]\n"
      "v_exp_f32 v233, v49\n"
      "v_mfma_f32_16x16x32_bf16 a[56:59], a[248:251], v[100:103], a[56:59]\n"
      "s_memrealtime s[82:83]\n"
      "v_cvt_pk_bf16_f32 v120, v232, v233\n"
      "v_mfma_f32_16x16x32_bf16 a[60:63], a[252:255], v[100:103], a[60:63]\n"
      "s_add_u32 m0, s78, 65536\n"
      "v_exp_f32 v234, v50\n"
      "v_mfma_f32_16x16x32_bf16 a[64:67], a[224:227], v[104:107], a[64:67]\n"
      "buffer_load_dwordx4 v208, s[44:47], s69 offen offset:0 nt lds\n"
      "v_mfma_f32_16x16x32_bf16 a[68:71], a[228:231], v[104:107], a[68:71]\n"
      "v_exp_f32 v235, v51\n"
      "v_mfma_f32_16x16x32_bf16 a[72:75], a[232:235], v[104:107], a[72:75]\n"
      "v_cvt_pk_bf16_f32 v121, v234, v235\n"
      "v_mfma_f32_16x16x32_bf16 a[76:79], a[236:239], v[104:107], a[76:79]\n"
      "v_exp_f32 v236, v52\n"
      "v_mfma_f32_16x16x32_bf16 a[80:83], a[240:243], v[104:107], a[80:83]\n"
      "buffer_load_dwordx4 v209, s[44:47], s69 offen offset:1024 nt lds\n"
      "v_mfma_f32_16x16x32_bf16 a[84:87], a[244:247], v[104:107], a[84:87]\n"
      "v_exp_f32 v237, v53\n"
      "v_mfma_f32_16x16x32_bf16 a[88:91], a[248:251], v[104:107], a[88:91]\n"
      "v_cvt_pk_bf16_f32 v122, v236, v237\n"
      "v_mfma_f32_16x16x32_bf16 a[92:95], a[252:255], v[104:107], a[92:95]\n"
      "v_exp_f32 v238, v54\n"
      "v_mfma_f32_16x16x32_bf16 a[96:99], a[224:227], v[108:111], a[96:99]\n"
      "buffer_load_dwordx4 v208, s[44:47], s69 offen offset:2048 nt lds\n"
      "v_mfma_f32_16x16x32_bf16 a[100:103], a[228:231], v[108:111], a[100:103]\n"
      "v_exp_f32 v239, v55\n"
      "v_mfma_f32_16x16x32_bf16 a[104:107], a[232:235], v[108:111], a[104:107]\n"
      "v_cvt_pk_bf16_f32 v123, v238, v239\n"
      "v_mfma_f32_16x16x32_bf16 a[108:111], a[236:239], v[108:111], a[108:111]\n"
      "buffer_load_dwordx4 v209, s[44:47], s69 offen offset:3072 nt lds\n"
      "v_mfma_f32_16x16x32_bf16 a[112:115], a[240:243], v[108:111], a[112:115]\n"
      "s_add_u32 s69, s69, 16384\n"
      "v_exp_f32 v232, v56\n"
      "v_mfma_f32_16x16x32_bf16 v[220:223], v[228:231], v[120:123], v[88:91]\n"
      "s_mov_b32 s87, 16384\n"
      "buffer_store_dwordx4 v[152:155], v254, s[56:59], s87 offen offset:1024 sc1\n"
      "v_mfma_f32_16x16x32_bf16 a[116:119], a[244:247], v[108:111], a[116:119]\n"
      "v_exp_f32 v233, v57\n"
      "v_mfma_f32_16x16x32_bf16 a[120:123], a[248:251], v[108:111], a[120:123]\n"
      "s_waitcnt lgkmcnt(0)\n"
      "v_cvt_pk_bf16_f32 v124, v232, v233\n"
      "v_mfma_f32_16x16x32_bf16 a[124:127], a[252:255], v[108:111], a[124:127]\n"
      "v_exp_f32 v234, v58\n"
      "s_cmp_lt_u32 s82, s95\n"
      "s_cbranch_scc1 STUB22_%=\n"
      "BACK22_%=:\n"
      "v_mfma_f32_16x16x32_bf16 v[0:3], a[192:195], a[128:131], v[64:67]\n"
      "ds_read_b64_tr_b16 a[224:225], v200 offset:24576\n"
      "ds_read_b64_tr_b16 a[226:227], v200 offset:28672\n"
      "v_mfma_f32_16x16x32_bf16 v[4:7], a[196:199], a[128:131], v[64:67]\n"
      "s_add_u32 s95, s82, 80\n"
      "v_exp_f32 v235, v59\n"
      "v_mfma_f32_16x16x32_bf16 v[0:3], a[200:203], a[132:135], v[0:3]\n"
      "ds_read_b64_tr_b16 a[228:229], v201 offset:24576\n"
      "v_cvt_pk_bf16_f32 v125, v234, v235\n"
      "v_mfma_f32_16x16x32_bf16 v[4:7], a[204:207], a[132:135], v[4:7]\n"
      "v_exp_f32 v236, v60\n"
      "v_mfma_f32_16x16x32_bf16 v[0:3], a[208:211], a[136:139], v[0:3]\n"
      "ds_read_b64_tr_b16 a[230:231], v201 offset:28672\n"
      "ds_read_b64_tr_b16 a[232:233], v202 offset:24576\n"
      "v_mfma_f32_16x16x32_bf16 v[4:7], a[212:215], a[136:139], v[4:7]\n"
      "v_exp_f32 v237, v61\n"
      "v_mfma_f32_16x16x32_bf16 v[0:3], a[216:219], a[140:143], v[0:3]\n"
      "ds_read_b64_tr_b16 a[234:235], v202 offset:28672\n"
      "v_cvt_pk_bf16_f32 v126, v236, v237\n"
      "v_mfma_f32_16x16x32_bf16 v[4:7], a[220:223], a[140:143], v[4:7]\n"
      "v_exp_f32 v238, v62\n"
      "v_mfma_f32_16x16x32_bf16 v[8:11], a[192:195], a[144:147], v[68:71]\n"
      "ds_read_b64_tr_b16 a[236:237], v203 offset:24576\n"
      "ds_read_b64_tr_b16 a[238:239], v203 offset:28672\n"
      "v_mfma_f32_16x16x32_bf16 v[12:15], a[196:199], a[144:147], v[68:71]\n"
      "v_exp_f32 v239, v63\n"
      "v_mfma_f32_16x16x32_bf16 v[8:11], a[200:203], a[148:151], v[8:11]\n"
      "ds_read_b64_tr_b16 a[240:241], v204 offset:24576\n"
      "v_cvt_pk_bf16_f32 v127, v238, v239\n"
      "v_mfma_f32_16x16x32_bf16 v[12:15], a[204:207], a[148:151], v[12:15]\n"
      "ds_read_b64_tr_b16 a[242:243], v204 offset:28672\n"
      "ds_read_b64_tr_b16 a[244:245], v205 offset:24576\n"
      "v_mfma_f32_16x16x32_bf16 v[8:11], a[208:211], a[152:155], v[8:11]\n"
      "v_exp_f32 v232, v0\n"
      "v_mfma_f32_16x16x32_bf16 v[224:227], v[228:231], v[124:127], v[92:95]\n"
      "ds_read_b64_tr_b16 a[246:247], v205 offset:28672\n"
      "ds_read_b64_tr_b16 a[248:249], v206 offset:24576\n"
      "v_mfma_f32_16x16x32_bf16 v[12:15], a[212:215], a[152:155], v[12:15]\n"
      "v_exp_f32 v233, v1\n"
      "v_mfma_f32_16x16x32_bf16 v[8:11], a[216:219], a[156:159], v[8:11]\n"
      "ds_read_b64_tr_b16 a[250:251], v206 offset:28672\n"
      "v_cvt_pk_bf16_f32 v96, v232, v233\n"
      "v_mfma_f32_16x16x32_bf16 v[12:15], a[220:223], a[156:159], v[12:15]\n"
      "v_exp_f32 v234, v2\n"
      "v_mfma_f32_16x16x32_bf16 v[16:19], a[192:195], a[160:163], v[72:75]\n"
      "ds_read_b64_tr_b16 a[252:253], v207 offset:24576\n"
      "ds_read_b64_tr_b16 a[254:255], v207 offset:28672\n"
      "v_mfma_f32_16x16x32_bf16 v[20:23], a[196:199], a[160:163], v[72:75]\n"
      "v_exp_f32 v235, v3\n"
      "v_mfma_f32_16x16x32_bf16 v[16:19], a[200:203], a[164:167], v[16:19]\n"
      "s_mov_b32 s87, 24576\n"
      "buffer_store_dwordx4 v[156:159], v254, s[56:59], s87 offen offset:1024 sc1\n"
      "v_cvt_pk_bf16_f32 v97, v234, v235\n"
      "v_mfma_f32_16x16x32_bf16 v[20:23], a[204:207], a[164:167], v[20:23]\n"
      "v_exp_f32 v236, v4\n"
      "v_mfma_f32_16x16x32_bf16 v[16:19], a[208:211], a[168:171], v[16:19]\n"
      "v_max3_f32 v240, v212, v216, v220\n"
      "v_max_f32 v240, v240, v224\n"
      "v_mfma_f32_16x16x32_bf16 v[20:23], a[212:215], a[168:171], v[20:23]\n"
      "v_exp_f32 v237, v5\n"
      "v_mfma_f32_16x16x32_bf16 v[16:19], a[216:219], a[172:175], v[16:19]\n"
      "v_cvt_pk_bf16_f32 v98, v236, v237\n"
      "v_mfma_f32_16x16x32_bf16 v[20:23], a[220:223], a[172:175], v[20:23]\n"
      "v_exp_f32 v238, v6\n"
      "v_mfma_f32_16x16x32_bf16 v[24:27], a[192:195], a[176:179], v[76:79]\n"
      "v_cmp_nge_f32 vcc, s74, v240\n"
      "v_mfma_f32_16x16x32_bf16 v[28:31], a[196:199], a[176:179], v[76:79]\n"
      "v_exp_f32 v239, v7\n"
      "v_mfma_f32_16x16x32_bf16 v[24:27], a[200:203], a[180:183], v[24:27]\n"
      "v_cvt_pk_bf16_f32 v99, v238, v239\n"
      "v_mfma_f32_16x16x32_bf16 v[28:31], a[204:207], a[180:183], v[28:31]\n"
      "v_mfma_f32_16x16x32_bf16 v[24:27], a[208:211], a[184:187], v[24:27]\n"
      "v_exp_f32 v232, v8\n"
      "v_mfma_f32_16x16x32_bf16 v[28:31], a[212:215], a[184:187], v[28:31]\n"
      "s_waitcnt lgkmcnt(0)\n"
      "v_mfma_f32_16x16x32_bf16 v[24:27], a[216:219], a[188:191], v[24:27]\n"
      "v_exp_f32 v233, v9\n"
      "v_mfma_f32_16x16x32_bf16 v[28:31], a[220:223], a[188:191], v[28:31]\n"
      "v_cvt_pk_bf16_f32 v100, v232, v233\n"
      "s_cbranch_vccnz STUB23_%=\n"
      "BACK23_%=:\n"
      "v_mfma_f32_16x16x32_bf16 a[0:3], a[224:227], v[112:115], a[0:3]\n"
      "v_exp_f32 v234, v10\n"
      "v_mfma_f32_16x16x32_bf16 a[4:7], a[228:231], v[112:115], a[4:7]\n"
      "ds_read_b128 a[192:195], v192 offset:40960\n"
      "v_mfma_f32_16x16x32_bf16 v[80:83], v[228:231], v[96:99], v[212:215]\n"
      "v_exp_f32 v235, v11\n"
      "v_mfma_f32_16x16x32_bf16 a[8:11], a[232:235], v[112:115], a[8:11]\n"
      "ds_read_b128 a[196:199], v192 offset:45056\n"
      "v_cvt_pk_bf16_f32 v101, v234, v235\n"
      "v_mfma_f32_16x16x32_bf16 a[12:15], a[236:239], v[112:115], a[12:15]\n"
      "v_exp_f32 v236, v12\n"
      "v_mfma_f32_16x16x32_bf16 a[16:19], a[240:243], v[112:115], a[16:19]\n"
      "ds_read_b128 a[200:203], v193 offset:40960\n"
      "v_mfma_f32_16x16x32_bf16 a[20:23], a[244:247], v[112:115], a[20:23]\n"
      "v_exp_f32 v237, v13\n"
      "v_mfma_f32_16x16x32_bf16 a[24:27], a[248:251], v[112:115], a[24:27]\n"
      "ds_read_b128 a[204:207], v193 offset:45056\n"
      "v_cvt_pk_bf16_f32 v102, v236, v237\n"
      "v_mfma_f32_16x16x32_bf16 a[28:31], a[252:255], v[112:115], a[28:31]\n"
      "v_exp_f32 v238, v14\n"
      "v_mfma_f32_16x16x32_bf16 a[32:35], a[224:227], v[116:119], a[32:35]\n"
      "ds_read_b128 a[208:211], v194 offset:40960\n"
      "v_mfma_f32_16x16x32_bf16 a[36:39], a[228:231], v[116:119], a[36:39]\n"
      "v_exp_f32 v239, v15\n"
      "v_mfma_f32_16x16x32_bf16 a[40:43], a[232:235], v[116:119], a[40:43]\n"
      "ds_read_b128 a[212:215], v194 offset:45056\n"
      "v_cvt_pk_bf16_f32 v103, v238, v239\n"
      "v_mfma_f32_16x16x32_bf16 a[44:47], a[236:239], v[116:119], a[44:47]\n"
      "ds_read_b128 a[216:219], v195 offset:40960\n"
      "v_mfma_f32_16x16x32_bf16 a[48:51], a[240:243], v[116:119], a[48:51]\n"
      "v_exp_f32 v232, v16\n"
      "v_mfma_f32_16x16x32_bf16 v[84:87], v[228:231], v[100:103], v[216:219]\n"
      "ds_read_b128 a[220:223], v195 offset:45056\n"
      "s_mov_b32 s87, 0\n"
      "buffer_store_dwordx4 v[160:163], v254, s[56:59], s87 offen offset:2048 sc1\n"
      "v_mfma_f32_16x16x32_bf16 a[52:55], a[244:247], v[116:119], a[52:55]\n"
      "v_exp_f32 v233, v17\n"
      "v_mfma_f32_16x16x32_bf16 a[56:59], a[248:251], v[116:119], a[56:59]\n"
      "s_memrealtime s[82:83]\n"
      "v_cvt_pk_bf16_f32 v104, v232, v233\n"
      "v_mfma_f32_16x16x32_bf16 a[60:63], a[252:255], v[116:119], a[60:63]\n"
      "s_add_u32 m0, s78, 16384\n"
      "v_exp_f32 v234, v18\n"
      "v_mfma_f32_16x16x32_bf16 a[64:67], a[224:227], v[120:123], a[64:67]\n"
      "buffer_load_dwordx4 v196, s[40:43], s68 offen offset:0 nt lds\n"
      "v_mfma_f32_16x16x32_bf16 a[68:71], a[228:231], v[120:123], a[68:71]\n"
      "v_exp_f32 v235, v19\n"
      "v_mfma_f32_16x16x32_bf16 a[72:75], a[232:235], v[120:123], a[72:75]\n"
      "v_cvt_pk_bf16_f32 v105, v234, v235\n"
      "v_mfma_f32_16x16x32_bf16 a[76:79], a[236:239], v[120:123], a[76:79]\n"
      "v_exp_f32 v236, v20\n"
      "v_mfma_f32_16x16x32_bf16 a[80:83], a[240:243], v[120:123], a[80:83]\n"
      "buffer_load_dwordx4 v197, s[40:43], s68 offen offset:1024 nt lds\n"
      "v_mfma_f32_16x16x32_bf16 a[84:87], a[244:247], v[120:123], a[84:87]\n"
      "v_exp_f32 v237, v21\n"
      "v_mfma_f32_16x16x32_bf16 a[88:91], a[248:251], v[120:123], a[88:91]\n"
      "v_cvt_pk_bf16_f32 v106, v236, v237\n"
      "v_mfma_f32_16x16x32_bf16 a[92:95], a[252:255], v[120:123], a[92:95]\n"
      "v_exp_f32 v238, v22\n"
      "v_mfma_f32_16x16x32_bf16 a[96:99], a[224:227], v[124:127], a[96:99]\n"
      "buffer_load_dwordx4 v198, s[40:43], s68 offen offset:2048 nt lds\n"
      "v_mfma_f32_16x16x32_bf16 a[100:103], a[228:231], v[124:127], a[100:103]\n"
      "v_exp_f32 v239, v23\n"
      "v_mfma_f32_16x16x32_bf16 a[104:107], a[232:235], v[124:127], a[104:107]\n"
      "v_cvt_pk_bf16_f32 v107, v238, v239\n"
      "v_mfma_f32_16x16x32_bf16 a[108:111], a[236:239], v[124:127], a[108:111]\n"
      "buffer_load_dwordx4 v199, s[40:43], s68 offen offset:3072 nt lds\n"
      "v_mfma_f32_16x16x32_bf16 a[112:115], a[240:243], v[124:127], a[112:115]\n"
      "s_add_u32 s68, s68, 16384\n"
      "v_exp_f32 v232, v24\n"
      "v_mfma_f32_16x16x32_bf16 v[88:91], v[228:231], v[104:107], v[220:223]\n"
      "v_mfma_f32_16x16x32_bf16 a[116:119], a[244:247], v[124:127], a[116:119]\n"
      "v_exp_f32 v233, v25\n"
      "v_mfma_f32_16x16x32_bf16 a[120:123], a[248:251], v[124:127], a[120:123]\n"
      "s_waitcnt lgkmcnt(0)\n"
      "v_cvt_pk_bf16_f32 v108, v232, v233\n"
      "v_mfma_f32_16x16x32_bf16 a[124:127], a[252:255], v[124:127], a[124:127]\n"
      "v_exp_f32 v234, v26\n"
      "s_cmp_lt_u32 s82, s95\n"
      "s_cbranch_scc1 STUB24_%=\n"
      "BACK24_%=:\n"
      "v_mfma_f32_16x16x32_bf16 v[32:35], a[192:195], a[128:131], v[64:67]\n"
      "ds_read_b64_tr_b16 a[224:225], v200 offset:32768\n"
      "ds_read_b64_tr_b16 a[226:227], v200 offset:36864\n"
      "v_mfma_f32_16x16x32_bf16 v[36:39], a[196:199], a[128:131], v[64:67]\n"
      "s_add_u32 s95, s82, 80\n"
      "v_exp_f32 v235, v27\n"
      "v_mfma_f32_16x16x32_bf16 v[32:35], a[200:203], a[132:135], v[32:35]\n"
      "ds_read_b64_tr_b16 a[228:229], v201 offset:32768\n"
      "v_cvt_pk_bf16_f32 v109, v234, v235\n"
      "v_mfma_f32_16x16x32_bf16 v[36:39], a[204:207], a[132:135], v[36:39]\n"
      "v_exp_f32 v236, v28\n"
      "v_mfma_f32_16x16x32_bf16 v[32:35], a[208:211], a[136:139], v[32:35]\n"
      "ds_read_b64_tr_b16 a[230:231], v201 offset:36864\n"
      "ds_read_b64_tr_b16 a[232:233], v202 offset:32768\n"
      "v_mfma_f32_16x16x32_bf16 v[36:39], a[212:215], a[136:139], v[36:39]\n"
      "v_exp_f32 v237, v29\n"
      "v_mfma_f32_16x16x32_bf16 v[32:35], a[216:219], a[140:143], v[32:35]\n"
      "ds_read_b64_tr_b16 a[234:235], v202 offset:36864\n"
      "v_cvt_pk_bf16_f32 v110, v236, v237\n"
      "v_mfma_f32_16x16x32_bf16 v[36:39], a[220:223], a[140:143], v[36:39]\n"
      "v_exp_f32 v238, v30\n"
      "v_mfma_f32_16x16x32_bf16 v[40:43], a[192:195], a[144:147], v[68:71]\n"
      "ds_read_b64_tr_b16 a[236:237], v203 offset:32768\n"
      "ds_read_b64_tr_b16 a[238:239], v203 offset:36864\n"
      "v_mfma_f32_16x16x32_bf16 v[44:47], a[196:199], a[144:147], v[68:71]\n"
      "v_exp_f32 v239, v31\n"
      "v_mfma_f32_16x16x32_bf16 v[40:43], a[200:203], a[148:151], v[40:43]\n"
      "ds_read_b64_tr_b16 a[240:241], v204 offset:32768\n"
      "v_cvt_pk_bf16_f32 v111, v238, v239\n"
      "v_mfma_f32_16x16x32_bf16 v[44:47], a[204:207], a[148:151], v[44:47]\n"
      "ds_read_b64_tr_b16 a[242:243], v204 offset:36864\n"
      "ds_read_b64_tr_b16 a[244:245], v205 offset:32768\n"
      "v_mfma_f32_16x16x32_bf16 v[40:43], a[208:211], a[152:155], v[40:43]\n"
      "v_exp_f32 v232, v32\n"
      "v_mfma_f32_16x16x32_bf16 v[92:95], v[228:231], v[108:111], v[224:227]\n"
      "ds_read_b64_tr_b16 a[246:247], v205 offset:36864\n"
      "ds_read_b64_tr_b16 a[248:249], v206 offset:32768\n"
      "v_mfma_f32_16x16x32_bf16 v[44:47], a[212:215], a[152:155], v[44:47]\n"
      "v_exp_f32 v233, v33\n"
      "v_mfma_f32_16x16x32_bf16 v[40:43], a[216:219], a[156:159], v[40:43]\n"
      "ds_read_b64_tr_b16 a[250:251], v206 offset:36864\n"
      "v_cvt_pk_bf16_f32 v112, v232, v233\n"
      "v_mfma_f32_16x16x32_bf16 v[44:47], a[220:223], a[156:159], v[44:47]\n"
      "v_exp_f32 v234, v34\n"
      "v_mfma_f32_16x16x32_bf16 v[48:51], a[192:195], a[160:163], v[72:75]\n"
      "ds_read_b64_tr_b16 a[252:253], v207 offset:32768\n"
      "ds_read_b64_tr_b16 a[254:255], v207 offset:36864\n"
      "v_mfma_f32_16x16x32_bf16 v[52:55], a[196:199], a[160:163], v[72:75]\n"
      "v_exp_f32 v235, v35\n"
      "v_mfma_f32_16x16x32_bf16 v[48:51], a[200:203], a[164:167], v[48:51]\n"
      "s_mov_b32 s87, 8192\n"
      "buffer_store_dwordx4 v[164:167], v254, s[56:59], s87 offen offset:2048 sc1\n"
      "v_cvt_pk_bf16_f32 v113, v234, v235\n"
      "v_mfma_f32_16x16x32_bf16 v[52:55], a[204:207], a[164:167], v[52:55]\n"
      "v_exp_f32 v236, v36\n"
      "v_mfma_f32_16x16x32_bf16 v[48:51], a[208:211], a[168:171], v[48:51]\n"
      "v_max3_f32 v240, v80, v84, v88\n"
      "v_max_f32 v240, v240, v92\n"
      "v_mfma_f32_16x16x32_bf16 v[52:55], a[212:215], a[168:171], v[52:55]\n"
      "v_exp_f32 v237, v37\n"
      "v_mfma_f32_16x16x32_bf16 v[48:51], a[216:219], a[172:175], v[48:51]\n"
      "v_cvt_pk_bf16_f32 v114, v236, v237\n"
      "v_mfma_f32_16x16x32_bf16 v[52:55], a[220:223], a[172:175], v[52:55]\n"
      "v_exp_f32 v238, v38\n"
      "v_mfma_f32_16x16x32_bf16 v[56:59], a[192:195], a[176:179], v[76:79]\n"
      "v_cmp_nge_f32 vcc, s74, v240\n"
      "v_mfma_f32_16x16x32_bf16 v[60:63], a[196:199], a[176:179], v[76:79]\n"
      "v_exp_f32 v239, v39\n"
      "v_mfma_f32_16x16x32_bf16 v[56:59], a[200:203], a[180:183], v[56:59]\n"
      "v_cvt_pk_bf16_f32 v115, v238, v239\n"
      "v_mfma_f32_16x16x32_bf16 v[60:63], a[204:207], a[180:183], v[60:63]\n"
      "v_mfma_f32_16x16x32_bf16 v[56:59], a[208:211], a[184:187], v[56:59]\n"
      "v_exp_f32 v232, v40\n"
      "v_mfma_f32_16x16x32_bf16 v[60:63], a[212:215], a[184:187], v[60:63]\n"
      "s_waitcnt vmcnt(21) lgkmcnt(0)\n"
      "s_barrier\n"
      "v_mfma_f32_16x16x32_bf16 v[56:59], a[216:219], a[188:191], v[56:59]\n"
      "v_exp_f32 v233, v41\n"
      "v_mfma_f32_16x16x32_bf16 v[60:63], a[220:223], a[188:191], v[60:63]\n"
      "v_cvt_pk_bf16_f32 v116, v232, v233\n"
      "s_cbranch_vccnz STUB25_%=\n"
      "BACK25_%=:\n"
      "v_mfma_f32_16x16x32_bf16 a[0:3], a[224:227], v[96:99], a[0:3]\n"
      "v_exp_f32 v234, v42\n"
      "v_mfma_f32_16x16x32_bf16 a[4:7], a[228:231], v[96:99], a[4:7]\n"
      "ds_read_b128 a[192:195], v192 offset:49152\n"
      "s_mov_b32 s87, 16384\n"
      "buffer_store_dwordx4 v[168:171], v254, s[56:59], s87 offen offset:2048 sc1\n"
      "v_mfma_f32_16x16x32_bf16 v[212:215], v[228:231], v[112:115], v[80:83]\n"
      "v_exp_f32 v235, v43\n"
      "v_mfma_f32_16x16x32_bf16 a[8:11], a[232:235], v[96:99], a[8:11]\n"
      "ds_read_b128 a[196:199], v192 offset:53248\n"
      "v_cvt_pk_bf16_f32 v117, v234, v235\n"
      "v_mfma_f32_16x16x32_bf16 a[12:15], a[236:239], v[96:99], a[12:15]\n"
      "v_exp_f32 v236, v44\n"
      "v_mfma_f32_16x16x32_bf16 a[16:19], a[240:243], v[96:99], a[16:19]\n"
      "ds_read_b128 a[200:203], v193 offset:49152\n"
      "v_mfma_f32_16x16x32_bf16 a[20:23], a[244:247], v[96:99], a[20:23]\n"
      "v_exp_f32 v237, v45\n"
      "v_mfma_f32_16x16x32_bf16 a[24:27], a[248:251], v[96:99], a[24:27]\n"
      "ds_read_b128 a[204:207], v193 offset:53248\n"
      "v_cvt_pk_bf16_f32 v118, v236, v237\n"
      "v_mfma_f32_16x16x32_bf16 a[28:31], a[252:255], v[96:99], a[28:31]\n"
      "v_exp_f32 v238, v46\n"
      "v_mfma_f32_16x16x32_bf16 a[32:35], a[224:227], v[100:103], a[32:35]\n"
      "ds_read_b128 a[208:211], v194 offset:49152\n"
      "v_mfma_f32_16x16x32_bf16 a[36:39], a[228:231], v[100:103], a[36:39]\n"
      "v_exp_f32 v239, v47\n"
      "v_mfma_f32_16x16x32_bf16 a[40:43], a[232:235], v[100:103], a[40:43]\n"
      "ds_read_b128 a[212:215], v194 offset:53248\n"
      "v_cvt_pk_bf16_f32 v119, v238, v239\n"
      "v_mfma_f32_16x16x32_bf16 a[44:47], a[236:239], v[100:103], a[44:47]\n"
      "ds_read_b128 a[216:219], v195 offset:49152\n"
      "v_mfma_f32_16x16x32_bf16 a[48:51], a[240:243], v[100:103], a[48:51]\n"
      "v_exp_f32 v232, v48\n"
      "v_mfma_f32_16x16x32_bf16 v[216:219], v[228:231], v[116:119], v[84:87]\n"
      "ds_read_b128 a[220:223], v195 offset:53248\n"
      "v_mfma_f32_16x16x32_bf16 a[52:55], a[244:247], v[100:103], a[52:55]\n"
      "v_exp_f32 v233, v49\n"
      "v_mfma_f32_16x16x32_bf16 a[56:59], a[248:251], v[100:103], a[56:59]\n"
      "s_memrealtime s[82:83]\n"
      "v_cvt_pk_bf16_f32 v120, v232, v233\n"
      "v_mfma_f32_16x16x32_bf16 a[60:63], a[252:255], v[100:103], a[60:63]\n"
      "s_add_u32 m0, s78, 81920\n"
      "v_exp_f32 v234, v50\n"
      "v_mfma_f32_16x16x32_bf16 a[64:67], a[224:227], v[104:107], a[64:67]\n"
      "buffer_load_dwordx4 v208, s[44:47], s69 offen offset:0 nt lds\n"
      "v_mfma_f32_16x16x32_bf16 a[68:71], a[228:231], v[104:107], a[68:71]\n"
      "v_exp_f32 v235, v51\n"
      "v_mfma_f32_16x16x32_bf16 a[72:75], a[232:235], v[104:107], a[72:75]\n"
      "v_cvt_pk_bf16_f32 v121, v234, v235\n"
      "v_mfma_f32_16x16x32_bf16 a[76:79], a[236:239], v[104:107], a[76:79]\n"
      "v_exp_f32 v236, v52\n"
      "v_mfma_f32_16x16x32_bf16 a[80:83], a[240:243], v[104:107], a[80:83]\n"
      "buffer_load_dwordx4 v209, s[44:47], s69 offen offset:1024 nt lds\n"
      "v_mfma_f32_16x16x32_bf16 a[84:87], a[244:247], v[104:107], a[84:87]\n"
      "v_exp_f32 v237, v53\n"
      "v_mfma_f32_16x16x32_bf16 a[88:91], a[248:251], v[104:107], a[88:91]\n"
      "v_cvt_pk_bf16_f32 v122, v236, v237\n"
      "v_mfma_f32_16x16x32_bf16 a[92:95], a[252:255], v[104:107], a[92:95]\n"
      "v_exp_f32 v238, v54\n"
      "v_mfma_f32_16x16x32_bf16 a[96:99], a[224:227], v[108:111], a[96:99]\n"
      "buffer_load_dwordx4 v208, s[44:47], s69 offen offset:2048 nt lds\n"
      "v_mfma_f32_16x16x32_bf16 a[100:103], a[228:231], v[108:111], a[100:103]\n"
      "v_exp_f32 v239, v55\n"
      "v_mfma_f32_16x16x32_bf16 a[104:107], a[232:235], v[108:111], a[104:107]\n"
      "s_mov_b32 s87, 24576\n"
      "buffer_store_dwordx4 v[172:175], v254, s[56:59], s87 offen offset:2048 sc1\n"
      "v_cvt_pk_bf16_f32 v123, v238, v239\n"
      "v_mfma_f32_16x16x32_bf16 a[108:111], a[236:239], v[108:111], a[108:111]\n"
      "buffer_load_dwordx4 v209, s[44:47], s69 offen offset:3072 nt lds\n"
      "v_mfma_f32_16x16x32_bf16 a[112:115], a[240:243], v[108:111], a[112:115]\n"
      "s_add_u32 s69, s69, 16384\n"
      "v_exp_f32 v232, v56\n"
      "v_mfma_f32_16x16x32_bf16 v[220:223], v[228:231], v[120:123], v[88:91]\n"
      "v_mfma_f32_16x16x32_bf16 a[116:119], a[244:247], v[108:111], a[116:119]\n"
      "v_exp_f32 v233, v57\n"
      "v_mfma_f32_16x16x32_bf16 a[120:123], a[248:251], v[108:111], a[120:123]\n"
      "s_waitcnt lgkmcnt(0)\n"
      "v_cvt_pk_bf16_f32 v124, v232, v233\n"
      "v_mfma_f32_16x16x32_bf16 a[124:127], a[252:255], v[108:111], a[124:127]\n"
      "v_exp_f32 v234, v58\n"
      "s_cmp_lt_u32 s82, s95\n"
      "s_cbranch_scc1 STUB26_%=\n"
      "BACK26_%=:\n"
      "v_mfma_f32_16x16x32_bf16 v[0:3], a[192:195], a[128:131], v[64:67]\n"
      "ds_read_b64_tr_b16 a[224:225], v200 offset:40960\n"
      "ds_read_b64_tr_b16 a[226:227], v200 offset:45056\n"
      "v_mfma_f32_16x16x32_bf16 v[4:7], a[196:199], a[128:131], v[64:67]\n"
      "s_add_u32 s95, s82, 80\n"
      "v_exp_f32 v235, v59\n"
      "v_mfma_f32_16x16x32_bf16 v[0:3], a[200:203], a[132:135], v[0:3]\n"
      "ds_read_b64_tr_b16 a[228:229], v201 offset:40960\n"
      "v_cvt_pk_bf16_f32 v125, v234, v235\n"
      "v_mfma_f32_16x16x32_bf16 v[4:7], a[204:207], a[132:135], v[4:7]\n"
      "v_exp_f32 v236, v60\n"
      "v_mfma_f32_16x16x32_bf16 v[0:3], a[208:211], a[136:139], v[0:3]\n"
      "ds_read_b64_tr_b16 a[230:231], v201 offset:45056\n"
      "ds_read_b64_tr_b16 a[232:233], v202 offset:40960\n"
      "v_mfma_f32_16x16x32_bf16 v[4:7], a[212:215], a[136:139], v[4:7]\n"
      "v_exp_f32 v237, v61\n"
      "v_mfma_f32_16x16x32_bf16 v[0:3], a[216:219], a[140:143], v[0:3]\n"
      "ds_read_b64_tr_b16 a[234:235], v202 offset:45056\n"
      "v_cvt_pk_bf16_f32 v126, v236, v237\n"
      "v_mfma_f32_16x16x32_bf16 v[4:7], a[220:223], a[140:143], v[4:7]\n"
      "v_exp_f32 v238, v62\n"
      "v_mfma_f32_16x16x32_bf16 v[8:11], a[192:195], a[144:147], v[68:71]\n"
      "ds_read_b64_tr_b16 a[236:237], v203 offset:40960\n"
      "ds_read_b64_tr_b16 a[238:239], v203 offset:45056\n"
      "v_mfma_f32_16x16x32_bf16 v[12:15], a[196:199], a[144:147], v[68:71]\n"
      "v_exp_f32 v239, v63\n"
      "v_mfma_f32_16x16x32_bf16 v[8:11], a[200:203], a[148:151], v[8:11]\n"
      "ds_read_b64_tr_b16 a[240:241], v204 offset:40960\n"
      "v_cvt_pk_bf16_f32 v127, v238, v239\n"
      "v_mfma_f32_16x16x32_bf16 v[12:15], a[204:207], a[148:151], v[12:15]\n"
      "ds_read_b64_tr_b16 a[242:243], v204 offset:45056\n"
      "ds_read_b64_tr_b16 a[244:245], v205 offset:40960\n"
      "v_mfma_f32_16x16x32_bf16 v[8:11], a[208:211], a[152:155], v[8:11]\n"
      "v_exp_f32 v232, v0\n"
      "v_mfma_f32_16x16x32_bf16 v[224:227], v[228:231], v[124:127], v[92:95]\n"
      "ds_read_b64_tr_b16 a[246:247], v205 offset:45056\n"
      "ds_read_b64_tr_b16 a[248:249], v206 offset:40960\n"
      "v_mfma_f32_16x16x32_bf16 v[12:15], a[212:215], a[152:155], v[12:15]\n"
      "v_exp_f32 v233, v1\n"
      "v_mfma_f32_16x16x32_bf16 v[8:11], a[216:219], a[156:159], v[8:11]\n"
      "ds_read_b64_tr_b16 a[250:251], v206 offset:45056\n"
      "v_cvt_pk_bf16_f32 v96, v232, v233\n"
      "v_mfma_f32_16x16x32_bf16 v[12:15], a[220:223], a[156:159], v[12:15]\n"
      "v_exp_f32 v234, v2\n"
      "v_mfma_f32_16x16x32_bf16 v[16:19], a[192:195], a[160:163], v[72:75]\n"
      "ds_read_b64_tr_b16 a[252:253], v207 offset:40960\n"
      "ds_read_b64_tr_b16 a[254:255], v207 offset:45056\n"
      "v_mfma_f32_16x16x32_bf16 v[20:23], a[196:199], a[160:163], v[72:75]\n"
      "v_exp_f32 v235, v3\n"
      "v_mfma_f32_16x16x32_bf16 v[16:19], a[200:203], a[164:167], v[16:19]\n"
      "s_mov_b32 s87, 0\n"
      "buffer_store_dwordx4 v[176:179], v254, s[56:59], s87 offen offset:3072 sc1\n"
      "v_cvt_pk_bf16_f32 v97, v234, v235\n"
      "v_mfma_f32_16x16x32_bf16 v[20:23], a[204:207], a[164:167], v[20:23]\n"
      "v_exp_f32 v236, v4\n"
      "v_mfma_f32_16x16x32_bf16 v[16:19], a[208:211], a[168:171], v[16:19]\n"
      "v_max3_f32 v240, v212, v216, v220\n"
      "v_max_f32 v240, v240, v224\n"
      "v_mfma_f32_16x16x32_bf16 v[20:23], a[212:215], a[168:171], v[20:23]\n"
      "v_exp_f32 v237, v5\n"
      "v_mfma_f32_16x16x32_bf16 v[16:19], a[216:219], a[172:175], v[16:19]\n"
      "v_cvt_pk_bf16_f32 v98, v236, v237\n"
      "v_mfma_f32_16x16x32_bf16 v[20:23], a[220:223], a[172:175], v[20:23]\n"
      "v_exp_f32 v238, v6\n"
      "v_mfma_f32_16x16x32_bf16 v[24:27], a[192:195], a[176:179], v[76:79]\n"
      "v_cmp_nge_f32 vcc, s74, v240\n"
      "v_mfma_f32_16x16x32_bf16 v[28:31], a[196:199], a[176:179], v[76:79]\n"
      "v_exp_f32 v239, v7\n"
      "v_mfma_f32_16x16x32_bf16 v[24:27], a[200:203], a[180:183], v[24:27]\n"
      "v_cvt_pk_bf16_f32 v99, v238, v239\n"
      "v_mfma_f32_16x16x32_bf16 v[28:31], a[204:207], a[180:183], v[28:31]\n"
      "v_mfma_f32_16x16x32_bf16 v[24:27], a[208:211], a[184:187], v[24:27]\n"
      "v_exp_f32 v232, v8\n"
      "v_mfma_f32_16x16x32_bf16 v[28:31], a[212:215], a[184:187], v[28:31]\n"
      "s_waitcnt lgkmcnt(0)\n"
      "v_mfma_f32_16x16x32_bf16 v[24:27], a[216:219], a[188:191], v[24:27]\n"
      "v_exp_f32 v233, v9\n"
      "v_mfma_f32_16x16x32_bf16 v[28:31], a[220:223], a[188:191], v[28:31]\n"
      "v_cvt_pk_bf16_f32 v100, v232, v233\n"
      "s_cbranch_vccnz STUB27_%=\n"
      "BACK27_%=:\n"
      "v_mfma_f32_16x16x32_bf16 a[0:3], a[224:227], v[112:115], a[0:3]\n"
      "v_exp_f32 v234, v10\n"
      "v_mfma_f32_16x16x32_bf16 a[4:7], a[228:231], v[112:115], a[4:7]\n"
      "ds_read_b128 a[192:195], v192 offset:57344\n"
      "v_mfma_f32_16x16x32_bf16 v[80:83], v[228:231], v[96:99], v[212:215]\n"
      "v_exp_f32 v235, v11\n"
      "v_mfma_f32_16x16x32_bf16 a[8:11], a[232:235], v[112:115], a[8:11]\n"
      "ds_read_b128 a[196:199], v192 offset:61440\n"
      "v_cvt_pk_bf16_f32 v101, v234, v235\n"
      "v_mfma_f32_16x16x32_bf16 a[12:15], a[236:239], v[112:115], a[12:15]\n"
      "v_exp_f32 v236, v12\n"
      "v_mfma_f32_16x16x32_bf16 a[16:19], a[240:243], v[112:115], a[16:19]\n"
      "ds_read_b128 a[200:203], v193 offset:57344\n"
      "v_mfma_f32_16x16x32_bf16 a[20:23], a[244:247], v[112:115], a[20:23]\n"
      "v_exp_f32 v237, v13\n"
      "v_mfma_f32_16x16x32_bf16 a[24:27], a[248:251], v[112:115], a[24:27]\n"
      "ds_read_b128 a[204:207], v193 offset:61440\n"
      "v_cvt_pk_bf16_f32 v102, v236, v237\n"
      "v_mfma_f32_16x16x32_bf16 a[28:31], a[252:255], v[112:115], a[28:31]\n"
      "v_exp_f32 v238, v14\n"
      "v_mfma_f32_16x16x32_bf16 a[32:35], a[224:227], v[116:119], a[32:35]\n"
      "ds_read_b128 a[208:211], v194 offset:57344\n"
      "v_mfma_f32_16x16x32_bf16 a[36:39], a[228:231], v[116:119], a[36:39]\n"
      "v_exp_f32 v239, v15\n"
      "v_mfma_f32_16x16x32_bf16 a[40:43], a[232:235], v[116:119], a[40:43]\n"
      "ds_read_b128 a[212:215], v194 offset:61440\n"
      "v_cvt_pk_bf16_f32 v103, v238, v239\n"
      "v_mfma_f32_16x16x32_bf16 a[44:47], a[236:239], v[116:119], a[44:47]\n"
      "ds_read_b128 a[216:219], v195 offset:57344\n"
      "s_mov_b32 s87, 8192\n"
      "buffer_store_dwordx4 v[180:183], v254, s[56:59], s87 offen offset:3072 sc1\n"
      "v_mfma_f32_16x16x32_bf16 a[48:51], a[240:243], v[116:119], a[48:51]\n"
      "v_exp_f32 v232, v16\n"
      "v_mfma_f32_16x16x32_bf16 v[84:87], v[228:231], v[100:103], v[216:219]\n"
      "ds_read_b128 a[220:223], v195 offset:61440\n"
      "v_mfma_f32_16x16x32_bf16 a[52:55], a[244:247], v[116:119], a[52:55]\n"
      "v_exp_f32 v233, v17\n"
      "v_mfma_f32_16x16x32_bf16 a[56:59], a[248:251], v[116:119], a[56:59]\n"
      "s_memrealtime s[82:83]\n"
      "v_cvt_pk_bf16_f32 v104, v232, v233\n"
      "v_mfma_f32_16x16x32_bf16 a[60:63], a[252:255], v[116:119], a[60:63]\n"
      "s_add_u32 m0, s78, 32768\n"
      "v_exp_f32 v234, v18\n"
      "v_mfma_f32_16x16x32_bf16 a[64:67], a[224:227], v[120:123], a[64:67]\n"
      "buffer_load_dwordx4 v196, s[40:43], s68 offen offset:0 nt lds\n"
      "v_mfma_f32_16x16x32_bf16 a[68:71], a[228:231], v[120:123], a[68:71]\n"
      "v_exp_f32 v235, v19\n"
      "v_mfma_f32_16x16x32_bf16 a[72:75], a[232:235], v[120:123], a[72:75]\n"
      "v_cvt_pk_bf16_f32 v105, v234, v235\n"
      "v_mfma_f32_16x16x32_bf16 a[76:79], a[236:239], v[120:123], a[76:79]\n"
      "v_exp_f32 v236, v20\n"
      "v_mfma_f32_16x16x32_bf16 a[80:83], a[240:243], v[120:123], a[80:83]\n"
      "buffer_load_dwordx4 v197, s[40:43], s68 offen offset:1024 nt lds\n"
      "v_mfma_f32_16x16x32_bf16 a[84:87], a[244:247], v[120:123], a[84:87]\n"
      "v_exp_f32 v237, v21\n"
      "v_mfma_f32_16x16x32_bf16 a[88:91], a[248:251], v[120:123], a[88:91]\n"
      "v_cvt_pk_bf16_f32 v106, v236, v237\n"
      "v_mfma_f32_16x16x32_bf16 a[92:95], a[252:255], v[120:123], a[92:95]\n"
      "v_exp_f32 v238, v22\n"
      "v_mfma_f32_16x16x32_bf16 a[96:99], a[224:227], v[124:127], a[96:99]\n"
      "buffer_load_dwordx4 v198, s[40:43], s68 offen offset:2048 nt lds\n"
      "v_mfma_f32_16x16x32_bf16 a[100:103], a[228:231], v[124:127], a[100:103]\n"
      "v_exp_f32 v239, v23\n"
      "v_mfma_f32_16x16x32_bf16 a[104:107], a[232:235], v[124:127], a[104:107]\n"
      "v_cvt_pk_bf16_f32 v107, v238, v239\n"
      "v_mfma_f32_16x16x32_bf16 a[108:111], a[236:239], v[124:127], a[108:111]\n"
      "buffer_load_dwordx4 v199, s[40:43], s68 offen offset:3072 nt lds\n"
      "v_mfma_f32_16x16x32_bf16 a[112:115], a[240:243], v[124:127], a[112:115]\n"
      "s_add_u32 s68, s68, 16384\n"
      "v_exp_f32 v232, v24\n"
      "v_mfma_f32_16x16x32_bf16 v[88:91], v[228:231], v[104:107], v[220:223]\n"
      "v_mfma_f32_16x16x32_bf16 a[116:119], a[244:247], v[124:127], a[116:119]\n"
      "v_exp_f32 v233, v25\n"
      "v_mfma_f32_16x16x32_bf16 a[120:123], a[248:251], v[124:127], a[120:123]\n"
      "s_waitcnt lgkmcnt(0)\n"
      "v_cvt_pk_bf16_f32 v108, v232, v233\n"
      "v_mfma_f32_16x16x32_bf16 a[124:127], a[252:255], v[124:127], a[124:127]\n"
      "v_exp_f32 v234, v26\n"
      "s_cmp_lt_u32 s82, s95\n"
      "s_cbranch_scc1 STUB28_%=\n"
      "BACK28_%=:\n"
      "v_mfma_f32_16x16x32_bf16 v[32:35], a[192:195], a[128:131], v[64:67]\n"
      "ds_read_b64_tr_b16 a[224:225], v200 offset:49152\n"
      "ds_read_b64_tr_b16 a[226:227], v200 offset:53248\n"
      "v_mfma_f32_16x16x32_bf16 v[36:39], a[196:199], a[128:131], v[64:67]\n"
      "s_add_u32 s95, s82, 80\n"
      "v_exp_f32 v235, v27\n"
      "v_mfma_f32_16x16x32_bf16 v[32:35], a[200:203], a[132:135], v[32:35]\n"
      "ds_read_b64_tr_b16 a[228:229], v201 offset:49152\n"
      "v_cvt_pk_bf16_f32 v109, v234, v235\n"
      "v_mfma_f32_16x16x32_bf16 v[36:39], a[204:207], a[132:135], v[36:39]\n"
      "v_exp_f32 v236, v28\n"
      "v_mfma_f32_16x16x32_bf16 v[32:35], a[208:211], a[136:139], v[32:35]\n"
      "ds_read_b64_tr_b16 a[230:231], v201 offset:53248\n"
      "ds_read_b64_tr_b16 a[232:233], v202 offset:49152\n"
      "v_mfma_f32_16x16x32_bf16 v[36:39], a[212:215], a[136:139], v[36:39]\n"
      "v_exp_f32 v237, v29\n"
      "v_mfma_f32_16x16x32_bf16 v[32:35], a[216:219], a[140:143], v[32:35]\n"
      "ds_read_b64_tr_b16 a[234:235], v202 offset:53248\n"
      "v_cvt_pk_bf16_f32 v110, v236, v237\n"
      "v_mfma_f32_16x16x32_bf16 v[36:39], a[220:223], a[140:143], v[36:39]\n"
      "v_exp_f32 v238, v30\n"
      "v_mfma_f32_16x16x32_bf16 v[40:43], a[192:195], a[144:147], v[68:71]\n"
      "ds_read_b64_tr_b16 a[236:237], v203 offset:49152\n"
      "ds_read_b64_tr_b16 a[238:239], v203 offset:53248\n"
      "v_mfma_f32_16x16x32_bf16 v[44:47], a[196:199], a[144:147], v[68:71]\n"
      "v_exp_f32 v239, v31\n"
      "v_mfma_f32_16x16x32_bf16 v[40:43], a[200:203], a[148:151], v[40:43]\n"
      "ds_read_b64_tr_b16 a[240:241], v204 offset:49152\n"
      "v_cvt_pk_bf16_f32 v111, v238, v239\n"
      "v_mfma_f32_16x16x32_bf16 v[44:47], a[204:207], a[148:151], v[44:47]\n"
      "ds_read_b64_tr_b16 a[242:243], v204 offset:53248\n"
      "ds_read_b64_tr_b16 a[244:245], v205 offset:49152\n"
      "v_mfma_f32_16x16x32_bf16 v[40:43], a[208:211], a[152:155], v[40:43]\n"
      "v_exp_f32 v232, v32\n"
      "v_mfma_f32_16x16x32_bf16 v[92:95], v[228:231], v[108:111], v[224:227]\n"
      "ds_read_b64_tr_b16 a[246:247], v205 offset:53248\n"
      "ds_read_b64_tr_b16 a[248:249], v206 offset:49152\n"
      "v_mfma_f32_16x16x32_bf16 v[44:47], a[212:215], a[152:155], v[44:47]\n"
      "v_exp_f32 v233, v33\n"
      "v_mfma_f32_16x16x32_bf16 v[40:43], a[216:219], a[156:159], v[40:43]\n"
      "ds_read_b64_tr_b16 a[250:251], v206 offset:53248\n"
      "v_cvt_pk_bf16_f32 v112, v232, v233\n"
      "v_mfma_f32_16x16x32_bf16 v[44:47], a[220:223], a[156:159], v[44:47]\n"
      "v_exp_f32 v234, v34\n"
      "v_mfma_f32_16x16x32_bf16 v[48:51], a[192:195], a[160:163], v[72:75]\n"
      "ds_read_b64_tr_b16 a[252:253], v207 offset:49152\n"
      "ds_read_b64_tr_b16 a[254:255], v207 offset:53248\n"
      "v_mfma_f32_16x16x32_bf16 v[52:55], a[196:199], a[160:163], v[72:75]\n"
      "v_exp_f32 v235, v35\n"
      "v_mfma_f32_16x16x32_bf16 v[48:51], a[200:203], a[164:167], v[48:51]\n"
      "s_mov_b32 s87, 16384\n"
      "buffer_store_dwordx4 v[184:187], v254, s[56:59], s87 offen offset:3072 sc1\n"
      "v_cvt_pk_bf16_f32 v113, v234, v235\n"
      "v_mfma_f32_16x16x32_bf16 v[52:55], a[204:207], a[164:167], v[52:55]\n"
      "v_exp_f32 v236, v36\n"
      "v_mfma_f32_16x16x32_bf16 v[48:51], a[208:211], a[168:171], v[48:51]\n"
      "v_max3_f32 v240, v80, v84, v88\n"
      "v_max_f32 v240, v240, v92\n"
      "v_mfma_f32_16x16x32_bf16 v[52:55], a[212:215], a[168:171], v[52:55]\n"
      "v_exp_f32 v237, v37\n"
      "v_mfma_f32_16x16x32_bf16 v[48:51], a[216:219], a[172:175], v[48:51]\n"
      "v_cvt_pk_bf16_f32 v114, v236, v237\n"
      "v_mfma_f32_16x16x32_bf16 v[52:55], a[220:223], a[172:175], v[52:55]\n"
      "v_exp_f32 v238, v38\n"
      "v_mfma_f32_16x16x32_bf16 v[56:59], a[192:195], a[176:179], v[76:79]\n"
      "v_cmp_nge_f32 vcc, s74, v240\n"
      "v_mfma_f32_16x16x32_bf16 v[60:63], a[196:199], a[176:179], v[76:79]\n"
      "v_exp_f32 v239, v39\n"
      "v_mfma_f32_16x16x32_bf16 v[56:59], a[200:203], a[180:183], v[56:59]\n"
      "v_cvt_pk_bf16_f32 v115, v238, v239\n"
      "v_mfma_f32_16x16x32_bf16 v[60:63], a[204:207], a[180:183], v[60:63]\n"
      "s_mov_b32 s87, 24576\n"
      "buffer_store_dwordx4 v[188:191], v254, s[56:59], s87 offen offset:3072 sc1\n"
      "v_mfma_f32_16x16x32_bf16 v[56:59], a[208:211], a[184:187], v[56:59]\n"
      "v_exp_f32 v232, v40\n"
      "v_mfma_f32_16x16x32_bf16 v[60:63], a[212:215], a[184:187], v[60:63]\n"
      "s_waitcnt vmcnt(22) lgkmcnt(0)\n"
      "s_barrier\n"
      "v_mfma_f32_16x16x32_bf16 v[56:59], a[216:219], a[188:191], v[56:59]\n"
      "v_exp_f32 v233, v41\n"
      "v_mfma_f32_16x16x32_bf16 v[60:63], a[220:223], a[188:191], v[60:63]\n"
      "v_cvt_pk_bf16_f32 v116, v232, v233\n"
      "s_cbranch_vccnz STUB29_%=\n"
      "BACK29_%=:\n"
      "v_mfma_f32_16x16x32_bf16 a[0:3], a[224:227], v[96:99], a[0:3]\n"
      "v_exp_f32 v234, v42\n"
      "v_mfma_f32_16x16x32_bf16 a[4:7], a[228:231], v[96:99], a[4:7]\n"
      "ds_read_b128 a[192:195], v192 offset:0\n"
      "v_mfma_f32_16x16x32_bf16 v[212:215], v[228:231], v[112:115], v[80:83]\n"
      "v_exp_f32 v235, v43\n"
      "v_mfma_f32_16x16x32_bf16 a[8:11], a[232:235], v[96:99], a[8:11]\n"
      "ds_read_b128 a[196:199], v192 offset:4096\n"
      "v_cvt_pk_bf16_f32 v117, v234, v235\n"
      "v_mfma_f32_16x16x32_bf16 a[12:15], a[236:239], v[96:99], a[12:15]\n"
      "v_exp_f32 v236, v44\n"
      "v_mfma_f32_16x16x32_bf16 a[16:19], a[240:243], v[96:99], a[16:19]\n"
      "ds_read_b128 a[200:203], v193 offset:0\n"
      "v_mfma_f32_16x16x32_bf16 a[20:23], a[244:247], v[96:99], a[20:23]\n"
      "v_exp_f32 v237, v45\n"
      "v_mfma_f32_16x16x32_bf16 a[24:27], a[248:251], v[96:99], a[24:27]\n"
      "ds_read_b128 a[204:207], v193 offset:4096\n"
      "v_cvt_pk_bf16_f32 v118, v236, v237\n"
      "v_mfma_f32_16x16x32_bf16 a[28:31], a[252:255], v[96:99], a[28:31]\n"
      "v_exp_f32 v238, v46\n"
      "v_mfma_f32_16x16x32_bf16 a[32:35], a[224:227], v[100:103], a[32:35]\n"
      "ds_read_b128 a[208:211], v194 offset:0\n"
      "v_mfma_f32_16x16x32_bf16 a[36:39], a[228:231], v[100:103], a[36:39]\n"
      "v_exp_f32 v239, v47\n"
      "v_mfma_f32_16x16x32_bf16 a[40:43], a[232:235], v[100:103], a[40:43]\n"
      "ds_read_b128 a[212:215], v194 offset:4096\n"
      "v_cvt_pk_bf16_f32 v119, v238, v239\n"
      "v_mfma_f32_16x16x32_bf16 a[44:47], a[236:239], v[100:103], a[44:47]\n"
      "ds_read_b128 a[216:219], v195 offset:0\n"
      "v_mfma_f32_16x16x32_bf16 a[48:51], a[240:243], v[100:103], a[48:51]\n"
      "v_exp_f32 v232, v48\n"
      "v_mfma_f32_16x16x32_bf16 v[216:219], v[228:231], v[116:119], v[84:87]\n"
      "ds_read_b128 a[220:223], v195 offset:4096\n"
      "v_mfma_f32_16x16x32_bf16 a[52:55], a[244:247], v[100:103], a[52:55]\n"
      "v_exp_f32 v233, v49\n"
      "v_mfma_f32_16x16x32_bf16 a[56:59], a[248:251], v[100:103], a[56:59]\n"
      "s_memrealtime s[82:83]\n"
      "v_cvt_pk_bf16_f32 v120, v232, v233\n"
      "v_mfma_f32_16x16x32_bf16 a[60:63], a[252:255], v[100:103], a[60:63]\n"
      "s_add_u32 m0, s78, 98304\n"
      "v_exp_f32 v234, v50\n"
      "v_mfma_f32_16x16x32_bf16 a[64:67], a[224:227], v[104:107], a[64:67]\n"
      "buffer_load_dwordx4 v208, s[44:47], s69 offen offset:0 nt lds\n"
      "v_mfma_f32_16x16x32_bf16 a[68:71], a[228:231], v[104:107], a[68:71]\n"
      "v_exp_f32 v235, v51\n"
      "v_mfma_f32_16x16x32_bf16 a[72:75], a[232:235], v[104:107], a[72:75]\n"
      "v_cvt_pk_bf16_f32 v121, v234, v235\n"
      "v_mfma_f32_16x16x32_bf16 a[76:79], a[236:239], v[104:107], a[76:79]\n"
      "v_exp_f32 v236, v52\n"
      "v_mfma_f32_16x16x32_bf16 a[80:83], a[240:243], v[104:107], a[80:83]\n"
      "buffer_load_dwordx4 v209, s[44:47], s69 offen offset:1024 nt lds\n"
      "v_mfma_f32_16x16x32_bf16 a[84:87], a[244:247], v[104:107], a[84:87]\n"
      "v_exp_f32 v237, v53\n"
      "v_mfma_f32_16x16x32_bf16 a[88:91], a[248:251], v[104:107], a[88:91]\n"
      "v_cvt_pk_bf16_f32 v122, v236, v237\n"
      "v_mfma_f32_16x16x32_bf16 a[92:95], a[252:255], v[104:107], a[92:95]\n"
      "v_exp_f32 v238, v54\n"
      "v_mfma_f32_16x16x32_bf16 a[96:99], a[224:227], v[108:111], a[96:99]\n"
      "buffer_load_dwordx4 v208, s[44:47], s69 offen offset:2048 nt lds\n"
      "v_mfma_f32_16x16x32_bf16 a[100:103], a[228:231], v[108:111], a[100:103]\n"
      "v_exp_f32 v239, v55\n"
      "v_mfma_f32_16x16x32_bf16 a[104:107], a[232:235], v[108:111], a[104:107]\n"
      "v_cvt_pk_bf16_f32 v123, v238, v239\n"
      "v_mfma_f32_16x16x32_bf16 a[108:111], a[236:239], v[108:111], a[108:111]\n"
      "buffer_load_dwordx4 v209, s[44:47], s69 offen offset:3072 nt lds\n"
      "v_mfma_f32_16x16x32_bf16 a[112:115], a[240:243], v[108:111], a[112:115]\n"
      "s_add_u32 s69, s69, 16384\n"
      "v_exp_f32 v232, v56\n"
      "v_mfma_f32_16x16x32_bf16 v[220:223], v[228:231], v[120:123], v[88:91]\n"
      "v_mfma_f32_16x16x32_bf16 a[116:119], a[244:247], v[108:111], a[116:119]\n"
      "v_exp_f32 v233, v57\n"
      "v_mfma_f32_16x16x32_bf16 a[120:123], a[248:251], v[108:111], a[120:123]\n"
      "s_waitcnt lgkmcnt(0)\n"
      "v_cvt_pk_bf16_f32 v124, v232, v233\n"
      "v_mfma_f32_16x16x32_bf16 a[124:127], a[252:255], v[108:111], a[124:127]\n"
      "v_exp_f32 v234, v58\n"
      "s_cmp_lt_u32 s82, s95\n"
      "s_cbranch_scc1 STUB30_%=\n"
      "BACK30_%=:\n"
      "v_lshlrev_b32 v184, 3, v255\n"
      "global_load_dwordx2 v[188:189], v184, %11 sc1\n"
      "s_mov_b32 s70, 2\n"
      "s_branch L11_%=\n"
      ".p2align 3\n"
      "L01_%=:\n"
      "L11_%=:\n"
      "v_mfma_f32_16x16x32_bf16 v[0:3], a[192:195], a[128:131], v[64:67]\n"
      "ds_read_b64_tr_b16 a[224:225], v200 offset:57344\n"
      "ds_read_b64_tr_b16 a[226:227], v200 offset:61440\n"
      "v_mfma_f32_16x16x32_bf16 v[4:7], a[196:199], a[128:131], v[64:67]\n"
      "v_exp_f32 v235, v59\n"
      "v_mfma_f32_16x16x32_bf16 v[0:3], a[200:203], a[132:135], v[0:3]\n"
      "ds_read_b64_tr_b16 a[228:229], v201 offset:57344\n"
      "v_cvt_pk_bf16_f32 v125, v234, v235\n"
      "v_mfma_f32_16x16x32_bf16 v[4:7], a[204:207], a[132:135], v[4:7]\n"
      "v_exp_f32 v236, v60\n"
      "v_mfma_f32_16x16x32_bf16 v[0:3], a[208:211], a[136:139], v[0:3]\n"
      "ds_read_b64_tr_b16 a[230:231], v201 offset:61440\n"
      "ds_read_b64_tr_b16 a[232:233], v202 offset:57344\n"
      "v_mfma_f32_16x16x32_bf16 v[4:7], a[212:215], a[136:139], v[4:7]\n"
      "v_exp_f32 v237, v61\n"
      "v_mfma_f32_16x16x32_bf16 v[0:3], a[216:219], a[140:143], v[0:3]\n"
      "ds_read_b64_tr_b16 a[234:235], v202 offset:61440\n"
      "v_cvt_pk_bf16_f32 v126, v236, v237\n"
      "v_mfma_f32_16x16x32_bf16 v[4:7], a[220:223], a[140:143], v[4:7]\n"
      "v_exp_f32 v238, v62\n"
      "v_mfma_f32_16x16x32_bf16 v[8:11], a[192:195], a[144:147], v[68:71]\n"
      "ds_read_b64_tr_b16 a[236:237], v203 offset:57344\n"
      "ds_read_b64_tr_b16 a[238:239], v203 offset:61440\n"
      "v_mfma_f32_16x16x32_bf16 v[12:15], a[196:199], a[144:147], v[68:71]\n"
      "v_exp_f32 v239, v63\n"
      "v_mfma_f32_16x16x32_bf16 v[8:11], a[200:203], a[148:151], v[8:11]\n"
      "ds_read_b64_tr_b16 a[240:241], v204 offset:57344\n"
      "v_cvt_pk_bf16_f32 v127, v238, v239\n"
      "v_mfma_f32_16x16x32_bf16 v[12:15], a[204:207], a[148:151], v[12:15]\n"
      "ds_read_b64_tr_b16 a[242:243], v204 offset:61440\n"
      "ds_read_b64_tr_b16 a[244:245], v205 offset:57344\n"
      "v_mfma_f32_16x16x32_bf16 v[8:11], a[208:211], a[152:155], v[8:11]\n"
      "v_exp_f32 v232, v0\n"
      "v_mfma_f32_16x16x32_bf16 v[224:227], v[228:231], v[124:127], v[92:95]\n"
      "ds_read_b64_tr_b16 a[246:247], v205 offset:61440\n"
      "ds_read_b64_tr_b16 a[248:249], v206 offset:57344\n"
      "v_mfma_f32_16x16x32_bf16 v[12:15], a[212:215], a[152:155], v[12:15]\n"
      "v_exp_f32 v233, v1\n"
      "v_mfma_f32_16x16x32_bf16 v[8:11], a[216:219], a[156:159], v[8:11]\n"
      "ds_read_b64_tr_b16 a[250:251], v206 offset:61440\n"
      "v_cvt_pk_bf16_f32 v96, v232, v233\n"
      "v_mfma_f32_16x16x32_bf16 v[12:15], a[220:223], a[156:159], v[12:15]\n"
      "v_exp_f32 v234, v2\n"
      "v_mfma_f32_16x16x32_bf16 v[16:19], a[192:195], a[160:163], v[72:75]\n"
      "ds_read_b64_tr_b16 a[252:253], v207 offset:57344\n"
      "ds_read_b64_tr_b16 a[254:255], v207 offset:61440\n"
      "v_mfma_f32_16x16x32_bf16 v[20:23], a[196:199], a[160:163], v[72:75]\n"
      "v_exp_f32 v235, v3\n"
      "v_mfma_f32_16x16x32_bf16 v[16:19], a[200:203], a[164:167], v[16:19]\n"
      "v_cvt_pk_bf16_f32 v97, v234, v235\n"
      "v_mfma_f32_16x16x32_bf16 v[20:23], a[204:207], a[164:167], v[20:23]\n"
      "v_exp_f32 v236, v4\n"
      "v_mfma_f32_16x16x32_bf16 v[16:19], a[208:211], a[168:171], v[16:19]\n"
      "v_max3_f32 v240, v212, v216, v220\n"
      "v_max_f32 v240, v240, v224\n"
      "v_mfma_f32_16x16x32_bf16 v[20:23], a[212:215], a[168:171], v[20:23]\n"
      "v_exp_f32 v237, v5\n"
      "v_mfma_f32_16x16x32_bf16 v[16:19], a[216:219], a[172:175], v[16:19]\n"
      "v_cvt_pk_bf16_f32 v98, v236, v237\n"
      "v_mfma_f32_16x16x32_bf16 v[20:23], a[220:223], a[172:175], v[20:23]\n"
      "v_exp_f32 v238, v6\n"
      "v_mfma_f32_16x16x32_bf16 v[24:27], a[192:195], a[176:179], v[76:79]\n"
      "v_cmp_nge_f32 vcc, s74, v240\n"
      "v_mfma_f32_16x16x32_bf16 v[28:31], a[196:199], a[176:179], v[76:79]\n"
      "v_exp_f32 v239, v7\n"
      "v_mfma_f32_16x16x32_bf16 v[24:27], a[200:203], a[180:183], v[24:27]\n"
      "v_cvt_pk_bf16_f32 v99, v238, v239\n"
      "v_mfma_f32_16x16x32_bf16 v[28:31], a[204:207], a[180:183], v[28:31]\n"
      "v_mfma_f32_16x16x32_bf16 v[24:27], a[208:211], a[184:187], v[24:27]\n"
      "v_exp_f32 v232, v8\n"
      "v_mfma_f32_16x16x32_bf16 v[28:31], a[212:215], a[184:187], v[28:31]\n"
      "s_waitcnt lgkmcnt(0)\n"
      "v_mfma_f32_16x16x32_bf16 v[24:27], a[216:219], a[188:191], v[24:27]\n"
      "v_exp_f32 v233, v9\n"
      "v_mfma_f32_16x16x32_bf16 v[28:31], a[220:223], a[188:191], v[28:31]\n"
      "v_cvt_pk_bf16_f32 v100, v232, v233\n"
      "s_cbranch_vccnz STUB31_%=\n"
      "BACK31_%=:\n"
      "v_mfma_f32_16x16x32_bf16 a[0:3], a[224:227], v[112:115], a[0:3]\n"
      "v_exp_f32 v234, v10\n"
      "v_mfma_f32_16x16x32_bf16 a[4:7], a[228:231], v[112:115], a[4:7]\n"
      "ds_read_b128 a[192:195], v192 offset:8192\n"
      "v_mfma_f32_16x16x32_bf16 v[80:83], v[228:231], v[96:99], v[212:215]\n"
      "v_exp_f32 v235, v11\n"
      "v_mfma_f32_16x16x32_bf16 a[8:11], a[232:235], v[112:115], a[8:11]\n"
      "ds_read_b128 a[196:199], v192 offset:12288\n"
      "v_cvt_pk_bf16_f32 v101, v234, v235\n"
      "v_mfma_f32_16x16x32_bf16 a[12:15], a[236:239], v[112:115], a[12:15]\n"
      "v_exp_f32 v236, v12\n"
      "v_mfma_f32_16x16x32_bf16 a[16:19], a[240:243], v[112:115], a[16:19]\n"
      "ds_read_b128 a[200:203], v193 offset:8192\n"
      "v_mfma_f32_16x16x32_bf16 a[20:23], a[244:247], v[112:115], a[20:23]\n"
      "v_exp_f32 v237, v13\n"
      "v_mfma_f32_16x16x32_bf16 a[24:27], a[248:251], v[112:115], a[24:27]\n"
      "ds_read_b128 a[204:207], v193 offset:12288\n"
      "v_cvt_pk_bf16_f32 v102, v236, v237\n"
      "v_mfma_f32_16x16x32_bf16 a[28:31], a[252:255], v[112:115], a[28:31]\n"
      "v_exp_f32 v238, v14\n"
      "v_mfma_f32_16x16x32_bf16 a[32:35], a[224:227], v[116:119], a[32:35]\n"
      "ds_read_b128 a[208:211], v194 offset:8192\n"
      "v_mfma_f32_16x16x32_bf16 a[36:39], a[228:231], v[116:119], a[36:39]\n"
      "v_exp_f32 v239, v15\n"
      "v_mfma_f32_16x16x32_bf16 a[40:43], a[232:235], v[116:119], a[40:43]\n"
      "ds_read_b128 a[212:215], v194 offset:12288\n"
      "v_cvt_pk_bf16_f32 v103, v238, v239\n"
      "v_mfma_f32_16x16x32_bf16 a[44:47], a[236:239], v[116:119], a[44:47]\n"
      "ds_read_b128 a[216:219], v195 offset:8192\n"
      "v_mfma_f32_16x16x32_bf16 a[48:51], a[240:243], v[116:119], a[48:51]\n"
      "v_exp_f32 v232, v16\n"
      "v_mfma_f32_16x16x32_bf16 v[84:87], v[228:231], v[100:103], v[216:219]\n"
      "ds_read_b128 a[220:223], v195 offset:12288\n"
      "v_mfma_f32_16x16x32_bf16 a[52:55], a[244:247], v[116:119], a[52:55]\n"
      "v_exp_f32 v233, v17\n"
      "v_mfma_f32_16x16x32_bf16 a[56:59], a[248:251], v[116:119], a[56:59]\n"
      "v_cvt_pk_bf16_f32 v104, v232, v233\n"
      "v_mfma_f32_16x16x32_bf16 a[60:63], a[252:255], v[116:119], a[60:63]\n"
      "s_add_u32 m0, s78, 49152\n"
      "v_exp_f32 v234, v18\n"
      "v_mfma_f32_16x16x32_bf16 a[64:67], a[224:227], v[120:123], a[64:67]\n"
      "buffer_load_dwordx4 v196, s[40:43], s68 offen offset:0 nt lds\n"
      "v_mfma_f32_16x16x32_bf16 a[68:71], a[228:231], v[120:123], a[68:71]\n"
      "v_exp_f32 v235, v19\n"
      "v_mfma_f32_16x16x32_bf16 a[72:75], a[232:235], v[120:123], a[72:75]\n"
      "v_cvt_pk_bf16_f32 v105, v234, v235\n"
      "v_mfma_f32_16x16x32_bf16 a[76:79], a[236:239], v[120:123], a[76:79]\n"
      "v_exp_f32 v236, v20\n"
      "v_mfma_f32_16x16x32_bf16 a[80:83], a[240:243], v[120:123], a[80:83]\n"
      "buffer_load_dwordx4 v197, s[40:43], s68 offen offset:1024 nt lds\n"
      "v_mfma_f32_16x16x32_bf16 a[84:87], a[244:247], v[120:123], a[84:87]\n"
      "v_exp_f32 v237, v21\n"
      "v_mfma_f32_16x16x32_bf16 a[88:91], a[248:251], v[120:123], a[88:91]\n"
      "v_cvt_pk_bf16_f32 v106, v236, v237\n"
      "v_mfma_f32_16x16x32_bf16 a[92:95], a[252:255], v[120:123], a[92:95]\n"
      "v_exp_f32 v238, v22\n"
      "v_mfma_f32_16x16x32_bf16 a[96:99], a[224:227], v[124:127], a[96:99]\n"
      "buffer_load_dwordx4 v198, s[40:43], s68 offen offset:2048 nt lds\n"
      "v_mfma_f32_16x16x32_bf16 a[100:103], a[228:231], v[124:127], a[100:103]\n"
      "v_exp_f32 v239, v23\n"
      "v_mfma_f32_16x16x32_bf16 a[104:107], a[232:235], v[124:127], a[104:107]\n"
      "v_cvt_pk_bf16_f32 v107, v238, v239\n"
      "v_mfma_f32_16x16x32_bf16 a[108:111], a[236:239], v[124:127], a[108:111]\n"
      "buffer_load_dwordx4 v199, s[40:43], s68 offen offset:3072 nt lds\n"
      "v_mfma_f32_16x16x32_bf16 a[112:115], a[240:243], v[124:127], a[112:115]\n"
      "s_add_u32 s68, s68, 16384\n"
      "v_exp_f32 v232, v24\n"
      "v_mfma_f32_16x16x32_bf16 v[88:91], v[228:231], v[104:107], v[220:223]\n"
      "v_mfma_f32_16x16x32_bf16 a[116:119], a[244:247], v[124:127], a[116:119]\n"
      "v_exp_f32 v233, v25\n"
      "v_mfma_f32_16x16x32_bf16 a[120:123], a[248:251], v[124:127], a[120:123]\n"
      "s_waitcnt lgkmcnt(0)\n"
      "v_cvt_pk_bf16_f32 v108, v232, v233\n"
      "v_mfma_f32_16x16x32_bf16 a[124:127], a[252:255], v[124:127], a[124:127]\n"
      "v_exp_f32 v234, v26\n"
      "v_mfma_f32_16x16x32_bf16 v[32:35], a[192:195], a[128:131], v[64:67]\n"
      "ds_read_b64_tr_b16 a[224:225], v200 offset:0\n"
      "ds_read_b64_tr_b16 a[226:227], v200 offset:4096\n"
      "v_mfma_f32_16x16x32_bf16 v[36:39], a[196:199], a[128:131], v[64:67]\n"
      "v_exp_f32 v235, v27\n"
      "v_mfma_f32_16x16x32_bf16 v[32:35], a[200:203], a[132:135], v[32:35]\n"
      "ds_read_b64_tr_b16 a[228:229], v201 offset:0\n"
      "v_cvt_pk_bf16_f32 v109, v234, v235\n"
      "v_mfma_f32_16x16x32_bf16 v[36:39], a[204:207], a[132:135], v[36:39]\n"
      "v_exp_f32 v236, v28\n"
      "v_mfma_f32_16x16x32_bf16 v[32:35], a[208:211], a[136:139], v[32:35]\n"
      "ds_read_b64_tr_b16 a[230:231], v201 offset:4096\n"
      "ds_read_b64_tr_b16 a[232:233], v202 offset:0\n"
      "v_mfma_f32_16x16x32_bf16 v[36:39], a[212:215], a[136:139], v[36:39]\n"
      "v_exp_f32 v237, v29\n"
      "v_mfma_f32_16x16x32_bf16 v[32:35], a[216:219], a[140:143], v[32:35]\n"
      "ds_read_b64_tr_b16 a[234:235], v202 offset:4096\n"
      "v_cvt_pk_bf16_f32 v110, v236, v237\n"
      "v_mfma_f32_16x16x32_bf16 v[36:39], a[220:223], a[140:143], v[36:39]\n"
      "v_exp_f32 v238, v30\n"
      "v_mfma_f32_16x16x32_bf16 v[40:43], a[192:195], a[144:147], v[68:71]\n"
      "ds_read_b64_tr_b16 a[236:237], v203 offset:0\n"
      "ds_read_b64_tr_b16 a[238:239], v203 offset:4096\n"
      "v_mfma_f32_16x16x32_bf16 v[44:47], a[196:199], a[144:147], v[68:71]\n"
      "v_exp_f32 v239, v31\n"
      "v_mfma_f32_16x16x32_bf16 v[40:43], a[200:203], a[148:151], v[40:43]\n"
      "ds_read_b64_tr_b16 a[240:241], v204 offset:0\n"
      "v_cvt_pk_bf16_f32 v111, v238, v239\n"
      "v_mfma_f32_16x16x32_bf16 v[44:47], a[204:207], a[148:151], v[44:47]\n"
      "ds_read_b64_tr_b16 a[242:243], v204 offset:4096\n"
      "ds_read_b64_tr_b16 a[244:245], v205 offset:0\n"
      "v_mfma_f32_16x16x32_bf16 v[40:43], a[208:211], a[152:155], v[40:43]\n"
      "v_exp_f32 v232, v32\n"
      "v_mfma_f32_16x16x32_bf16 v[92:95], v[228:231], v[108:111], v[224:227]\n"
      "ds_read_b64_tr_b16 a[246:247], v205 offset:4096\n"
      "ds_read_b64_tr_b16 a[248:249], v206 offset:0\n"
      "v_mfma_f32_16x16x32_bf16 v[44:47], a[212:215], a[152:155], v[44:47]\n"
      "v_exp_f32 v233, v33\n"
      "v_mfma_f32_16x16x32_bf16 v[40:43], a[216:219], a[156:159], v[40:43]\n"
      "ds_read_b64_tr_b16 a[250:251], v206 offset:4096\n"
      "v_cvt_pk_bf16_f32 v112, v232, v233\n"
      "v_mfma_f32_16x16x32_bf16 v[44:47], a[220:223], a[156:159], v[44:47]\n"
      "v_exp_f32 v234, v34\n"
      "v_mfma_f32_16x16x32_bf16 v[48:51], a[192:195], a[160:163], v[72:75]\n"
      "ds_read_b64_tr_b16 a[252:253], v207 offset:0\n"
      "ds_read_b64_tr_b16 a[254:255], v207 offset:4096\n"
      "v_mfma_f32_16x16x32_bf16 v[52:55], a[196:199], a[160:163], v[72:75]\n"
      "v_exp_f32 v235, v35\n"
      "v_mfma_f32_16x16x32_bf16 v[48:51], a[200:203], a[164:167], v[48:51]\n"
      "v_cvt_pk_bf16_f32 v113, v234, v235\n"
      "v_mfma_f32_16x16x32_bf16 v[52:55], a[204:207], a[164:167], v[52:55]\n"
      "v_exp_f32 v236, v36\n"
      "v_mfma_f32_16x16x32_bf16 v[48:51], a[208:211], a[168:171], v[48:51]\n"
      "v_max3_f32 v240, v80, v84, v88\n"
      "v_max_f32 v240, v240, v92\n"
      "v_mfma_f32_16x16x32_bf16 v[52:55], a[212:215], a[168:171], v[52:55]\n"
      "v_exp_f32 v237, v37\n"
      "v_mfma_f32_16x16x32_bf16 v[48:51], a[216:219], a[172:175], v[48:51]\n"
      "v_cvt_pk_bf16_f32 v114, v236, v237\n"
      "v_mfma_f32_16x16x32_bf16 v[52:55], a[220:223], a[172:175], v[52:55]\n"
      "v_exp_f32 v238, v38\n"
      "v_mfma_f32_16x16x32_bf16 v[56:59], a[192:195], a[176:179], v[76:79]\n"
      "v_cmp_nge_f32 vcc, s74, v240\n"
      "v_mfma_f32_16x16x32_bf16 v[60:63], a[196:199], a[176:179], v[76:79]\n"
      "v_exp_f32 v239, v39\n"
      "v_mfma_f32_16x16x32_bf16 v[56:59], a[200:203], a[180:183], v[56:59]\n"
      "v_cvt_pk_bf16_f32 v115, v238, v239\n"
      "v_mfma_f32_16x16x32_bf16 v[60:63], a[204:207], a[180:183], v[60:63]\n"
      "v_mfma_f32_16x16x32_bf16 v[56:59], a[208:211], a[184:187], v[56:59]\n"
      "v_exp_f32 v232, v40\n"
      "v_mfma_f32_16x16x32_bf16 v[60:63], a[212:215], a[184:187], v[60:63]\n"
      "s_waitcnt vmcnt(12) lgkmcnt(0)\n"
      "s_barrier\n"
      "v_mfma_f32_16x16x32_bf16 v[56:59], a[216:219], a[188:191], v[56:59]\n"
      "v_exp_f32 v233, v41\n"
      "v_mfma_f32_16x16x32_bf16 v[60:63], a[220:223], a[188:191], v[60:63]\n"
      "v_cvt_pk_bf16_f32 v116, v232, v233\n"
      "s_cbranch_vccnz STUB32_%=\n"
      "BACK32_%=:\n"
      "v_mfma_f32_16x16x32_bf16 a[0:3], a[224:227], v[96:99], a[0:3]\n"
      "v_exp_f32 v234, v42\n"
      "v_mfma_f32_16x16x32_bf16 a[4:7], a[228:231], v[96:99], a[4:7]\n"
      "ds_read_b128 a[192:195], v192 offset:16384\n"
      "v_mfma_f32_16x16x32_bf16 v[212:215], v[228:231], v[112:115], v[80:83]\n"
      "v_exp_f32 v235, v43\n"
      "v_mfma_f32_16x16x32_bf16 a[8:11], a[232:235], v[96:99], a[8:11]\n"
      "ds_read_b128 a[196:199], v192 offset:20480\n"
      "v_cvt_pk_bf16_f32 v117, v234, v235\n"
      "v_mfma_f32_16x16x32_bf16 a[12:15], a[236:239], v[96:99], a[12:15]\n"
      "v_exp_f32 v236, v44\n"
      "v_mfma_f32_16x16x32_bf16 a[16:19], a[240:243], v[96:99], a[16:19]\n"
      "ds_read_b128 a[200:203], v193 offset:16384\n"
      "v_mfma_f32_16x16x32_bf16 a[20:23], a[244:247], v[96:99], a[20:23]\n"
      "v_exp_f32 v237, v45\n"
      "v_mfma_f32_16x16x32_bf16 a[24:27], a[248:251], v[96:99], a[24:27]\n"
      "ds_read_b128 a[204:207], v193 offset:20480\n"
      "v_cvt_pk_bf16_f32 v118, v236, v237\n"
      "v_mfma_f32_16x16x32_bf16 a[28:31], a[252:255], v[96:99], a[28:31]\n"
      "v_exp_f32 v238, v46\n"
      "v_mfma_f32_16x16x32_bf16 a[32:35], a[224:227], v[100:103], a[32:35]\n"
      "ds_read_b128 a[208:211], v194 offset:16384\n"
      "v_mfma_f32_16x16x32_bf16 a[36:39], a[228:231], v[100:103], a[36:39]\n"
      "v_exp_f32 v239, v47\n"
      "v_mfma_f32_16x16x32_bf16 a[40:43], a[232:235], v[100:103], a[40:43]\n"
      "ds_read_b128 a[212:215], v194 offset:20480\n"
      "v_cvt_pk_bf16_f32 v119, v238, v239\n"
      "v_mfma_f32_16x16x32_bf16 a[44:47], a[236:239], v[100:103], a[44:47]\n"
      "ds_read_b128 a[216:219], v195 offset:16384\n"
      "v_mfma_f32_16x16x32_bf16 a[48:51], a[240:243], v[100:103], a[48:51]\n"
      "v_exp_f32 v232, v48\n"
      "v_mfma_f32_16x16x32_bf16 v[216:219], v[228:231], v[116:119], v[84:87]\n"
      "ds_read_b128 a[220:223], v195 offset:20480\n"
      "v_mfma_f32_16x16x32_bf16 a[52:55], a[244:247], v[100:103], a[52:55]\n"
      "v_exp_f32 v233, v49\n"
      "v_mfma_f32_16x16x32_bf16 a[56:59], a[248:251], v[100:103], a[56:59]\n"
      "v_cvt_pk_bf16_f32 v120, v232, v233\n"
      "v_mfma_f32_16x16x32_bf16 a[60:63], a[252:255], v[100:103], a[60:63]\n"
      "s_add_u32 m0, s78, 114688\n"
      "v_exp_f32 v234, v50\n"
      "v_mfma_f32_16x16x32_bf16 a[64:67], a[224:227], v[104:107], a[64:67]\n"
      "buffer_load_dwordx4 v208, s[44:47], s69 offen offset:0 nt lds\n"
      "v_mfma_f32_16x16x32_bf16 a[68:71], a[228:231], v[104:107], a[68:71]\n"
      "v_exp_f32 v235, v51\n"
      "v_mfma_f32_16x16x32_bf16 a[72:75], a[232:235], v[104:107], a[72:75]\n"
      "v_cvt_pk_bf16_f32 v121, v234, v235\n"
      "v_mfma_f32_16x16x32_bf16 a[76:79], a[236:239], v[104:107], a[76:79]\n"
      "v_exp_f32 v236, v52\n"
      "v_mfma_f32_16x16x32_bf16 a[80:83], a[240:243], v[104:107], a[80:83]\n"
      "buffer_load_dwordx4 v209, s[44:47], s69 offen offset:1024 nt lds\n"
      "v_mfma_f32_16x16x32_bf16 a[84:87], a[244:247], v[104:107], a[84:87]\n"
      "v_exp_f32 v237, v53\n"
      "v_mfma_f32_16x16x32_bf16 a[88:91], a[248:251], v[104:107], a[88:91]\n"
      "v_cvt_pk_bf16_f32 v122, v236, v237\n"
      "v_mfma_f32_16x16x32_bf16 a[92:95], a[252:255], v[104:107], a[92:95]\n"
      "v_exp_f32 v238, v54\n"
      "v_mfma_f32_16x16x32_bf16 a[96:99], a[224:227], v[108:111], a[96:99]\n"
      "buffer_load_dwordx4 v208, s[44:47], s69 offen offset:2048 nt lds\n"
      "v_mfma_f32_16x16x32_bf16 a[100:103], a[228:231], v[108:111], a[100:103]\n"
      "v_exp_f32 v239, v55\n"
      "v_mfma_f32_16x16x32_bf16 a[104:107], a[232:235], v[108:111], a[104:107]\n"
      "v_cvt_pk_bf16_f32 v123, v238, v239\n"
      "v_mfma_f32_16x16x32_bf16 a[108:111], a[236:239], v[108:111], a[108:111]\n"
      "buffer_load_dwordx4 v209, s[44:47], s69 offen offset:3072 nt lds\n"
      "v_mfma_f32_16x16x32_bf16 a[112:115], a[240:243], v[108:111], a[112:115]\n"
      "s_add_u32 s69, s69, 16384\n"
      "v_exp_f32 v232, v56\n"
      "v_mfma_f32_16x16x32_bf16 v[220:223], v[228:231], v[120:123], v[88:91]\n"
      "v_mfma_f32_16x16x32_bf16 a[116:119], a[244:247], v[108:111], a[116:119]\n"
      "v_exp_f32 v233, v57\n"
      "v_mfma_f32_16x16x32_bf16 a[120:123], a[248:251], v[108:111], a[120:123]\n"
      "s_waitcnt lgkmcnt(0)\n"
      "v_cvt_pk_bf16_f32 v124, v232, v233\n"
      "v_mfma_f32_16x16x32_bf16 a[124:127], a[252:255], v[108:111], a[124:127]\n"
      "v_exp_f32 v234, v58\n"
      "v_mfma_f32_16x16x32_bf16 v[0:3], a[192:195], a[128:131], v[64:67]\n"
      "ds_read_b64_tr_b16 a[224:225], v200 offset:8192\n"
      "ds_read_b64_tr_b16 a[226:227], v200 offset:12288\n"
      "v_mfma_f32_16x16x32_bf16 v[4:7], a[196:199], a[128:131], v[64:67]\n"
      "v_exp_f32 v235, v59\n"
      "v_mfma_f32_16x16x32_bf16 v[0:3], a[200:203], a[132:135], v[0:3]\n"
      "ds_read_b64_tr_b16 a[228:229], v201 offset:8192\n"
      "v_cvt_pk_bf16_f32 v125, v234, v235\n"
      "v_mfma_f32_16x16x32_bf16 v[4:7], a[204:207], a[132:135], v[4:7]\n"
      "v_exp_f32 v236, v60\n"
      "v_mfma_f32_16x16x32_bf16 v[0:3], a[208:211], a[136:139], v[0:3]\n"
      "ds_read_b64_tr_b16 a[230:231], v201 offset:12288\n"
      "ds_read_b64_tr_b16 a[232:233], v202 offset:8192\n"
      "v_mfma_f32_16x16x32_bf16 v[4:7], a[212:215], a[136:139], v[4:7]\n"
      "v_exp_f32 v237, v61\n"
      "v_mfma_f32_16x16x32_bf16 v[0:3], a[216:219], a[140:143], v[0:3]\n"
      "ds_read_b64_tr_b16 a[234:235], v202 offset:12288\n"
      "v_cvt_pk_bf16_f32 v126, v236, v237\n"
      "v_mfma_f32_16x16x32_bf16 v[4:7], a[220:223], a[140:143], v[4:7]\n"
      "v_exp_f32 v238, v62\n"
      "v_mfma_f32_16x16x32_bf16 v[8:11], a[192:195], a[144:147], v[68:71]\n"
      "ds_read_b64_tr_b16 a[236:237], v203 offset:8192\n"
      "ds_read_b64_tr_b16 a[238:239], v203 offset:12288\n"
      "v_mfma_f32_16x16x32_bf16 v[12:15], a[196:199], a[144:147], v[68:71]\n"
      "v_exp_f32 v239, v63\n"
      "v_mfma_f32_16x16x32_bf16 v[8:11], a[200:203], a[148:151], v[8:11]\n"
      "ds_read_b64_tr_b16 a[240:241], v204 offset:8192\n"
      "v_cvt_pk_bf16_f32 v127, v238, v239\n"
      "v_mfma_f32_16x16x32_bf16 v[12:15], a[204:207], a[148:151], v[12:15]\n"
      "ds_read_b64_tr_b16 a[242:243], v204 offset:12288\n"
      "ds_read_b64_tr_b16 a[244:245], v205 offset:8192\n"
      "v_mfma_f32_16x16x32_bf16 v[8:11], a[208:211], a[152:155], v[8:11]\n"
      "v_exp_f32 v232, v0\n"
      "v_mfma_f32_16x16x32_bf16 v[224:227], v[228:231], v[124:127], v[92:95]\n"
      "ds_read_b64_tr_b16 a[246:247], v205 offset:12288\n"
      "ds_read_b64_tr_b16 a[248:249], v206 offset:8192\n"
      "v_mfma_f32_16x16x32_bf16 v[12:15], a[212:215], a[152:155], v[12:15]\n"
      "v_exp_f32 v233, v1\n"
      "v_mfma_f32_16x16x32_bf16 v[8:11], a[216:219], a[156:159], v[8:11]\n"
      "ds_read_b64_tr_b16 a[250:251], v206 offset:12288\n"
      "v_cvt_pk_bf16_f32 v96, v232, v233\n"
      "v_mfma_f32_16x16x32_bf16 v[12:15], a[220:223], a[156:159], v[12:15]\n"
      "v_exp_f32 v234, v2\n"
      "v_mfma_f32_16x16x32_bf16 v[16:19], a[192:195], a[160:163], v[72:75]\n"
      "ds_read_b64_tr_b16 a[252:253], v207 offset:8192\n"
      "ds_read_b64_tr_b16 a[254:255], v207 offset:12288\n"
      "v_mfma_f32_16x16x32_bf16 v[20:23], a[196:199], a[160:163], v[72:75]\n"
      "v_exp_f32 v235, v3\n"
      "v_mfma_f32_16x16x32_bf16 v[16:19], a[200:203], a[164:167], v[16:19]\n"
      "v_cvt_pk_bf16_f32 v97, v234, v235\n"
      "v_mfma_f32_16x16x32_bf16 v[20:23], a[204:207], a[164:167], v[20:23]\n"
      "v_exp_f32 v236, v4\n"
      "v_mfma_f32_16x16x32_bf16 v[16:19], a[208:211], a[168:171], v[16:19]\n"
      "v_max3_f32 v240, v212, v216, v220\n"
      "v_max_f32 v240, v240, v224\n"
      "v_mfma_f32_16x16x32_bf16 v[20:23], a[212:215], a[168:171], v[20:23]\n"
      "v_exp_f32 v237, v5\n"
      "v_mfma_f32_16x16x32_bf16 v[16:19], a[216:219], a[172:175], v[16:19]\n"
      "v_cvt_pk_bf16_f32 v98, v236, v237\n"
      "v_mfma_f32_16x16x32_bf16 v[20:23], a[220:223], a[172:175], v[20:23]\n"
      "v_exp_f32 v238, v6\n"
      "v_mfma_f32_16x16x32_bf16 v[24:27], a[192:195], a[176:179], v[76:79]\n"
      "v_cmp_nge_f32 vcc, s74, v240\n"
      "v_mfma_f32_16x16x32_bf16 v[28:31], a[196:199], a[176:179], v[76:79]\n"
      "v_exp_f32 v239, v7\n"
      "v_mfma_f32_16x16x32_bf16 v[24:27], a[200:203], a[180:183], v[24:27]\n"
      "v_cvt_pk_bf16_f32 v99, v238, v239\n"
      "v_mfma_f32_16x16x32_bf16 v[28:31], a[204:207], a[180:183], v[28:31]\n"
      "v_mfma_f32_16x16x32_bf16 v[24:27], a[208:211], a[184:187], v[24:27]\n"
      "v_exp_f32 v232, v8\n"
      "v_mfma_f32_16x16x32_bf16 v[28:31], a[212:215], a[184:187], v[28:31]\n"
      "s_waitcnt lgkmcnt(0)\n"
      "v_mfma_f32_16x16x32_bf16 v[24:27], a[216:219], a[188:191], v[24:27]\n"
      "v_exp_f32 v233, v9\n"
      "v_mfma_f32_16x16x32_bf16 v[28:31], a[220:223], a[188:191], v[28:31]\n"
      "v_cvt_pk_bf16_f32 v100, v232, v233\n"
      "s_cbranch_vccnz STUB33_%=\n"
      "BACK33_%=:\n"
      "v_mfma_f32_16x16x32_bf16 a[0:3], a[224:227], v[112:115], a[0:3]\n"
      "v_exp_f32 v234, v10\n"
      "v_mfma_f32_16x16x32_bf16 a[4:7], a[228:231], v[112:115], a[4:7]\n"
      "ds_read_b128 a[192:195], v192 offset:24576\n"
      "v_mfma_f32_16x16x32_bf16 v[80:83], v[228:231], v[96:99], v[212:215]\n"
      "v_exp_f32 v235, v11\n"
      "v_mfma_f32_16x16x32_bf16 a[8:11], a[232:235], v[112:115], a[8:11]\n"
      "ds_read_b128 a[196:199], v192 offset:28672\n"
      "v_cvt_pk_bf16_f32 v101, v234, v235\n"
      "v_mfma_f32_16x16x32_bf16 a[12:15], a[236:239], v[112:115], a[12:15]\n"
      "v_exp_f32 v236, v12\n"
      "v_mfma_f32_16x16x32_bf16 a[16:19], a[240:243], v[112:115], a[16:19]\n"
      "ds_read_b128 a[200:203], v193 offset:24576\n"
      "v_mfma_f32_16x16x32_bf16 a[20:23], a[244:247], v[112:115], a[20:23]\n"
      "v_exp_f32 v237, v13\n"
      "v_mfma_f32_16x16x32_bf16 a[24:27], a[248:251], v[112:115], a[24:27]\n"
      "ds_read_b128 a[204:207], v193 offset:28672\n"
      "v_cvt_pk_bf16_f32 v102, v236, v237\n"
      "v_mfma_f32_16x16x32_bf16 a[28:31], a[252:255], v[112:115], a[28:31]\n"
      "v_exp_f32 v238, v14\n"
      "v_mfma_f32_16x16x32_bf16 a[32:35], a[224:227], v[116:119], a[32:35]\n"
      "ds_read_b128 a[208:211], v194 offset:24576\n"
      "v_mfma_f32_16x16x32_bf16 a[36:39], a[228:231], v[116:119], a[36:39]\n"
      "v_exp_f32 v239, v15\n"
      "v_mfma_f32_16x16x32_bf16 a[40:43], a[232:235], v[116:119], a[40:43]\n"
      "ds_read_b128 a[212:215], v194 offset:28672\n"
      "v_cvt_pk_bf16_f32 v103, v238, v239\n"
      "v_mfma_f32_16x16x32_bf16 a[44:47], a[236:239], v[116:119], a[44:47]\n"
      "ds_read_b128 a[216:219], v195 offset:24576\n"
      "v_mfma_f32_16x16x32_bf16 a[48:51], a[240:243], v[116:119], a[48:51]\n"
      "v_exp_f32 v232, v16\n"
      "v_mfma_f32_16x16x32_bf16 v[84:87], v[228:231], v[100:103], v[216:219]\n"
      "ds_read_b128 a[220:223], v195 offset:28672\n"
      "v_mfma_f32_16x16x32_bf16 a[52:55], a[244:247], v[116:119], a[52:55]\n"
      "v_exp_f32 v233, v17\n"
      "v_mfma_f32_16x16x32_bf16 a[56:59], a[248:251], v[116:119], a[56:59]\n"
      "v_cvt_pk_bf16_f32 v104, v232, v233\n"
      "v_mfma_f32_16x16x32_bf16 a[60:63], a[252:255], v[116:119], a[60:63]\n"
      "s_add_u32 m0, s78, 0\n"
      "v_exp_f32 v234, v18\n"
      "v_mfma_f32_16x16x32_bf16 a[64:67], a[224:227], v[120:123], a[64:67]\n"
      "buffer_load_dwordx4 v196, s[40:43], s68 offen offset:0 nt lds\n"
      "v_mfma_f32_16x16x32_bf16 a[68:71], a[228:231], v[120:123], a[68:71]\n"
      "v_exp_f32 v235, v19\n"
      "v_mfma_f32_16x16x32_bf16 a[72:75], a[232:235], v[120:123], a[72:75]\n"
      "v_cvt_pk_bf16_f32 v105, v234, v235\n"
      "v_mfma_f32_16x16x32_bf16 a[76:79], a[236:239], v[120:123], a[76:79]\n"
      "v_exp_f32 v236, v20\n"
      "v_mfma_f32_16x16x32_bf16 a[80:83], a[240:243], v[120:123], a[80:83]\n"
      "buffer_load_dwordx4 v197, s[40:43], s68 offen offset:1024 nt lds\n"
      "v_mfma_f32_16x16x32_bf16 a[84:87], a[244:247], v[120:123], a[84:87]\n"
      "v_exp_f32 v237, v21\n"
      "v_mfma_f32_16x16x32_bf16 a[88:91], a[248:251], v[120:123], a[88:91]\n"
      "v_cvt_pk_bf16_f32 v106, v236, v237\n"
      "v_mfma_f32_16x16x32_bf16 a[92:95], a[252:255], v[120:123], a[92:95]\n"
      "v_exp_f32 v238, v22\n"
      "v_mfma_f32_16x16x32_bf16 a[96:99], a[224:227], v[124:127], a[96:99]\n"
      "buffer_load_dwordx4 v198, s[40:43], s68 offen offset:2048 nt lds\n"
      "v_mfma_f32_16x16x32_bf16 a[100:103], a[228:231], v[124:127], a[100:103]\n"
      "v_exp_f32 v239, v23\n"
      "v_mfma_f32_16x16x32_bf16 a[104:107], a[232:235], v[124:127], a[104:107]\n"
      "v_cvt_pk_bf16_f32 v107, v238, v239\n"
      "v_mfma_f32_16x16x32_bf16 a[108:111], a[236:239], v[124:127], a[108:111]\n"
      "buffer_load_dwordx4 v199, s[40:43], s68 offen offset:3072 nt lds\n"
      "v_mfma_f32_16x16x32_bf16 a[112:115], a[240:243], v[124:127], a[112:115]\n"
      "s_add_u32 s68, s68, 16384\n"
      "v_exp_f32 v232, v24\n"
      "v_mfma_f32_16x16x32_bf16 v[88:91], v[228:231], v[104:107], v[220:223]\n"
      "v_mfma_f32_16x16x32_bf16 a[116:119], a[244:247], v[124:127], a[116:119]\n"
      "v_exp_f32 v233, v25\n"
      "v_mfma_f32_16x16x32_bf16 a[120:123], a[248:251], v[124:127], a[120:123]\n"
      "s_waitcnt lgkmcnt(0)\n"
      "v_cvt_pk_bf16_f32 v108, v232, v233\n"
      "v_mfma_f32_16x16x32_bf16 a[124:127], a[252:255], v[124:127], a[124:127]\n"
      "v_exp_f32 v234, v26\n"
      "v_mfma_f32_16x16x32_bf16 v[32:35], a[192:195], a[128:131], v[64:67]\n"
      "ds_read_b64_tr_b16 a[224:225], v200 offset:16384\n"
      "ds_read_b64_tr_b16 a[226:227], v200 offset:20480\n"
      "v_mfma_f32_16x16x32_bf16 v[36:39], a[196:199], a[128:131], v[64:67]\n"
      "v_exp_f32 v235, v27\n"
      "v_mfma_f32_16x16x32_bf16 v[32:35], a[200:203], a[132:135], v[32:35]\n"
      "ds_read_b64_tr_b16 a[228:229], v201 offset:16384\n"
      "v_cvt_pk_bf16_f32 v109, v234, v235\n"
      "v_mfma_f32_16x16x32_bf16 v[36:39], a[204:207], a[132:135], v[36:39]\n"
      "v_exp_f32 v236, v28\n"
      "v_mfma_f32_16x16x32_bf16 v[32:35], a[208:211], a[136:139], v[32:35]\n"
      "ds_read_b64_tr_b16 a[230:231], v201 offset:20480\n"
      "ds_read_b64_tr_b16 a[232:233], v202 offset:16384\n"
      "v_mfma_f32_16x16x32_bf16 v[36:39], a[212:215], a[136:139], v[36:39]\n"
      "v_exp_f32 v237, v29\n"
      "v_mfma_f32_16x16x32_bf16 v[32:35], a[216:219], a[140:143], v[32:35]\n"
      "ds_read_b64_tr_b16 a[234:235], v202 offset:20480\n"
      "v_cvt_pk_bf16_f32 v110, v236, v237\n"
      "v_mfma_f32_16x16x32_bf16 v[36:39], a[220:223], a[140:143], v[36:39]\n"
      "v_exp_f32 v238, v30\n"
      "v_mfma_f32_16x16x32_bf16 v[40:43], a[192:195], a[144:147], v[68:71]\n"
      "ds_read_b64_tr_b16 a[236:237], v203 offset:16384\n"
      "ds_read_b64_tr_b16 a[238:239], v203 offset:20480\n"
      "v_mfma_f32_16x16x32_bf16 v[44:47], a[196:199], a[144:147], v[68:71]\n"
      "v_exp_f32 v239, v31\n"
      "v_mfma_f32_16x16x32_bf16 v[40:43], a[200:203], a[148:151], v[40:43]\n"
      "ds_read_b64_tr_b16 a[240:241], v204 offset:16384\n"
      "v_cvt_pk_bf16_f32 v111, v238, v239\n"
      "v_mfma_f32_16x16x32_bf16 v[44:47], a[204:207], a[148:151], v[44:47]\n"
      "ds_read_b64_tr_b16 a[242:243], v204 offset:20480\n"
      "ds_read_b64_tr_b16 a[244:245], v205 offset:16384\n"
      "v_mfma_f32_16x16x32_bf16 v[40:43], a[208:211], a[152:155], v[40:43]\n"
      "v_exp_f32 v232, v32\n"
      "v_mfma_f32_16x16x32_bf16 v[92:95], v[228:231], v[108:111], v[224:227]\n"
      "ds_read_b64_tr_b16 a[246:247], v205 offset:20480\n"
      "ds_read_b64_tr_b16 a[248:249], v206 offset:16384\n"
      "v_mfma_f32_16x16x32_bf16 v[44:47], a[212:215], a[152:155], v[44:47]\n"
      "v_exp_f32 v233, v33\n"
      "v_mfma_f32_16x16x32_bf16 v[40:43], a[216:219], a[156:159], v[40:43]\n"
      "ds_read_b64_tr_b16 a[250:251], v206 offset:20480\n"
      "v_cvt_pk_bf16_f32 v112, v232, v233\n"
      "v_mfma_f32_16x16x32_bf16 v[44:47], a[220:223], a[156:159], v[44:47]\n"
      "v_exp_f32 v234, v34\n"
      "v_mfma_f32_16x16x32_bf16 v[48:51], a[192:195], a[160:163], v[72:75]\n"
      "ds_read_b64_tr_b16 a[252:253], v207 offset:16384\n"
      "ds_read_b64_tr_b16 a[254:255], v207 offset:20480\n"
      "v_mfma_f32_16x16x32_bf16 v[52:55], a[196:199], a[160:163], v[72:75]\n"
      "v_exp_f32 v235, v35\n"
      "v_mfma_f32_16x16x32_bf16 v[48:51], a[200:203], a[164:167], v[48:51]\n"
      "v_cvt_pk_bf16_f32 v113, v234, v235\n"
      "v_mfma_f32_16x16x32_bf16 v[52:55], a[204:207], a[164:167], v[52:55]\n"
      "v_exp_f32 v236, v36\n"
      "v_mfma_f32_16x16x32_bf16 v[48:51], a[208:211], a[168:171], v[48:51]\n"
      "v_max3_f32 v240, v80, v84, v88\n"
      "v_max_f32 v240, v240, v92\n"
      "v_mfma_f32_16x16x32_bf16 v[52:55], a[212:215], a[168:171], v[52:55]\n"
      "v_exp_f32 v237, v37\n"
      "v_mfma_f32_16x16x32_bf16 v[48:51], a[216:219], a[172:175], v[48:51]\n"
      "v_cvt_pk_bf16_f32 v114, v236, v237\n"
      "v_mfma_f32_16x16x32_bf16 v[52:55], a[220:223], a[172:175], v[52:55]\n"
      "v_exp_f32 v238, v38\n"
      "v_mfma_f32_16x16x32_bf16 v[56:59], a[192:195], a[176:179], v[76:79]\n"
      "v_cmp_nge_f32 vcc, s74, v240\n"
      "v_mfma_f32_16x16x32_bf16 v[60:63], a[196:199], a[176:179], v[76:79]\n"
      "v_exp_f32 v239, v39\n"
      "v_mfma_f32_16x16x32_bf16 v[56:59], a[200:203], a[180:183], v[56:59]\n"
      "v_cvt_pk_bf16_f32 v115, v238, v239\n"
      "v_mfma_f32_16x16x32_bf16 v[60:63], a[204:207], a[180:183], v[60:63]\n"
      "v_mfma_f32_16x16x32_bf16 v[56:59], a[208:211], a[184:187], v[56:59]\n"
      "v_exp_f32 v232, v40\n"
      "v_mfma_f32_16x16x32_bf16 v[60:63], a[212:215], a[184:187], v[60:63]\n"
      "s_waitcnt vmcnt(12) lgkmcnt(0)\n"
      "s_barrier\n"
      "v_mfma_f32_16x16x32_bf16 v[56:59], a[216:219], a[188:191], v[56:59]\n"
      "v_exp_f32 v233, v41\n"
      "v_mfma_f32_16x16x32_bf16 v[60:63], a[220:223], a[188:191], v[60:63]\n"
      "v_cvt_pk_bf16_f32 v116, v232, v233\n"
      "s_cbranch_vccnz STUB34_%=\n"
      "BACK34_%=:\n"
      "v_mfma_f32_16x16x32_bf16 a[0:3], a[224:227], v[96:99], a[0:3]\n"
      "v_exp_f32 v234, v42\n"
      "v_mfma_f32_16x16x32_bf16 a[4:7], a[228:231], v[96:99], a[4:7]\n"
      "ds_read_b128 a[192:195], v192 offset:32768\n"
      "v_mfma_f32_16x16x32_bf16 v[212:215], v[228:231], v[112:115], v[80:83]\n"
      "v_exp_f32 v235, v43\n"
      "v_mfma_f32_16x16x32_bf16 a[8:11], a[232:235], v[96:99], a[8:11]\n"
      "ds_read_b128 a[196:199], v192 offset:36864\n"
      "v_cvt_pk_bf16_f32 v117, v234, v235\n"
      "v_mfma_f32_16x16x32_bf16 a[12:15], a[236:239], v[96:99], a[12:15]\n"
      "v_exp_f32 v236, v44\n"
      "v_mfma_f32_16x16x32_bf16 a[16:19], a[240:243], v[96:99], a[16:19]\n"
      "ds_read_b128 a[200:203], v193 offset:32768\n"
      "v_mfma_f32_16x16x32_bf16 a[20:23], a[244:247], v[96:99], a[20:23]\n"
      "v_exp_f32 v237, v45\n"
      "v_mfma_f32_16x16x32_bf16 a[24:27], a[248:251], v[96:99], a[24:27]\n"
      "ds_read_b128 a[204:207], v193 offset:36864\n"
      "v_cvt_pk_bf16_f32 v118, v236, v237\n"
      "v_mfma_f32_16x16x32_bf16 a[28:31], a[252:255], v[96:99], a[28:31]\n"
      "v_exp_f32 v238, v46\n"
      "v_mfma_f32_16x16x32_bf16 a[32:35], a[224:227], v[100:103], a[32:35]\n"
      "ds_read_b128 a[208:211], v194 offset:32768\n"
      "v_mfma_f32_16x16x32_bf16 a[36:39], a[228:231], v[100:103], a[36:39]\n"
      "v_exp_f32 v239, v47\n"
      "v_mfma_f32_16x16x32_bf16 a[40:43], a[232:235], v[100:103], a[40:43]\n"
      "ds_read_b128 a[212:215], v194 offset:36864\n"
      "v_cvt_pk_bf16_f32 v119, v238, v239\n"
      "v_mfma_f32_16x16x32_bf16 a[44:47], a[236:239], v[100:103], a[44:47]\n"
      "ds_read_b128 a[216:219], v195 offset:32768\n"
      "v_mfma_f32_16x16x32_bf16 a[48:51], a[240:243], v[100:103], a[48:51]\n"
      "v_exp_f32 v232, v48\n"
      "v_mfma_f32_16x16x32_bf16 v[216:219], v[228:231], v[116:119], v[84:87]\n"
      "ds_read_b128 a[220:223], v195 offset:36864\n"
      "v_mfma_f32_16x16x32_bf16 a[52:55], a[244:247], v[100:103], a[52:55]\n"
      "v_exp_f32 v233, v49\n"
      "v_mfma_f32_16x16x32_bf16 a[56:59], a[248:251], v[100:103], a[56:59]\n"
      "v_cvt_pk_bf16_f32 v120, v232, v233\n"
      "v_mfma_f32_16x16x32_bf16 a[60:63], a[252:255], v[100:103], a[60:63]\n"
      "s_add_u32 m0, s78, 65536\n"
      "v_exp_f32 v234, v50\n"
      "v_mfma_f32_16x16x32_bf16 a[64:67], a[224:227], v[104:107], a[64:67]\n"
      "buffer_load_dwordx4 v208, s[44:47], s69 offen offset:0 nt lds\n"
      "v_mfma_f32_16x16x32_bf16 a[68:71], a[228:231], v[104:107], a[68:71]\n"
      "v_exp_f32 v235, v51\n"
      "v_mfma_f32_16x16x32_bf16 a[72:75], a[232:235], v[104:107], a[72:75]\n"
      "v_cvt_pk_bf16_f32 v121, v234, v235\n"
      "v_mfma_f32_16x16x32_bf16 a[76:79], a[236:239], v[104:107], a[76:79]\n"
      "v_exp_f32 v236, v52\n"
      "v_mfma_f32_16x16x32_bf16 a[80:83], a[240:243], v[104:107], a[80:83]\n"
      "buffer_load_dwordx4 v209, s[44:47], s69 offen offset:1024 nt lds\n"
      "v_mfma_f32_16x16x32_bf16 a[84:87], a[244:247], v[104:107], a[84:87]\n"
      "v_exp_f32 v237, v53\n"
      "v_mfma_f32_16x16x32_bf16 a[88:91], a[248:251], v[104:107], a[88:91]\n"
      "v_cvt_pk_bf16_f32 v122, v236, v237\n"
      "v_mfma_f32_16x16x32_bf16 a[92:95], a[252:255], v[104:107], a[92:95]\n"
      "v_exp_f32 v238, v54\n"
      "v_mfma_f32_16x16x32_bf16 a[96:99], a[224:227], v[108:111], a[96:99]\n"
      "buffer_load_dwordx4 v208, s[44:47], s69 offen offset:2048 nt lds\n"
      "v_mfma_f32_16x16x32_bf16 a[100:103], a[228:231], v[108:111], a[100:103]\n"
      "v_exp_f32 v239, v55\n"
      "v_mfma_f32_16x16x32_bf16 a[104:107], a[232:235], v[108:111], a[104:107]\n"
      "v_cvt_pk_bf16_f32 v123, v238, v239\n"
      "v_mfma_f32_16x16x32_bf16 a[108:111], a[236:239], v[108:111], a[108:111]\n"
      "buffer_load_dwordx4 v209, s[44:47], s69 offen offset:3072 nt lds\n"
      "v_mfma_f32_16x16x32_bf16 a[112:115], a[240:243], v[108:111], a[112:115]\n"
      "s_add_u32 s69, s69, 16384\n"
      "v_exp_f32 v232, v56\n"
      "v_mfma_f32_16x16x32_bf16 v[220:223], v[228:231], v[120:123], v[88:91]\n"
      "v_mfma_f32_16x16x32_bf16 a[116:119], a[244:247], v[108:111], a[116:119]\n"
      "v_exp_f32 v233, v57\n"
      "v_mfma_f32_16x16x32_bf16 a[120:123], a[248:251], v[108:111], a[120:123]\n"
      "s_waitcnt lgkmcnt(0)\n"
      "v_cvt_pk_bf16_f32 v124, v232, v233\n"
      "v_mfma_f32_16x16x32_bf16 a[124:127], a[252:255], v[108:111], a[124:127]\n"
      "v_exp_f32 v234, v58\n"
      "s_cmp_eq_u32 s70, s71\n"
      "s_cbranch_scc1 SIG0_%=\n"
      "SIG0B_%=:\n"
      "s_cmp_eq_u32 s70, 0\n"
      "s_cbranch_scc1 POL0_%=\n"
      "POL0B_%=:\n"
      "v_mfma_f32_16x16x32_bf16 v[0:3], a[192:195], a[128:131], v[64:67]\n"
      "ds_read_b64_tr_b16 a[224:225], v200 offset:24576\n"
      "ds_read_b64_tr_b16 a[226:227], v200 offset:28672\n"
      "v_mfma_f32_16x16x32_bf16 v[4:7], a[196:199], a[128:131], v[64:67]\n"
      "v_exp_f32 v235, v59\n"
      "v_mfma_f32_16x16x32_bf16 v[0:3], a[200:203], a[132:135], v[0:3]\n"
      "ds_read_b64_tr_b16 a[228:229], v201 offset:24576\n"
      "v_cvt_pk_bf16_f32 v125, v234, v235\n"
      "v_mfma_f32_16x16x32_bf16 v[4:7], a[204:207], a[132:135], v[4:7]\n"
      "v_exp_f32 v236, v60\n"
      "v_mfma_f32_16x16x32_bf16 v[0:3], a[208:211], a[136:139], v[0:3]\n"
      "ds_read_b64_tr_b16 a[230:231], v201 offset:28672\n"
      "ds_read_b64_tr_b16 a[232:233], v202 offset:24576\n"
      "v_mfma_f32_16x16x32_bf16 v[4:7], a[212:215], a[136:139], v[4:7]\n"
      "v_exp_f32 v237, v61\n"
      "v_mfma_f32_16x16x32_bf16 v[0:3], a[216:219], a[140:143], v[0:3]\n"
      "ds_read_b64_tr_b16 a[234:235], v202 offset:28672\n"
      "v_cvt_pk_bf16_f32 v126, v236, v237\n"
      "v_mfma_f32_16x16x32_bf16 v[4:7], a[220:223], a[140:143], v[4:7]\n"
      "v_exp_f32 v238, v62\n"
      "v_mfma_f32_16x16x32_bf16 v[8:11], a[192:195], a[144:147], v[68:71]\n"
      "ds_read_b64_tr_b16 a[236:237], v203 offset:24576\n"
      "ds_read_b64_tr_b16 a[238:239], v203 offset:28672\n"
      "v_mfma_f32_16x16x32_bf16 v[12:15], a[196:199], a[144:147], v[68:71]\n"
      "v_exp_f32 v239, v63\n"
      "v_mfma_f32_16x16x32_bf16 v[8:11], a[200:203], a[148:151], v[8:11]\n"
      "ds_read_b64_tr_b16 a[240:241], v204 offset:24576\n"
      "v_cvt_pk_bf16_f32 v127, v238, v239\n"
      "v_mfma_f32_16x16x32_bf16 v[12:15], a[204:207], a[148:151], v[12:15]\n"
      "ds_read_b64_tr_b16 a[242:243], v204 offset:28672\n"
      "ds_read_b64_tr_b16 a[244:245], v205 offset:24576\n"
      "v_mfma_f32_16x16x32_bf16 v[8:11], a[208:211], a[152:155], v[8:11]\n"
      "v_exp_f32 v232, v0\n"
      "v_mfma_f32_16x16x32_bf16 v[224:227], v[228:231], v[124:127], v[92:95]\n"
      "ds_read_b64_tr_b16 a[246:247], v205 offset:28672\n"
      "ds_read_b64_tr_b16 a[248:249], v206 offset:24576\n"
      "v_mfma_f32_16x16x32_bf16 v[12:15], a[212:215], a[152:155], v[12:15]\n"
      "v_exp_f32 v233, v1\n"
      "v_mfma_f32_16x16x32_bf16 v[8:11], a[216:219], a[156:159], v[8:11]\n"
      "ds_read_b64_tr_b16 a[250:251], v206 offset:28672\n"
      "v_cvt_pk_bf16_f32 v96, v232, v233\n"
      "v_mfma_f32_16x16x32_bf16 v[12:15], a[220:223], a[156:159], v[12:15]\n"
      "v_exp_f32 v234, v2\n"
      "v_mfma_f32_16x16x32_bf16 v[16:19], a[192:195], a[160:163], v[72:75]\n"
      "ds_read_b64_tr_b16 a[252:253], v207 offset:24576\n"
      "ds_read_b64_tr_b16 a[254:255], v207 offset:28672\n"
      "v_mfma_f32_16x16x32_bf16 v[20:23], a[196:199], a[160:163], v[72:75]\n"
      "v_exp_f32 v235, v3\n"
      "v_mfma_f32_16x16x32_bf16 v[16:19], a[200:203], a[164:167], v[16:19]\n"
      "v_cvt_pk_bf16_f32 v97, v234, v235\n"
      "v_mfma_f32_16x16x32_bf16 v[20:23], a[204:207], a[164:167], v[20:23]\n"
      "v_exp_f32 v236, v4\n"
      "v_mfma_f32_16x16x32_bf16 v[16:19], a[208:211], a[168:171], v[16:19]\n"
      "v_max3_f32 v240, v212, v216, v220\n"
      "v_max_f32 v240, v240, v224\n"
      "v_mfma_f32_16x16x32_bf16 v[20:23], a[212:215], a[168:171], v[20:23]\n"
      "v_exp_f32 v237, v5\n"
      "v_mfma_f32_16x16x32_bf16 v[16:19], a[216:219], a[172:175], v[16:19]\n"
      "v_cvt_pk_bf16_f32 v98, v236, v237\n"
      "v_mfma_f32_16x16x32_bf16 v[20:23], a[220:223], a[172:175], v[20:23]\n"
      "v_exp_f32 v238, v6\n"
      "v_mfma_f32_16x16x32_bf16 v[24:27], a[192:195], a[176:179], v[76:79]\n"
      "v_cmp_nge_f32 vcc, s74, v240\n"
      "v_mfma_f32_16x16x32_bf16 v[28:31], a[196:199], a[176:179], v[76:79]\n"
      "v_exp_f32 v239, v7\n"
      "v_mfma_f32_16x16x32_bf16 v[24:27], a[200:203], a[180:183], v[24:27]\n"
      "v_cvt_pk_bf16_f32 v99, v238, v239\n"
      "v_mfma_f32_16x16x32_bf16 v[28:31], a[204:207], a[180:183], v[28:31]\n"
      "v_mfma_f32_16x16x32_bf16 v[24:27], a[208:211], a[184:187], v[24:27]\n"
      "v_exp_f32 v232, v8\n"
      "v_mfma_f32_16x16x32_bf16 v[28:31], a[212:215], a[184:187], v[28:31]\n"
      "s_waitcnt lgkmcnt(0)\n"
      "v_mfma_f32_16x16x32_bf16 v[24:27], a[216:219], a[188:191], v[24:27]\n"
      "v_exp_f32 v233, v9\n"
      "v_mfma_f32_16x16x32_bf16 v[28:31], a[220:223], a[188:191], v[28:31]\n"
      "v_cvt_pk_bf16_f32 v100, v232, v233\n"
      "s_cbranch_vccnz STUB35_%=\n"
      "BACK35_%=:\n"
      "v_mfma_f32_16x16x32_bf16 a[0:3], a[224:227], v[112:115], a[0:3]\n"
      "v_exp_f32 v234, v10\n"
      "v_mfma_f32_16x16x32_bf16 a[4:7], a[228:231], v[112:115], a[4:7]\n"
      "ds_read_b128 a[192:195], v192 offset:40960\n"
      "v_mfma_f32_16x16x32_bf16 v[80:83], v[228:231], v[96:99], v[212:215]\n"
      "v_exp_f32 v235, v11\n"
      "v_mfma_f32_16x16x32_bf16 a[8:11], a[232:235], v[112:115], a[8:11]\n"
      "ds_read_b128 a[196:199], v192 offset:45056\n"
      "v_cvt_pk_bf16_f32 v101, v234, v235\n"
      "v_mfma_f32_16x16x32_bf16 a[12:15], a[236:239], v[112:115], a[12:15]\n"
      "v_exp_f32 v236, v12\n"
      "v_mfma_f32_16x16x32_bf16 a[16:19], a[240:243], v[112:115], a[16:19]\n"
      "ds_read_b128 a[200:203], v193 offset:40960\n"
      "v_mfma_f32_16x16x32_bf16 a[20:23], a[244:247], v[112:115], a[20:23]\n"
      "v_exp_f32 v237, v13\n"
      "v_mfma_f32_16x16x32_bf16 a[24:27], a[248:251], v[112:115], a[24:27]\n"
      "ds_read_b128 a[204:207], v193 offset:45056\n"
      "v_cvt_pk_bf16_f32 v102, v236, v237\n"
      "v_mfma_f32_16x16x32_bf16 a[28:31], a[252:255], v[112:115], a[28:31]\n"
      "v_exp_f32 v238, v14\n"
      "v_mfma_f32_16x16x32_bf16 a[32:35], a[224:227], v[116:119], a[32:35]\n"
      "ds_read_b128 a[208:211], v194 offset:40960\n"
      "v_mfma_f32_16x16x32_bf16 a[36:39], a[228:231], v[116:119], a[36:39]\n"
      "v_exp_f32 v239, v15\n"
      "v_mfma_f32_16x16x32_bf16 a[40:43], a[232:235], v[116:119], a[40:43]\n"
      "ds_read_b128 a[212:215], v194 offset:45056\n"
      "v_cvt_pk_bf16_f32 v103, v238, v239\n"
      "v_mfma_f32_16x16x32_bf16 a[44:47], a[236:239], v[116:119], a[44:47]\n"
      "ds_read_b128 a[216:219], v195 offset:40960\n"
      "v_mfma_f32_16x16x32_bf16 a[48:51], a[240:243], v[116:119], a[48:51]\n"
      "v_exp_f32 v232, v16\n"
      "v_mfma_f32_16x16x32_bf16 v[84:87], v[228:231], v[100:103], v[216:219]\n"
      "ds_read_b128 a[220:223], v195 offset:45056\n"
      "v_mfma_f32_16x16x32_bf16 a[52:55], a[244:247], v[116:119], a[52:55]\n"
      "v_exp_f32 v233, v17\n"
      "v_mfma_f32_16x16x32_bf16 a[56:59], a[248:251], v[116:119], a[56:59]\n"
      "v_cvt_pk_bf16_f32 v104, v232, v233\n"
      "v_mfma_f32_16x16x32_bf16 a[60:63], a[252:255], v[116:119], a[60:63]\n"
      "s_add_u32 m0, s78, 16384\n"
      "v_exp_f32 v234, v18\n"
      "v_mfma_f32_16x16x32_bf16 a[64:67], a[224:227], v[120:123], a[64:67]\n"
      "buffer_load_dwordx4 v196, s[40:43], s68 offen offset:0 nt lds\n"
      "v_mfma_f32_16x16x32_bf16 a[68:71], a[228:231], v[120:123], a[68:71]\n"
      "v_exp_f32 v235, v19\n"
      "v_mfma_f32_16x16x32_bf16 a[72:75], a[232:235], v[120:123], a[72:75]\n"
      "v_cvt_pk_bf16_f32 v105, v234, v235\n"
      "v_mfma_f32_16x16x32_bf16 a[76:79], a[236:239], v[120:123], a[76:79]\n"
      "v_exp_f32 v236, v20\n"
      "v_mfma_f32_16x16x32_bf16 a[80:83], a[240:243], v[120:123], a[80:83]\n"
      "buffer_load_dwordx4 v197, s[40:43], s68 offen offset:1024 nt lds\n"
      "v_mfma_f32_16x16x32_bf16 a[84:87], a[244:247], v[120:123], a[84:87]\n"
      "v_exp_f32 v237, v21\n"
      "v_mfma_f32_16x16x32_bf16 a[88:91], a[248:251], v[120:123], a[88:91]\n"
      "v_cvt_pk_bf16_f32 v106, v236, v237\n"
      "v_mfma_f32_16x16x32_bf16 a[92:95], a[252:255], v[120:123], a[92:95]\n"
      "v_exp_f32 v238, v22\n"
      "v_mfma_f32_16x16x32_bf16 a[96:99], a[224:227], v[124:127], a[96:99]\n"
      "buffer_load_dwordx4 v198, s[40:43], s68 offen offset:2048 nt lds\n"
      "v_mfma_f32_16x16x32_bf16 a[100:103], a[228:231], v[124:127], a[100:103]\n"
      "v_exp_f32 v239, v23\n"
      "v_mfma_f32_16x16x32_bf16 a[104:107], a[232:235], v[124:127], a[104:107]\n"
      "v_cvt_pk_bf16_f32 v107, v238, v239\n"
      "v_mfma_f32_16x16x32_bf16 a[108:111], a[236:239], v[124:127], a[108:111]\n"
      "buffer_load_dwordx4 v199, s[40:43], s68 offen offset:3072 nt lds\n"
      "v_mfma_f32_16x16x32_bf16 a[112:115], a[240:243], v[124:127], a[112:115]\n"
      "s_add_u32 s68, s68, 16384\n"
      "v_exp_f32 v232, v24\n"
      "v_mfma_f32_16x16x32_bf16 v[88:91], v[228:231], v[104:107], v[220:223]\n"
      "v_mfma_f32_16x16x32_bf16 a[116:119], a[244:247], v[124:127], a[116:119]\n"
      "v_exp_f32 v233, v25\n"
      "v_mfma_f32_16x16x32_bf16 a[120:123], a[248:251], v[124:127], a[120:123]\n"
      "s_waitcnt lgkmcnt(0)\n"
      "v_cvt_pk_bf16_f32 v108, v232, v233\n"
      "v_mfma_f32_16x16x32_bf16 a[124:127], a[252:255], v[124:127], a[124:127]\n"
      "v_exp_f32 v234, v26\n"
      "v_mfma_f32_16x16x32_bf16 v[32:35], a[192:195], a[128:131], v[64:67]\n"
      "ds_read_b64_tr_b16 a[224:225], v200 offset:32768\n"
      "ds_read_b64_tr_b16 a[226:227], v200 offset:36864\n"
      "v_mfma_f32_16x16x32_bf16 v[36:39], a[196:199], a[128:131], v[64:67]\n"
      "v_exp_f32 v235, v27\n"
      "v_mfma_f32_16x16x32_bf16 v[32:35], a[200:203], a[132:135], v[32:35]\n"
      "ds_read_b64_tr_b16 a[228:229], v201 offset:32768\n"
      "v_cvt_pk_bf16_f32 v109, v234, v235\n"
      "v_mfma_f32_16x16x32_bf16 v[36:39], a[204:207], a[132:135], v[36:39]\n"
      "v_exp_f32 v236, v28\n"
      "v_mfma_f32_16x16x32_bf16 v[32:35], a[208:211], a[136:139], v[32:35]\n"
      "ds_read_b64_tr_b16 a[230:231], v201 offset:36864\n"
      "ds_read_b64_tr_b16 a[232:233], v202 offset:32768\n"
      "v_mfma_f32_16x16x32_bf16 v[36:39], a[212:215], a[136:139], v[36:39]\n"
      "v_exp_f32 v237, v29\n"
      "v_mfma_f32_16x16x32_bf16 v[32:35], a[216:219], a[140:143], v[32:35]\n"
      "ds_read_b64_tr_b16 a[234:235], v202 offset:36864\n"
      "v_cvt_pk_bf16_f32 v110, v236, v237\n"
      "v_mfma_f32_16x16x32_bf16 v[36:39], a[220:223], a[140:143], v[36:39]\n"
      "v_exp_f32 v238, v30\n"
      "v_mfma_f32_16x16x32_bf16 v[40:43], a[192:195], a[144:147], v[68:71]\n"
      "ds_read_b64_tr_b16 a[236:237], v203 offset:32768\n"
      "ds_read_b64_tr_b16 a[238:239], v203 offset:36864\n"
      "v_mfma_f32_16x16x32_bf16 v[44:47], a[196:199], a[144:147], v[68:71]\n"
      "v_exp_f32 v239, v31\n"
      "v_mfma_f32_16x16x32_bf16 v[40:43], a[200:203], a[148:151], v[40:43]\n"
      "ds_read_b64_tr_b16 a[240:241], v204 offset:32768\n"
      "v_cvt_pk_bf16_f32 v111, v238, v239\n"
      "v_mfma_f32_16x16x32_bf16 v[44:47], a[204:207], a[148:151], v[44:47]\n"
      "ds_read_b64_tr_b16 a[242:243], v204 offset:36864\n"
      "ds_read_b64_tr_b16 a[244:245], v205 offset:32768\n"
      "v_mfma_f32_16x16x32_bf16 v[40:43], a[208:211], a[152:155], v[40:43]\n"
      "v_exp_f32 v232, v32\n"
      "v_mfma_f32_16x16x32_bf16 v[92:95], v[228:231], v[108:111], v[224:227]\n"
      "ds_read_b64_tr_b16 a[246:247], v205 offset:36864\n"
      "ds_read_b64_tr_b16 a[248:249], v206 offset:32768\n"
      "v_mfma_f32_16x16x32_bf16 v[44:47], a[212:215], a[152:155], v[44:47]\n"
      "v_exp_f32 v233, v33\n"
      "v_mfma_f32_16x16x32_bf16 v[40:43], a[216:219], a[156:159], v[40:43]\n"
      "ds_read_b64_tr_b16 a[250:251], v206 offset:36864\n"
      "v_cvt_pk_bf16_f32 v112, v232, v233\n"
      "v_mfma_f32_16x16x32_bf16 v[44:47], a[220:223], a[156:159], v[44:47]\n"
      "v_exp_f32 v234, v34\n"
      "v_mfma_f32_16x16x32_bf16 v[48:51], a[192:195], a[160:163], v[72:75]\n"
      "ds_read_b64_tr_b16 a[252:253], v207 offset:32768\n"
      "ds_read_b64_tr_b16 a[254:255], v207 offset:36864\n"
      "v_mfma_f32_16x16x32_bf16 v[52:55], a[196:199], a[160:163], v[72:75]\n"
      "v_exp_f32 v235, v35\n"
      "v_mfma_f32_16x16x32_bf16 v[48:51], a[200:203], a[164:167], v[48:51]\n"
      "v_cvt_pk_bf16_f32 v113, v234, v235\n"
      "v_mfma_f32_16x16x32_bf16 v[52:55], a[204:207], a[164:167], v[52:55]\n"
      "v_exp_f32 v236, v36\n"
      "v_mfma_f32_16x16x32_bf16 v[48:51], a[208:211], a[168:171], v[48:51]\n"
      "v_max3_f32 v240, v80, v84, v88\n"
      "v_max_f32 v240, v240, v92\n"
      "v_mfma_f32_16x16x32_bf16 v[52:55], a[212:215], a[168:171], v[52:55]\n"
      "v_exp_f32 v237, v37\n"
      "v_mfma_f32_16x16x32_bf16 v[48:51], a[216:219], a[172:175], v[48:51]\n"
      "v_cvt_pk_bf16_f32 v114, v236, v237\n"
      "v_mfma_f32_16x16x32_bf16 v[52:55], a[220:223], a[172:175], v[52:55]\n"
      "v_exp_f32 v238, v38\n"
      "v_mfma_f32_16x16x32_bf16 v[56:59], a[192:195], a[176:179], v[76:79]\n"
      "v_cmp_nge_f32 vcc, s74, v240\n"
      "v_mfma_f32_16x16x32_bf16 v[60:63], a[196:199], a[176:179], v[76:79]\n"
      "v_exp_f32 v239, v39\n"
      "v_mfma_f32_16x16x32_bf16 v[56:59], a[200:203], a[180:183], v[56:59]\n"
      "v_cvt_pk_bf16_f32 v115, v238, v239\n"
      "v_mfma_f32_16x16x32_bf16 v[60:63], a[204:207], a[180:183], v[60:63]\n"
      "v_mfma_f32_16x16x32_bf16 v[56:59], a[208:211], a[184:187], v[56:59]\n"
      "v_exp_f32 v232, v40\n"
      "v_mfma_f32_16x16x32_bf16 v[60:63], a[212:215], a[184:187], v[60:63]\n"
      "s_waitcnt vmcnt(12) lgkmcnt(0)\n"
      "s_barrier\n"
      "v_mfma_f32_16x16x32_bf16 v[56:59], a[216:219], a[188:191], v[56:59]\n"
      "v_exp_f32 v233, v41\n"
      "v_mfma_f32_16x16x32_bf16 v[60:63], a[220:223], a[188:191], v[60:63]\n"
      "v_cvt_pk_bf16_f32 v116, v232, v233\n"
      "s_cbranch_vccnz STUB36_%=\n"
      "BACK36_%=:\n"
      "v_mfma_f32_16x16x32_bf16 a[0:3], a[224:227], v[96:99], a[0:3]\n"
      "v_exp_f32 v234, v42\n"
      "v_mfma_f32_16x16x32_bf16 a[4:7], a[228:231], v[96:99], a[4:7]\n"
      "ds_read_b128 a[192:195], v192 offset:49152\n"
      "v_mfma_f32_16x16x32_bf16 v[212:215], v[228:231], v[112:115], v[80:83]\n"
      "v_exp_f32 v235, v43\n"
      "v_mfma_f32_16x16x32_bf16 a[8:11], a[232:235], v[96:99], a[8:11]\n"
      "ds_read_b128 a[196:199], v192 offset:53248\n"
      "v_cvt_pk_bf16_f32 v117, v234, v235\n"
      "v_mfma_f32_16x16x32_bf16 a[12:15], a[236:239], v[96:99], a[12:15]\n"
      "v_exp_f32 v236, v44\n"
      "v_mfma_f32_16x16x32_bf16 a[16:19], a[240:243], v[96:99], a[16:19]\n"
      "ds_read_b128 a[200:203], v193 offset:49152\n"
      "v_mfma_f32_16x16x32_bf16 a[20:23], a[244:247], v[96:99], a[20:23]\n"
      "v_exp_f32 v237, v45\n"
      "v_mfma_f32_16x16x32_bf16 a[24:27], a[248:251], v[96:99], a[24:27]\n"
      "ds_read_b128 a[204:207], v193 offset:53248\n"
      "v_cvt_pk_bf16_f32 v118, v236, v237\n"
      "v_mfma_f32_16x16x32_bf16 a[28:31], a[252:255], v[96:99], a[28:31]\n"
      "v_exp_f32 v238, v46\n"
      "v_mfma_f32_16x16x32_bf16 a[32:35], a[224:227], v[100:103], a[32:35]\n"
      "ds_read_b128 a[208:211], v194 offset:49152\n"
      "v_mfma_f32_16x16x32_bf16 a[36:39], a[228:231], v[100:103], a[36:39]\n"
      "v_exp_f32 v239, v47\n"
      "v_mfma_f32_16x16x32_bf16 a[40:43], a[232:235], v[100:103], a[40:43]\n"
      "ds_read_b128 a[212:215], v194 offset:53248\n"
      "v_cvt_pk_bf16_f32 v119, v238, v239\n"
      "v_mfma_f32_16x16x32_bf16 a[44:47], a[236:239], v[100:103], a[44:47]\n"
      "ds_read_b128 a[216:219], v195 offset:49152\n"
      "v_mfma_f32_16x16x32_bf16 a[48:51], a[240:243], v[100:103], a[48:51]\n"
      "v_exp_f32 v232, v48\n"
      "v_mfma_f32_16x16x32_bf16 v[216:219], v[228:231], v[116:119], v[84:87]\n"
      "ds_read_b128 a[220:223], v195 offset:53248\n"
      "v_mfma_f32_16x16x32_bf16 a[52:55], a[244:247], v[100:103], a[52:55]\n"
      "v_exp_f32 v233, v49\n"
      "v_mfma_f32_16x16x32_bf16 a[56:59], a[248:251], v[100:103], a[56:59]\n"
      "v_cvt_pk_bf16_f32 v120, v232, v233\n"
      "v_mfma_f32_16x16x32_bf16 a[60:63], a[252:255], v[100:103], a[60:63]\n"
      "s_add_u32 m0, s78, 81920\n"
      "v_exp_f32 v234, v50\n"
      "v_mfma_f32_16x16x32_bf16 a[64:67], a[224:227], v[104:107], a[64:67]\n"
      "buffer_load_dwordx4 v208, s[44:47], s69 offen offset:0 nt lds\n"
      "v_mfma_f32_16x16x32_bf16 a[68:71], a[228:231], v[104:107], a[68:71]\n"
      "v_exp_f32 v235, v51\n"
      "v_mfma_f32_16x16x32_bf16 a[72:75], a[232:235], v[104:107], a[72:75]\n"
      "v_cvt_pk_bf16_f32 v121, v234, v235\n"
      "v_mfma_f32_16x16x32_bf16 a[76:79], a[236:239], v[104:107], a[76:79]\n"
      "v_exp_f32 v236, v52\n"
      "v_mfma_f32_16x16x32_bf16 a[80:83], a[240:243], v[104:107], a[80:83]\n"
      "buffer_load_dwordx4 v209, s[44:47], s69 offen offset:1024 nt lds\n"
      "v_mfma_f32_16x16x32_bf16 a[84:87], a[244:247], v[104:107], a[84:87]\n"
      "v_exp_f32 v237, v53\n"
      "v_mfma_f32_16x16x32_bf16 a[88:91], a[248:251], v[104:107], a[88:91]\n"
      "v_cvt_pk_bf16_f32 v122, v236, v237\n"
      "v_mfma_f32_16x16x32_bf16 a[92:95], a[252:255], v[104:107], a[92:95]\n"
      "v_exp_f32 v238, v54\n"
      "v_mfma_f32_16x16x32_bf16 a[96:99], a[224:227], v[108:111], a[96:99]\n"
      "buffer_load_dwordx4 v208, s[44:47], s69 offen offset:2048 nt lds\n"
      "v_mfma_f32_16x16x32_bf16 a[100:103], a[228:231], v[108:111], a[100:103]\n"
      "v_exp_f32 v239, v55\n"
      "v_mfma_f32_16x16x32_bf16 a[104:107], a[232:235], v[108:111], a[104:107]\n"
      "v_cvt_pk_bf16_f32 v123, v238, v239\n"
      "v_mfma_f32_16x16x32_bf16 a[108:111], a[236:239], v[108:111], a[108:111]\n"
      "buffer_load_dwordx4 v209, s[44:47], s69 offen offset:3072 nt lds\n"
      "v_mfma_f32_16x16x32_bf16 a[112:115], a[240:243], v[108:111], a[112:115]\n"
      "s_add_u32 s69, s69, 16384\n"
      "v_exp_f32 v232, v56\n"
      "v_mfma_f32_16x16x32_bf16 v[220:223], v[228:231], v[120:123], v[88:91]\n"
      "v_mfma_f32_16x16x32_bf16 a[116:119], a[244:247], v[108:111], a[116:119]\n"
      "v_exp_f32 v233, v57\n"
      "v_mfma_f32_16x16x32_bf16 a[120:123], a[248:251], v[108:111], a[120:123]\n"
      "s_waitcnt lgkmcnt(0)\n"
      "v_cvt_pk_bf16_f32 v124, v232, v233\n"
      "v_mfma_f32_16x16x32_bf16 a[124:127], a[252:255], v[108:111], a[124:127]\n"
      "v_exp_f32 v234, v58\n"
      "v_mfma_f32_16x16x32_bf16 v[0:3], a[192:195], a[128:131], v[64:67]\n"
      "ds_read_b64_tr_b16 a[224:225], v200 offset:40960\n"
      "ds_read_b64_tr_b16 a[226:227], v200 offset:45056\n"
      "v_mfma_f32_16x16x32_bf16 v[4:7], a[196:199], a[128:131], v[64:67]\n"
      "v_exp_f32 v235, v59\n"
      "v_mfma_f32_16x16x32_bf16 v[0:3], a[200:203], a[132:135], v[0:3]\n"
      "ds_read_b64_tr_b16 a[228:229], v201 offset:40960\n"
      "v_cvt_pk_bf16_f32 v125, v234, v235\n"
      "v_mfma_f32_16x16x32_bf16 v[4:7], a[204:207], a[132:135], v[4:7]\n"
      "v_exp_f32 v236, v60\n"
      "v_mfma_f32_16x16x32_bf16 v[0:3], a[208:211], a[136:139], v[0:3]\n"
      "ds_read_b64_tr_b16 a[230:231], v201 offset:45056\n"
      "ds_read_b64_tr_b16 a[232:233], v202 offset:40960\n"
      "v_mfma_f32_16x16x32_bf16 v[4:7], a[212:215], a[136:139], v[4:7]\n"
      "v_exp_f32 v237, v61\n"
      "v_mfma_f32_16x16x32_bf16 v[0:3], a[216:219], a[140:143], v[0:3]\n"
      "ds_read_b64_tr_b16 a[234:235], v202 offset:45056\n"
      "v_cvt_pk_bf16_f32 v126, v236, v237\n"
      "v_mfma_f32_16x16x32_bf16 v[4:7], a[220:223], a[140:143], v[4:7]\n"
      "v_exp_f32 v238, v62\n"
      "v_mfma_f32_16x16x32_bf16 v[8:11], a[192:195], a[144:147], v[68:71]\n"
      "ds_read_b64_tr_b16 a[236:237], v203 offset:40960\n"
      "ds_read_b64_tr_b16 a[238:239], v203 offset:45056\n"
      "v_mfma_f32_16x16x32_bf16 v[12:15], a[196:199], a[144:147], v[68:71]\n"
      "v_exp_f32 v239, v63\n"
      "v_mfma_f32_16x16x32_bf16 v[8:11], a[200:203], a[148:151], v[8:11]\n"
      "ds_read_b64_tr_b16 a[240:241], v204 offset:40960\n"
      "v_cvt_pk_bf16_f32 v127, v238, v239\n"
      "v_mfma_f32_16x16x32_bf16 v[12:15], a[204:207], a[148:151], v[12:15]\n"
      "ds_read_b64_tr_b16 a[242:243], v204 offset:45056\n"
      "ds_read_b64_tr_b16 a[244:245], v205 offset:40960\n"
      "v_mfma_f32_16x16x32_bf16 v[8:11], a[208:211], a[152:155], v[8:11]\n"
      "v_exp_f32 v232, v0\n"
      "v_mfma_f32_16x16x32_bf16 v[224:227], v[228:231], v[124:127], v[92:95]\n"
      "ds_read_b64_tr_b16 a[246:247], v205 offset:45056\n"
      "ds_read_b64_tr_b16 a[248:249], v206 offset:40960\n"
      "v_mfma_f32_16x16x32_bf16 v[12:15], a[212:215], a[152:155], v[12:15]\n"
      "v_exp_f32 v233, v1\n"
      "v_mfma_f32_16x16x32_bf16 v[8:11], a[216:219], a[156:159], v[8:11]\n"
      "ds_read_b64_tr_b16 a[250:251], v206 offset:45056\n"
      "v_cvt_pk_bf16_f32 v96, v232, v233\n"
      "v_mfma_f32_16x16x32_bf16 v[12:15], a[220:223], a[156:159], v[12:15]\n"
      "v_exp_f32 v234, v2\n"
      "v_mfma_f32_16x16x32_bf16 v[16:19], a[192:195], a[160:163], v[72:75]\n"
      "ds_read_b64_tr_b16 a[252:253], v207 offset:40960\n"
      "ds_read_b64_tr_b16 a[254:255], v207 offset:45056\n"
      "v_mfma_f32_16x16x32_bf16 v[20:23], a[196:199], a[160:163], v[72:75]\n"
      "v_exp_f32 v235, v3\n"
      "v_mfma_f32_16x16x32_bf16 v[16:19], a[200:203], a[164:167], v[16:19]\n"
      "v_cvt_pk_bf16_f32 v97, v234, v235\n"
      "v_mfma_f32_16x16x32_bf16 v[20:23], a[204:207], a[164:167], v[20:23]\n"
      "v_exp_f32 v236, v4\n"
      "v_mfma_f32_16x16x32_bf16 v[16:19], a[208:211], a[168:171], v[16:19]\n"
      "v_max3_f32 v240, v212, v216, v220\n"
      "v_max_f32 v240, v240, v224\n"
      "v_mfma_f32_16x16x32_bf16 v[20:23], a[212:215], a[168:171], v[20:23]\n"
      "v_exp_f32 v237, v5\n"
      "v_mfma_f32_16x16x32_bf16 v[16:19], a[216:219], a[172:175], v[16:19]\n"
      "v_cvt_pk_bf16_f32 v98, v236, v237\n"
      "v_mfma_f32_16x16x32_bf16 v[20:23], a[220:223], a[172:175], v[20:23]\n"
      "v_exp_f32 v238, v6\n"
      "v_mfma_f32_16x16x32_bf16 v[24:27], a[192:195], a[176:179], v[76:79]\n"
      "v_cmp_nge_f32 vcc, s74, v240\n"
      "v_mfma_f32_16x16x32_bf16 v[28:31], a[196:199], a[176:179], v[76:79]\n"
      "v_exp_f32 v239, v7\n"
      "v_mfma_f32_16x16x32_bf16 v[24:27], a[200:203], a[180:183], v[24:27]\n"
      "v_cvt_pk_bf16_f32 v99, v238, v239\n"
      "v_mfma_f32_16x16x32_bf16 v[28:31], a[204:207], a[180:183], v[28:31]\n"
      "v_mfma_f32_16x16x32_bf16 v[24:27], a[208:211], a[184:187], v[24:27]\n"
      "v_exp_f32 v232, v8\n"
      "v_mfma_f32_16x16x32_bf16 v[28:31], a[212:215], a[184:187], v[28:31]\n"
      "s_waitcnt lgkmcnt(0)\n"
      "v_mfma_f32_16x16x32_bf16 v[24:27], a[216:219], a[188:191], v[24:27]\n"
      "v_exp_f32 v233, v9\n"
      "v_mfma_f32_16x16x32_bf16 v[28:31], a[220:223], a[188:191], v[28:31]\n"
      "v_cvt_pk_bf16_f32 v100, v232, v233\n"
      "s_cbranch_vccnz STUB37_%=\n"
      "BACK37_%=:\n"
      "v_mfma_f32_16x16x32_bf16 a[0:3], a[224:227], v[112:115], a[0:3]\n"
      "v_exp_f32 v234, v10\n"
      "v_mfma_f32_16x16x32_bf16 a[4:7], a[228:231], v[112:115], a[4:7]\n"
      "ds_read_b128 a[192:195], v192 offset:57344\n"
      "v_mfma_f32_16x16x32_bf16 v[80:83], v[228:231], v[96:99], v[212:215]\n"
      "v_exp_f32 v235, v11\n"
      "v_mfma_f32_16x16x32_bf16 a[8:11], a[232:235], v[112:115], a[8:11]\n"
      "ds_read_b128 a[196:199], v192 offset:61440\n"
      "v_cvt_pk_bf16_f32 v101, v234, v235\n"
      "v_mfma_f32_16x16x32_bf16 a[12:15], a[236:239], v[112:115], a[12:15]\n"
      "v_exp_f32 v236, v12\n"
      "v_mfma_f32_16x16x32_bf16 a[16:19], a[240:243], v[112:115], a[16:19]\n"
      "ds_read_b128 a[200:203], v193 offset:57344\n"
      "v_mfma_f32_16x16x32_bf16 a[20:23], a[244:247], v[112:115], a[20:23]\n"
      "v_exp_f32 v237, v13\n"
      "v_mfma_f32_16x16x32_bf16 a[24:27], a[248:251], v[112:115], a[24:27]\n"
      "ds_read_b128 a[204:207], v193 offset:61440\n"
      "v_cvt_pk_bf16_f32 v102, v236, v237\n"
      "v_mfma_f32_16x16x32_bf16 a[28:31], a[252:255], v[112:115], a[28:31]\n"
      "v_exp_f32 v238, v14\n"
      "v_mfma_f32_16x16x32_bf16 a[32:35], a[224:227], v[116:119], a[32:35]\n"
      "ds_read_b128 a[208:211], v194 offset:57344\n"
      "v_mfma_f32_16x16x32_bf16 a[36:39], a[228:231], v[116:119], a[36:39]\n"
      "v_exp_f32 v239, v15\n"
      "v_mfma_f32_16x16x32_bf16 a[40:43], a[232:235], v[116:119], a[40:43]\n"
      "ds_read_b128 a[212:215], v194 offset:61440\n"
      "v_cvt_pk_bf16_f32 v103, v238, v239\n"
      "v_mfma_f32_16x16x32_bf16 a[44:47], a[236:239], v[116:119], a[44:47]\n"
      "ds_read_b128 a[216:219], v195 offset:57344\n"
      "v_mfma_f32_16x16x32_bf16 a[48:51], a[240:243], v[116:119], a[48:51]\n"
      "v_exp_f32 v232, v16\n"
      "v_mfma_f32_16x16x32_bf16 v[84:87], v[228:231], v[100:103], v[216:219]\n"
      "ds_read_b128 a[220:223], v195 offset:61440\n"
      "v_mfma_f32_16x16x32_bf16 a[52:55], a[244:247], v[116:119], a[52:55]\n"
      "v_exp_f32 v233, v17\n"
      "v_mfma_f32_16x16x32_bf16 a[56:59], a[248:251], v[116:119], a[56:59]\n"
      "v_cvt_pk_bf16_f32 v104, v232, v233\n"
      "v_mfma_f32_16x16x32_bf16 a[60:63], a[252:255], v[116:119], a[60:63]\n"
      "s_add_u32 m0, s78, 32768\n"
      "v_exp_f32 v234, v18\n"
      "v_mfma_f32_16x16x32_bf16 a[64:67], a[224:227], v[120:123], a[64:67]\n"
      "buffer_load_dwordx4 v196, s[40:43], s68 offen offset:0 nt lds\n"
      "v_mfma_f32_16x16x32_bf16 a[68:71], a[228:231], v[120:123], a[68:71]\n"
      "v_exp_f32 v235, v19\n"
      "v_mfma_f32_16x16x32_bf16 a[72:75], a[232:235], v[120:123], a[72:75]\n"
      "v_cvt_pk_bf16_f32 v105, v234, v235\n"
      "v_mfma_f32_16x16x32_bf16 a[76:79], a[236:239], v[120:123], a[76:79]\n"
      "v_exp_f32 v236, v20\n"
      "v_mfma_f32_16x16x32_bf16 a[80:83], a[240:243], v[120:123], a[80:83]\n"
      "buffer_load_dwordx4 v197, s[40:43], s68 offen offset:1024 nt lds\n"
      "v_mfma_f32_16x16x32_bf16 a[84:87], a[244:247], v[120:123], a[84:87]\n"
      "v_exp_f32 v237, v21\n"
      "v_mfma_f32_16x16x32_bf16 a[88:91], a[248:251], v[120:123], a[88:91]\n"
      "v_cvt_pk_bf16_f32 v106, v236, v237\n"
      "v_mfma_f32_16x16x32_bf16 a[92:95], a[252:255], v[120:123], a[92:95]\n"
      "v_exp_f32 v238, v22\n"
      "v_mfma_f32_16x16x32_bf16 a[96:99], a[224:227], v[124:127], a[96:99]\n"
      "buffer_load_dwordx4 v198, s[40:43], s68 offen offset:2048 nt lds\n"
      "v_mfma_f32_16x16x32_bf16 a[100:103], a[228:231], v[124:127], a[100:103]\n"
      "v_exp_f32 v239, v23\n"
      "v_mfma_f32_16x16x32_bf16 a[104:107], a[232:235], v[124:127], a[104:107]\n"
      "v_cvt_pk_bf16_f32 v107, v238, v239\n"
      "v_mfma_f32_16x16x32_bf16 a[108:111], a[236:239], v[124:127], a[108:111]\n"
      "buffer_load_dwordx4 v199, s[40:43], s68 offen offset:3072 nt lds\n"
      "v_mfma_f32_16x16x32_bf16 a[112:115], a[240:243], v[124:127], a[112:115]\n"
      "s_add_u32 s68, s68, 16384\n"
      "v_exp_f32 v232, v24\n"
      "v_mfma_f32_16x16x32_bf16 v[88:91], v[228:231], v[104:107], v[220:223]\n"
      "v_mfma_f32_16x16x32_bf16 a[116:119], a[244:247], v[124:127], a[116:119]\n"
      "v_exp_f32 v233, v25\n"
      "v_mfma_f32_16x16x32_bf16 a[120:123], a[248:251], v[124:127], a[120:123]\n"
      "s_waitcnt lgkmcnt(0)\n"
      "v_cvt_pk_bf16_f32 v108, v232, v233\n"
      "v_mfma_f32_16x16x32_bf16 a[124:127], a[252:255], v[124:127], a[124:127]\n"
      "v_exp_f32 v234, v26\n"
      "v_mfma_f32_16x16x32_bf16 v[32:35], a[192:195], a[128:131], v[64:67]\n"
      "ds_read_b64_tr_b16 a[224:225], v200 offset:49152\n"
      "ds_read_b64_tr_b16 a[226:227], v200 offset:53248\n"
      "v_mfma_f32_16x16x32_bf16 v[36:39], a[196:199], a[128:131], v[64:67]\n"
      "v_exp_f32 v235, v27\n"
      "v_mfma_f32_16x16x32_bf16 v[32:35], a[200:203], a[132:135], v[32:35]\n"
      "ds_read_b64_tr_b16 a[228:229], v201 offset:49152\n"
      "v_cvt_pk_bf16_f32 v109, v234, v235\n"
      "v_mfma_f32_16x16x32_bf16 v[36:39], a[204:207], a[132:135], v[36:39]\n"
      "v_exp_f32 v236, v28\n"
      "v_mfma_f32_16x16x32_bf16 v[32:35], a[208:211], a[136:139], v[32:35]\n"
      "ds_read_b64_tr_b16 a[230:231], v201 offset:53248\n"
      "ds_read_b64_tr_b16 a[232:233], v202 offset:49152\n"
      "v_mfma_f32_16x16x32_bf16 v[36:39], a[212:215], a[136:139], v[36:39]\n"
      "v_exp_f32 v237, v29\n"
      "v_mfma_f32_16x16x32_bf16 v[32:35], a[216:219], a[140:143], v[32:35]\n"
      "ds_read_b64_tr_b16 a[234:235], v202 offset:53248\n"
      "v_cvt_pk_bf16_f32 v110, v236, v237\n"
      "v_mfma_f32_16x16x32_bf16 v[36:39], a[220:223], a[140:143], v[36:39]\n"
      "v_exp_f32 v238, v30\n"
      "v_mfma_f32_16x16x32_bf16 v[40:43], a[192:195], a[144:147], v[68:71]\n"
      "ds_read_b64_tr_b16 a[236:237], v203 offset:49152\n"
      "ds_read_b64_tr_b16 a[238:239], v203 offset:53248\n"
      "v_mfma_f32_16x16x32_bf16 v[44:47], a[196:199], a[144:147], v[68:71]\n"
      "v_exp_f32 v239, v31\n"
      "v_mfma_f32_16x16x32_bf16 v[40:43], a[200:203], a[148:151], v[40:43]\n"
      "ds_read_b64_tr_b16 a[240:241], v204 offset:49152\n"
      "v_cvt_pk_bf16_f32 v111, v238, v239\n"
      "v_mfma_f32_16x16x32_bf16 v[44:47], a[204:207], a[148:151], v[44:47]\n"
      "ds_read_b64_tr_b16 a[242:243], v204 offset:53248\n"
      "ds_read_b64_tr_b16 a[244:245], v205 offset:49152\n"
      "v_mfma_f32_16x16x32_bf16 v[40:43], a[208:211], a[152:155], v[40:43]\n"
      "v_exp_f32 v232, v32\n"
      "v_mfma_f32_16x16x32_bf16 v[92:95], v[228:231], v[108:111], v[224:227]\n"
      "ds_read_b64_tr_b16 a[246:247], v205 offset:53248\n"
      "ds_read_b64_tr_b16 a[248:249], v206 offset:49152\n"
      "v_mfma_f32_16x16x32_bf16 v[44:47], a[212:215], a[152:155], v[44:47]\n"
      "v_exp_f32 v233, v33\n"
      "v_mfma_f32_16x16x32_bf16 v[40:43], a[216:219], a[156:159], v[40:43]\n"
      "ds_read_b64_tr_b16 a[250:251], v206 offset:53248\n"
      "v_cvt_pk_bf16_f32 v112, v232, v233\n"
      "v_mfma_f32_16x16x32_bf16 v[44:47], a[220:223], a[156:159], v[44:47]\n"
      "v_exp_f32 v234, v34\n"
      "v_mfma_f32_16x16x32_bf16 v[48:51], a[192:195], a[160:163], v[72:75]\n"
      "ds_read_b64_tr_b16 a[252:253], v207 offset:49152\n"
      "ds_read_b64_tr_b16 a[254:255], v207 offset:53248\n"
      "v_mfma_f32_16x16x32_bf16 v[52:55], a[196:199], a[160:163], v[72:75]\n"
      "v_exp_f32 v235, v35\n"
      "v_mfma_f32_16x16x32_bf16 v[48:51], a[200:203], a[164:167], v[48:51]\n"
      "v_cvt_pk_bf16_f32 v113, v234, v235\n"
      "v_mfma_f32_16x16x32_bf16 v[52:55], a[204:207], a[164:167], v[52:55]\n"
      "v_exp_f32 v236, v36\n"
      "v_mfma_f32_16x16x32_bf16 v[48:51], a[208:211], a[168:171], v[48:51]\n"
      "v_max3_f32 v240, v80, v84, v88\n"
      "v_max_f32 v240, v240, v92\n"
      "v_mfma_f32_16x16x32_bf16 v[52:55], a[212:215], a[168:171], v[52:55]\n"
      "v_exp_f32 v237, v37\n"
      "v_mfma_f32_16x16x32_bf16 v[48:51], a[216:219], a[172:175], v[48:51]\n"
      "v_cvt_pk_bf16_f32 v114, v236, v237\n"
      "v_mfma_f32_16x16x32_bf16 v[52:55], a[220:223], a[172:175], v[52:55]\n"
      "v_exp_f32 v238, v38\n"
      "v_mfma_f32_16x16x32_bf16 v[56:59], a[192:195], a[176:179], v[76:79]\n"
      "v_cmp_nge_f32 vcc, s74, v240\n"
      "v_mfma_f32_16x16x32_bf16 v[60:63], a[196:199], a[176:179], v[76:79]\n"
      "v_exp_f32 v239, v39\n"
      "v_mfma_f32_16x16x32_bf16 v[56:59], a[200:203], a[180:183], v[56:59]\n"
      "v_cvt_pk_bf16_f32 v115, v238, v239\n"
      "v_mfma_f32_16x16x32_bf16 v[60:63], a[204:207], a[180:183], v[60:63]\n"
      "v_mfma_f32_16x16x32_bf16 v[56:59], a[208:211], a[184:187], v[56:59]\n"
      "v_exp_f32 v232, v40\n"
      "v_mfma_f32_16x16x32_bf16 v[60:63], a[212:215], a[184:187], v[60:63]\n"
      "s_waitcnt vmcnt(12) lgkmcnt(0)\n"
      "s_barrier\n"
      "v_mfma_f32_16x16x32_bf16 v[56:59], a[216:219], a[188:191], v[56:59]\n"
      "v_exp_f32 v233, v41\n"
      "v_mfma_f32_16x16x32_bf16 v[60:63], a[220:223], a[188:191], v[60:63]\n"
      "v_cvt_pk_bf16_f32 v116, v232, v233\n"
      "s_cbranch_vccnz STUB38_%=\n"
      "BACK38_%=:\n"
      "v_mfma_f32_16x16x32_bf16 a[0:3], a[224:227], v[96:99], a[0:3]\n"
      "v_exp_f32 v234, v42\n"
      "v_mfma_f32_16x16x32_bf16 a[4:7], a[228:231], v[96:99], a[4:7]\n"
      "ds_read_b128 a[192:195], v192 offset:0\n"
      "v_mfma_f32_16x16x32_bf16 v[212:215], v[228:231], v[112:115], v[80:83]\n"
      "v_exp_f32 v235, v43\n"
      "v_mfma_f32_16x16x32_bf16 a[8:11], a[232:235], v[96:99], a[8:11]\n"
      "ds_read_b128 a[196:199], v192 offset:4096\n"
      "v_cvt_pk_bf16_f32 v117, v234, v235\n"
      "v_mfma_f32_16x16x32_bf16 a[12:15], a[236:239], v[96:99], a[12:15]\n"
      "v_exp_f32 v236, v44\n"
      "v_mfma_f32_16x16x32_bf16 a[16:19], a[240:243], v[96:99], a[16:19]\n"
      "ds_read_b128 a[200:203], v193 offset:0\n"
      "v_mfma_f32_16x16x32_bf16 a[20:23], a[244:247], v[96:99], a[20:23]\n"
      "v_exp_f32 v237, v45\n"
      "v_mfma_f32_16x16x32_bf16 a[24:27], a[248:251], v[96:99], a[24:27]\n"
      "ds_read_b128 a[204:207], v193 offset:4096\n"
      "v_cvt_pk_bf16_f32 v118, v236, v237\n"
      "v_mfma_f32_16x16x32_bf16 a[28:31], a[252:255], v[96:99], a[28:31]\n"
      "v_exp_f32 v238, v46\n"
      "v_mfma_f32_16x16x32_bf16 a[32:35], a[224:227], v[100:103], a[32:35]\n"
      "ds_read_b128 a[208:211], v194 offset:0\n"
      "v_mfma_f32_16x16x32_bf16 a[36:39], a[228:231], v[100:103], a[36:39]\n"
      "v_exp_f32 v239, v47\n"
      "v_mfma_f32_16x16x32_bf16 a[40:43], a[232:235], v[100:103], a[40:43]\n"
      "ds_read_b128 a[212:215], v194 offset:4096\n"
      "v_cvt_pk_bf16_f32 v119, v238, v239\n"
      "v_mfma_f32_16x16x32_bf16 a[44:47], a[236:239], v[100:103], a[44:47]\n"
      "ds_read_b128 a[216:219], v195 offset:0\n"
      "v_mfma_f32_16x16x32_bf16 a[48:51], a[240:243], v[100:103], a[48:51]\n"
      "v_exp_f32 v232, v48\n"
      "v_mfma_f32_16x16x32_bf16 v[216:219], v[228:231], v[116:119], v[84:87]\n"
      "ds_read_b128 a[220:223], v195 offset:4096\n"
      "v_mfma_f32_16x16x32_bf16 a[52:55], a[244:247], v[100:103], a[52:55]\n"
      "v_exp_f32 v233, v49\n"
      "v_mfma_f32_16x16x32_bf16 a[56:59], a[248:251], v[100:103], a[56:59]\n"
      "v_cvt_pk_bf16_f32 v120, v232, v233\n"
      "v_mfma_f32_16x16x32_bf16 a[60:63], a[252:255], v[100:103], a[60:63]\n"
      "s_add_u32 m0, s78, 98304\n"
      "v_exp_f32 v234, v50\n"
      "v_mfma_f32_16x16x32_bf16 a[64:67], a[224:227], v[104:107], a[64:67]\n"
      "buffer_load_dwordx4 v208, s[44:47], s69 offen offset:0 nt lds\n"
      "v_mfma_f32_16x16x32_bf16 a[68:71], a[228:231], v[104:107], a[68:71]\n"
      "v_exp_f32 v235, v51\n"
      "v_mfma_f32_16x16x32_bf16 a[72:75], a[232:235], v[104:107], a[72:75]\n"
      "v_cvt_pk_bf16_f32 v121, v234, v235\n"
      "v_mfma_f32_16x16x32_bf16 a[76:79], a[236:239], v[104:107], a[76:79]\n"
      "v_exp_f32 v236, v52\n"
      "v_mfma_f32_16x16x32_bf16 a[80:83], a[240:243], v[104:107], a[80:83]\n"
      "buffer_load_dwordx4 v209, s[44:47], s69 offen offset:1024 nt lds\n"
      "v_mfma_f32_16x16x32_bf16 a[84:87], a[244:247], v[104:107], a[84:87]\n"
      "v_exp_f32 v237, v53\n"
      "v_mfma_f32_16x16x32_bf16 a[88:91], a[248:251], v[104:107], a[88:91]\n"
      "v_cvt_pk_bf16_f32 v122, v236, v237\n"
      "v_mfma_f32_16x16x32_bf16 a[92:95], a[252:255], v[104:107], a[92:95]\n"
      "v_exp_f32 v238, v54\n"
      "v_mfma_f32_16x16x32_bf16 a[96:99], a[224:227], v[108:111], a[96:99]\n"
      "buffer_load_dwordx4 v208, s[44:47], s69 offen offset:2048 nt lds\n"
      "v_mfma_f32_16x16x32_bf16 a[100:103], a[228:231], v[108:111], a[100:103]\n"
      "v_exp_f32 v239, v55\n"
      "v_mfma_f32_16x16x32_bf16 a[104:107], a[232:235], v[108:111], a[104:107]\n"
      "v_cvt_pk_bf16_f32 v123, v238, v239\n"
      "v_mfma_f32_16x16x32_bf16 a[108:111], a[236:239], v[108:111], a[108:111]\n"
      "buffer_load_dwordx4 v209, s[44:47], s69 offen offset:3072 nt lds\n"
      "v_mfma_f32_16x16x32_bf16 a[112:115], a[240:243], v[108:111], a[112:115]\n"
      "s_add_u32 s69, s69, 16384\n"
      "v_exp_f32 v232, v56\n"
      "v_mfma_f32_16x16x32_bf16 v[220:223], v[228:231], v[120:123], v[88:91]\n"
      "v_mfma_f32_16x16x32_bf16 a[116:119], a[244:247], v[108:111], a[116:119]\n"
      "v_exp_f32 v233, v57\n"
      "v_mfma_f32_16x16x32_bf16 a[120:123], a[248:251], v[108:111], a[120:123]\n"
      "s_waitcnt lgkmcnt(0)\n"
      "v_cvt_pk_bf16_f32 v124, v232, v233\n"
      "v_mfma_f32_16x16x32_bf16 a[124:127], a[252:255], v[108:111], a[124:127]\n"
      "v_exp_f32 v234, v58\n"
      "s_sub_u32 s70, s70, 1\n"
      "s_cbranch_scc0 L01_%=\n"
      "s_mov_b32 s75, 1\n"
      "ds_read_b64_tr_b16 a[224:225], v200 offset:57344\n"
      "ds_read_b64_tr_b16 a[226:227], v200 offset:61440\n"
      "v_exp_f32 v235, v59\n"
      "s_nop 1\n"
      "ds_read_b64_tr_b16 a[228:229], v201 offset:57344\n"
      "v_cvt_pk_bf16_f32 v125, v234, v235\n"
      "s_nop 1\n"
      "v_exp_f32 v236, v60\n"
      "s_nop 1\n"
      "ds_read_b64_tr_b16 a[230:231], v201 offset:61440\n"
      "ds_read_b64_tr_b16 a[232:233], v202 offset:57344\n"
      "v_exp_f32 v237, v61\n"
      "s_nop 1\n"
      "ds_read_b64_tr_b16 a[234:235], v202 offset:61440\n"
      "v_cvt_pk_bf16_f32 v126, v236, v237\n"
      "s_nop 1\n"
      "v_exp_f32 v238, v62\n"
      "s_nop 1\n"
      "ds_read_b64_tr_b16 a[236:237], v203 offset:57344\n"
      "ds_read_b64_tr_b16 a[238:239], v203 offset:61440\n"
      "v_exp_f32 v239, v63\n"
      "s_nop 1\n"
      "ds_read_b64_tr_b16 a[240:241], v204 offset:57344\n"
      "v_cvt_pk_bf16_f32 v127, v238, v239\n"
      "s_nop 1\n"
      "ds_read_b64_tr_b16 a[242:243], v204 offset:61440\n"
      "ds_read_b64_tr_b16 a[244:245], v205 offset:57344\n"
      "v_mfma_f32_16x16x32_bf16 v[224:227], v[228:231], v[124:127], v[92:95]\n"
      "ds_read_b64_tr_b16 a[246:247], v205 offset:61440\n"
      "ds_read_b64_tr_b16 a[248:249], v206 offset:57344\n"
      "ds_read_b64_tr_b16 a[250:251], v206 offset:61440\n"
      "s_nop 3\n"
      "ds_read_b64_tr_b16 a[252:253], v207 offset:57344\n"
      "ds_read_b64_tr_b16 a[254:255], v207 offset:61440\n"
      "v_max3_f32 v240, v212, v216, v220\n"
      "v_max_f32 v240, v240, v224\n"
      "s_nop 1\n"
      "v_cmp_nge_f32 vcc, s74, v240\n"
      "s_nop 1\n"
      "s_waitcnt lgkmcnt(0)\n"
      "s_cbranch_vccnz STUB39_%=\n"
      "BACK39_%=:\n"
      "v_readfirstlane_b32 s94, v185\n"
      "s_mov_b64 s[40:41], %8\n"
      "s_and_b32 s41, s41, 0xffff\n"
      "s_mov_b32 s42, 1048576\n"
      "s_mov_b64 s[44:45], %9\n"
      "s_and_b32 s45, s45, 0xffff\n"
      "s_mov_b32 s46, 655360\n"
      "s_mov_b64 s[48:49], %10\n"
      "s_and_b32 s49, s49, 0xffff\n"
      "s_mov_b32 s50, 325280\n"
      "s_lshl_b32 s68, s80, 13\n"
      "s_mul_i32 s69, s80, 5120\n"
      "v_add_u32 v248, 4096, v252\n"
      "s_add_u32 s87, s80, 1\n"
      "s_and_b32 s87, s87, 3\n"
      "s_lshl_b32 s87, s87, 11\n"
      "v_add_u32 v244, s87, v252\n"
      "s_add_u32 s87, s80, 2\n"
      "s_and_b32 s87, s87, 3\n"
      "s_lshl_b32 s87, s87, 11\n"
      "v_add_u32 v245, s87, v252\n"
      "s_add_u32 s87, s80, 3\n"
      "s_and_b32 s87, s87, 3\n"
      "s_lshl_b32 s87, s87, 11\n"
      "v_add_u32 v246, s87, v252\n"
      "s_add_u32 s87, s80, 4\n"
      "s_and_b32 s87, s87, 3\n"
      "s_lshl_b32 s87, s87, 11\n"
      "v_add_u32 v247, s87, v252\n"
      "v_cmp_ne_u32 vcc, s94, v186\n"
      "v_cmp_ne_u32 s[92:93], s94, v187\n"
      "s_or_b64 vcc, vcc, s[92:93]\n"
      "s_cmp_eq_u64 vcc, 0\n"
      "s_cbranch_scc0 SPIN0_%=\n"
      "SPB0_%=:\n"
      "global_load_dwordx2 v[238:239], v184, %11 sc1\n"
      "v_mfma_f32_16x16x32_bf16 a[0:3], a[224:227], v[112:115], a[0:3]\n"
      "s_add_u32 s83, s69, 0\n"
      "buffer_load_dwordx4 v[32:35], v252, s[44:47], s83 offen offset:0 sc1\n"
      "buffer_load_dwordx4 v[36:39], v252, s[44:47], s83 offen offset:1024 sc1\n"
      "buffer_load_dwordx4 v[40:43], v252, s[44:47], s83 offen offset:2048 sc1\n"
      "buffer_load_dwordx4 v[44:47], v252, s[44:47], s83 offen offset:3072 sc1\n"
      "buffer_load_dwordx4 v[48:51], v248, s[44:47], s83 offen offset:0 sc1\n"
      "v_mfma_f32_16x16x32_bf16 a[4:7], a[228:231], v[112:115], a[4:7]\n"
      "s_add_u32 s84, s68, 0\n"
      "buffer_load_dwordx4 v[0:3], v244, s[40:43], s84 offen offset:0 sc1\n"
      "buffer_load_dwordx4 v[4:7], v244, s[40:43], s84 offen offset:1024 sc1\n"
      "buffer_load_dwordx4 v[8:11], v245, s[40:43], s84 offen offset:0 sc1\n"
      "v_rcp_f32 v214, v212\n"
      "v_mfma_f32_16x16x32_bf16 a[8:11], a[232:235], v[112:115], a[8:11]\n"
      "v_rcp_f32 v218, v216\n"
      "v_rcp_f32 v222, v220\n"
      "v_rcp_f32 v226, v224\n"
      "v_mfma_f32_16x16x32_bf16 a[12:15], a[236:239], v[112:115], a[12:15]\n"
      "s_nop 0\n"
      "v_mov_b32 v215, v214\n"
      "v_mov_b32 v219, v218\n"
      "v_mov_b32 v223, v222\n"
      "v_mov_b32 v227, v226\n"
      "v_mfma_f32_16x16x32_bf16 a[16:19], a[240:243], v[112:115], a[16:19]\n"
      "v_mfma_f32_16x16x32_bf16 a[20:23], a[244:247], v[112:115], a[20:23]\n"
      "v_mfma_f32_16x16x32_bf16 a[24:27], a[248:251], v[112:115], a[24:27]\n"
      "v_mfma_f32_16x16x32_bf16 a[28:31], a[252:255], v[112:115], a[28:31]\n"
      "v_mfma_f32_16x16x32_bf16 a[32:35], a[224:227], v[116:119], a[32:35]\n"
      "v_mfma_f32_16x16x32_bf16 a[36:39], a[228:231], v[116:119], a[36:39]\n"
      "v_mfma_f32_16x16x32_bf16 a[40:43], a[232:235], v[116:119], a[40:43]\n"
      "v_mfma_f32_16x16x32_bf16 a[44:47], a[236:239], v[116:119], a[44:47]\n"
      "v_mfma_f32_16x16x32_bf16 a[48:51], a[240:243], v[116:119], a[48:51]\n"
      "v_mfma_f32_16x16x32_bf16 a[52:55], a[244:247], v[116:119], a[52:55]\n"
      "v_accvgpr_read_b32 v128, a0\n"
      "v_accvgpr_read_b32 v129, a1\n"
      "v_accvgpr_read_b32 v130, a2\n"
      "v_accvgpr_read_b32 v131, a3\n"
      "v_accvgpr_read_b32 v132, a4\n"
      "v_accvgpr_read_b32 v133, a5\n"
      "v_mfma_f32_16x16x32_bf16 a[56:59], a[248:251], v[116:119], a[56:59]\n"
      "v_accvgpr_read_b32 v134, a6\n"
      "v_accvgpr_read_b32 v135, a7\n"
      "buffer_load_dwordx4 v[12:15], v245, s[40:43], s84 offen offset:1024 sc1\n"
      "buffer_load_dwordx4 v[16:19], v246, s[40:43], s84 offen offset:0 sc1\n"
      "v_pk_mul_f32 v[128:129], v[128:129], v[214:215]\n"
      "v_mfma_f32_16x16x32_bf16 a[60:63], a[252:255], v[116:119], a[60:63]\n"
      "v_pk_mul_f32 v[130:131], v[130:131], v[214:215]\n"
      "v_pk_mul_f32 v[132:133], v[132:133], v[214:215]\n"
      "v_pk_mul_f32 v[134:135], v[134:135], v[214:215]\n"
      "v_mfma_f32_16x16x32_bf16 a[64:67], a[224:227], v[120:123], a[64:67]\n"
      "v_cvt_pk_bf16_f32 v144, v128, v129\n"
      "v_cvt_pk_bf16_f32 v145, v130, v131\n"
      "v_cvt_pk_bf16_f32 v146, v132, v133\n"
      "v_cvt_pk_bf16_f32 v147, v134, v135\n"
      "s_mov_b32 s87, 32768\n"
      "buffer_store_dwordx4 v[144:147], v254, s[56:59], s87 offen offset:0 sc1\n"
      "v_mfma_f32_16x16x32_bf16 a[68:71], a[228:231], v[120:123], a[68:71]\n"
      "v_accvgpr_read_b32 v136, a8\n"
      "v_accvgpr_read_b32 v137, a9\n"
      "v_accvgpr_read_b32 v138, a10\n"
      "v_accvgpr_read_b32 v139, a11\n"
      "v_accvgpr_read_b32 v140, a12\n"
      "v_accvgpr_read_b32 v141, a13\n"
      "v_mfma_f32_16x16x32_bf16 a[72:75], a[232:235], v[120:123], a[72:75]\n"
      "v_accvgpr_read_b32 v142, a14\n"
      "v_accvgpr_read_b32 v143, a15\n"
      "buffer_load_dwordx4 v[20:23], v246, s[40:43], s84 offen offset:1024 sc1\n"
      "buffer_load_dwordx4 v[24:27], v247, s[40:43], s84 offen offset:0 sc1\n"
      "v_pk_mul_f32 v[136:137], v[136:137], v[214:215]\n"
      "v_mfma_f32_16x16x32_bf16 a[76:79], a[236:239], v[120:123], a[76:79]\n"
      "v_pk_mul_f32 v[138:139], v[138:139], v[214:215]\n"
      "v_pk_mul_f32 v[140:141], v[140:141], v[214:215]\n"
      "v_pk_mul_f32 v[142:143], v[142:143], v[214:215]\n"
      "v_mfma_f32_16x16x32_bf16 a[80:83], a[240:243], v[120:123], a[80:83]\n"
      "v_cvt_pk_bf16_f32 v148, v136, v137\n"
      "v_cvt_pk_bf16_f32 v149, v138, v139\n"
      "v_cvt_pk_bf16_f32 v150, v140, v141\n"
      "v_cvt_pk_bf16_f32 v151, v142, v143\n"
      "s_mov_b32 s87, 40960\n"
      "buffer_store_dwordx4 v[148:151], v254, s[56:59], s87 offen offset:0 sc1\n"
      "v_mfma_f32_16x16x32_bf16 a[84:87], a[244:247], v[120:123], a[84:87]\n"
      "v_accvgpr_read_b32 v128, a16\n"
      "v_accvgpr_read_b32 v129, a17\n"
      "v_accvgpr_read_b32 v130, a18\n"
      "v_accvgpr_read_b32 v131, a19\n"
      "v_accvgpr_read_b32 v132, a20\n"
      "v_accvgpr_read_b32 v133, a21\n"
      "v_mfma_f32_16x16x32_bf16 a[88:91], a[248:251], v[120:123], a[88:91]\n"
      "v_accvgpr_read_b32 v134, a22\n"
      "v_accvgpr_read_b32 v135, a23\n"
      "buffer_load_dwordx4 v[28:31], v247, s[40:43], s84 offen offset:1024 sc1\n"
      "s_add_u32 s83, s69, 40960\n"
      "buffer_load_dwordx4 v[84:87], v252, s[44:47], s83 offen offset:0 sc1\n"
      "v_mfma_f32_16x16x32_bf16 a[92:95], a[252:255], v[120:123], a[92:95]\n"
      "v_pk_mul_f32 v[128:129], v[128:129], v[214:215]\n"
      "v_pk_mul_f32 v[130:131], v[130:131], v[214:215]\n"
      "v_pk_mul_f32 v[132:133], v[132:133], v[214:215]\n"
      "v_mfma_f32_16x16x32_bf16 a[96:99], a[224:227], v[124:127], a[96:99]\n"
      "v_pk_mul_f32 v[134:135], v[134:135], v[214:215]\n"
      "v_cvt_pk_bf16_f32 v152, v128, v129\n"
      "v_cvt_pk_bf16_f32 v153, v130, v131\n"
      "v_cvt_pk_bf16_f32 v154, v132, v133\n"
      "v_cvt_pk_bf16_f32 v155, v134, v135\n"
      "v_mfma_f32_16x16x32_bf16 a[100:103], a[228:231], v[124:127], a[100:103]\n"
      "s_mov_b32 s87, 49152\n"
      "buffer_store_dwordx4 v[152:155], v254, s[56:59], s87 offen offset:0 sc1\n"
      "v_accvgpr_read_b32 v136, a24\n"
      "v_accvgpr_read_b32 v137, a25\n"
      "v_accvgpr_read_b32 v138, a26\n"
      "v_accvgpr_read_b32 v139, a27\n"
      "v_mfma_f32_16x16x32_bf16 a[104:107], a[232:235], v[124:127], a[104:107]\n"
      "v_accvgpr_read_b32 v140, a28\n"
      "v_accvgpr_read_b32 v141, a29\n"
      "v_accvgpr_read_b32 v142, a30\n"
      "v_accvgpr_read_b32 v143, a31\n"
      "buffer_load_dwordx4 v[88:91], v252, s[44:47], s83 offen offset:1024 sc1\n"
      "buffer_load_dwordx4 v[92:95], v252, s[44:47], s83 offen offset:2048 sc1\n"
      "v_mfma_f32_16x16x32_bf16 a[108:111], a[236:239], v[124:127], a[108:111]\n"
      "v_pk_mul_f32 v[136:137], v[136:137], v[214:215]\n"
      "v_pk_mul_f32 v[138:139], v[138:139], v[214:215]\n"
      "v_pk_mul_f32 v[140:141], v[140:141], v[214:215]\n"
      "v_mfma_f32_16x16x32_bf16 a[112:115], a[240:243], v[124:127], a[112:115]\n"
      "v_pk_mul_f32 v[142:143], v[142:143], v[214:215]\n"
      "v_cvt_pk_bf16_f32 v156, v136, v137\n"
      "v_cvt_pk_bf16_f32 v157, v138, v139\n"
      "v_cvt_pk_bf16_f32 v158, v140, v141\n"
      "v_cvt_pk_bf16_f32 v159, v142, v143\n"
      "v_mfma_f32_16x16x32_bf16 a[116:119], a[244:247], v[124:127], a[116:119]\n"
      "s_mov_b32 s87, 57344\n"
      "buffer_store_dwordx4 v[156:159], v254, s[56:59], s87 offen offset:0 sc1\n"
      "v_accvgpr_read_b32 v128, a32\n"
      "v_accvgpr_read_b32 v129, a33\n"
      "v_accvgpr_read_b32 v130, a34\n"
      "v_accvgpr_read_b32 v131, a35\n"
      "v_mfma_f32_16x16x32_bf16 a[120:123], a[248:251], v[124:127], a[120:123]\n"
      "v_accvgpr_read_b32 v132, a36\n"
      "v_accvgpr_read_b32 v133, a37\n"
      "v_accvgpr_read_b32 v134, a38\n"
      "v_accvgpr_read_b32 v135, a39\n"
      "buffer_load_dwordx4 v[96:99], v252, s[44:47], s83 offen offset:3072 sc1\n"
      "buffer_load_dwordx4 v[100:103], v248, s[44:47], s83 offen offset:0 sc1\n"
      "v_mfma_f32_16x16x32_bf16 a[124:127], a[252:255], v[124:127], a[124:127]\n"
      "v_pk_mul_f32 v[128:129], v[128:129], v[218:219]\n"
      "v_pk_mul_f32 v[130:131], v[130:131], v[218:219]\n"
      "v_pk_mul_f32 v[132:133], v[132:133], v[218:219]\n"
      "v_pk_mul_f32 v[134:135], v[134:135], v[218:219]\n"
      "v_cvt_pk_bf16_f32 v144, v128, v129\n"
      "v_cvt_pk_bf16_f32 v145, v130, v131\n"
      "v_cvt_pk_bf16_f32 v146, v132, v133\n"
      "v_cvt_pk_bf16_f32 v147, v134, v135\n"
      "s_mov_b32 s87, 32768\n"
      "buffer_store_dwordx4 v[144:147], v254, s[56:59], s87 offen offset:1024 sc1\n"
      "v_accvgpr_read_b32 v136, a40\n"
      "v_accvgpr_read_b32 v137, a41\n"
      "v_accvgpr_read_b32 v138, a42\n"
      "v_accvgpr_read_b32 v139, a43\n"
      "v_accvgpr_read_b32 v140, a44\n"
      "v_accvgpr_read_b32 v141, a45\n"
      "v_accvgpr_read_b32 v142, a46\n"
      "v_accvgpr_read_b32 v143, a47\n"
      "s_add_u32 s84, s68, 65536\n"
      "buffer_load_dwordx4 v[52:55], v244, s[40:43], s84 offen offset:0 sc1\n"
      "buffer_load_dwordx4 v[56:59], v244, s[40:43], s84 offen offset:1024 sc1\n"
      "v_pk_mul_f32 v[136:137], v[136:137], v[218:219]\n"
      "v_pk_mul_f32 v[138:139], v[138:139], v[218:219]\n"
      "v_pk_mul_f32 v[140:141], v[140:141], v[218:219]\n"
      "v_pk_mul_f32 v[142:143], v[142:143], v[218:219]\n"
      "v_cvt_pk_bf16_f32 v148, v136, v137\n"
      "v_cvt_pk_bf16_f32 v149, v138, v139\n"
      "v_cvt_pk_bf16_f32 v150, v140, v141\n"
      "v_cvt_pk_bf16_f32 v151, v142, v143\n"
      "s_mov_b32 s87, 40960\n"
      "buffer_store_dwordx4 v[148:151], v254, s[56:59], s87 offen offset:1024 sc1\n"
      "v_accvgpr_read_b32 v128, a48\n"
      "v_accvgpr_read_b32 v129, a49\n"
      "v_accvgpr_read_b32 v130, a50\n"
      "v_accvgpr_read_b32 v131, a51\n"
      "v_accvgpr_read_b32 v132, a52\n"
      "v_accvgpr_read_b32 v133, a53\n"
      "v_accvgpr_read_b32 v134, a54\n"
      "v_accvgpr_read_b32 v135, a55\n"
      "buffer_load_dwordx4 v[60:63], v245, s[40:43], s84 offen offset:0 sc1\n"
      "buffer_load_dwordx4 v[64:67], v245, s[40:43], s84 offen offset:1024 sc1\n"
      "v_pk_mul_f32 v[128:129], v[128:129], v[218:219]\n"
      "v_pk_mul_f32 v[130:131], v[130:131], v[218:219]\n"
      "v_pk_mul_f32 v[132:133], v[132:133], v[218:219]\n"
      "v_pk_mul_f32 v[134:135], v[134:135], v[218:219]\n"
      "v_cvt_pk_bf16_f32 v152, v128, v129\n"
      "v_cvt_pk_bf16_f32 v153, v130, v131\n"
      "v_cvt_pk_bf16_f32 v154, v132, v133\n"
      "v_cvt_pk_bf16_f32 v155, v134, v135\n"
      "s_mov_b32 s87, 49152\n"
      "buffer_store_dwordx4 v[152:155], v254, s[56:59], s87 offen offset:1024 sc1\n"
      "v_accvgpr_read_b32 v136, a56\n"
      "v_accvgpr_read_b32 v137, a57\n"
      "v_accvgpr_read_b32 v138, a58\n"
      "v_accvgpr_read_b32 v139, a59\n"
      "v_accvgpr_read_b32 v140, a60\n"
      "v_accvgpr_read_b32 v141, a61\n"
      "v_accvgpr_read_b32 v142, a62\n"
      "v_accvgpr_read_b32 v143, a63\n"
      "buffer_load_dwordx4 v[68:71], v246, s[40:43], s84 offen offset:0 sc1\n"
      "buffer_load_dwordx4 v[72:75], v246, s[40:43], s84 offen offset:1024 sc1\n"
      "v_pk_mul_f32 v[136:137], v[136:137], v[218:219]\n"
      "v_pk_mul_f32 v[138:139], v[138:139], v[218:219]\n"
      "v_pk_mul_f32 v[140:141], v[140:141], v[218:219]\n"
      "v_pk_mul_f32 v[142:143], v[142:143], v[218:219]\n"
      "v_cvt_pk_bf16_f32 v156, v136, v137\n"
      "v_cvt_pk_bf16_f32 v157, v138, v139\n"
      "v_cvt_pk_bf16_f32 v158, v140, v141\n"
      "v_cvt_pk_bf16_f32 v159, v142, v143\n"
      "s_mov_b32 s87, 57344\n"
      "buffer_store_dwordx4 v[156:159], v254, s[56:59], s87 offen offset:1024 sc1\n"
      "v_accvgpr_read_b32 v128, a64\n"
      "v_accvgpr_read_b32 v129, a65\n"
      "v_accvgpr_read_b32 v130, a66\n"
      "v_accvgpr_read_b32 v131, a67\n"
      "v_accvgpr_read_b32 v132, a68\n"
      "v_accvgpr_read_b32 v133, a69\n"
      "v_accvgpr_read_b32 v134, a70\n"
      "v_accvgpr_read_b32 v135, a71\n"
      "buffer_load_dwordx4 v[76:79], v247, s[40:43], s84 offen offset:0 sc1\n"
      "buffer_load_dwordx4 v[80:83], v247, s[40:43], s84 offen offset:1024 sc1\n"
      "v_pk_mul_f32 v[128:129], v[128:129], v[222:223]\n"
      "v_pk_mul_f32 v[130:131], v[130:131], v[222:223]\n"
      "v_pk_mul_f32 v[132:133], v[132:133], v[222:223]\n"
      "v_pk_mul_f32 v[134:135], v[134:135], v[222:223]\n"
      "v_cvt_pk_bf16_f32 v144, v128, v129\n"
      "v_cvt_pk_bf16_f32 v145, v130, v131\n"
      "v_cvt_pk_bf16_f32 v146, v132, v133\n"
      "v_cvt_pk_bf16_f32 v147, v134, v135\n"
      "v_accvgpr_write_b32 a172, v144\n"
      "v_accvgpr_write_b32 a173, v145\n"
      "v_accvgpr_write_b32 a174, v146\n"
      "v_accvgpr_write_b32 a175, v147\n"
      "v_accvgpr_read_b32 v136, a72\n"
      "v_accvgpr_read_b32 v137, a73\n"
      "v_accvgpr_read_b32 v138, a74\n"
      "v_accvgpr_read_b32 v139, a75\n"
      "v_accvgpr_read_b32 v140, a76\n"
      "v_accvgpr_read_b32 v141, a77\n"
      "v_accvgpr_read_b32 v142, a78\n"
      "v_accvgpr_read_b32 v143, a79\n"
      "v_pk_mul_f32 v[136:137], v[136:137], v[222:223]\n"
      "v_pk_mul_f32 v[138:139], v[138:139], v[222:223]\n"
      "v_pk_mul_f32 v[140:141], v[140:141], v[222:223]\n"
      "v_pk_mul_f32 v[142:143], v[142:143], v[222:223]\n"
      "v_cvt_pk_bf16_f32 v148, v136, v137\n"
      "v_cvt_pk_bf16_f32 v149, v138, v139\n"
      "v_cvt_pk_bf16_f32 v150, v140, v141\n"
      "v_cvt_pk_bf16_f32 v151, v142, v143\n"
      "v_accvgpr_write_b32 a176, v148\n"
      "v_accvgpr_write_b32 a177, v149\n"
      "v_accvgpr_write_b32 a178, v150\n"
      "v_accvgpr_write_b32 a179, v151\n"
      "v_accvgpr_read_b32 v128, a80\n"
      "v_accvgpr_read_b32 v129, a81\n"
      "v_accvgpr_read_b32 v130, a82\n"
      "v_accvgpr_read_b32 v131, a83\n"
      "v_accvgpr_read_b32 v132, a84\n"
      "v_accvgpr_read_b32 v133, a85\n"
      "v_accvgpr_read_b32 v134, a86\n"
      "v_accvgpr_read_b32 v135, a87\n"
      "v_pk_mul_f32 v[128:129], v[128:129], v[222:223]\n"
      "v_pk_mul_f32 v[130:131], v[130:131], v[222:223]\n"
      "v_pk_mul_f32 v[132:133], v[132:133], v[222:223]\n"
      "v_pk_mul_f32 v[134:135], v[134:135], v[222:223]\n"
      "v_cvt_pk_bf16_f32 v152, v128, v129\n"
      "v_cvt_pk_bf16_f32 v153, v130, v131\n"
      "v_cvt_pk_bf16_f32 v154, v132, v133\n"
      "v_cvt_pk_bf16_f32 v155, v134, v135\n"
      "v_accvgpr_write_b32 a180, v152\n"
      "v_accvgpr_write_b32 a181, v153\n"
      "v_accvgpr_write_b32 a182, v154\n"
      "v_accvgpr_write_b32 a183, v155\n"
      "v_accvgpr_read_b32 v136, a88\n"
      "v_accvgpr_read_b32 v137, a89\n"
      "v_accvgpr_read_b32 v138, a90\n"
      "v_accvgpr_read_b32 v139, a91\n"
      "v_accvgpr_read_b32 v140, a92\n"
      "v_accvgpr_read_b32 v141, a93\n"
      "v_accvgpr_read_b32 v142, a94\n"
      "v_accvgpr_read_b32 v143, a95\n"
      "v_pk_mul_f32 v[136:137], v[136:137], v[222:223]\n"
      "v_pk_mul_f32 v[138:139], v[138:139], v[222:223]\n"
      "v_pk_mul_f32 v[140:141], v[140:141], v[222:223]\n"
      "v_pk_mul_f32 v[142:143], v[142:143], v[222:223]\n"
      "v_cvt_pk_bf16_f32 v156, v136, v137\n"
      "v_cvt_pk_bf16_f32 v157, v138, v139\n"
      "v_cvt_pk_bf16_f32 v158, v140, v141\n"
      "v_cvt_pk_bf16_f32 v159, v142, v143\n"
      "v_accvgpr_write_b32 a184, v156\n"
      "v_accvgpr_write_b32 a185, v157\n"
      "v_accvgpr_write_b32 a186, v158\n"
      "v_accvgpr_write_b32 a187, v159\n"
      "s_nop 7\n"
      "v_accvgpr_read_b32 v128, a96\n"
      "v_accvgpr_read_b32 v129, a97\n"
      "v_accvgpr_read_b32 v130, a98\n"
      "v_accvgpr_read_b32 v131, a99\n"
      "v_accvgpr_read_b32 v132, a100\n"
      "v_accvgpr_read_b32 v133, a101\n"
      "v_accvgpr_read_b32 v134, a102\n"
      "v_accvgpr_read_b32 v135, a103\n"
      "v_pk_mul_f32 v[128:129], v[128:129], v[226:227]\n"
      "v_pk_mul_f32 v[130:131], v[130:131], v[226:227]\n"
      "v_pk_mul_f32 v[132:133], v[132:133], v[226:227]\n"
      "v_pk_mul_f32 v[134:135], v[134:135], v[226:227]\n"
      "v_cvt_pk_bf16_f32 v144, v128, v129\n"
      "v_cvt_pk_bf16_f32 v145, v130, v131\n"
      "v_cvt_pk_bf16_f32 v146, v132, v133\n"
      "v_cvt_pk_bf16_f32 v147, v134, v135\n"
      "v_accvgpr_write_b32 a188, v144\n"
      "v_accvgpr_write_b32 a189, v145\n"
      "v_accvgpr_write_b32 a190, v146\n"
      "v_accvgpr_write_b32 a191, v147\n"
      "v_accvgpr_read_b32 v136, a104\n"
      "v_accvgpr_read_b32 v137, a105\n"
      "v_accvgpr_read_b32 v138, a106\n"
      "v_accvgpr_read_b32 v139, a107\n"
      "v_accvgpr_read_b32 v140, a108\n"
      "v_accvgpr_read_b32 v141, a109\n"
      "v_accvgpr_read_b32 v142, a110\n"
      "v_accvgpr_read_b32 v143, a111\n"
      "v_pk_mul_f32 v[136:137], v[136:137], v[226:227]\n"
      "v_pk_mul_f32 v[138:139], v[138:139], v[226:227]\n"
      "v_pk_mul_f32 v[140:141], v[140:141], v[226:227]\n"
      "v_pk_mul_f32 v[142:143], v[142:143], v[226:227]\n"
      "v_cvt_pk_bf16_f32 v148, v136, v137\n"
      "v_cvt_pk_bf16_f32 v149, v138, v139\n"
      "v_cvt_pk_bf16_f32 v150, v140, v141\n"
      "v_cvt_pk_bf16_f32 v151, v142, v143\n"
      "v_accvgpr_write_b32 a192, v148\n"
      "v_accvgpr_write_b32 a193, v149\n"
      "v_accvgpr_write_b32 a194, v150\n"
      "v_accvgpr_write_b32 a195, v151\n"
      "v_accvgpr_read_b32 v128, a112\n"
      "v_accvgpr_read_b32 v129, a113\n"
      "v_accvgpr_read_b32 v130, a114\n"
      "v_accvgpr_read_b32 v131, a115\n"
      "v_accvgpr_read_b32 v132, a116\n"
      "v_accvgpr_read_b32 v133, a117\n"
      "v_accvgpr_read_b32 v134, a118\n"
      "v_accvgpr_read_b32 v135, a119\n"
      "v_pk_mul_f32 v[128:129], v[128:129], v[226:227]\n"
      "v_pk_mul_f32 v[130:131], v[130:131], v[226:227]\n"
      "v_pk_mul_f32 v[132:133], v[132:133], v[226:227]\n"
      "v_pk_mul_f32 v[134:135], v[134:135], v[226:227]\n"
      "v_cvt_pk_bf16_f32 v152, v128, v129\n"
      "v_cvt_pk_bf16_f32 v153, v130, v131\n"
      "v_cvt_pk_bf16_f32 v154, v132, v133\n"
      "v_cvt_pk_bf16_f32 v155, v134, v135\n"
      "v_accvgpr_write_b32 a196, v152\n"
      "v_accvgpr_write_b32 a197, v153\n"
      "v_accvgpr_write_b32 a198, v154\n"
      "v_accvgpr_write_b32 a199, v155\n"
      "v_accvgpr_read_b32 v136, a120\n"
      "v_accvgpr_read_b32 v137, a121\n"
      "v_accvgpr_read_b32 v138, a122\n"
      "v_accvgpr_read_b32 v139, a123\n"
      "v_accvgpr_read_b32 v140, a124\n"
      "v_accvgpr_read_b32 v141, a125\n"
      "v_accvgpr_read_b32 v142, a126\n"
      "v_accvgpr_read_b32 v143, a127\n"
      "v_pk_mul_f32 v[136:137], v[136:137], v[226:227]\n"
      "v_pk_mul_f32 v[138:139], v[138:139], v[226:227]\n"
      "v_pk_mul_f32 v[140:141], v[140:141], v[226:227]\n"
      "v_pk_mul_f32 v[142:143], v[142:143], v[226:227]\n"
      "v_cvt_pk_bf16_f32 v156, v136, v137\n"
      "v_cvt_pk_bf16_f32 v157, v138, v139\n"
      "v_cvt_pk_bf16_f32 v158, v140, v141\n"
      "v_cvt_pk_bf16_f32 v159, v142, v143\n"
      "v_accvgpr_write_b32 a200, v156\n"
      "v_accvgpr_write_b32 a201, v157\n"
      "v_accvgpr_write_b32 a202, v158\n"
      "v_accvgpr_write_b32 a203, v159\n"
      "s_add_u32 s83, s69, 81920\n"
      "buffer_load_dwordx4 v[136:139], v252, s[44:47], s83 offen offset:0 sc1\n"
      "buffer_load_dwordx4 v[140:143], v252, s[44:47], s83 offen offset:1024 sc1\n"
      "buffer_load_dwordx4 v[144:147], v252, s[44:47], s83 offen offset:2048 sc1\n"
      "buffer_load_dwordx4 v[148:151], v252, s[44:47], s83 offen offset:3072 sc1\n"
      "buffer_load_dwordx4 v[152:155], v248, s[44:47], s83 offen offset:0 sc1\n"
      "s_add_u32 s84, s68, 131072\n"
      "buffer_load_dwordx4 v[104:107], v244, s[40:43], s84 offen offset:0 sc1\n"
      "buffer_load_dwordx4 v[108:111], v244, s[40:43], s84 offen offset:1024 sc1\n"
      "buffer_load_dwordx4 v[112:115], v245, s[40:43], s84 offen offset:0 sc1\n"
      "buffer_load_dwordx4 v[116:119], v245, s[40:43], s84 offen offset:1024 sc1\n"
      "buffer_load_dwordx4 v[120:123], v246, s[40:43], s84 offen offset:0 sc1\n"
      "buffer_load_dwordx4 v[124:127], v246, s[40:43], s84 offen offset:1024 sc1\n"
      "buffer_load_dwordx4 v[128:131], v247, s[40:43], s84 offen offset:0 sc1\n"
      "buffer_load_dwordx4 v[132:135], v247, s[40:43], s84 offen offset:1024 sc1\n"
      "s_waitcnt vmcnt(40)\n"
      "v_cmp_ne_u64 s[92:93], v[188:189], v[238:239]\n"
      "v_mfma_f32_16x16x32_bf16 a[0:3], v[32:35], v[0:3], 0\n"
      "s_add_u32 s83, s69, 122880\n"
      "buffer_load_dwordx4 v[188:191], v252, s[44:47], s83 offen offset:0 sc1\n"
      "v_mfma_f32_16x16x32_bf16 a[4:7], v[36:39], v[0:3], 0\n"
      "v_mfma_f32_16x16x32_bf16 a[8:11], v[40:43], v[0:3], 0\n"
      "v_mfma_f32_16x16x32_bf16 a[12:15], v[44:47], v[0:3], 0\n"
      "buffer_load_dwordx4 v[192:195], v252, s[44:47], s83 offen offset:1024 sc1\n"
      "v_mfma_f32_16x16x32_bf16 a[16:19], v[48:51], v[0:3], 0\n"
      "s_mov_b32 s87, 32768\n"
      "buffer_store_dwordx4 a[172:175], v254, s[56:59], s87 offen offset:2048 sc1\n"
      "v_mfma_f32_16x16x32_bf16 a[20:23], v[32:35], v[4:7], 0\n"
      "v_mfma_f32_16x16x32_bf16 a[24:27], v[36:39], v[4:7], 0\n"
      "buffer_load_dwordx4 v[196:199], v252, s[44:47], s83 offen offset:2048 sc1\n"
      "v_mfma_f32_16x16x32_bf16 a[28:31], v[40:43], v[4:7], 0\n"
      "v_mfma_f32_16x16x32_bf16 a[32:35], v[44:47], v[4:7], 0\n"
      "v_mfma_f32_16x16x32_bf16 a[36:39], v[48:51], v[4:7], 0\n"
      "buffer_load_dwordx4 v[200:203], v252, s[44:47], s83 offen offset:3072 sc1\n"
      "s_waitcnt vmcnt(43)\n"
      "v_mfma_f32_16x16x32_bf16 a[40:43], v[32:35], v[8:11], 0\n"
      "v_mfma_f32_16x16x32_bf16 a[44:47], v[36:39], v[8:11], 0\n"
      "v_mfma_f32_16x16x32_bf16 a[48:51], v[40:43], v[8:11], 0\n"
      "buffer_load_dwordx4 v[204:207], v248, s[44:47], s83 offen offset:0 sc1\n"
      "v_mfma_f32_16x16x32_bf16 a[52:55], v[44:47], v[8:11], 0\n"
      "s_mov_b32 s87, 40960\n"
      "buffer_store_dwordx4 a[176:179], v254, s[56:59], s87 offen offset:2048 sc1\n"
      "v_mfma_f32_16x16x32_bf16 a[56:59], v[48:51], v[8:11], 0\n"
      "v_mfma_f32_16x16x32_bf16 a[60:63], v[32:35], v[12:15], 0\n"
      "s_add_u32 s84, s68, 196608\n"
      "buffer_load_dwordx4 v[156:159], v244, s[40:43], s84 offen offset:0 sc1\n"
      "v_mfma_f32_16x16x32_bf16 a[64:67], v[36:39], v[12:15], 0\n"
      "v_mfma_f32_16x16x32_bf16 a[68:71], v[40:43], v[12:15], 0\n"
      "v_mfma_f32_16x16x32_bf16 a[72:75], v[44:47], v[12:15], 0\n"
      "buffer_load_dwordx4 v[160:163], v244, s[40:43], s84 offen offset:1024 sc1\n"
      "v_mfma_f32_16x16x32_bf16 a[76:79], v[48:51], v[12:15], 0\n"
      "s_waitcnt vmcnt(44)\n"
      "v_mfma_f32_16x16x32_bf16 a[80:83], v[32:35], v[16:19], 0\n"
      "v_mfma_f32_16x16x32_bf16 a[84:87], v[36:39], v[16:19], 0\n"
      "buffer_load_dwordx4 v[164:167], v245, s[40:43], s84 offen offset:0 sc1\n"
      "v_mfma_f32_16x16x32_bf16 a[88:91], v[40:43], v[16:19], 0\n"
      "v_mfma_f32_16x16x32_bf16 a[92:95], v[44:47], v[16:19], 0\n"
      "v_mfma_f32_16x16x32_bf16 a[96:99], v[48:51], v[16:19], 0\n"
      "buffer_load_dwordx4 v[168:171], v245, s[40:43], s84 offen offset:1024 sc1\n"
      "v_mfma_f32_16x16x32_bf16 a[100:103], v[32:35], v[20:23], 0\n"
      "s_mov_b32 s87, 49152\n"
      "buffer_store_dwordx4 a[180:183], v254, s[56:59], s87 offen offset:2048 sc1\n"
      "v_mfma_f32_16x16x32_bf16 a[104:107], v[36:39], v[20:23], 0\n"
      "v_mfma_f32_16x16x32_bf16 a[108:111], v[40:43], v[20:23], 0\n"
      "buffer_load_dwordx4 v[172:175], v246, s[40:43], s84 offen offset:0 sc1\n"
      "v_mfma_f32_16x16x32_bf16 a[112:115], v[44:47], v[20:23], 0\n"
      "v_mfma_f32_16x16x32_bf16 a[116:119], v[48:51], v[20:23], 0\n"
      "s_waitcnt vmcnt(45)\n"
      "v_mfma_f32_16x16x32_bf16 a[120:123], v[32:35], v[24:27], 0\n"
      "buffer_load_dwordx4 v[176:179], v246, s[40:43], s84 offen offset:1024 sc1\n"
      "v_mfma_f32_16x16x32_bf16 a[124:127], v[36:39], v[24:27], 0\n"
      "v_mfma_f32_16x16x32_bf16 a[128:131], v[40:43], v[24:27], 0\n"
      "v_mfma_f32_16x16x32_bf16 a[132:135], v[44:47], v[24:27], 0\n"
      "buffer_load_dwordx4 v[180:183], v247, s[40:43], s84 offen offset:0 sc1\n"
      "v_mfma_f32_16x16x32_bf16 a[136:139], v[48:51], v[24:27], 0\n"
      "s_mov_b32 s87, 57344\n"
      "buffer_store_dwordx4 a[184:187], v254, s[56:59], s87 offen offset:2048 sc1\n"
      "v_mfma_f32_16x16x32_bf16 v[208:211], v[32:35], v[28:31], 0\n"
      "v_mfma_f32_16x16x32_bf16 v[212:215], v[36:39], v[28:31], 0\n"
      "buffer_load_dwordx4 v[184:187], v247, s[40:43], s84 offen offset:1024 sc1\n"
      "v_mfma_f32_16x16x32_bf16 v[216:219], v[40:43], v[28:31], 0\n"
      "v_mfma_f32_16x16x32_bf16 v[220:223], v[44:47], v[28:31], 0\n"
      "v_mfma_f32_16x16x32_bf16 v[224:227], v[48:51], v[28:31], 0\n"
      "s_waitcnt vmcnt(39)\n"
      "v_mfma_f32_16x16x32_bf16 a[0:3], v[84:87], v[52:55], a[0:3]\n"
      "s_add_u32 s83, s69, 163840\n"
      "buffer_load_dwordx4 v[32:35], v252, s[44:47], s83 offen offset:0 sc1\n"
      "v_mfma_f32_16x16x32_bf16 a[4:7], v[88:91], v[52:55], a[4:7]\n"
      "v_mfma_f32_16x16x32_bf16 a[8:11], v[92:95], v[52:55], a[8:11]\n"
      "v_mfma_f32_16x16x32_bf16 a[12:15], v[96:99], v[52:55], a[12:15]\n"
      "buffer_load_dwordx4 v[36:39], v252, s[44:47], s83 offen offset:1024 sc1\n"
      "v_mfma_f32_16x16x32_bf16 a[16:19], v[100:103], v[52:55], a[16:19]\n"
      "s_mov_b32 s87, 32768\n"
      "buffer_store_dwordx4 a[188:191], v254, s[56:59], s87 offen offset:3072 sc1\n"
      "v_mfma_f32_16x16x32_bf16 a[20:23], v[84:87], v[56:59], a[20:23]\n"
      "v_mfma_f32_16x16x32_bf16 a[24:27], v[88:91], v[56:59], a[24:27]\n"
      "buffer_load_dwordx4 v[40:43], v252, s[44:47], s83 offen offset:2048 sc1\n"
      "v_mfma_f32_16x16x32_bf16 a[28:31], v[92:95], v[56:59], a[28:31]\n"
      "v_mfma_f32_16x16x32_bf16 a[32:35], v[96:99], v[56:59], a[32:35]\n"
      "v_mfma_f32_16x16x32_bf16 a[36:39], v[100:103], v[56:59], a[36:39]\n"
      "buffer_load_dwordx4 v[44:47], v252, s[44:47], s83 offen offset:3072 sc1\n"
      "s_waitcnt vmcnt(41)\n"
      "v_mfma_f32_16x16x32_bf16 a[40:43], v[84:87], v[60:63], a[40:43]\n"
      "v_mfma_f32_16x16x32_bf16 a[44:47], v[88:91], v[60:63], a[44:47]\n"
      "v_mfma_f32_16x16x32_bf16 a[48:51], v[92:95], v[60:63], a[48:51]\n"
      "buffer_load_dwordx4 v[48:51], v248, s[44:47], s83 offen offset:0 sc1\n"
      "v_mfma_f32_16x16x32_bf16 a[52:55], v[96:99], v[60:63], a[52:55]\n"
      "s_mov_b32 s87, 40960\n"
      "buffer_store_dwordx4 a[192:195], v254, s[56:59], s87 offen offset:3072 sc1\n"
      "v_mfma_f32_16x16x32_bf16 a[56:59], v[100:103], v[60:63], a[56:59]\n"
      "v_mfma_f32_16x16x32_bf16 a[60:63], v[84:87], v[64:67], a[60:63]\n"
      "s_add_u32 s84, s68, 262144\n"
      "buffer_load_dwordx4 v[0:3], v244, s[40:43], s84 offen offset:0 sc1\n"
      "v_mfma_f32_16x16x32_bf16 a[64:67], v[88:91], v[64:67], a[64:67]\n"
      "v_mfma_f32_16x16x32_bf16 a[68:71], v[92:95], v[64:67], a[68:71]\n"
      "v_mfma_f32_16x16x32_bf16 a[72:75], v[96:99], v[64:67], a[72:75]\n"
      "buffer_load_dwordx4 v[4:7], v244, s[40:43], s84 offen offset:1024 sc1\n"
      "v_mfma_f32_16x16x32_bf16 a[76:79], v[100:103], v[64:67], a[76:79]\n"
      "s_waitcnt vmcnt(42)\n"
      "v_mfma_f32_16x16x32_bf16 a[80:83], v[84:87], v[68:71], a[80:83]\n"
      "v_mfma_f32_16x16x32_bf16 a[84:87], v[88:91], v[68:71], a[84:87]\n"
      "buffer_load_dwordx4 v[8:11], v245, s[40:43], s84 offen offset:0 sc1\n"
      "v_mfma_f32_16x16x32_bf16 a[88:91], v[92:95], v[68:71], a[88:91]\n"
      "v_mfma_f32_16x16x32_bf16 a[92:95], v[96:99], v[68:71], a[92:95]\n"
      "v_mfma_f32_16x16x32_bf16 a[96:99], v[100:103], v[68:71], a[96:99]\n"
      "buffer_load_dwordx4 v[12:15], v245, s[40:43], s84 offen offset:1024 sc1\n"
      "v_mfma_f32_16x16x32_bf16 a[100:103], v[84:87], v[72:75], a[100:103]\n"
      "s_mov_b32 s87, 49152\n"
      "buffer_store_dwordx4 a[196:199], v254, s[56:59], s87 offen offset:3072 sc1\n"
      "v_mfma_f32_16x16x32_bf16 a[104:107], v[88:91], v[72:75], a[104:107]\n"
      "v_mfma_f32_16x16x32_bf16 a[108:111], v[92:95], v[72:75], a[108:111]\n"
      "buffer_load_dwordx4 v[16:19], v246, s[40:43], s84 offen offset:0 sc1\n"
      "v_mfma_f32_16x16x32_bf16 a[112:115], v[96:99], v[72:75], a[112:115]\n"
      "v_mfma_f32_16x16x32_bf16 a[116:119], v[100:103], v[72:75], a[116:119]\n"
      "s_waitcnt vmcnt(43)\n"
      "v_mfma_f32_16x16x32_bf16 a[120:123], v[84:87], v[76:79], a[120:123]\n"
      "buffer_load_dwordx4 v[20:23], v246, s[40:43], s84 offen offset:1024 sc1\n"
      "v_mfma_f32_16x16x32_bf16 a[124:127], v[88:91], v[76:79], a[124:127]\n"
      "v_mfma_f32_16x16x32_bf16 a[128:131], v[92:95], v[76:79], a[128:131]\n"
      "v_mfma_f32_16x16x32_bf16 a[132:135], v[96:99], v[76:79], a[132:135]\n"
      "buffer_load_dwordx4 v[24:27], v247, s[40:43], s84 offen offset:0 sc1\n"
      "v_mfma_f32_16x16x32_bf16 a[136:139], v[100:103], v[76:79], a[136:139]\n"
      "s_mov_b32 s87, 57344\n"
      "buffer_store_dwordx4 a[200:203], v254, s[56:59], s87 offen offset:3072 sc1\n"
      "v_mfma_f32_16x16x32_bf16 v[208:211], v[84:87], v[80:83], v[208:211]\n"
      "v_mfma_f32_16x16x32_bf16 v[212:215], v[88:91], v[80:83], v[212:215]\n"
      "buffer_load_dwordx4 v[28:31], v247, s[40:43], s84 offen offset:1024 sc1\n"
      "v_mfma_f32_16x16x32_bf16 v[216:219], v[92:95], v[80:83], v[216:219]\n"
      "v_mfma_f32_16x16x32_bf16 v[220:223], v[96:99], v[80:83], v[220:223]\n"
      "v_mfma_f32_16x16x32_bf16 v[224:227], v[100:103], v[80:83], v[224:227]\n"
      "s_waitcnt vmcnt(40)\n"
      "v_mfma_f32_16x16x32_bf16 a[0:3], v[136:139], v[104:107], a[0:3]\n"
      "s_add_u32 s83, s69, 204800\n"
      "buffer_load_dwordx4 v[84:87], v252, s[44:47], s83 offen offset:0 sc1\n"
      "v_mfma_f32_16x16x32_bf16 a[4:7], v[140:143], v[104:107], a[4:7]\n"
      "v_mfma_f32_16x16x32_bf16 a[8:11], v[144:147], v[104:107], a[8:11]\n"
      "v_mfma_f32_16x16x32_bf16 a[12:15], v[148:151], v[104:107], a[12:15]\n"
      "buffer_load_dwordx4 v[88:91], v252, s[44:47], s83 offen offset:1024 sc1\n"
      "v_mfma_f32_16x16x32_bf16 a[16:19], v[152:155], v[104:107], a[16:19]\n"
      "v_mfma_f32_16x16x32_bf16 a[20:23], v[136:139], v[108:111], a[20:23]\n"
      "v_mfma_f32_16x16x32_bf16 a[24:27], v[140:143], v[108:111], a[24:27]\n"
      "buffer_load_dwordx4 v[92:95], v252, s[44:47], s83 offen offset:2048 sc1\n"
      "v_mfma_f32_16x16x32_bf16 a[28:31], v[144:147], v[108:111], a[28:31]\n"
      "v_mfma_f32_16x16x32_bf16 a[32:35], v[148:151], v[108:111], a[32:35]\n"
      "v_mfma_f32_16x16x32_bf16 a[36:39], v[152:155], v[108:111], a[36:39]\n"
      "buffer_load_dwordx4 v[96:99], v252, s[44:47], s83 offen offset:3072 sc1\n"
      "s_waitcnt vmcnt(42)\n"
      "v_mfma_f32_16x16x32_bf16 a[40:43], v[136:139], v[112:115], a[40:43]\n"
      "v_mfma_f32_16x16x32_bf16 a[44:47], v[140:143], v[112:115], a[44:47]\n"
      "v_mfma_f32_16x16x32_bf16 a[48:51], v[144:147], v[112:115], a[48:51]\n"
      "buffer_load_dwordx4 v[100:103], v248, s[44:47], s83 offen offset:0 sc1\n"
      "v_mfma_f32_16x16x32_bf16 a[52:55], v[148:151], v[112:115], a[52:55]\n"
      "v_mfma_f32_16x16x32_bf16 a[56:59], v[152:155], v[112:115], a[56:59]\n"
      "v_mfma_f32_16x16x32_bf16 a[60:63], v[136:139], v[116:119], a[60:63]\n"
      "s_add_u32 s84, s68, 327680\n"
      "buffer_load_dwordx4 v[52:55], v244, s[40:43], s84 offen offset:0 sc1\n"
      "v_mfma_f32_16x16x32_bf16 a[64:67], v[140:143], v[116:119], a[64:67]\n"
      "v_mfma_f32_16x16x32_bf16 a[68:71], v[144:147], v[116:119], a[68:71]\n"
      "v_mfma_f32_16x16x32_bf16 a[72:75], v[148:151], v[116:119], a[72:75]\n"
      "buffer_load_dwordx4 v[56:59], v244, s[40:43], s84 offen offset:1024 sc1\n"
      "v_mfma_f32_16x16x32_bf16 a[76:79], v[152:155], v[116:119], a[76:79]\n"
      "s_waitcnt vmcnt(43)\n"
      "v_mfma_f32_16x16x32_bf16 a[80:83], v[136:139], v[120:123], a[80:83]\n"
      "v_mfma_f32_16x16x32_bf16 a[84:87], v[140:143], v[120:123], a[84:87]\n"
      "buffer_load_dwordx4 v[60:63], v245, s[40:43], s84 offen offset:0 sc1\n"
      "v_mfma_f32_16x16x32_bf16 a[88:91], v[144:147], v[120:123], a[88:91]\n"
      "v_mfma_f32_16x16x32_bf16 a[92:95], v[148:151], v[120:123], a[92:95]\n"
      "v_mfma_f32_16x16x32_bf16 a[96:99], v[152:155], v[120:123], a[96:99]\n"
      "buffer_load_dwordx4 v[64:67], v245, s[40:43], s84 offen offset:1024 sc1\n"
      "v_mfma_f32_16x16x32_bf16 a[100:103], v[136:139], v[124:127], a[100:103]\n"
      "v_mfma_f32_16x16x32_bf16 a[104:107], v[140:143], v[124:127], a[104:107]\n"
      "v_mfma_f32_16x16x32_bf16 a[108:111], v[144:147], v[124:127], a[108:111]\n"
      "buffer_load_dwordx4 v[68:71], v246, s[40:43], s84 offen offset:0 sc1\n"
      "v_mfma_f32_16x16x32_bf16 a[112:115], v[148:151], v[124:127], a[112:115]\n"
      "v_mfma_f32_16x16x32_bf16 a[116:119], v[152:155], v[124:127], a[116:119]\n"
      "s_waitcnt vmcnt(44)\n"
      "v_mfma_f32_16x16x32_bf16 a[120:123], v[136:139], v[128:131], a[120:123]\n"
      "buffer_load_dwordx4 v[72:75], v246, s[40:43], s84 offen offset:1024 sc1\n"
      "v_mfma_f32_16x16x32_bf16 a[124:127], v[140:143], v[128:131], a[124:127]\n"
      "v_mfma_f32_16x16x32_bf16 a[128:131], v[144:147], v[128:131], a[128:131]\n"
      "v_mfma_f32_16x16x32_bf16 a[132:135], v[148:151], v[128:131], a[132:135]\n"
      "buffer_load_dwordx4 v[76:79], v247, s[40:43], s84 offen offset:0 sc1\n"
      "v_mfma_f32_16x16x32_bf16 a[136:139], v[152:155], v[128:131], a[136:139]\n"
      "v_mfma_f32_16x16x32_bf16 v[208:211], v[136:139], v[132:135], v[208:211]\n"
      "v_mfma_f32_16x16x32_bf16 v[212:215], v[140:143], v[132:135], v[212:215]\n"
      "buffer_load_dwordx4 v[80:83], v247, s[40:43], s84 offen offset:1024 sc1\n"
      "v_mfma_f32_16x16x32_bf16 v[216:219], v[144:147], v[132:135], v[216:219]\n"
      "v_mfma_f32_16x16x32_bf16 v[220:223], v[148:151], v[132:135], v[220:223]\n"
      "v_mfma_f32_16x16x32_bf16 v[224:227], v[152:155], v[132:135], v[224:227]\n"
      "s_waitcnt vmcnt(38)\n"
      "v_mfma_f32_16x16x32_bf16 a[0:3], v[188:191], v[156:159], a[0:3]\n"
      "s_add_u32 s83, s69, 245760\n"
      "buffer_load_dwordx4 v[136:139], v252, s[44:47], s83 offen offset:0 sc1\n"
      "v_mfma_f32_16x16x32_bf16 a[4:7], v[192:195], v[156:159], a[4:7]\n"
      "v_mfma_f32_16x16x32_bf16 a[8:11], v[196:199], v[156:159], a[8:11]\n"
      "v_mfma_f32_16x16x32_bf16 a[12:15], v[200:203], v[156:159], a[12:15]\n"
      "buffer_load_dwordx4 v[140:143], v252, s[44:47], s83 offen offset:1024 sc1\n"
      "v_mfma_f32_16x16x32_bf16 a[16:19], v[204:207], v[156:159], a[16:19]\n"
      "v_mfma_f32_16x16x32_bf16 a[20:23], v[188:191], v[160:163], a[20:23]\n"
      "v_mfma_f32_16x16x32_bf16 a[24:27], v[192:195], v[160:163], a[24:27]\n"
      "buffer_load_dwordx4 v[144:147], v252, s[44:47], s83 offen offset:2048 sc1\n"
      "v_mfma_f32_16x16x32_bf16 a[28:31], v[196:199], v[160:163], a[28:31]\n"
      "v_mfma_f32_16x16x32_bf16 a[32:35], v[200:203], v[160:163], a[32:35]\n"
      "v_mfma_f32_16x16x32_bf16 a[36:39], v[204:207], v[160:163], a[36:39]\n"
      "buffer_load_dwordx4 v[148:151], v252, s[44:47], s83 offen offset:3072 sc1\n"
      "s_waitcnt vmcnt(40)\n"
      "v_mfma_f32_16x16x32_bf16 a[40:43], v[188:191], v[164:167], a[40:43]\n"
      "v_mfma_f32_16x16x32_bf16 a[44:47], v[192:195], v[164:167], a[44:47]\n"
      "v_mfma_f32_16x16x32_bf16 a[48:51], v[196:199], v[164:167], a[48:51]\n"
      "buffer_load_dwordx4 v[152:155], v248, s[44:47], s83 offen offset:0 sc1\n"
      "v_mfma_f32_16x16x32_bf16 a[52:55], v[200:203], v[164:167], a[52:55]\n"
      "v_mfma_f32_16x16x32_bf16 a[56:59], v[204:207], v[164:167], a[56:59]\n"
      "v_mfma_f32_16x16x32_bf16 a[60:63], v[188:191], v[168:171], a[60:63]\n"
      "s_add_u32 s84, s68, 393216\n"
      "buffer_load_dwordx4 v[104:107], v244, s[40:43], s84 offen offset:0 sc1\n"
      "v_mfma_f32_16x16x32_bf16 a[64:67], v[192:195], v[168:171], a[64:67]\n"
      "v_mfma_f32_16x16x32_bf16 a[68:71], v[196:199], v[168:171], a[68:71]\n"
      "v_mfma_f32_16x16x32_bf16 a[72:75], v[200:203], v[168:171], a[72:75]\n"
      "buffer_load_dwordx4 v[108:111], v244, s[40:43], s84 offen offset:1024 sc1\n"
      "v_mfma_f32_16x16x32_bf16 a[76:79], v[204:207], v[168:171], a[76:79]\n"
      "s_waitcnt vmcnt(40)\n"
      "v_mfma_f32_16x16x32_bf16 a[80:83], v[188:191], v[172:175], a[80:83]\n"
      "v_mfma_f32_16x16x32_bf16 a[84:87], v[192:195], v[172:175], a[84:87]\n"
      "buffer_load_dwordx4 v[112:115], v245, s[40:43], s84 offen offset:0 sc1\n"
      "v_mfma_f32_16x16x32_bf16 a[88:91], v[196:199], v[172:175], a[88:91]\n"
      "v_mfma_f32_16x16x32_bf16 a[92:95], v[200:203], v[172:175], a[92:95]\n"
      "v_mfma_f32_16x16x32_bf16 a[96:99], v[204:207], v[172:175], a[96:99]\n"
      "buffer_load_dwordx4 v[116:119], v245, s[40:43], s84 offen offset:1024 sc1\n"
      "v_mfma_f32_16x16x32_bf16 a[100:103], v[188:191], v[176:179], a[100:103]\n"
      "v_mfma_f32_16x16x32_bf16 a[104:107], v[192:195], v[176:179], a[104:107]\n"
      "v_mfma_f32_16x16x32_bf16 a[108:111], v[196:199], v[176:179], a[108:111]\n"
      "buffer_load_dwordx4 v[120:123], v246, s[40:43], s84 offen offset:0 sc1\n"
      "v_mfma_f32_16x16x32_bf16 a[112:115], v[200:203], v[176:179], a[112:115]\n"
      "v_mfma_f32_16x16x32_bf16 a[116:119], v[204:207], v[176:179], a[116:119]\n"
      "s_waitcnt vmcnt(40)\n"
      "v_mfma_f32_16x16x32_bf16 a[120:123], v[188:191], v[180:183], a[120:123]\n"
      "buffer_load_dwordx4 v[124:127], v246, s[40:43], s84 offen offset:1024 sc1\n"
      "v_mfma_f32_16x16x32_bf16 a[124:127], v[192:195], v[180:183], a[124:127]\n"
      "v_mfma_f32_16x16x32_bf16 a[128:131], v[196:199], v[180:183], a[128:131]\n"
      "v_mfma_f32_16x16x32_bf16 a[132:135], v[200:203], v[180:183], a[132:135]\n"
      "buffer_load_dwordx4 v[128:131], v247, s[40:43], s84 offen offset:0 sc1\n"
      "v_mfma_f32_16x16x32_bf16 a[136:139], v[204:207], v[180:183], a[136:139]\n"
      "v_mfma_f32_16x16x32_bf16 v[208:211], v[188:191], v[184:187], v[208:211]\n"
      "v_mfma_f32_16x16x32_bf16 v[212:215], v[192:195], v[184:187], v[212:215]\n"
      "buffer_load_dwordx4 v[132:135], v247, s[40:43], s84 offen offset:1024 sc1\n"
      "v_mfma_f32_16x16x32_bf16 v[216:219], v[196:199], v[184:187], v[216:219]\n"
      "v_mfma_f32_16x16x32_bf16 v[220:223], v[200:203], v[184:187], v[220:223]\n"
      "v_mfma_f32_16x16x32_bf16 v[224:227], v[204:207], v[184:187], v[224:227]\n"
      "s_waitcnt vmcnt(27)\n"
      "s_barrier\n"
      "s_cmp_lg_u32 s80, 0\n"
      "s_cbranch_scc1 NOSIG_%=\n"
      "v_lshlrev_b32 v240, 11, v255\n"
      "s_mov_b64 exec, %16\n"
      "global_store_dword v240, v238, %15 sc1\n"
      "global_store_dword v240, v239, %15 offset:1024 sc1\n"
      "s_mov_b64 exec, -1\n"
      "s_cmp_eq_u64 s[92:93], 0\n"
      "s_cbranch_scc1 NOSIG_%=\n"
      "global_store_dword v240, v238, %14 sc1\n"
      "global_store_dword v240, v239, %14 offset:1024 sc1\n"
      "NOSIG_%=:\n"
      "v_mfma_f32_16x16x32_bf16 a[0:3], v[32:35], v[0:3], a[0:3]\n"
      "s_add_u32 s83, s69, 286720\n"
      "buffer_load_dwordx4 v[188:191], v252, s[44:47], s83 offen offset:0 sc1\n"
      "v_mfma_f32_16x16x32_bf16 a[4:7], v[36:39], v[0:3], a[4:7]\n"
      "v_mfma_f32_16x16x32_bf16 a[8:11], v[40:43], v[0:3], a[8:11]\n"
      "v_mfma_f32_16x16x32_bf16 a[12:15], v[44:47], v[0:3], a[12:15]\n"
      "buffer_load_dwordx4 v[192:195], v252, s[44:47], s83 offen offset:1024 sc1\n"
      "v_mfma_f32_16x16x32_bf16 a[16:19], v[48:51], v[0:3], a[16:19]\n"
      "v_mfma_f32_16x16x32_bf16 a[20:23], v[32:35], v[4:7], a[20:23]\n"
      "v_mfma_f32_16x16x32_bf16 a[24:27], v[36:39], v[4:7], a[24:27]\n"
      "buffer_load_dwordx4 v[196:199], v252, s[44:47], s83 offen offset:2048 sc1\n"
      "v_mfma_f32_16x16x32_bf16 a[28:31], v[40:43], v[4:7], a[28:31]\n"
      "v_mfma_f32_16x16x32_bf16 a[32:35], v[44:47], v[4:7], a[32:35]\n"
      "v_mfma_f32_16x16x32_bf16 a[36:39], v[48:51], v[4:7], a[36:39]\n"
      "buffer_load_dwordx4 v[200:203], v252, s[44:47], s83 offen offset:3072 sc1\n"
      "s_waitcnt vmcnt(31)\n"
      "v_mfma_f32_16x16x32_bf16 a[40:43], v[32:35], v[8:11], a[40:43]\n"
      "v_mfma_f32_16x16x32_bf16 a[44:47], v[36:39], v[8:11], a[44:47]\n"
      "v_mfma_f32_16x16x32_bf16 a[48:51], v[40:43], v[8:11], a[48:51]\n"
      "buffer_load_dwordx4 v[204:207], v248, s[44:47], s83 offen offset:0 sc1\n"
      "v_mfma_f32_16x16x32_bf16 a[52:55], v[44:47], v[8:11], a[52:55]\n"
      "v_mfma_f32_16x16x32_bf16 a[56:59], v[48:51], v[8:11], a[56:59]\n"
      "v_mfma_f32_16x16x32_bf16 a[60:63], v[32:35], v[12:15], a[60:63]\n"
      "s_add_u32 s84, s68, 458752\n"
      "buffer_load_dwordx4 v[156:159], v244, s[40:43], s84 offen offset:0 sc1\n"
      "v_mfma_f32_16x16x32_bf16 a[64:67], v[36:39], v[12:15], a[64:67]\n"
      "v_mfma_f32_16x16x32_bf16 a[68:71], v[40:43], v[12:15], a[68:71]\n"
      "v_mfma_f32_16x16x32_bf16 a[72:75], v[44:47], v[12:15], a[72:75]\n"
      "buffer_load_dwordx4 v[160:163], v244, s[40:43], s84 offen offset:1024 sc1\n"
      "v_mfma_f32_16x16x32_bf16 a[76:79], v[48:51], v[12:15], a[76:79]\n"
      "s_waitcnt vmcnt(34)\n"
      "v_mfma_f32_16x16x32_bf16 a[80:83], v[32:35], v[16:19], a[80:83]\n"
      "v_mfma_f32_16x16x32_bf16 a[84:87], v[36:39], v[16:19], a[84:87]\n"
      "buffer_load_dwordx4 v[164:167], v245, s[40:43], s84 offen offset:0 sc1\n"
      "v_mfma_f32_16x16x32_bf16 a[88:91], v[40:43], v[16:19], a[88:91]\n"
      "v_mfma_f32_16x16x32_bf16 a[92:95], v[44:47], v[16:19], a[92:95]\n"
      "v_mfma_f32_16x16x32_bf16 a[96:99], v[48:51], v[16:19], a[96:99]\n"
      "buffer_load_dwordx4 v[168:171], v245, s[40:43], s84 offen offset:1024 sc1\n"
      "v_mfma_f32_16x16x32_bf16 a[100:103], v[32:35], v[20:23], a[100:103]\n"
      "v_mfma_f32_16x16x32_bf16 a[104:107], v[36:39], v[20:23], a[104:107]\n"
      "v_mfma_f32_16x16x32_bf16 a[108:111], v[40:43], v[20:23], a[108:111]\n"
      "buffer_load_dwordx4 v[172:175], v246, s[40:43], s84 offen offset:0 sc1\n"
      "v_mfma_f32_16x16x32_bf16 a[112:115], v[44:47], v[20:23], a[112:115]\n"
      "v_mfma_f32_16x16x32_bf16 a[116:119], v[48:51], v[20:23], a[116:119]\n"
      "s_waitcnt vmcnt(36)\n"
      "v_mfma_f32_16x16x32_bf16 a[120:123], v[32:35], v[24:27], a[120:123]\n"
      "buffer_load_dwordx4 v[176:179], v246, s[40:43], s84 offen offset:1024 sc1\n"
      "v_mfma_f32_16x16x32_bf16 a[124:127], v[36:39], v[24:27], a[124:127]\n"
      "v_mfma_f32_16x16x32_bf16 a[128:131], v[40:43], v[24:27], a[128:131]\n"
      "v_mfma_f32_16x16x32_bf16 a[132:135], v[44:47], v[24:27], a[132:135]\n"
      "buffer_load_dwordx4 v[180:183], v247, s[40:43], s84 offen offset:0 sc1\n"
      "v_mfma_f32_16x16x32_bf16 a[136:139], v[48:51], v[24:27], a[136:139]\n"
      "v_mfma_f32_16x16x32_bf16 v[208:211], v[32:35], v[28:31], v[208:211]\n"
      "v_mfma_f32_16x16x32_bf16 v[212:215], v[36:39], v[28:31], v[212:215]\n"
      "buffer_load_dwordx4 v[184:187], v247, s[40:43], s84 offen offset:1024 sc1\n"
      "v_mfma_f32_16x16x32_bf16 v[216:219], v[40:43], v[28:31], v[216:219]\n"
      "v_mfma_f32_16x16x32_bf16 v[220:223], v[44:47], v[28:31], v[220:223]\n"
      "v_mfma_f32_16x16x32_bf16 v[224:227], v[48:51], v[28:31], v[224:227]\n"
      "s_waitcnt vmcnt(32)\n"
      "v_mfma_f32_16x16x32_bf16 a[0:3], v[84:87], v[52:55], a[0:3]\n"
      "s_add_u32 s83, s69, 327680\n"
      "buffer_load_dwordx4 v[32:35], v252, s[44:47], s83 offen offset:0 sc1\n"
      "v_mfma_f32_16x16x32_bf16 a[4:7], v[88:91], v[52:55], a[4:7]\n"
      "v_mfma_f32_16x16x32_bf16 a[8:11], v[92:95], v[52:55], a[8:11]\n"
      "v_mfma_f32_16x16x32_bf16 a[12:15], v[96:99], v[52:55], a[12:15]\n"
      "buffer_load_dwordx4 v[36:39], v252, s[44:47], s83 offen offset:1024 sc1\n"
      "v_mfma_f32_16x16x32_bf16 a[16:19], v[100:103], v[52:55], a[16:19]\n"
      "v_mfma_f32_16x16x32_bf16 a[20:23], v[84:87], v[56:59], a[20:23]\n"
      "v_mfma_f32_16x16x32_bf16 a[24:27], v[88:91], v[56:59], a[24:27]\n"
      "buffer_load_dwordx4 v[40:43], v252, s[44:47], s83 offen offset:2048 sc1\n"
      "v_mfma_f32_16x16x32_bf16 a[28:31], v[92:95], v[56:59], a[28:31]\n"
      "v_mfma_f32_16x16x32_bf16 a[32:35], v[96:99], v[56:59], a[32:35]\n"
      "v_mfma_f32_16x16x32_bf16 a[36:39], v[100:103], v[56:59], a[36:39]\n"
      "buffer_load_dwordx4 v[44:47], v252, s[44:47], s83 offen offset:3072 sc1\n"
      "s_waitcnt vmcnt(34)\n"
      "v_mfma_f32_16x16x32_bf16 a[40:43], v[84:87], v[60:63], a[40:43]\n"
      "v_mfma_f32_16x16x32_bf16 a[44:47], v[88:91], v[60:63], a[44:47]\n"
      "v_mfma_f32_16x16x32_bf16 a[48:51], v[92:95], v[60:63], a[48:51]\n"
      "buffer_load_dwordx4 v[48:51], v248, s[44:47], s83 offen offset:0 sc1\n"
      "v_mfma_f32_16x16x32_bf16 a[52:55], v[96:99], v[60:63], a[52:55]\n"
      "v_mfma_f32_16x16x32_bf16 a[56:59], v[100:103], v[60:63], a[56:59]\n"
      "v_mfma_f32_16x16x32_bf16 a[60:63], v[84:87], v[64:67], a[60:63]\n"
      "s_add_u32 s84, s68, 524288\n"
      "buffer_load_dwordx4 v[0:3], v244, s[40:43], s84 offen offset:0 sc1\n"
      "v_mfma_f32_16x16x32_bf16 a[64:67], v[88:91], v[64:67], a[64:67]\n"
      "v_mfma_f32_16x16x32_bf16 a[68:71], v[92:95], v[64:67], a[68:71]\n"
      "v_mfma_f32_16x16x32_bf16 a[72:75], v[96:99], v[64:67], a[72:75]\n"
      "buffer_load_dwordx4 v[4:7], v244, s[40:43], s84 offen offset:1024 sc1\n"
      "v_mfma_f32_16x16x32_bf16 a[76:79], v[100:103], v[64:67], a[76:79]\n"
      "s_waitcnt vmcnt(35)\n"
      "v_mfma_f32_16x16x32_bf16 a[80:83], v[84:87], v[68:71], a[80:83]\n"
      "v_mfma_f32_16x16x32_bf16 a[84:87], v[88:91], v[68:71], a[84:87]\n"
      "buffer_load_dwordx4 v[8:11], v245, s[40:43], s84 offen offset:0 sc1\n"
      "v_mfma_f32_16x16x32_bf16 a[88:91], v[92:95], v[68:71], a[88:91]\n"
      "v_mfma_f32_16x16x32_bf16 a[92:95], v[96:99], v[68:71], a[92:95]\n"
      "v_mfma_f32_16x16x32_bf16 a[96:99], v[100:103], v[68:71], a[96:99]\n"
      "buffer_load_dwordx4 v[12:15], v245, s[40:43], s84 offen offset:1024 sc1\n"
      "v_mfma_f32_16x16x32_bf16 a[100:103], v[84:87], v[72:75], a[100:103]\n"
      "v_mfma_f32_16x16x32_bf16 a[104:107], v[88:91], v[72:75], a[104:107]\n"
      "v_mfma_f32_16x16x32_bf16 a[108:111], v[92:95], v[72:75], a[108:111]\n"
      "buffer_load_dwordx4 v[16:19], v246, s[40:43], s84 offen offset:0 sc1\n"
      "v_mfma_f32_16x16x32_bf16 a[112:115], v[96:99], v[72:75], a[112:115]\n"
      "v_mfma_f32_16x16x32_bf16 a[116:119], v[100:103], v[72:75], a[116:119]\n"
      "s_waitcnt vmcnt(36)\n"
      "v_mfma_f32_16x16x32_bf16 a[120:123], v[84:87], v[76:79], a[120:123]\n"
      "buffer_load_dwordx4 v[20:23], v246, s[40:43], s84 offen offset:1024 sc1\n"
      "v_mfma_f32_16x16x32_bf16 a[124:127], v[88:91], v[76:79], a[124:127]\n"
      "v_mfma_f32_16x16x32_bf16 a[128:131], v[92:95], v[76:79], a[128:131]\n"
      "v_mfma_f32_16x16x32_bf16 a[132:135], v[96:99], v[76:79], a[132:135]\n"
      "buffer_load_dwordx4 v[24:27], v247, s[40:43], s84 offen offset:0 sc1\n"
      "v_mfma_f32_16x16x32_bf16 a[136:139], v[100:103], v[76:79], a[136:139]\n"
      "v_mfma_f32_16x16x32_bf16 v[208:211], v[84:87], v[80:83], v[208:211]\n"
      "v_mfma_f32_16x16x32_bf16 v[212:215], v[88:91], v[80:83], v[212:215]\n"
      "buffer_load_dwordx4 v[28:31], v247, s[40:43], s84 offen offset:1024 sc1\n"
      "v_mfma_f32_16x16x32_bf16 v[216:219], v[92:95], v[80:83], v[216:219]\n"
      "v_mfma_f32_16x16x32_bf16 v[220:223], v[96:99], v[80:83], v[220:223]\n"
      "v_mfma_f32_16x16x32_bf16 v[224:227], v[100:103], v[80:83], v[224:227]\n"
      "s_waitcnt vmcnt(32)\n"
      "v_mfma_f32_16x16x32_bf16 a[0:3], v[136:139], v[104:107], a[0:3]\n"
      "s_add_u32 s83, s69, 368640\n"
      "buffer_load_dwordx4 v[84:87], v252, s[44:47], s83 offen offset:0 sc1\n"
      "v_mfma_f32_16x16x32_bf16 a[4:7], v[140:143], v[104:107], a[4:7]\n"
      "v_mfma_f32_16x16x32_bf16 a[8:11], v[144:147], v[104:107], a[8:11]\n"
      "v_mfma_f32_16x16x32_bf16 a[12:15], v[148:151], v[104:107], a[12:15]\n"
      "buffer_load_dwordx4 v[88:91], v252, s[44:47], s83 offen offset:1024 sc1\n"
      "v_mfma_f32_16x16x32_bf16 a[16:19], v[152:155], v[104:107], a[16:19]\n"
      "v_mfma_f32_16x16x32_bf16 a[20:23], v[136:139], v[108:111], a[20:23]\n"
      "v_mfma_f32_16x16x32_bf16 a[24:27], v[140:143], v[108:111], a[24:27]\n"
      "buffer_load_dwordx4 v[92:95], v252, s[44:47], s83 offen offset:2048 sc1\n"
      "v_mfma_f32_16x16x32_bf16 a[28:31], v[144:147], v[108:111], a[28:31]\n"
      "v_mfma_f32_16x16x32_bf16 a[32:35], v[148:151], v[108:111], a[32:35]\n"
      "v_mfma_f32_16x16x32_bf16 a[36:39], v[152:155], v[108:111], a[36:39]\n"
      "buffer_load_dwordx4 v[96:99], v252, s[44:47], s83 offen offset:3072 sc1\n"
      "s_waitcnt vmcnt(34)\n"
      "v_mfma_f32_16x16x32_bf16 a[40:43], v[136:139], v[112:115], a[40:43]\n"
      "v_mfma_f32_16x16x32_bf16 a[44:47], v[140:143], v[112:115], a[44:47]\n"
      "v_mfma_f32_16x16x32_bf16 a[48:51], v[144:147], v[112:115], a[48:51]\n"
      "buffer_load_dwordx4 v[100:103], v248, s[44:47], s83 offen offset:0 sc1\n"
      "v_mfma_f32_16x16x32_bf16 a[52:55], v[148:151], v[112:115], a[52:55]\n"
      "v_mfma_f32_16x16x32_bf16 a[56:59], v[152:155], v[112:115], a[56:59]\n"
      "v_mfma_f32_16x16x32_bf16 a[60:63], v[136:139], v[116:119], a[60:63]\n"
      "s_add_u32 s84, s68, 589824\n"
      "buffer_load_dwordx4 v[52:55], v244, s[40:43], s84 offen offset:0 sc1\n"
      "v_mfma_f32_16x16x32_bf16 a[64:67], v[140:143], v[116:119], a[64:67]\n"
      "v_mfma_f32_16x16x32_bf16 a[68:71], v[144:147], v[116:119], a[68:71]\n"
      "v_mfma_f32_16x16x32_bf16 a[72:75], v[148:151], v[116:119], a[72:75]\n"
      "buffer_load_dwordx4 v[56:59], v244, s[40:43], s84 offen offset:1024 sc1\n"
      "v_mfma_f32_16x16x32_bf16 a[76:79], v[152:155], v[116:119], a[76:79]\n"
      "s_waitcnt vmcnt(35)\n"
      "v_mfma_f32_16x16x32_bf16 a[80:83], v[136:139], v[120:123], a[80:83]\n"
      "v_mfma_f32_16x16x32_bf16 a[84:87], v[140:143], v[120:123], a[84:87]\n"
      "buffer_load_dwordx4 v[60:63], v245, s[40:43], s84 offen offset:0 sc1\n"
      "v_mfma_f32_16x16x32_bf16 a[88:91], v[144:147], v[120:123], a[88:91]\n"
      "v_mfma_f32_16x16x32_bf16 a[92:95], v[148:151], v[120:123], a[92:95]\n"
      "v_mfma_f32_16x16x32_bf16 a[96:99], v[152:155], v[120:123], a[96:99]\n"
      "buffer_load_dwordx4 v[64:67], v245, s[40:43], s84 offen offset:1024 sc1\n"
      "v_mfma_f32_16x16x32_bf16 a[100:103], v[136:139], v[124:127], a[100:103]\n"
      "v_mfma_f32_16x16x32_bf16 a[104:107], v[140:143], v[124:127], a[104:107]\n"
      "v_mfma_f32_16x16x32_bf16 a[108:111], v[144:147], v[124:127], a[108:111]\n"
      "buffer_load_dwordx4 v[68:71], v246, s[40:43], s84 offen offset:0 sc1\n"
      "v_mfma_f32_16x16x32_bf16 a[112:115], v[148:151], v[124:127], a[112:115]\n"
      "v_mfma_f32_16x16x32_bf16 a[116:119], v[152:155], v[124:127], a[116:119]\n"
      "s_waitcnt vmcnt(36)\n"
      "v_mfma_f32_16x16x32_bf16 a[120:123], v[136:139], v[128:131], a[120:123]\n"
      "buffer_load_dwordx4 v[72:75], v246, s[40:43], s84 offen offset:1024 sc1\n"
      "v_mfma_f32_16x16x32_bf16 a[124:127], v[140:143], v[128:131], a[124:127]\n"
      "v_mfma_f32_16x16x32_bf16 a[128:131], v[144:147], v[128:131], a[128:131]\n"
      "v_mfma_f32_16x16x32_bf16 a[132:135], v[148:151], v[128:131], a[132:135]\n"
      "buffer_load_dwordx4 v[76:79], v247, s[40:43], s84 offen offset:0 sc1\n"
      "v_mfma_f32_16x16x32_bf16 a[136:139], v[152:155], v[128:131], a[136:139]\n"
      "v_mfma_f32_16x16x32_bf16 v[208:211], v[136:139], v[132:135], v[208:211]\n"
      "v_mfma_f32_16x16x32_bf16 v[212:215], v[140:143], v[132:135], v[212:215]\n"
      "buffer_load_dwordx4 v[80:83], v247, s[40:43], s84 offen offset:1024 sc1\n"
      "v_mfma_f32_16x16x32_bf16 v[216:219], v[144:147], v[132:135], v[216:219]\n"
      "v_mfma_f32_16x16x32_bf16 v[220:223], v[148:151], v[132:135], v[220:223]\n"
      "v_mfma_f32_16x16x32_bf16 v[224:227], v[152:155], v[132:135], v[224:227]\n"
      "s_waitcnt vmcnt(32)\n"
      "v_mfma_f32_16x16x32_bf16 a[0:3], v[188:191], v[156:159], a[0:3]\n"
      "s_add_u32 s83, s69, 409600\n"
      "buffer_load_dwordx4 v[136:139], v252, s[44:47], s83 offen offset:0 sc1\n"
      "v_mfma_f32_16x16x32_bf16 a[4:7], v[192:195], v[156:159], a[4:7]\n"
      "v_mfma_f32_16x16x32_bf16 a[8:11], v[196:199], v[156:159], a[8:11]\n"
      "v_mfma_f32_16x16x32_bf16 a[12:15], v[200:203], v[156:159], a[12:15]\n"
      "buffer_load_dwordx4 v[140:143], v252, s[44:47], s83 offen offset:1024 sc1\n"
      "v_mfma_f32_16x16x32_bf16 a[16:19], v[204:207], v[156:159], a[16:19]\n"
      "v_mfma_f32_16x16x32_bf16 a[20:23], v[188:191], v[160:163], a[20:23]\n"
      "v_mfma_f32_16x16x32_bf16 a[24:27], v[192:195], v[160:163], a[24:27]\n"
      "buffer_load_dwordx4 v[144:147], v252, s[44:47], s83 offen offset:2048 sc1\n"
      "v_mfma_f32_16x16x32_bf16 a[28:31], v[196:199], v[160:163], a[28:31]\n"
      "v_mfma_f32_16x16x32_bf16 a[32:35], v[200:203], v[160:163], a[32:35]\n"
      "v_mfma_f32_16x16x32_bf16 a[36:39], v[204:207], v[160:163], a[36:39]\n"
      "buffer_load_dwordx4 v[148:151], v252, s[44:47], s83 offen offset:3072 sc1\n"
      "s_waitcnt vmcnt(34)\n"
      "v_mfma_f32_16x16x32_bf16 a[40:43], v[188:191], v[164:167], a[40:43]\n"
      "v_mfma_f32_16x16x32_bf16 a[44:47], v[192:195], v[164:167], a[44:47]\n"
      "v_mfma_f32_16x16x32_bf16 a[48:51], v[196:199], v[164:167], a[48:51]\n"
      "buffer_load_dwordx4 v[152:155], v248, s[44:47], s83 offen offset:0 sc1\n"
      "v_mfma_f32_16x16x32_bf16 a[52:55], v[200:203], v[164:167], a[52:55]\n"
      "v_mfma_f32_16x16x32_bf16 a[56:59], v[204:207], v[164:167], a[56:59]\n"
      "v_mfma_f32_16x16x32_bf16 a[60:63], v[188:191], v[168:171], a[60:63]\n"
      "s_add_u32 s84, s68, 655360\n"
      "buffer_load_dwordx4 v[104:107], v244, s[40:43], s84 offen offset:0 sc1\n"
      "v_mfma_f32_16x16x32_bf16 a[64:67], v[192:195], v[168:171], a[64:67]\n"
      "v_mfma_f32_16x16x32_bf16 a[68:71], v[196:199], v[168:171], a[68:71]\n"
      "v_mfma_f32_16x16x32_bf16 a[72:75], v[200:203], v[168:171], a[72:75]\n"
      "buffer_load_dwordx4 v[108:111], v244, s[40:43], s84 offen offset:1024 sc1\n"
      "v_mfma_f32_16x16x32_bf16 a[76:79], v[204:207], v[168:171], a[76:79]\n"
      "s_waitcnt vmcnt(35)\n"
      "v_mfma_f32_16x16x32_bf16 a[80:83], v[188:191], v[172:175], a[80:83]\n"
      "v_mfma_f32_16x16x32_bf16 a[84:87], v[192:195], v[172:175], a[84:87]\n"
      "buffer_load_dwordx4 v[112:115], v245, s[40:43], s84 offen offset:0 sc1\n"
      "v_mfma_f32_16x16x32_bf16 a[88:91], v[196:199], v[172:175], a[88:91]\n"
      "v_mfma_f32_16x16x32_bf16 a[92:95], v[200:203], v[172:175], a[92:95]\n"
      "v_mfma_f32_16x16x32_bf16 a[96:99], v[204:207], v[172:175], a[96:99]\n"
      "buffer_load_dwordx4 v[116:119], v245, s[40:43], s84 offen offset:1024 sc1\n"
      "v_mfma_f32_16x16x32_bf16 a[100:103], v[188:191], v[176:179], a[100:103]\n"
      "v_mfma_f32_16x16x32_bf16 a[104:107], v[192:195], v[176:179], a[104:107]\n"
      "v_mfma_f32_16x16x32_bf16 a[108:111], v[196:199], v[176:179], a[108:111]\n"
      "buffer_load_dwordx4 v[120:123], v246, s[40:43], s84 offen offset:0 sc1\n"
      "v_mfma_f32_16x16x32_bf16 a[112:115], v[200:203], v[176:179], a[112:115]\n"
      "v_mfma_f32_16x16x32_bf16 a[116:119], v[204:207], v[176:179], a[116:119]\n"
      "s_waitcnt vmcnt(36)\n"
      "v_mfma_f32_16x16x32_bf16 a[120:123], v[188:191], v[180:183], a[120:123]\n"
      "buffer_load_dwordx4 v[124:127], v246, s[40:43], s84 offen offset:1024 sc1\n"
      "v_mfma_f32_16x16x32_bf16 a[124:127], v[192:195], v[180:183], a[124:127]\n"
      "v_mfma_f32_16x16x32_bf16 a[128:131], v[196:199], v[180:183], a[128:131]\n"
      "v_mfma_f32_16x16x32_bf16 a[132:135], v[200:203], v[180:183], a[132:135]\n"
      "buffer_load_dwordx4 v[128:131], v247, s[40:43], s84 offen offset:0 sc1\n"
      "v_mfma_f32_16x16x32_bf16 a[136:139], v[204:207], v[180:183], a[136:139]\n"
      "v_mfma_f32_16x16x32_bf16 v[208:211], v[188:191], v[184:187], v[208:211]\n"
      "v_mfma_f32_16x16x32_bf16 v[212:215], v[192:195], v[184:187], v[212:215]\n"
      "buffer_load_dwordx4 v[132:135], v247, s[40:43], s84 offen offset:1024 sc1\n"
      "v_mfma_f32_16x16x32_bf16 v[216:219], v[196:199], v[184:187], v[216:219]\n"
      "v_mfma_f32_16x16x32_bf16 v[220:223], v[200:203], v[184:187], v[220:223]\n"
      "v_mfma_f32_16x16x32_bf16 v[224:227], v[204:207], v[184:187], v[224:227]\n"
      "s_waitcnt vmcnt(32)\n"
      "v_mfma_f32_16x16x32_bf16 a[0:3], v[32:35], v[0:3], a[0:3]\n"
      "s_add_u32 s83, s69, 450560\n"
      "buffer_load_dwordx4 v[188:191], v252, s[44:47], s83 offen offset:0 sc1\n"
      "v_mfma_f32_16x16x32_bf16 a[4:7], v[36:39], v[0:3], a[4:7]\n"
      "v_mfma_f32_16x16x32_bf16 a[8:11], v[40:43], v[0:3], a[8:11]\n"
      "v_mfma_f32_16x16x32_bf16 a[12:15], v[44:47], v[0:3], a[12:15]\n"
      "buffer_load_dwordx4 v[192:195], v252, s[44:47], s83 offen offset:1024 sc1\n"
      "v_mfma_f32_16x16x32_bf16 a[16:19], v[48:51], v[0:3], a[16:19]\n"
      "v_mfma_f32_16x16x32_bf16 a[20:23], v[32:35], v[4:7], a[20:23]\n"
      "v_mfma_f32_16x16x32_bf16 a[24:27], v[36:39], v[4:7], a[24:27]\n"
      "buffer_load_dwordx4 v[196:199], v252, s[44:47], s83 offen offset:2048 sc1\n"
      "v_mfma_f32_16x16x32_bf16 a[28:31], v[40:43], v[4:7], a[28:31]\n"
      "v_mfma_f32_16x16x32_bf16 a[32:35], v[44:47], v[4:7], a[32:35]\n"
      "v_mfma_f32_16x16x32_bf16 a[36:39], v[48:51], v[4:7], a[36:39]\n"
      "buffer_load_dwordx4 v[200:203], v252, s[44:47], s83 offen offset:3072 sc1\n"
      "s_waitcnt vmcnt(34)\n"
      "v_mfma_f32_16x16x32_bf16 a[40:43], v[32:35], v[8:11], a[40:43]\n"
      "v_mfma_f32_16x16x32_bf16 a[44:47], v[36:39], v[8:11], a[44:47]\n"
      "v_mfma_f32_16x16x32_bf16 a[48:51], v[40:43], v[8:11], a[48:51]\n"
      "buffer_load_dwordx4 v[204:207], v248, s[44:47], s83 offen offset:0 sc1\n"
      "v_mfma_f32_16x16x32_bf16 a[52:55], v[44:47], v[8:11], a[52:55]\n"
      "v_mfma_f32_16x16x32_bf16 a[56:59], v[48:51], v[8:11], a[56:59]\n"
      "v_mfma_f32_16x16x32_bf16 a[60:63], v[32:35], v[12:15], a[60:63]\n"
      "s_add_u32 s84, s68, 720896\n"
      "buffer_load_dwordx4 v[156:159], v244, s[40:43], s84 offen offset:0 sc1\n"
      "v_mfma_f32_16x16x32_bf16 a[64:67], v[36:39], v[12:15], a[64:67]\n"
      "v_mfma_f32_16x16x32_bf16 a[68:71], v[40:43], v[12:15], a[68:71]\n"
      "v_mfma_f32_16x16x32_bf16 a[72:75], v[44:47], v[12:15], a[72:75]\n"
      "buffer_load_dwordx4 v[160:163], v244, s[40:43], s84 offen offset:1024 sc1\n"
      "v_mfma_f32_16x16x32_bf16 a[76:79], v[48:51], v[12:15], a[76:79]\n"
      "s_waitcnt vmcnt(35)\n"
      "v_mfma_f32_16x16x32_bf16 a[80:83], v[32:35], v[16:19], a[80:83]\n"
      "v_mfma_f32_16x16x32_bf16 a[84:87], v[36:39], v[16:19], a[84:87]\n"
      "buffer_load_dwordx4 v[164:167], v245, s[40:43], s84 offen offset:0 sc1\n"
      "v_mfma_f32_16x16x32_bf16 a[88:91], v[40:43], v[16:19], a[88:91]\n"
      "v_mfma_f32_16x16x32_bf16 a[92:95], v[44:47], v[16:19], a[92:95]\n"
      "v_mfma_f32_16x16x32_bf16 a[96:99], v[48:51], v[16:19], a[96:99]\n"
      "buffer_load_dwordx4 v[168:171], v245, s[40:43], s84 offen offset:1024 sc1\n"
      "v_mfma_f32_16x16x32_bf16 a[100:103], v[32:35], v[20:23], a[100:103]\n"
      "v_mfma_f32_16x16x32_bf16 a[104:107], v[36:39], v[20:23], a[104:107]\n"
      "v_mfma_f32_16x16x32_bf16 a[108:111], v[40:43], v[20:23], a[108:111]\n"
      "buffer_load_dwordx4 v[172:175], v246, s[40:43], s84 offen offset:0 sc1\n"
      "v_mfma_f32_16x16x32_bf16 a[112:115], v[44:47], v[20:23], a[112:115]\n"
      "v_mfma_f32_16x16x32_bf16 a[116:119], v[48:51], v[20:23], a[116:119]\n"
      "s_waitcnt vmcnt(36)\n"
      "v_mfma_f32_16x16x32_bf16 a[120:123], v[32:35], v[24:27], a[120:123]\n"
      "buffer_load_dwordx4 v[176:179], v246, s[40:43], s84 offen offset:1024 sc1\n"
      "v_mfma_f32_16x16x32_bf16 a[124:127], v[36:39], v[24:27], a[124:127]\n"
      "v_mfma_f32_16x16x32_bf16 a[128:131], v[40:43], v[24:27], a[128:131]\n"
      "v_mfma_f32_16x16x32_bf16 a[132:135], v[44:47], v[24:27], a[132:135]\n"
      "buffer_load_dwordx4 v[180:183], v247, s[40:43], s84 offen offset:0 sc1\n"
      "v_mfma_f32_16x16x32_bf16 a[136:139], v[48:51], v[24:27], a[136:139]\n"
      "v_mfma_f32_16x16x32_bf16 v[208:211], v[32:35], v[28:31], v[208:211]\n"
      "v_mfma_f32_16x16x32_bf16 v[212:215], v[36:39], v[28:31], v[212:215]\n"
      "buffer_load_dwordx4 v[184:187], v247, s[40:43], s84 offen offset:1024 sc1\n"
      "v_mfma_f32_16x16x32_bf16 v[216:219], v[40:43], v[28:31], v[216:219]\n"
      "v_mfma_f32_16x16x32_bf16 v[220:223], v[44:47], v[28:31], v[220:223]\n"
      "v_mfma_f32_16x16x32_bf16 v[224:227], v[48:51], v[28:31], v[224:227]\n"
      "s_waitcnt vmcnt(32)\n"
      "v_mfma_f32_16x16x32_bf16 a[0:3], v[84:87], v[52:55], a[0:3]\n"
      "s_add_u32 s83, s69, 491520\n"
      "buffer_load_dwordx4 v[32:35], v252, s[44:47], s83 offen offset:0 sc1\n"
      "v_mfma_f32_16x16x32_bf16 a[4:7], v[88:91], v[52:55], a[4:7]\n"
      "v_mfma_f32_16x16x32_bf16 a[8:11], v[92:95], v[52:55], a[8:11]\n"
      "v_mfma_f32_16x16x32_bf16 a[12:15], v[96:99], v[52:55], a[12:15]\n"
      "buffer_load_dwordx4 v[36:39], v252, s[44:47], s83 offen offset:1024 sc1\n"
      "v_mfma_f32_16x16x32_bf16 a[16:19], v[100:103], v[52:55], a[16:19]\n"
      "v_mfma_f32_16x16x32_bf16 a[20:23], v[84:87], v[56:59], a[20:23]\n"
      "v_mfma_f32_16x16x32_bf16 a[24:27], v[88:91], v[56:59], a[24:27]\n"
      "buffer_load_dwordx4 v[40:43], v252, s[44:47], s83 offen offset:2048 sc1\n"
      "v_mfma_f32_16x16x32_bf16 a[28:31], v[92:95], v[56:59], a[28:31]\n"
      "v_mfma_f32_16x16x32_bf16 a[32:35], v[96:99], v[56:59], a[32:35]\n"
      "v_mfma_f32_16x16x32_bf16 a[36:39], v[100:103], v[56:59], a[36:39]\n"
      "buffer_load_dwordx4 v[44:47], v252, s[44:47], s83 offen offset:3072 sc1\n"
      "s_waitcnt vmcnt(34)\n"
      "v_mfma_f32_16x16x32_bf16 a[40:43], v[84:87], v[60:63], a[40:43]\n"
      "v_mfma_f32_16x16x32_bf16 a[44:47], v[88:91], v[60:63], a[44:47]\n"
      "v_mfma_f32_16x16x32_bf16 a[48:51], v[92:95], v[60:63], a[48:51]\n"
      "buffer_load_dwordx4 v[48:51], v248, s[44:47], s83 offen offset:0 sc1\n"
      "v_mfma_f32_16x16x32_bf16 a[52:55], v[96:99], v[60:63], a[52:55]\n"
      "v_mfma_f32_16x16x32_bf16 a[56:59], v[100:103], v[60:63], a[56:59]\n"
      "v_mfma_f32_16x16x32_bf16 a[60:63], v[84:87], v[64:67], a[60:63]\n"
      "s_add_u32 s84, s68, 786432\n"
      "buffer_load_dwordx4 v[0:3], v244, s[40:43], s84 offen offset:0 sc1\n"
      "v_mfma_f32_16x16x32_bf16 a[64:67], v[88:91], v[64:67], a[64:67]\n"
      "v_mfma_f32_16x16x32_bf16 a[68:71], v[92:95], v[64:67], a[68:71]\n"
      "v_mfma_f32_16x16x32_bf16 a[72:75], v[96:99], v[64:67], a[72:75]\n"
      "buffer_load_dwordx4 v[4:7], v244, s[40:43], s84 offen offset:1024 sc1\n"
      "v_mfma_f32_16x16x32_bf16 a[76:79], v[100:103], v[64:67], a[76:79]\n"
      "s_waitcnt vmcnt(35)\n"
      "v_mfma_f32_16x16x32_bf16 a[80:83], v[84:87], v[68:71], a[80:83]\n"
      "v_mfma_f32_16x16x32_bf16 a[84:87], v[88:91], v[68:71], a[84:87]\n"
      "buffer_load_dwordx4 v[8:11], v245, s[40:43], s84 offen offset:0 sc1\n"
      "v_mfma_f32_16x16x32_bf16 a[88:91], v[92:95], v[68:71], a[88:91]\n"
      "v_mfma_f32_16x16x32_bf16 a[92:95], v[96:99], v[68:71], a[92:95]\n"
      "v_mfma_f32_16x16x32_bf16 a[96:99], v[100:103], v[68:71], a[96:99]\n"
      "buffer_load_dwordx4 v[12:15], v245, s[40:43], s84 offen offset:1024 sc1\n"
      "v_mfma_f32_16x16x32_bf16 a[100:103], v[84:87], v[72:75], a[100:103]\n"
      "v_mfma_f32_16x16x32_bf16 a[104:107], v[88:91], v[72:75], a[104:107]\n"
      "v_mfma_f32_16x16x32_bf16 a[108:111], v[92:95], v[72:75], a[108:111]\n"
      "buffer_load_dwordx4 v[16:19], v246, s[40:43], s84 offen offset:0 sc1\n"
      "v_mfma_f32_16x16x32_bf16 a[112:115], v[96:99], v[72:75], a[112:115]\n"
      "v_mfma_f32_16x16x32_bf16 a[116:119], v[100:103], v[72:75], a[116:119]\n"
      "s_waitcnt vmcnt(36)\n"
      "v_mfma_f32_16x16x32_bf16 a[120:123], v[84:87], v[76:79], a[120:123]\n"
      "buffer_load_dwordx4 v[20:23], v246, s[40:43], s84 offen offset:1024 sc1\n"
      "v_mfma_f32_16x16x32_bf16 a[124:127], v[88:91], v[76:79], a[124:127]\n"
      "v_mfma_f32_16x16x32_bf16 a[128:131], v[92:95], v[76:79], a[128:131]\n"
      "v_mfma_f32_16x16x32_bf16 a[132:135], v[96:99], v[76:79], a[132:135]\n"
      "buffer_load_dwordx4 v[24:27], v247, s[40:43], s84 offen offset:0 sc1\n"
      "v_mfma_f32_16x16x32_bf16 a[136:139], v[100:103], v[76:79], a[136:139]\n"
      "v_mfma_f32_16x16x32_bf16 v[208:211], v[84:87], v[80:83], v[208:211]\n"
      "v_mfma_f32_16x16x32_bf16 v[212:215], v[88:91], v[80:83], v[212:215]\n"
      "buffer_load_dwordx4 v[28:31], v247, s[40:43], s84 offen offset:1024 sc1\n"
      "v_mfma_f32_16x16x32_bf16 v[216:219], v[92:95], v[80:83], v[216:219]\n"
      "v_mfma_f32_16x16x32_bf16 v[220:223], v[96:99], v[80:83], v[220:223]\n"
      "v_mfma_f32_16x16x32_bf16 v[224:227], v[100:103], v[80:83], v[224:227]\n"
      "s_waitcnt vmcnt(32)\n"
      "v_mfma_f32_16x16x32_bf16 a[0:3], v[136:139], v[104:107], a[0:3]\n"
      "s_add_u32 s83, s69, 532480\n"
      "buffer_load_dwordx4 v[84:87], v252, s[44:47], s83 offen offset:0 sc1\n"
      "v_mfma_f32_16x16x32_bf16 a[4:7], v[140:143], v[104:107], a[4:7]\n"
      "v_mfma_f32_16x16x32_bf16 a[8:11], v[144:147], v[104:107], a[8:11]\n"
      "v_mfma_f32_16x16x32_bf16 a[12:15], v[148:151], v[104:107], a[12:15]\n"
      "buffer_load_dwordx4 v[88:91], v252, s[44:47], s83 offen offset:1024 sc1\n"
      "v_mfma_f32_16x16x32_bf16 a[16:19], v[152:155], v[104:107], a[16:19]\n"
      "v_mfma_f32_16x16x32_bf16 a[20:23], v[136:139], v[108:111], a[20:23]\n"
      "v_mfma_f32_16x16x32_bf16 a[24:27], v[140:143], v[108:111], a[24:27]\n"
      "buffer_load_dwordx4 v[92:95], v252, s[44:47], s83 offen offset:2048 sc1\n"
      "v_mfma_f32_16x16x32_bf16 a[28:31], v[144:147], v[108:111], a[28:31]\n"
      "v_mfma_f32_16x16x32_bf16 a[32:35], v[148:151], v[108:111], a[32:35]\n"
      "v_mfma_f32_16x16x32_bf16 a[36:39], v[152:155], v[108:111], a[36:39]\n"
      "buffer_load_dwordx4 v[96:99], v252, s[44:47], s83 offen offset:3072 sc1\n"
      "s_waitcnt vmcnt(34)\n"
      "v_mfma_f32_16x16x32_bf16 a[40:43], v[136:139], v[112:115], a[40:43]\n"
      "v_mfma_f32_16x16x32_bf16 a[44:47], v[140:143], v[112:115], a[44:47]\n"
      "v_mfma_f32_16x16x32_bf16 a[48:51], v[144:147], v[112:115], a[48:51]\n"
      "buffer_load_dwordx4 v[100:103], v248, s[44:47], s83 offen offset:0 sc1\n"
      "v_mfma_f32_16x16x32_bf16 a[52:55], v[148:151], v[112:115], a[52:55]\n"
      "v_mfma_f32_16x16x32_bf16 a[56:59], v[152:155], v[112:115], a[56:59]\n"
      "v_mfma_f32_16x16x32_bf16 a[60:63], v[136:139], v[116:119], a[60:63]\n"
      "s_add_u32 s84, s68, 851968\n"
      "buffer_load_dwordx4 v[52:55], v244, s[40:43], s84 offen offset:0 sc1\n"
      "v_mfma_f32_16x16x32_bf16 a[64:67], v[140:143], v[116:119], a[64:67]\n"
      "v_mfma_f32_16x16x32_bf16 a[68:71], v[144:147], v[116:119], a[68:71]\n"
      "v_mfma_f32_16x16x32_bf16 a[72:75], v[148:151], v[116:119], a[72:75]\n"
      "buffer_load_dwordx4 v[56:59], v244, s[40:43], s84 offen offset:1024 sc1\n"
      "v_mfma_f32_16x16x32_bf16 a[76:79], v[152:155], v[116:119], a[76:79]\n"
      "s_waitcnt vmcnt(35)\n"
      "v_mfma_f32_16x16x32_bf16 a[80:83], v[136:139], v[120:123], a[80:83]\n"
      "v_mfma_f32_16x16x32_bf16 a[84:87], v[140:143], v[120:123], a[84:87]\n"
      "buffer_load_dwordx4 v[60:63], v245, s[40:43], s84 offen offset:0 sc1\n"
      "v_mfma_f32_16x16x32_bf16 a[88:91], v[144:147], v[120:123], a[88:91]\n"
      "v_mfma_f32_16x16x32_bf16 a[92:95], v[148:151], v[120:123], a[92:95]\n"
      "v_mfma_f32_16x16x32_bf16 a[96:99], v[152:155], v[120:123], a[96:99]\n"
      "buffer_load_dwordx4 v[64:67], v245, s[40:43], s84 offen offset:1024 sc1\n"
      "v_mfma_f32_16x16x32_bf16 a[100:103], v[136:139], v[124:127], a[100:103]\n"
      "v_mfma_f32_16x16x32_bf16 a[104:107], v[140:143], v[124:127], a[104:107]\n"
      "v_mfma_f32_16x16x32_bf16 a[108:111], v[144:147], v[124:127], a[108:111]\n"
      "buffer_load_dwordx4 v[68:71], v246, s[40:43], s84 offen offset:0 sc1\n"
      "v_mfma_f32_16x16x32_bf16 a[112:115], v[148:151], v[124:127], a[112:115]\n"
      "v_mfma_f32_16x16x32_bf16 a[116:119], v[152:155], v[124:127], a[116:119]\n"
      "s_waitcnt vmcnt(36)\n"
      "v_mfma_f32_16x16x32_bf16 a[120:123], v[136:139], v[128:131], a[120:123]\n"
      "buffer_load_dwordx4 v[72:75], v246, s[40:43], s84 offen offset:1024 sc1\n"
      "v_mfma_f32_16x16x32_bf16 a[124:127], v[140:143], v[128:131], a[124:127]\n"
      "v_mfma_f32_16x16x32_bf16 a[128:131], v[144:147], v[128:131], a[128:131]\n"
      "v_mfma_f32_16x16x32_bf16 a[132:135], v[148:151], v[128:131], a[132:135]\n"
      "buffer_load_dwordx4 v[76:79], v247, s[40:43], s84 offen offset:0 sc1\n"
      "v_mfma_f32_16x16x32_bf16 a[136:139], v[152:155], v[128:131], a[136:139]\n"
      "v_mfma_f32_16x16x32_bf16 v[208:211], v[136:139], v[132:135], v[208:211]\n"
      "v_mfma_f32_16x16x32_bf16 v[212:215], v[140:143], v[132:135], v[212:215]\n"
      "buffer_load_dwordx4 v[80:83], v247, s[40:43], s84 offen offset:1024 sc1\n"
      "v_mfma_f32_16x16x32_bf16 v[216:219], v[144:147], v[132:135], v[216:219]\n"
      "v_mfma_f32_16x16x32_bf16 v[220:223], v[148:151], v[132:135], v[220:223]\n"
      "global_load_dword v250, v249, %13 offset:512 sc1\n"
      "v_mfma_f32_16x16x32_bf16 v[224:227], v[152:155], v[132:135], v[224:227]\n"
      "s_waitcnt vmcnt(33)\n"
      "v_mfma_f32_16x16x32_bf16 a[0:3], v[188:191], v[156:159], a[0:3]\n"
      "s_add_u32 s83, s69, 573440\n"
      "buffer_load_dwordx4 v[136:139], v252, s[44:47], s83 offen offset:0 sc1\n"
      "v_mfma_f32_16x16x32_bf16 a[4:7], v[192:195], v[156:159], a[4:7]\n"
      "v_mfma_f32_16x16x32_bf16 a[8:11], v[196:199], v[156:159], a[8:11]\n"
      "v_mfma_f32_16x16x32_bf16 a[12:15], v[200:203], v[156:159], a[12:15]\n"
      "buffer_load_dwordx4 v[140:143], v252, s[44:47], s83 offen offset:1024 sc1\n"
      "v_mfma_f32_16x16x32_bf16 a[16:19], v[204:207], v[156:159], a[16:19]\n"
      "v_mfma_f32_16x16x32_bf16 a[20:23], v[188:191], v[160:163], a[20:23]\n"
      "v_mfma_f32_16x16x32_bf16 a[24:27], v[192:195], v[160:163], a[24:27]\n"
      "buffer_load_dwordx4 v[144:147], v252, s[44:47], s83 offen offset:2048 sc1\n"
      "v_mfma_f32_16x16x32_bf16 a[28:31], v[196:199], v[160:163], a[28:31]\n"
      "v_mfma_f32_16x16x32_bf16 a[32:35], v[200:203], v[160:163], a[32:35]\n"
      "v_mfma_f32_16x16x32_bf16 a[36:39], v[204:207], v[160:163], a[36:39]\n"
      "buffer_load_dwordx4 v[148:151], v252, s[44:47], s83 offen offset:3072 sc1\n"
      "s_waitcnt vmcnt(35)\n"
      "v_mfma_f32_16x16x32_bf16 a[40:43], v[188:191], v[164:167], a[40:43]\n"
      "v_mfma_f32_16x16x32_bf16 a[44:47], v[192:195], v[164:167], a[44:47]\n"
      "v_mfma_f32_16x16x32_bf16 a[48:51], v[196:199], v[164:167], a[48:51]\n"
      "buffer_load_dwordx4 v[152:155], v248, s[44:47], s83 offen offset:0 sc1\n"
      "v_mfma_f32_16x16x32_bf16 a[52:55], v[200:203], v[164:167], a[52:55]\n"
      "v_mfma_f32_16x16x32_bf16 a[56:59], v[204:207], v[164:167], a[56:59]\n"
      "v_mfma_f32_16x16x32_bf16 a[60:63], v[188:191], v[168:171], a[60:63]\n"
      "s_add_u32 s84, s68, 917504\n"
      "buffer_load_dwordx4 v[104:107], v244, s[40:43], s84 offen offset:0 sc1\n"
      "v_mfma_f32_16x16x32_bf16 a[64:67], v[192:195], v[168:171], a[64:67]\n"
      "v_mfma_f32_16x16x32_bf16 a[68:71], v[196:199], v[168:171], a[68:71]\n"
      "v_mfma_f32_16x16x32_bf16 a[72:75], v[200:203], v[168:171], a[72:75]\n"
      "buffer_load_dwordx4 v[108:111], v244, s[40:43], s84 offen offset:1024 sc1\n"
      "v_mfma_f32_16x16x32_bf16 a[76:79], v[204:207], v[168:171], a[76:79]\n"
      "s_waitcnt vmcnt(36)\n"
      "v_mfma_f32_16x16x32_bf16 a[80:83], v[188:191], v[172:175], a[80:83]\n"
      "v_mfma_f32_16x16x32_bf16 a[84:87], v[192:195], v[172:175], a[84:87]\n"
      "buffer_load_dwordx4 v[112:115], v245, s[40:43], s84 offen offset:0 sc1\n"
      "v_mfma_f32_16x16x32_bf16 a[88:91], v[196:199], v[172:175], a[88:91]\n"
      "v_mfma_f32_16x16x32_bf16 a[92:95], v[200:203], v[172:175], a[92:95]\n"
      "v_mfma_f32_16x16x32_bf16 a[96:99], v[204:207], v[172:175], a[96:99]\n"
      "buffer_load_dwordx4 v[116:119], v245, s[40:43], s84 offen offset:1024 sc1\n"
      "v_mfma_f32_16x16x32_bf16 a[100:103], v[188:191], v[176:179], a[100:103]\n"
      "v_mfma_f32_16x16x32_bf16 a[104:107], v[192:195], v[176:179], a[104:107]\n"
      "v_mfma_f32_16x16x32_bf16 a[108:111], v[196:199], v[176:179], a[108:111]\n"
      "buffer_load_dwordx4 v[120:123], v246, s[40:43], s84 offen offset:0 sc1\n"
      "v_mfma_f32_16x16x32_bf16 a[112:115], v[200:203], v[176:179], a[112:115]\n"
      "v_mfma_f32_16x16x32_bf16 a[116:119], v[204:207], v[176:179], a[116:119]\n"
      "s_waitcnt vmcnt(37)\n"
      "v_mfma_f32_16x16x32_bf16 a[120:123], v[188:191], v[180:183], a[120:123]\n"
      "buffer_load_dwordx4 v[124:127], v246, s[40:43], s84 offen offset:1024 sc1\n"
      "v_mfma_f32_16x16x32_bf16 a[124:127], v[192:195], v[180:183], a[124:127]\n"
      "v_mfma_f32_16x16x32_bf16 a[128:131], v[196:199], v[180:183], a[128:131]\n"
      "v_mfma_f32_16x16x32_bf16 a[132:135], v[200:203], v[180:183], a[132:135]\n"
      "buffer_load_dwordx4 v[128:131], v247, s[40:43], s84 offen offset:0 sc1\n"
      "v_mfma_f32_16x16x32_bf16 a[136:139], v[204:207], v[180:183], a[136:139]\n"
      "v_mfma_f32_16x16x32_bf16 v[208:211], v[188:191], v[184:187], v[208:211]\n"
      "v_mfma_f32_16x16x32_bf16 v[212:215], v[192:195], v[184:187], v[212:215]\n"
      "buffer_load_dwordx4 v[132:135], v247, s[40:43], s84 offen offset:1024 sc1\n"
      "v_mfma_f32_16x16x32_bf16 v[216:219], v[196:199], v[184:187], v[216:219]\n"
      "v_mfma_f32_16x16x32_bf16 v[220:223], v[200:203], v[184:187], v[220:223]\n"
      "v_mfma_f32_16x16x32_bf16 v[224:227], v[204:207], v[184:187], v[224:227]\n"
      "s_waitcnt vmcnt(33)\n"
      "v_mfma_f32_16x16x32_bf16 a[0:3], v[32:35], v[0:3], a[0:3]\n"
      "s_add_u32 s83, s69, 614400\n"
      "buffer_load_dwordx4 v[188:191], v252, s[44:47], s83 offen offset:0 sc1\n"
      "v_mfma_f32_16x16x32_bf16 a[4:7], v[36:39], v[0:3], a[4:7]\n"
      "v_mfma_f32_16x16x32_bf16 a[8:11], v[40:43], v[0:3], a[8:11]\n"
      "v_mfma_f32_16x16x32_bf16 a[12:15], v[44:47], v[0:3], a[12:15]\n"
      "buffer_load_dwordx4 v[192:195], v252, s[44:47], s83 offen offset:1024 sc1\n"
      "v_mfma_f32_16x16x32_bf16 a[16:19], v[48:51], v[0:3], a[16:19]\n"
      "v_mfma_f32_16x16x32_bf16 a[20:23], v[32:35], v[4:7], a[20:23]\n"
      "v_mfma_f32_16x16x32_bf16 a[24:27], v[36:39], v[4:7], a[24:27]\n"
      "buffer_load_dwordx4 v[196:199], v252, s[44:47], s83 offen offset:2048 sc1\n"
      "v_mfma_f32_16x16x32_bf16 a[28:31], v[40:43], v[4:7], a[28:31]\n"
      "v_mfma_f32_16x16x32_bf16 a[32:35], v[44:47], v[4:7], a[32:35]\n"
      "v_mfma_f32_16x16x32_bf16 a[36:39], v[48:51], v[4:7], a[36:39]\n"
      "buffer_load_dwordx4 v[200:203], v252, s[44:47], s83 offen offset:3072 sc1\n"
      "s_waitcnt vmcnt(35)\n"
      "v_mfma_f32_16x16x32_bf16 a[40:43], v[32:35], v[8:11], a[40:43]\n"
      "v_mfma_f32_16x16x32_bf16 a[44:47], v[36:39], v[8:11], a[44:47]\n"
      "v_mfma_f32_16x16x32_bf16 a[48:51], v[40:43], v[8:11], a[48:51]\n"
      "buffer_load_dwordx4 v[204:207], v248, s[44:47], s83 offen offset:0 sc1\n"
      "v_mfma_f32_16x16x32_bf16 a[52:55], v[44:47], v[8:11], a[52:55]\n"
      "v_mfma_f32_16x16x32_bf16 a[56:59], v[48:51], v[8:11], a[56:59]\n"
      "v_mfma_f32_16x16x32_bf16 a[60:63], v[32:35], v[12:15], a[60:63]\n"
      "s_add_u32 s84, s68, 983040\n"
      "buffer_load_dwordx4 v[156:159], v244, s[40:43], s84 offen offset:0 sc1\n"
      "v_mfma_f32_16x16x32_bf16 a[64:67], v[36:39], v[12:15], a[64:67]\n"
      "v_mfma_f32_16x16x32_bf16 a[68:71], v[40:43], v[12:15], a[68:71]\n"
      "v_mfma_f32_16x16x32_bf16 a[72:75], v[44:47], v[12:15], a[72:75]\n"
      "buffer_load_dwordx4 v[160:163], v244, s[40:43], s84 offen offset:1024 sc1\n"
      "v_mfma_f32_16x16x32_bf16 a[76:79], v[48:51], v[12:15], a[76:79]\n"
      "s_waitcnt vmcnt(36)\n"
      "v_mfma_f32_16x16x32_bf16 a[80:83], v[32:35], v[16:19], a[80:83]\n"
      "v_mfma_f32_16x16x32_bf16 a[84:87], v[36:39], v[16:19], a[84:87]\n"
      "buffer_load_dwordx4 v[164:167], v245, s[40:43], s84 offen offset:0 sc1\n"
      "v_mfma_f32_16x16x32_bf16 a[88:91], v[40:43], v[16:19], a[88:91]\n"
      "v_mfma_f32_16x16x32_bf16 a[92:95], v[44:47], v[16:19], a[92:95]\n"
      "v_mfma_f32_16x16x32_bf16 a[96:99], v[48:51], v[16:19], a[96:99]\n"
      "buffer_load_dwordx4 v[168:171], v245, s[40:43], s84 offen offset:1024 sc1\n"
      "v_mfma_f32_16x16x32_bf16 a[100:103], v[32:35], v[20:23], a[100:103]\n"
      "v_mfma_f32_16x16x32_bf16 a[104:107], v[36:39], v[20:23], a[104:107]\n"
      "v_mfma_f32_16x16x32_bf16 a[108:111], v[40:43], v[20:23], a[108:111]\n"
      "buffer_load_dwordx4 v[172:175], v246, s[40:43], s84 offen offset:0 sc1\n"
      "v_mfma_f32_16x16x32_bf16 a[112:115], v[44:47], v[20:23], a[112:115]\n"
      "v_mfma_f32_16x16x32_bf16 a[116:119], v[48:51], v[20:23], a[116:119]\n"
      "s_waitcnt vmcnt(37)\n"
      "v_mfma_f32_16x16x32_bf16 a[120:123], v[32:35], v[24:27], a[120:123]\n"
      "buffer_load_dwordx4 v[176:179], v246, s[40:43], s84 offen offset:1024 sc1\n"
      "v_mfma_f32_16x16x32_bf16 a[124:127], v[36:39], v[24:27], a[124:127]\n"
      "v_mfma_f32_16x16x32_bf16 a[128:131], v[40:43], v[24:27], a[128:131]\n"
      "v_mfma_f32_16x16x32_bf16 a[132:135], v[44:47], v[24:27], a[132:135]\n"
      "buffer_load_dwordx4 v[180:183], v247, s[40:43], s84 offen offset:0 sc1\n"
      "v_mfma_f32_16x16x32_bf16 a[136:139], v[48:51], v[24:27], a[136:139]\n"
      "v_mfma_f32_16x16x32_bf16 v[208:211], v[32:35], v[28:31], v[208:211]\n"
      "v_mfma_f32_16x16x32_bf16 v[212:215], v[36:39], v[28:31], v[212:215]\n"
      "buffer_load_dwordx4 v[184:187], v247, s[40:43], s84 offen offset:1024 sc1\n"
      "v_mfma_f32_16x16x32_bf16 v[216:219], v[40:43], v[28:31], v[216:219]\n"
      "v_mfma_f32_16x16x32_bf16 v[220:223], v[44:47], v[28:31], v[220:223]\n"
      "v_mfma_f32_16x16x32_bf16 v[224:227], v[48:51], v[28:31], v[224:227]\n"
      "s_waitcnt vmcnt(26)\n"
      "v_cmp_ne_u32 vcc, s94, v250\n"
      "s_cmp_eq_u64 vcc, 0\n"
      "s_cbranch_scc0 SPIN1_%=\n"
      "SPB1_%=:\n"
      "v_mfma_f32_16x16x32_bf16 a[0:3], v[84:87], v[52:55], a[0:3]\n"
      "s_add_u32 s83, s69, 20480\n"
      "buffer_load_dwordx4 v[32:35], v252, s[44:47], s83 offen offset:0 sc1\n"
      "v_mfma_f32_16x16x32_bf16 a[4:7], v[88:91], v[52:55], a[4:7]\n"
      "v_mfma_f32_16x16x32_bf16 a[8:11], v[92:95], v[52:55], a[8:11]\n"
      "v_mfma_f32_16x16x32_bf16 a[12:15], v[96:99], v[52:55], a[12:15]\n"
      "buffer_load_dwordx4 v[36:39], v252, s[44:47], s83 offen offset:1024 sc1\n"
      "v_mfma_f32_16x16x32_bf16 a[16:19], v[100:103], v[52:55], a[16:19]\n"
      "v_mfma_f32_16x16x32_bf16 a[20:23], v[84:87], v[56:59], a[20:23]\n"
      "v_mfma_f32_16x16x32_bf16 a[24:27], v[88:91], v[56:59], a[24:27]\n"
      "buffer_load_dwordx4 v[40:43], v252, s[44:47], s83 offen offset:2048 sc1\n"
      "v_mfma_f32_16x16x32_bf16 a[28:31], v[92:95], v[56:59], a[28:31]\n"
      "v_mfma_f32_16x16x32_bf16 a[32:35], v[96:99], v[56:59], a[32:35]\n"
      "v_mfma_f32_16x16x32_bf16 a[36:39], v[100:103], v[56:59], a[36:39]\n"
      "buffer_load_dwordx4 v[44:47], v252, s[44:47], s83 offen offset:3072 sc1\n"
      "s_waitcnt vmcnt(30)\n"
      "v_mfma_f32_16x16x32_bf16 a[40:43], v[84:87], v[60:63], a[40:43]\n"
      "v_mfma_f32_16x16x32_bf16 a[44:47], v[88:91], v[60:63], a[44:47]\n"
      "v_mfma_f32_16x16x32_bf16 a[48:51], v[92:95], v[60:63], a[48:51]\n"
      "buffer_load_dwordx4 v[48:51], v248, s[44:47], s83 offen offset:0 sc1\n"
      "v_mfma_f32_16x16x32_bf16 a[52:55], v[96:99], v[60:63], a[52:55]\n"
      "v_mfma_f32_16x16x32_bf16 a[56:59], v[100:103], v[60:63], a[56:59]\n"
      "v_mfma_f32_16x16x32_bf16 a[60:63], v[84:87], v[64:67], a[60:63]\n"
      "s_add_u32 s84, s68, 32768\n"
      "buffer_load_dwordx4 v[0:3], v244, s[40:43], s84 offen offset:0 sc1\n"
      "v_mfma_f32_16x16x32_bf16 a[64:67], v[88:91], v[64:67], a[64:67]\n"
      "v_mfma_f32_16x16x32_bf16 a[68:71], v[92:95], v[64:67], a[68:71]\n"
      "v_mfma_f32_16x16x32_bf16 a[72:75], v[96:99], v[64:67], a[72:75]\n"
      "buffer_load_dwordx4 v[4:7], v244, s[40:43], s84 offen offset:1024 sc1\n"
      "v_mfma_f32_16x16x32_bf16 a[76:79], v[100:103], v[64:67], a[76:79]\n"
      "s_waitcnt vmcnt(33)\n"
      "v_mfma_f32_16x16x32_bf16 a[80:83], v[84:87], v[68:71], a[80:83]\n"
      "v_mfma_f32_16x16x32_bf16 a[84:87], v[88:91], v[68:71], a[84:87]\n"
      "buffer_load_dwordx4 v[8:11], v245, s[40:43], s84 offen offset:0 sc1\n"
      "v_mfma_f32_16x16x32_bf16 a[88:91], v[92:95], v[68:71], a[88:91]\n"
      "v_mfma_f32_16x16x32_bf16 a[92:95], v[96:99], v[68:71], a[92:95]\n"
      "v_mfma_f32_16x16x32_bf16 a[96:99], v[100:103], v[68:71], a[96:99]\n"
      "buffer_load_dwordx4 v[12:15], v245, s[40:43], s84 offen offset:1024 sc1\n"
      "v_mfma_f32_16x16x32_bf16 a[100:103], v[84:87], v[72:75], a[100:103]\n"
      "v_mfma_f32_16x16x32_bf16 a[104:107], v[88:91], v[72:75], a[104:107]\n"
      "v_mfma_f32_16x16x32_bf16 a[108:111], v[92:95], v[72:75], a[108:111]\n"
      "buffer_load_dwordx4 v[16:19], v246, s[40:43], s84 offen offset:0 sc1\n"
      "v_mfma_f32_16x16x32_bf16 a[112:115], v[96:99], v[72:75], a[112:115]\n"
      "v_mfma_f32_16x16x32_bf16 a[116:119], v[100:103], v[72:75], a[116:119]\n"
      "s_waitcnt vmcnt(36)\n"
      "v_mfma_f32_16x16x32_bf16 a[120:123], v[84:87], v[76:79], a[120:123]\n"
      "buffer_load_dwordx4 v[20:23], v246, s[40:43], s84 offen offset:1024 sc1\n"
      "v_mfma_f32_16x16x32_bf16 a[124:127], v[88:91], v[76:79], a[124:127]\n"
      "v_mfma_f32_16x16x32_bf16 a[128:131], v[92:95], v[76:79], a[128:131]\n"
      "v_mfma_f32_16x16x32_bf16 a[132:135], v[96:99], v[76:79], a[132:135]\n"
      "buffer_load_dwordx4 v[24:27], v247, s[40:43], s84 offen offset:0 sc1\n"
      "v_mfma_f32_16x16x32_bf16 a[136:139], v[100:103], v[76:79], a[136:139]\n"
      "v_mfma_f32_16x16x32_bf16 v[208:211], v[84:87], v[80:83], v[208:211]\n"
      "v_mfma_f32_16x16x32_bf16 v[212:215], v[88:91], v[80:83], v[212:215]\n"
      "buffer_load_dwordx4 v[28:31], v247, s[40:43], s84 offen offset:1024 sc1\n"
      "v_mfma_f32_16x16x32_bf16 v[216:219], v[92:95], v[80:83], v[216:219]\n"
      "v_mfma_f32_16x16x32_bf16 v[220:223], v[96:99], v[80:83], v[220:223]\n"
      "v_mfma_f32_16x16x32_bf16 v[224:227], v[100:103], v[80:83], v[224:227]\n"
      "s_waitcnt vmcnt(32)\n"
      "v_mfma_f32_16x16x32_bf16 a[0:3], v[136:139], v[104:107], a[0:3]\n"
      "s_add_u32 s83, s69, 61440\n"
      "buffer_load_dwordx4 v[84:87], v252, s[44:47], s83 offen offset:0 sc1\n"
      "v_mfma_f32_16x16x32_bf16 a[4:7], v[140:143], v[104:107], a[4:7]\n"
      "v_mfma_f32_16x16x32_bf16 a[8:11], v[144:147], v[104:107], a[8:11]\n"
      "v_mfma_f32_16x16x32_bf16 a[12:15], v[148:151], v[104:107], a[12:15]\n"
      "buffer_load_dwordx4 v[88:91], v252, s[44:47], s83 offen offset:1024 sc1\n"
      "v_mfma_f32_16x16x32_bf16 a[16:19], v[152:155], v[104:107], a[16:19]\n"
      "v_mfma_f32_16x16x32_bf16 a[20:23], v[136:139], v[108:111], a[20:23]\n"
      "v_mfma_f32_16x16x32_bf16 a[24:27], v[140:143], v[108:111], a[24:27]\n"
      "buffer_load_dwordx4 v[92:95], v252, s[44:47], s83 offen offset:2048 sc1\n"
      "v_mfma_f32_16x16x32_bf16 a[28:31], v[144:147], v[108:111], a[28:31]\n"
      "v_mfma_f32_16x16x32_bf16 a[32:35], v[148:151], v[108:111], a[32:35]\n"
      "v_mfma_f32_16x16x32_bf16 a[36:39], v[152:155], v[108:111], a[36:39]\n"
      "buffer_load_dwordx4 v[96:99], v252, s[44:47], s83 offen offset:3072 sc1\n"
      "s_waitcnt vmcnt(34)\n"
      "v_mfma_f32_16x16x32_bf16 a[40:43], v[136:139], v[112:115], a[40:43]\n"
      "v_mfma_f32_16x16x32_bf16 a[44:47], v[140:143], v[112:115], a[44:47]\n"
      "v_mfma_f32_16x16x32_bf16 a[48:51], v[144:147], v[112:115], a[48:51]\n"
      "buffer_load_dwordx4 v[100:103], v248, s[44:47], s83 offen offset:0 sc1\n"
      "v_mfma_f32_16x16x32_bf16 a[52:55], v[148:151], v[112:115], a[52:55]\n"
      "v_mfma_f32_16x16x32_bf16 a[56:59], v[152:155], v[112:115], a[56:59]\n"
      "v_mfma_f32_16x16x32_bf16 a[60:63], v[136:139], v[116:119], a[60:63]\n"
      "s_add_u32 s84, s68, 98304\n"
      "buffer_load_dwordx4 v[52:55], v244, s[40:43], s84 offen offset:0 sc1\n"
      "v_mfma_f32_16x16x32_bf16 a[64:67], v[140:143], v[116:119], a[64:67]\n"
      "v_mfma_f32_16x16x32_bf16 a[68:71], v[144:147], v[116:119], a[68:71]\n"
      "v_mfma_f32_16x16x32_bf16 a[72:75], v[148:151], v[116:119], a[72:75]\n"
      "buffer_load_dwordx4 v[56:59], v244, s[40:43], s84 offen offset:1024 sc1\n"
      "v_mfma_f32_16x16x32_bf16 a[76:79], v[152:155], v[116:119], a[76:79]\n"
      "s_waitcnt vmcnt(35)\n"
      "v_mfma_f32_16x16x32_bf16 a[80:83], v[136:139], v[120:123], a[80:83]\n"
      "v_mfma_f32_16x16x32_bf16 a[84:87], v[140:143], v[120:123], a[84:87]\n"
      "buffer_load_dwordx4 v[60:63], v245, s[40:43], s84 offen offset:0 sc1\n"
      "v_mfma_f32_16x16x32_bf16 a[88:91], v[144:147], v[120:123], a[88:91]\n"
      "v_mfma_f32_16x16x32_bf16 a[92:95], v[148:151], v[120:123], a[92:95]\n"
      "v_mfma_f32_16x16x32_bf16 a[96:99], v[152:155], v[120:123], a[96:99]\n"
      "buffer_load_dwordx4 v[64:67], v245, s[40:43], s84 offen offset:1024 sc1\n"
      "v_mfma_f32_16x16x32_bf16 a[100:103], v[136:139], v[124:127], a[100:103]\n"
      "v_mfma_f32_16x16x32_bf16 a[104:107], v[140:143], v[124:127], a[104:107]\n"
      "v_mfma_f32_16x16x32_bf16 a[108:111], v[144:147], v[124:127], a[108:111]\n"
      "buffer_load_dwordx4 v[68:71], v246, s[40:43], s84 offen offset:0 sc1\n"
      "v_mfma_f32_16x16x32_bf16 a[112:115], v[148:151], v[124:127], a[112:115]\n"
      "v_mfma_f32_16x16x32_bf16 a[116:119], v[152:155], v[124:127], a[116:119]\n"
      "s_waitcnt vmcnt(36)\n"
      "v_mfma_f32_16x16x32_bf16 a[120:123], v[136:139], v[128:131], a[120:123]\n"
      "buffer_load_dwordx4 v[72:75], v246, s[40:43], s84 offen offset:1024 sc1\n"
      "v_mfma_f32_16x16x32_bf16 a[124:127], v[140:143], v[128:131], a[124:127]\n"
      "v_mfma_f32_16x16x32_bf16 a[128:131], v[144:147], v[128:131], a[128:131]\n"
      "v_mfma_f32_16x16x32_bf16 a[132:135], v[148:151], v[128:131], a[132:135]\n"
      "buffer_load_dwordx4 v[76:79], v247, s[40:43], s84 offen offset:0 sc1\n"
      "v_mfma_f32_16x16x32_bf16 a[136:139], v[152:155], v[128:131], a[136:139]\n"
      "v_mfma_f32_16x16x32_bf16 v[208:211], v[136:139], v[132:135], v[208:211]\n"
      "v_mfma_f32_16x16x32_bf16 v[212:215], v[140:143], v[132:135], v[212:215]\n"
      "buffer_load_dwordx4 v[80:83], v247, s[40:43], s84 offen offset:1024 sc1\n"
      "v_mfma_f32_16x16x32_bf16 v[216:219], v[144:147], v[132:135], v[216:219]\n"
      "v_mfma_f32_16x16x32_bf16 v[220:223], v[148:151], v[132:135], v[220:223]\n"
      "v_mfma_f32_16x16x32_bf16 v[224:227], v[152:155], v[132:135], v[224:227]\n"
      "s_waitcnt vmcnt(32)\n"
      "v_mfma_f32_16x16x32_bf16 a[0:3], v[188:191], v[156:159], a[0:3]\n"
      "s_add_u32 s83, s69, 102400\n"
      "buffer_load_dwordx4 v[136:139], v252, s[44:47], s83 offen offset:0 sc1\n"
      "v_mfma_f32_16x16x32_bf16 a[4:7], v[192:195], v[156:159], a[4:7]\n"
      "v_mfma_f32_16x16x32_bf16 a[8:11], v[196:199], v[156:159], a[8:11]\n"
      "v_mfma_f32_16x16x32_bf16 a[12:15], v[200:203], v[156:159], a[12:15]\n"
      "buffer_load_dwordx4 v[140:143], v252, s[44:47], s83 offen offset:1024 sc1\n"
      "v_mfma_f32_16x16x32_bf16 a[16:19], v[204:207], v[156:159], a[16:19]\n"
      "v_mfma_f32_16x16x32_bf16 a[20:23], v[188:191], v[160:163], a[20:23]\n"
      "v_mfma_f32_16x16x32_bf16 a[24:27], v[192:195], v[160:163], a[24:27]\n"
      "buffer_load_dwordx4 v[144:147], v252, s[44:47], s83 offen offset:2048 sc1\n"
      "v_mfma_f32_16x16x32_bf16 a[28:31], v[196:199], v[160:163], a[28:31]\n"
      "v_mfma_f32_16x16x32_bf16 a[32:35], v[200:203], v[160:163], a[32:35]\n"
      "v_mfma_f32_16x16x32_bf16 a[36:39], v[204:207], v[160:163], a[36:39]\n"
      "buffer_load_dwordx4 v[148:151], v252, s[44:47], s83 offen offset:3072 sc1\n"
      "s_waitcnt vmcnt(34)\n"
      "v_mfma_f32_16x16x32_bf16 a[40:43], v[188:191], v[164:167], a[40:43]\n"
      "v_mfma_f32_16x16x32_bf16 a[44:47], v[192:195], v[164:167], a[44:47]\n"
      "v_mfma_f32_16x16x32_bf16 a[48:51], v[196:199], v[164:167], a[48:51]\n"
      "buffer_load_dwordx4 v[152:155], v248, s[44:47], s83 offen offset:0 sc1\n"
      "v_mfma_f32_16x16x32_bf16 a[52:55], v[200:203], v[164:167], a[52:55]\n"
      "v_mfma_f32_16x16x32_bf16 a[56:59], v[204:207], v[164:167], a[56:59]\n"
      "v_mfma_f32_16x16x32_bf16 a[60:63], v[188:191], v[168:171], a[60:63]\n"
      "s_add_u32 s84, s68, 163840\n"
      "buffer_load_dwordx4 v[104:107], v244, s[40:43], s84 offen offset:0 sc1\n"
      "v_mfma_f32_16x16x32_bf16 a[64:67], v[192:195], v[168:171], a[64:67]\n"
      "v_mfma_f32_16x16x32_bf16 a[68:71], v[196:199], v[168:171], a[68:71]\n"
      "v_mfma_f32_16x16x32_bf16 a[72:75], v[200:203], v[168:171], a[72:75]\n"
      "buffer_load_dwordx4 v[108:111], v244, s[40:43], s84 offen offset:1024 sc1\n"
      "v_mfma_f32_16x16x32_bf16 a[76:79], v[204:207], v[168:171], a[76:79]\n"
      "s_waitcnt vmcnt(35)\n"
      "v_mfma_f32_16x16x32_bf16 a[80:83], v[188:191], v[172:175], a[80:83]\n"
      "v_mfma_f32_16x16x32_bf16 a[84:87], v[192:195], v[172:175], a[84:87]\n"
      "buffer_load_dwordx4 v[112:115], v245, s[40:43], s84 offen offset:0 sc1\n"
      "v_mfma_f32_16x16x32_bf16 a[88:91], v[196:199], v[172:175], a[88:91]\n"
      "v_mfma_f32_16x16x32_bf16 a[92:95], v[200:203], v[172:175], a[92:95]\n"
      "v_mfma_f32_16x16x32_bf16 a[96:99], v[204:207], v[172:175], a[96:99]\n"
      "buffer_load_dwordx4 v[116:119], v245, s[40:43], s84 offen offset:1024 sc1\n"
      "v_mfma_f32_16x16x32_bf16 a[100:103], v[188:191], v[176:179], a[100:103]\n"
      "v_mfma_f32_16x16x32_bf16 a[104:107], v[192:195], v[176:179], a[104:107]\n"
      "v_mfma_f32_16x16x32_bf16 a[108:111], v[196:199], v[176:179], a[108:111]\n"
      "buffer_load_dwordx4 v[120:123], v246, s[40:43], s84 offen offset:0 sc1\n"
      "v_mfma_f32_16x16x32_bf16 a[112:115], v[200:203], v[176:179], a[112:115]\n"
      "v_mfma_f32_16x16x32_bf16 a[116:119], v[204:207], v[176:179], a[116:119]\n"
      "s_waitcnt vmcnt(36)\n"
      "v_mfma_f32_16x16x32_bf16 a[120:123], v[188:191], v[180:183], a[120:123]\n"
      "buffer_load_dwordx4 v[124:127], v246, s[40:43], s84 offen offset:1024 sc1\n"
      "v_mfma_f32_16x16x32_bf16 a[124:127], v[192:195], v[180:183], a[124:127]\n"
      "v_mfma_f32_16x16x32_bf16 a[128:131], v[196:199], v[180:183], a[128:131]\n"
      "v_mfma_f32_16x16x32_bf16 a[132:135], v[200:203], v[180:183], a[132:135]\n"
      "buffer_load_dwordx4 v[128:131], v247, s[40:43], s84 offen offset:0 sc1\n"
      "v_mfma_f32_16x16x32_bf16 a[136:139], v[204:207], v[180:183], a[136:139]\n"
      "v_mfma_f32_16x16x32_bf16 v[208:211], v[188:191], v[184:187], v[208:211]\n"
      "v_mfma_f32_16x16x32_bf16 v[212:215], v[192:195], v[184:187], v[212:215]\n"
      "buffer_load_dwordx4 v[132:135], v247, s[40:43], s84 offen offset:1024 sc1\n"
      "v_mfma_f32_16x16x32_bf16 v[216:219], v[196:199], v[184:187], v[216:219]\n"
      "v_mfma_f32_16x16x32_bf16 v[220:223], v[200:203], v[184:187], v[220:223]\n"
      "v_mfma_f32_16x16x32_bf16 v[224:227], v[204:207], v[184:187], v[224:227]\n"
      "s_waitcnt vmcnt(32)\n"
      "v_mfma_f32_16x16x32_bf16 a[0:3], v[32:35], v[0:3], a[0:3]\n"
      "s_add_u32 s83, s69, 143360\n"
      "buffer_load_dwordx4 v[188:191], v252, s[44:47], s83 offen offset:0 sc1\n"
      "v_mfma_f32_16x16x32_bf16 a[4:7], v[36:39], v[0:3], a[4:7]\n"
      "v_mfma_f32_16x16x32_bf16 a[8:11], v[40:43], v[0:3], a[8:11]\n"
      "v_mfma_f32_16x16x32_bf16 a[12:15], v[44:47], v[0:3], a[12:15]\n"
      "buffer_load_dwordx4 v[192:195], v252, s[44:47], s83 offen offset:1024 sc1\n"
      "v_mfma_f32_16x16x32_bf16 a[16:19], v[48:51], v[0:3], a[16:19]\n"
      "v_mfma_f32_16x16x32_bf16 a[20:23], v[32:35], v[4:7], a[20:23]\n"
      "v_mfma_f32_16x16x32_bf16 a[24:27], v[36:39], v[4:7], a[24:27]\n"
      "buffer_load_dwordx4 v[196:199], v252, s[44:47], s83 offen offset:2048 sc1\n"
      "v_mfma_f32_16x16x32_bf16 a[28:31], v[40:43], v[4:7], a[28:31]\n"
      "v_mfma_f32_16x16x32_bf16 a[32:35], v[44:47], v[4:7], a[32:35]\n"
      "v_mfma_f32_16x16x32_bf16 a[36:39], v[48:51], v[4:7], a[36:39]\n"
      "buffer_load_dwordx4 v[200:203], v252, s[44:47], s83 offen offset:3072 sc1\n"
      "s_waitcnt vmcnt(34)\n"
      "v_mfma_f32_16x16x32_bf16 a[40:43], v[32:35], v[8:11], a[40:43]\n"
      "v_mfma_f32_16x16x32_bf16 a[44:47], v[36:39], v[8:11], a[44:47]\n"
      "v_mfma_f32_16x16x32_bf16 a[48:51], v[40:43], v[8:11], a[48:51]\n"
      "buffer_load_dwordx4 v[204:207], v248, s[44:47], s83 offen offset:0 sc1\n"
      "v_mfma_f32_16x16x32_bf16 a[52:55], v[44:47], v[8:11], a[52:55]\n"
      "v_mfma_f32_16x16x32_bf16 a[56:59], v[48:51], v[8:11], a[56:59]\n"
      "v_mfma_f32_16x16x32_bf16 a[60:63], v[32:35], v[12:15], a[60:63]\n"
      "s_add_u32 s84, s68, 229376\n"
      "buffer_load_dwordx4 v[156:159], v244, s[40:43], s84 offen offset:0 sc1\n"
      "v_mfma_f32_16x16x32_bf16 a[64:67], v[36:39], v[12:15], a[64:67]\n"
      "v_mfma_f32_16x16x32_bf16 a[68:71], v[40:43], v[12:15], a[68:71]\n"
      "v_mfma_f32_16x16x32_bf16 a[72:75], v[44:47], v[12:15], a[72:75]\n"
      "buffer_load_dwordx4 v[160:163], v244, s[40:43], s84 offen offset:1024 sc1\n"
      "v_mfma_f32_16x16x32_bf16 a[76:79], v[48:51], v[12:15], a[76:79]\n"
      "s_waitcnt vmcnt(35)\n"
      "v_mfma_f32_16x16x32_bf16 a[80:83], v[32:35], v[16:19], a[80:83]\n"
      "v_mfma_f32_16x16x32_bf16 a[84:87], v[36:39], v[16:19], a[84:87]\n"
      "buffer_load_dwordx4 v[164:167], v245, s[40:43], s84 offen offset:0 sc1\n"
      "v_mfma_f32_16x16x32_bf16 a[88:91], v[40:43], v[16:19], a[88:91]\n"
      "v_mfma_f32_16x16x32_bf16 a[92:95], v[44:47], v[16:19], a[92:95]\n"
      "v_mfma_f32_16x16x32_bf16 a[96:99], v[48:51], v[16:19], a[96:99]\n"
      "buffer_load_dwordx4 v[168:171], v245, s[40:43], s84 offen offset:1024 sc1\n"
      "v_mfma_f32_16x16x32_bf16 a[100:103], v[32:35], v[20:23], a[100:103]\n"
      "v_mfma_f32_16x16x32_bf16 a[104:107], v[36:39], v[20:23], a[104:107]\n"
      "v_mfma_f32_16x16x32_bf16 a[108:111], v[40:43], v[20:23], a[108:111]\n"
      "buffer_load_dwordx4 v[172:175], v246, s[40:43], s84 offen offset:0 sc1\n"
      "v_mfma_f32_16x16x32_bf16 a[112:115], v[44:47], v[20:23], a[112:115]\n"
      "v_mfma_f32_16x16x32_bf16 a[116:119], v[48:51], v[20:23], a[116:119]\n"
      "s_waitcnt vmcnt(36)\n"
      "v_mfma_f32_16x16x32_bf16 a[120:123], v[32:35], v[24:27], a[120:123]\n"
      "buffer_load_dwordx4 v[176:179], v246, s[40:43], s84 offen offset:1024 sc1\n"
      "v_mfma_f32_16x16x32_bf16 a[124:127], v[36:39], v[24:27], a[124:127]\n"
      "v_mfma_f32_16x16x32_bf16 a[128:131], v[40:43], v[24:27], a[128:131]\n"
      "v_mfma_f32_16x16x32_bf16 a[132:135], v[44:47], v[24:27], a[132:135]\n"
      "buffer_load_dwordx4 v[180:183], v247, s[40:43], s84 offen offset:0 sc1\n"
      "v_mfma_f32_16x16x32_bf16 a[136:139], v[48:51], v[24:27], a[136:139]\n"
      "v_mfma_f32_16x16x32_bf16 v[208:211], v[32:35], v[28:31], v[208:211]\n"
      "v_mfma_f32_16x16x32_bf16 v[212:215], v[36:39], v[28:31], v[212:215]\n"
      "buffer_load_dwordx4 v[184:187], v247, s[40:43], s84 offen offset:1024 sc1\n"
      "v_mfma_f32_16x16x32_bf16 v[216:219], v[40:43], v[28:31], v[216:219]\n"
      "v_mfma_f32_16x16x32_bf16 v[220:223], v[44:47], v[28:31], v[220:223]\n"
      "v_mfma_f32_16x16x32_bf16 v[224:227], v[48:51], v[28:31], v[224:227]\n"
      "s_waitcnt vmcnt(32)\n"
      "v_mfma_f32_16x16x32_bf16 a[0:3], v[84:87], v[52:55], a[0:3]\n"
      "s_add_u32 s83, s69, 184320\n"
      "buffer_load_dwordx4 v[32:35], v252, s[44:47], s83 offen offset:0 sc1\n"
      "v_mfma_f32_16x16x32_bf16 a[4:7], v[88:91], v[52:55], a[4:7]\n"
      "v_mfma_f32_16x16x32_bf16 a[8:11], v[92:95], v[52:55], a[8:11]\n"
      "v_mfma_f32_16x16x32_bf16 a[12:15], v[96:99], v[52:55], a[12:15]\n"
      "buffer_load_dwordx4 v[36:39], v252, s[44:47], s83 offen offset:1024 sc1\n"
      "v_mfma_f32_16x16x32_bf16 a[16:19], v[100:103], v[52:55], a[16:19]\n"
      "v_mfma_f32_16x16x32_bf16 a[20:23], v[84:87], v[56:59], a[20:23]\n"
      "v_mfma_f32_16x16x32_bf16 a[24:27], v[88:91], v[56:59], a[24:27]\n"
      "buffer_load_dwordx4 v[40:43], v252, s[44:47], s83 offen offset:2048 sc1\n"
      "v_mfma_f32_16x16x32_bf16 a[28:31], v[92:95], v[56:59], a[28:31]\n"
      "v_mfma_f32_16x16x32_bf16 a[32:35], v[96:99], v[56:59], a[32:35]\n"
      "v_mfma_f32_16x16x32_bf16 a[36:39], v[100:103], v[56:59], a[36:39]\n"
      "buffer_load_dwordx4 v[44:47], v252, s[44:47], s83 offen offset:3072 sc1\n"
      "s_waitcnt vmcnt(34)\n"
      "v_mfma_f32_16x16x32_bf16 a[40:43], v[84:87], v[60:63], a[40:43]\n"
      "v_mfma_f32_16x16x32_bf16 a[44:47], v[88:91], v[60:63], a[44:47]\n"
      "v_mfma_f32_16x16x32_bf16 a[48:51], v[92:95], v[60:63], a[48:51]\n"
      "buffer_load_dwordx4 v[48:51], v248, s[44:47], s83 offen offset:0 sc1\n"
      "v_mfma_f32_16x16x32_bf16 a[52:55], v[96:99], v[60:63], a[52:55]\n"
      "v_mfma_f32_16x16x32_bf16 a[56:59], v[100:103], v[60:63], a[56:59]\n"
      "v_mfma_f32_16x16x32_bf16 a[60:63], v[84:87], v[64:67], a[60:63]\n"
      "s_add_u32 s84, s68, 294912\n"
      "buffer_load_dwordx4 v[0:3], v244, s[40:43], s84 offen offset:0 sc1\n"
      "v_mfma_f32_16x16x32_bf16 a[64:67], v[88:91], v[64:67], a[64:67]\n"
      "v_mfma_f32_16x16x32_bf16 a[68:71], v[92:95], v[64:67], a[68:71]\n"
      "v_mfma_f32_16x16x32_bf16 a[72:75], v[96:99], v[64:67], a[72:75]\n"
      "buffer_load_dwordx4 v[4:7], v244, s[40:43], s84 offen offset:1024 sc1\n"
      "v_mfma_f32_16x16x32_bf16 a[76:79], v[100:103], v[64:67], a[76:79]\n"
      "s_waitcnt vmcnt(35)\n"
      "v_mfma_f32_16x16x32_bf16 a[80:83], v[84:87], v[68:71], a[80:83]\n"
      "v_mfma_f32_16x16x32_bf16 a[84:87], v[88:91], v[68:71], a[84:87]\n"
      "buffer_load_dwordx4 v[8:11], v245, s[40:43], s84 offen offset:0 sc1\n"
      "v_mfma_f32_16x16x32_bf16 a[88:91], v[92:95], v[68:71], a[88:91]\n"
      "v_mfma_f32_16x16x32_bf16 a[92:95], v[96:99], v[68:71], a[92:95]\n"
      "v_mfma_f32_16x16x32_bf16 a[96:99], v[100:103], v[68:71], a[96:99]\n"
      "buffer_load_dwordx4 v[12:15], v245, s[40:43], s84 offen offset:1024 sc1\n"
      "v_mfma_f32_16x16x32_bf16 a[100:103], v[84:87], v[72:75], a[100:103]\n"
      "v_mfma_f32_16x16x32_bf16 a[104:107], v[88:91], v[72:75], a[104:107]\n"
      "v_mfma_f32_16x16x32_bf16 a[108:111], v[92:95], v[72:75], a[108:111]\n"
      "buffer_load_dwordx4 v[16:19], v246, s[40:43], s84 offen offset:0 sc1\n"
      "v_mfma_f32_16x16x32_bf16 a[112:115], v[96:99], v[72:75], a[112:115]\n"
      "v_mfma_f32_16x16x32_bf16 a[116:119], v[100:103], v[72:75], a[116:119]\n"
      "s_waitcnt vmcnt(36)\n"
      "v_mfma_f32_16x16x32_bf16 a[120:123], v[84:87], v[76:79], a[120:123]\n"
      "buffer_load_dwordx4 v[20:23], v246, s[40:43], s84 offen offset:1024 sc1\n"
      "v_mfma_f32_16x16x32_bf16 a[124:127], v[88:91], v[76:79], a[124:127]\n"
      "v_mfma_f32_16x16x32_bf16 a[128:131], v[92:95], v[76:79], a[128:131]\n"
      "v_mfma_f32_16x16x32_bf16 a[132:135], v[96:99], v[76:79], a[132:135]\n"
      "buffer_load_dwordx4 v[24:27], v247, s[40:43], s84 offen offset:0 sc1\n"
      "v_mfma_f32_16x16x32_bf16 a[136:139], v[100:103], v[76:79], a[136:139]\n"
      "v_mfma_f32_16x16x32_bf16 v[208:211], v[84:87], v[80:83], v[208:211]\n"
      "v_mfma_f32_16x16x32_bf16 v[212:215], v[88:91], v[80:83], v[212:215]\n"
      "buffer_load_dwordx4 v[28:31], v247, s[40:43], s84 offen offset:1024 sc1\n"
      "v_mfma_f32_16x16x32_bf16 v[216:219], v[92:95], v[80:83], v[216:219]\n"
      "v_mfma_f32_16x16x32_bf16 v[220:223], v[96:99], v[80:83], v[220:223]\n"
      "v_mfma_f32_16x16x32_bf16 v[224:227], v[100:103], v[80:83], v[224:227]\n"
      "s_waitcnt vmcnt(32)\n"
      "v_mfma_f32_16x16x32_bf16 a[0:3], v[136:139], v[104:107], a[0:3]\n"
      "s_add_u32 s83, s69, 225280\n"
      "buffer_load_dwordx4 v[84:87], v252, s[44:47], s83 offen offset:0 sc1\n"
      "v_mfma_f32_16x16x32_bf16 a[4:7], v[140:143], v[104:107], a[4:7]\n"
      "v_mfma_f32_16x16x32_bf16 a[8:11], v[144:147], v[104:107], a[8:11]\n"
      "v_mfma_f32_16x16x32_bf16 a[12:15], v[148:151], v[104:107], a[12:15]\n"
      "buffer_load_dwordx4 v[88:91], v252, s[44:47], s83 offen offset:1024 sc1\n"
      "v_mfma_f32_16x16x32_bf16 a[16:19], v[152:155], v[104:107], a[16:19]\n"
      "v_mfma_f32_16x16x32_bf16 a[20:23], v[136:139], v[108:111], a[20:23]\n"
      "v_mfma_f32_16x16x32_bf16 a[24:27], v[140:143], v[108:111], a[24:27]\n"
      "buffer_load_dwordx4 v[92:95], v252, s[44:47], s83 offen offset:2048 sc1\n"
      "v_mfma_f32_16x16x32_bf16 a[28:31], v[144:147], v[108:111], a[28:31]\n"
      "v_mfma_f32_16x16x32_bf16 a[32:35], v[148:151], v[108:111], a[32:35]\n"
      "v_mfma_f32_16x16x32_bf16 a[36:39], v[152:155], v[108:111], a[36:39]\n"
      "buffer_load_dwordx4 v[96:99], v252, s[44:47], s83 offen offset:3072 sc1\n"
      "s_waitcnt vmcnt(34)\n"
      "v_mfma_f32_16x16x32_bf16 a[40:43], v[136:139], v[112:115], a[40:43]\n"
      "v_mfma_f32_16x16x32_bf16 a[44:47], v[140:143], v[112:115], a[44:47]\n"
      "v_mfma_f32_16x16x32_bf16 a[48:51], v[144:147], v[112:115], a[48:51]\n"
      "buffer_load_dwordx4 v[100:103], v248, s[44:47], s83 offen offset:0 sc1\n"
      "v_mfma_f32_16x16x32_bf16 a[52:55], v[148:151], v[112:115], a[52:55]\n"
      "v_mfma_f32_16x16x32_bf16 a[56:59], v[152:155], v[112:115], a[56:59]\n"
      "v_mfma_f32_16x16x32_bf16 a[60:63], v[136:139], v[116:119], a[60:63]\n"
      "s_add_u32 s84, s68, 360448\n"
      "buffer_load_dwordx4 v[52:55], v244, s[40:43], s84 offen offset:0 sc1\n"
      "v_mfma_f32_16x16x32_bf16 a[64:67], v[140:143], v[116:119], a[64:67]\n"
      "v_mfma_f32_16x16x32_bf16 a[68:71], v[144:147], v[116:119], a[68:71]\n"
      "v_mfma_f32_16x16x32_bf16 a[72:75], v[148:151], v[116:119], a[72:75]\n"
      "buffer_load_dwordx4 v[56:59], v244, s[40:43], s84 offen offset:1024 sc1\n"
      "v_mfma_f32_16x16x32_bf16 a[76:79], v[152:155], v[116:119], a[76:79]\n"
      "s_waitcnt vmcnt(35)\n"
      "v_mfma_f32_16x16x32_bf16 a[80:83], v[136:139], v[120:123], a[80:83]\n"
      "v_mfma_f32_16x16x32_bf16 a[84:87], v[140:143], v[120:123], a[84:87]\n"
      "buffer_load_dwordx4 v[60:63], v245, s[40:43], s84 offen offset:0 sc1\n"
      "v_mfma_f32_16x16x32_bf16 a[88:91], v[144:147], v[120:123], a[88:91]\n"
      "v_mfma_f32_16x16x32_bf16 a[92:95], v[148:151], v[120:123], a[92:95]\n"
      "v_mfma_f32_16x16x32_bf16 a[96:99], v[152:155], v[120:123], a[96:99]\n"
      "buffer_load_dwordx4 v[64:67], v245, s[40:43], s84 offen offset:1024 sc1\n"
      "v_mfma_f32_16x16x32_bf16 a[100:103], v[136:139], v[124:127], a[100:103]\n"
      "v_mfma_f32_16x16x32_bf16 a[104:107], v[140:143], v[124:127], a[104:107]\n"
      "v_mfma_f32_16x16x32_bf16 a[108:111], v[144:147], v[124:127], a[108:111]\n"
      "buffer_load_dwordx4 v[68:71], v246, s[40:43], s84 offen offset:0 sc1\n"
      "v_mfma_f32_16x16x32_bf16 a[112:115], v[148:151], v[124:127], a[112:115]\n"
      "v_mfma_f32_16x16x32_bf16 a[116:119], v[152:155], v[124:127], a[116:119]\n"
      "s_waitcnt vmcnt(36)\n"
      "v_mfma_f32_16x16x32_bf16 a[120:123], v[136:139], v[128:131], a[120:123]\n"
      "buffer_load_dwordx4 v[72:75], v246, s[40:43], s84 offen offset:1024 sc1\n"
      "v_mfma_f32_16x16x32_bf16 a[124:127], v[140:143], v[128:131], a[124:127]\n"
      "v_mfma_f32_16x16x32_bf16 a[128:131], v[144:147], v[128:131], a[128:131]\n"
      "v_mfma_f32_16x16x32_bf16 a[132:135], v[148:151], v[128:131], a[132:135]\n"
      "buffer_load_dwordx4 v[76:79], v247, s[40:43], s84 offen offset:0 sc1\n"
      "v_mfma_f32_16x16x32_bf16 a[136:139], v[152:155], v[128:131], a[136:139]\n"
      "v_mfma_f32_16x16x32_bf16 v[208:211], v[136:139], v[132:135], v[208:211]\n"
      "v_mfma_f32_16x16x32_bf16 v[212:215], v[140:143], v[132:135], v[212:215]\n"
      "buffer_load_dwordx4 v[80:83], v247, s[40:43], s84 offen offset:1024 sc1\n"
      "v_mfma_f32_16x16x32_bf16 v[216:219], v[144:147], v[132:135], v[216:219]\n"
      "v_mfma_f32_16x16x32_bf16 v[220:223], v[148:151], v[132:135], v[220:223]\n"
      "v_mfma_f32_16x16x32_bf16 v[224:227], v[152:155], v[132:135], v[224:227]\n"
      "s_waitcnt vmcnt(32)\n"
      "v_mfma_f32_16x16x32_bf16 a[0:3], v[188:191], v[156:159], a[0:3]\n"
      "s_add_u32 s83, s69, 266240\n"
      "buffer_load_dwordx4 v[136:139], v252, s[44:47], s83 offen offset:0 sc1\n"
      "v_mfma_f32_16x16x32_bf16 a[4:7], v[192:195], v[156:159], a[4:7]\n"
      "v_mfma_f32_16x16x32_bf16 a[8:11], v[196:199], v[156:159], a[8:11]\n"
      "v_mfma_f32_16x16x32_bf16 a[12:15], v[200:203], v[156:159], a[12:15]\n"
      "buffer_load_dwordx4 v[140:143], v252, s[44:47], s83 offen offset:1024 sc1\n"
      "v_mfma_f32_16x16x32_bf16 a[16:19], v[204:207], v[156:159], a[16:19]\n"
      "v_mfma_f32_16x16x32_bf16 a[20:23], v[188:191], v[160:163], a[20:23]\n"
      "v_mfma_f32_16x16x32_bf16 a[24:27], v[192:195], v[160:163], a[24:27]\n"
      "buffer_load_dwordx4 v[144:147], v252, s[44:47], s83 offen offset:2048 sc1\n"
      "v_mfma_f32_16x16x32_bf16 a[28:31], v[196:199], v[160:163], a[28:31]\n"
      "v_mfma_f32_16x16x32_bf16 a[32:35], v[200:203], v[160:163], a[32:35]\n"
      "v_mfma_f32_16x16x32_bf16 a[36:39], v[204:207], v[160:163], a[36:39]\n"
      "buffer_load_dwordx4 v[148:151], v252, s[44:47], s83 offen offset:3072 sc1\n"
      "s_waitcnt vmcnt(34)\n"
      "v_mfma_f32_16x16x32_bf16 a[40:43], v[188:191], v[164:167], a[40:43]\n"
      "v_mfma_f32_16x16x32_bf16 a[44:47], v[192:195], v[164:167], a[44:47]\n"
      "v_mfma_f32_16x16x32_bf16 a[48:51], v[196:199], v[164:167], a[48:51]\n"
      "buffer_load_dwordx4 v[152:155], v248, s[44:47], s83 offen offset:0 sc1\n"
      "v_mfma_f32_16x16x32_bf16 a[52:55], v[200:203], v[164:167], a[52:55]\n"
      "v_mfma_f32_16x16x32_bf16 a[56:59], v[204:207], v[164:167], a[56:59]\n"
      "v_mfma_f32_16x16x32_bf16 a[60:63], v[188:191], v[168:171], a[60:63]\n"
      "s_add_u32 s84, s68, 425984\n"
      "buffer_load_dwordx4 v[104:107], v244, s[40:43], s84 offen offset:0 sc1\n"
      "v_mfma_f32_16x16x32_bf16 a[64:67], v[192:195], v[168:171], a[64:67]\n"
      "v_mfma_f32_16x16x32_bf16 a[68:71], v[196:199], v[168:171], a[68:71]\n"
      "v_mfma_f32_16x16x32_bf16 a[72:75], v[200:203], v[168:171], a[72:75]\n"
      "buffer_load_dwordx4 v[108:111], v244, s[40:43], s84 offen offset:1024 sc1\n"
      "v_mfma_f32_16x16x32_bf16 a[76:79], v[204:207], v[168:171], a[76:79]\n"
      "s_waitcnt vmcnt(35)\n"
      "v_mfma_f32_16x16x32_bf16 a[80:83], v[188:191], v[172:175], a[80:83]\n"
      "v_mfma_f32_16x16x32_bf16 a[84:87], v[192:195], v[172:175], a[84:87]\n"
      "buffer_load_dwordx4 v[112:115], v245, s[40:43], s84 offen offset:0 sc1\n"
      "v_mfma_f32_16x16x32_bf16 a[88:91], v[196:199], v[172:175], a[88:91]\n"
      "v_mfma_f32_16x16x32_bf16 a[92:95], v[200:203], v[172:175], a[92:95]\n"
      "v_mfma_f32_16x16x32_bf16 a[96:99], v[204:207], v[172:175], a[96:99]\n"
      "buffer_load_dwordx4 v[116:119], v245, s[40:43], s84 offen offset:1024 sc1\n"
      "v_mfma_f32_16x16x32_bf16 a[100:103], v[188:191], v[176:179], a[100:103]\n"
      "v_mfma_f32_16x16x32_bf16 a[104:107], v[192:195], v[176:179], a[104:107]\n"
      "v_mfma_f32_16x16x32_bf16 a[108:111], v[196:199], v[176:179], a[108:111]\n"
      "buffer_load_dwordx4 v[120:123], v246, s[40:43], s84 offen offset:0 sc1\n"
      "v_mfma_f32_16x16x32_bf16 a[112:115], v[200:203], v[176:179], a[112:115]\n"
      "v_mfma_f32_16x16x32_bf16 a[116:119], v[204:207], v[176:179], a[116:119]\n"
      "s_waitcnt vmcnt(36)\n"
      "v_mfma_f32_16x16x32_bf16 a[120:123], v[188:191], v[180:183], a[120:123]\n"
      "buffer_load_dwordx4 v[124:127], v246, s[40:43], s84 offen offset:1024 sc1\n"
      "v_mfma_f32_16x16x32_bf16 a[124:127], v[192:195], v[180:183], a[124:127]\n"
      "v_mfma_f32_16x16x32_bf16 a[128:131], v[196:199], v[180:183], a[128:131]\n"
      "v_mfma_f32_16x16x32_bf16 a[132:135], v[200:203], v[180:183], a[132:135]\n"
      "buffer_load_dwordx4 v[128:131], v247, s[40:43], s84 offen offset:0 sc1\n"
      "v_mfma_f32_16x16x32_bf16 a[136:139], v[204:207], v[180:183], a[136:139]\n"
      "v_mfma_f32_16x16x32_bf16 v[208:211], v[188:191], v[184:187], v[208:211]\n"
      "v_mfma_f32_16x16x32_bf16 v[212:215], v[192:195], v[184:187], v[212:215]\n"
      "buffer_load_dwordx4 v[132:135], v247, s[40:43], s84 offen offset:1024 sc1\n"
      "v_mfma_f32_16x16x32_bf16 v[216:219], v[196:199], v[184:187], v[216:219]\n"
      "v_mfma_f32_16x16x32_bf16 v[220:223], v[200:203], v[184:187], v[220:223]\n"
      "v_mfma_f32_16x16x32_bf16 v[224:227], v[204:207], v[184:187], v[224:227]\n"
      "s_waitcnt vmcnt(32)\n"
      "v_mfma_f32_16x16x32_bf16 a[0:3], v[32:35], v[0:3], a[0:3]\n"
      "s_add_u32 s83, s69, 307200\n"
      "buffer_load_dwordx4 v[188:191], v252, s[44:47], s83 offen offset:0 sc1\n"
      "v_mfma_f32_16x16x32_bf16 a[4:7], v[36:39], v[0:3], a[4:7]\n"
      "v_mfma_f32_16x16x32_bf16 a[8:11], v[40:43], v[0:3], a[8:11]\n"
      "v_mfma_f32_16x16x32_bf16 a[12:15], v[44:47], v[0:3], a[12:15]\n"
      "buffer_load_dwordx4 v[192:195], v252, s[44:47], s83 offen offset:1024 sc1\n"
      "v_mfma_f32_16x16x32_bf16 a[16:19], v[48:51], v[0:3], a[16:19]\n"
      "v_mfma_f32_16x16x32_bf16 a[20:23], v[32:35], v[4:7], a[20:23]\n"
      "v_mfma_f32_16x16x32_bf16 a[24:27], v[36:39], v[4:7], a[24:27]\n"
      "buffer_load_dwordx4 v[196:199], v252, s[44:47], s83 offen offset:2048 sc1\n"
      "v_mfma_f32_16x16x32_bf16 a[28:31], v[40:43], v[4:7], a[28:31]\n"
      "v_mfma_f32_16x16x32_bf16 a[32:35], v[44:47], v[4:7], a[32:35]\n"
      "v_mfma_f32_16x16x32_bf16 a[36:39], v[48:51], v[4:7], a[36:39]\n"
      "buffer_load_dwordx4 v[200:203], v252, s[44:47], s83 offen offset:3072 sc1\n"
      "s_waitcnt vmcnt(34)\n"
      "v_mfma_f32_16x16x32_bf16 a[40:43], v[32:35], v[8:11], a[40:43]\n"
      "v_mfma_f32_16x16x32_bf16 a[44:47], v[36:39], v[8:11], a[44:47]\n"
      "v_mfma_f32_16x16x32_bf16 a[48:51], v[40:43], v[8:11], a[48:51]\n"
      "buffer_load_dwordx4 v[204:207], v248, s[44:47], s83 offen offset:0 sc1\n"
      "v_mfma_f32_16x16x32_bf16 a[52:55], v[44:47], v[8:11], a[52:55]\n"
      "v_mfma_f32_16x16x32_bf16 a[56:59], v[48:51], v[8:11], a[56:59]\n"
      "v_mfma_f32_16x16x32_bf16 a[60:63], v[32:35], v[12:15], a[60:63]\n"
      "s_add_u32 s84, s68, 491520\n"
      "buffer_load_dwordx4 v[156:159], v244, s[40:43], s84 offen offset:0 sc1\n"
      "v_mfma_f32_16x16x32_bf16 a[64:67], v[36:39], v[12:15], a[64:67]\n"
      "v_mfma_f32_16x16x32_bf16 a[68:71], v[40:43], v[12:15], a[68:71]\n"
      "v_mfma_f32_16x16x32_bf16 a[72:75], v[44:47], v[12:15], a[72:75]\n"
      "buffer_load_dwordx4 v[160:163], v244, s[40:43], s84 offen offset:1024 sc1\n"
      "v_mfma_f32_16x16x32_bf16 a[76:79], v[48:51], v[12:15], a[76:79]\n"
      "s_waitcnt vmcnt(35)\n"
      "v_mfma_f32_16x16x32_bf16 a[80:83], v[32:35], v[16:19], a[80:83]\n"
      "v_mfma_f32_16x16x32_bf16 a[84:87], v[36:39], v[16:19], a[84:87]\n"
      "buffer_load_dwordx4 v[164:167], v245, s[40:43], s84 offen offset:0 sc1\n"
      "v_mfma_f32_16x16x32_bf16 a[88:91], v[40:43], v[16:19], a[88:91]\n"
      "v_mfma_f32_16x16x32_bf16 a[92:95], v[44:47], v[16:19], a[92:95]\n"
      "v_mfma_f32_16x16x32_bf16 a[96:99], v[48:51], v[16:19], a[96:99]\n"
      "buffer_load_dwordx4 v[168:171], v245, s[40:43], s84 offen offset:1024 sc1\n"
      "v_mfma_f32_16x16x32_bf16 a[100:103], v[32:35], v[20:23], a[100:103]\n"
      "v_mfma_f32_16x16x32_bf16 a[104:107], v[36:39], v[20:23], a[104:107]\n"
      "v_mfma_f32_16x16x32_bf16 a[108:111], v[40:43], v[20:23], a[108:111]\n"
      "buffer_load_dwordx4 v[172:175], v246, s[40:43], s84 offen offset:0 sc1\n"
      "v_mfma_f32_16x16x32_bf16 a[112:115], v[44:47], v[20:23], a[112:115]\n"
      "v_mfma_f32_16x16x32_bf16 a[116:119], v[48:51], v[20:23], a[116:119]\n"
      "s_waitcnt vmcnt(36)\n"
      "v_mfma_f32_16x16x32_bf16 a[120:123], v[32:35], v[24:27], a[120:123]\n"
      "buffer_load_dwordx4 v[176:179], v246, s[40:43], s84 offen offset:1024 sc1\n"
      "v_mfma_f32_16x16x32_bf16 a[124:127], v[36:39], v[24:27], a[124:127]\n"
      "v_mfma_f32_16x16x32_bf16 a[128:131], v[40:43], v[24:27], a[128:131]\n"
      "v_mfma_f32_16x16x32_bf16 a[132:135], v[44:47], v[24:27], a[132:135]\n"
      "buffer_load_dwordx4 v[180:183], v247, s[40:43], s84 offen offset:0 sc1\n"
      "v_mfma_f32_16x16x32_bf16 a[136:139], v[48:51], v[24:27], a[136:139]\n"
      "v_mfma_f32_16x16x32_bf16 v[208:211], v[32:35], v[28:31], v[208:211]\n"
      "v_mfma_f32_16x16x32_bf16 v[212:215], v[36:39], v[28:31], v[212:215]\n"
      "buffer_load_dwordx4 v[184:187], v247, s[40:43], s84 offen offset:1024 sc1\n"
      "v_mfma_f32_16x16x32_bf16 v[216:219], v[40:43], v[28:31], v[216:219]\n"
      "v_mfma_f32_16x16x32_bf16 v[220:223], v[44:47], v[28:31], v[220:223]\n"
      "v_mfma_f32_16x16x32_bf16 v[224:227], v[48:51], v[28:31], v[224:227]\n"
      "s_waitcnt vmcnt(32)\n"
      "v_mfma_f32_16x16x32_bf16 a[0:3], v[84:87], v[52:55], a[0:3]\n"
      "s_add_u32 s83, s69, 348160\n"
      "buffer_load_dwordx4 v[32:35], v252, s[44:47], s83 offen offset:0 sc1\n"
      "v_mfma_f32_16x16x32_bf16 a[4:7], v[88:91], v[52:55], a[4:7]\n"
      "v_mfma_f32_16x16x32_bf16 a[8:11], v[92:95], v[52:55], a[8:11]\n"
      "v_mfma_f32_16x16x32_bf16 a[12:15], v[96:99], v[52:55], a[12:15]\n"
      "buffer_load_dwordx4 v[36:39], v252, s[44:47], s83 offen offset:1024 sc1\n"
      "v_mfma_f32_16x16x32_bf16 a[16:19], v[100:103], v[52:55], a[16:19]\n"
      "v_mfma_f32_16x16x32_bf16 a[20:23], v[84:87], v[56:59], a[20:23]\n"
      "v_mfma_f32_16x16x32_bf16 a[24:27], v[88:91], v[56:59], a[24:27]\n"
      "buffer_load_dwordx4 v[40:43], v252, s[44:47], s83 offen offset:2048 sc1\n"
      "v_mfma_f32_16x16x32_bf16 a[28:31], v[92:95], v[56:59], a[28:31]\n"
      "v_mfma_f32_16x16x32_bf16 a[32:35], v[96:99], v[56:59], a[32:35]\n"
      "v_mfma_f32_16x16x32_bf16 a[36:39], v[100:103], v[56:59], a[36:39]\n"
      "buffer_load_dwordx4 v[44:47], v252, s[44:47], s83 offen offset:3072 sc1\n"
      "s_waitcnt vmcnt(34)\n"
      "v_mfma_f32_16x16x32_bf16 a[40:43], v[84:87], v[60:63], a[40:43]\n"
      "v_mfma_f32_16x16x32_bf16 a[44:47], v[88:91], v[60:63], a[44:47]\n"
      "v_mfma_f32_16x16x32_bf16 a[48:51], v[92:95], v[60:63], a[48:51]\n"
      "buffer_load_dwordx4 v[48:51], v248, s[44:47], s83 offen offset:0 sc1\n"
      "v_mfma_f32_16x16x32_bf16 a[52:55], v[96:99], v[60:63], a[52:55]\n"
      "v_mfma_f32_16x16x32_bf16 a[56:59], v[100:103], v[60:63], a[56:59]\n"
      "v_mfma_f32_16x16x32_bf16 a[60:63], v[84:87], v[64:67], a[60:63]\n"
      "s_add_u32 s84, s68, 557056\n"
      "buffer_load_dwordx4 v[0:3], v244, s[40:43], s84 offen offset:0 sc1\n"
      "v_mfma_f32_16x16x32_bf16 a[64:67], v[88:91], v[64:67], a[64:67]\n"
      "v_mfma_f32_16x16x32_bf16 a[68:71], v[92:95], v[64:67], a[68:71]\n"
      "v_mfma_f32_16x16x32_bf16 a[72:75], v[96:99], v[64:67], a[72:75]\n"
      "buffer_load_dwordx4 v[4:7], v244, s[40:43], s84 offen offset:1024 sc1\n"
      "v_mfma_f32_16x16x32_bf16 a[76:79], v[100:103], v[64:67], a[76:79]\n"
      "s_waitcnt vmcnt(35)\n"
      "v_mfma_f32_16x16x32_bf16 a[80:83], v[84:87], v[68:71], a[80:83]\n"
      "v_mfma_f32_16x16x32_bf16 a[84:87], v[88:91], v[68:71], a[84:87]\n"
      "buffer_load_dwordx4 v[8:11], v245, s[40:43], s84 offen offset:0 sc1\n"
      "v_mfma_f32_16x16x32_bf16 a[88:91], v[92:95], v[68:71], a[88:91]\n"
      "v_mfma_f32_16x16x32_bf16 a[92:95], v[96:99], v[68:71], a[92:95]\n"
      "v_mfma_f32_16x16x32_bf16 a[96:99], v[100:103], v[68:71], a[96:99]\n"
      "buffer_load_dwordx4 v[12:15], v245, s[40:43], s84 offen offset:1024 sc1\n"
      "v_mfma_f32_16x16x32_bf16 a[100:103], v[84:87], v[72:75], a[100:103]\n"
      "v_mfma_f32_16x16x32_bf16 a[104:107], v[88:91], v[72:75], a[104:107]\n"
      "v_mfma_f32_16x16x32_bf16 a[108:111], v[92:95], v[72:75], a[108:111]\n"
      "buffer_load_dwordx4 v[16:19], v246, s[40:43], s84 offen offset:0 sc1\n"
      "v_mfma_f32_16x16x32_bf16 a[112:115], v[96:99], v[72:75], a[112:115]\n"
      "v_mfma_f32_16x16x32_bf16 a[116:119], v[100:103], v[72:75], a[116:119]\n"
      "s_waitcnt vmcnt(36)\n"
      "v_mfma_f32_16x16x32_bf16 a[120:123], v[84:87], v[76:79], a[120:123]\n"
      "buffer_load_dwordx4 v[20:23], v246, s[40:43], s84 offen offset:1024 sc1\n"
      "v_mfma_f32_16x16x32_bf16 a[124:127], v[88:91], v[76:79], a[124:127]\n"
      "v_mfma_f32_16x16x32_bf16 a[128:131], v[92:95], v[76:79], a[128:131]\n"
      "v_mfma_f32_16x16x32_bf16 a[132:135], v[96:99], v[76:79], a[132:135]\n"
      "buffer_load_dwordx4 v[24:27], v247, s[40:43], s84 offen offset:0 sc1\n"
      "v_mfma_f32_16x16x32_bf16 a[136:139], v[100:103], v[76:79], a[136:139]\n"
      "v_mfma_f32_16x16x32_bf16 v[208:211], v[84:87], v[80:83], v[208:211]\n"
      "v_mfma_f32_16x16x32_bf16 v[212:215], v[88:91], v[80:83], v[212:215]\n"
      "buffer_load_dwordx4 v[28:31], v247, s[40:43], s84 offen offset:1024 sc1\n"
      "v_mfma_f32_16x16x32_bf16 v[216:219], v[92:95], v[80:83], v[216:219]\n"
      "v_mfma_f32_16x16x32_bf16 v[220:223], v[96:99], v[80:83], v[220:223]\n"
      "v_mfma_f32_16x16x32_bf16 v[224:227], v[100:103], v[80:83], v[224:227]\n"
      "s_waitcnt vmcnt(32)\n"
      "v_mfma_f32_16x16x32_bf16 a[0:3], v[136:139], v[104:107], a[0:3]\n"
      "s_add_u32 s83, s69, 389120\n"
      "buffer_load_dwordx4 v[84:87], v252, s[44:47], s83 offen offset:0 sc1\n"
      "v_mfma_f32_16x16x32_bf16 a[4:7], v[140:143], v[104:107], a[4:7]\n"
      "v_mfma_f32_16x16x32_bf16 a[8:11], v[144:147], v[104:107], a[8:11]\n"
      "v_mfma_f32_16x16x32_bf16 a[12:15], v[148:151], v[104:107], a[12:15]\n"
      "buffer_load_dwordx4 v[88:91], v252, s[44:47], s83 offen offset:1024 sc1\n"
      "v_mfma_f32_16x16x32_bf16 a[16:19], v[152:155], v[104:107], a[16:19]\n"
      "v_mfma_f32_16x16x32_bf16 a[20:23], v[136:139], v[108:111], a[20:23]\n"
      "v_mfma_f32_16x16x32_bf16 a[24:27], v[140:143], v[108:111], a[24:27]\n"
      "buffer_load_dwordx4 v[92:95], v252, s[44:47], s83 offen offset:2048 sc1\n"
      "v_mfma_f32_16x16x32_bf16 a[28:31], v[144:147], v[108:111], a[28:31]\n"
      "v_mfma_f32_16x16x32_bf16 a[32:35], v[148:151], v[108:111], a[32:35]\n"
      "v_mfma_f32_16x16x32_bf16 a[36:39], v[152:155], v[108:111], a[36:39]\n"
      "buffer_load_dwordx4 v[96:99], v252, s[44:47], s83 offen offset:3072 sc1\n"
      "s_waitcnt vmcnt(34)\n"
      "v_mfma_f32_16x16x32_bf16 a[40:43], v[136:139], v[112:115], a[40:43]\n"
      "v_mfma_f32_16x16x32_bf16 a[44:47], v[140:143], v[112:115], a[44:47]\n"
      "v_mfma_f32_16x16x32_bf16 a[48:51], v[144:147], v[112:115], a[48:51]\n"
      "buffer_load_dwordx4 v[100:103], v248, s[44:47], s83 offen offset:0 sc1\n"
      "v_mfma_f32_16x16x32_bf16 a[52:55], v[148:151], v[112:115], a[52:55]\n"
      "v_mfma_f32_16x16x32_bf16 a[56:59], v[152:155], v[112:115], a[56:59]\n"
      "v_mfma_f32_16x16x32_bf16 a[60:63], v[136:139], v[116:119], a[60:63]\n"
      "s_add_u32 s84, s68, 622592\n"
      "buffer_load_dwordx4 v[52:55], v244, s[40:43], s84 offen offset:0 sc1\n"
      "v_mfma_f32_16x16x32_bf16 a[64:67], v[140:143], v[116:119], a[64:67]\n"
      "v_mfma_f32_16x16x32_bf16 a[68:71], v[144:147], v[116:119], a[68:71]\n"
      "v_mfma_f32_16x16x32_bf16 a[72:75], v[148:151], v[116:119], a[72:75]\n"
      "buffer_load_dwordx4 v[56:59], v244, s[40:43], s84 offen offset:1024 sc1\n"
      "v_mfma_f32_16x16x32_bf16 a[76:79], v[152:155], v[116:119], a[76:79]\n"
      "s_waitcnt vmcnt(35)\n"
      "v_mfma_f32_16x16x32_bf16 a[80:83], v[136:139], v[120:123], a[80:83]\n"
      "v_mfma_f32_16x16x32_bf16 a[84:87], v[140:143], v[120:123], a[84:87]\n"
      "buffer_load_dwordx4 v[60:63], v245, s[40:43], s84 offen offset:0 sc1\n"
      "v_mfma_f32_16x16x32_bf16 a[88:91], v[144:147], v[120:123], a[88:91]\n"
      "v_mfma_f32_16x16x32_bf16 a[92:95], v[148:151], v[120:123], a[92:95]\n"
      "v_mfma_f32_16x16x32_bf16 a[96:99], v[152:155], v[120:123], a[96:99]\n"
      "buffer_load_dwordx4 v[64:67], v245, s[40:43], s84 offen offset:1024 sc1\n"
      "v_mfma_f32_16x16x32_bf16 a[100:103], v[136:139], v[124:127], a[100:103]\n"
      "v_mfma_f32_16x16x32_bf16 a[104:107], v[140:143], v[124:127], a[104:107]\n"
      "v_mfma_f32_16x16x32_bf16 a[108:111], v[144:147], v[124:127], a[108:111]\n"
      "buffer_load_dwordx4 v[68:71], v246, s[40:43], s84 offen offset:0 sc1\n"
      "v_mfma_f32_16x16x32_bf16 a[112:115], v[148:151], v[124:127], a[112:115]\n"
      "v_mfma_f32_16x16x32_bf16 a[116:119], v[152:155], v[124:127], a[116:119]\n"
      "s_waitcnt vmcnt(36)\n"
      "v_mfma_f32_16x16x32_bf16 a[120:123], v[136:139], v[128:131], a[120:123]\n"
      "buffer_load_dwordx4 v[72:75], v246, s[40:43], s84 offen offset:1024 sc1\n"
      "v_mfma_f32_16x16x32_bf16 a[124:127], v[140:143], v[128:131], a[124:127]\n"
      "v_mfma_f32_16x16x32_bf16 a[128:131], v[144:147], v[128:131], a[128:131]\n"
      "v_mfma_f32_16x16x32_bf16 a[132:135], v[148:151], v[128:131], a[132:135]\n"
      "buffer_load_dwordx4 v[76:79], v247, s[40:43], s84 offen offset:0 sc1\n"
      "v_mfma_f32_16x16x32_bf16 a[136:139], v[152:155], v[128:131], a[136:139]\n"
      "v_mfma_f32_16x16x32_bf16 v[208:211], v[136:139], v[132:135], v[208:211]\n"
      "v_mfma_f32_16x16x32_bf16 v[212:215], v[140:143], v[132:135], v[212:215]\n"
      "buffer_load_dwordx4 v[80:83], v247, s[40:43], s84 offen offset:1024 sc1\n"
      "v_mfma_f32_16x16x32_bf16 v[216:219], v[144:147], v[132:135], v[216:219]\n"
      "v_mfma_f32_16x16x32_bf16 v[220:223], v[148:151], v[132:135], v[220:223]\n"
      "v_mfma_f32_16x16x32_bf16 v[224:227], v[152:155], v[132:135], v[224:227]\n"
      "s_waitcnt vmcnt(32)\n"
      "v_mfma_f32_16x16x32_bf16 a[0:3], v[188:191], v[156:159], a[0:3]\n"
      "s_add_u32 s83, s69, 430080\n"
      "buffer_load_dwordx4 v[136:139], v252, s[44:47], s83 offen offset:0 sc1\n"
      "v_mfma_f32_16x16x32_bf16 a[4:7], v[192:195], v[156:159], a[4:7]\n"
      "v_mfma_f32_16x16x32_bf16 a[8:11], v[196:199], v[156:159], a[8:11]\n"
      "v_mfma_f32_16x16x32_bf16 a[12:15], v[200:203], v[156:159], a[12:15]\n"
      "buffer_load_dwordx4 v[140:143], v252, s[44:47], s83 offen offset:1024 sc1\n"
      "v_mfma_f32_16x16x32_bf16 a[16:19], v[204:207], v[156:159], a[16:19]\n"
      "v_mfma_f32_16x16x32_bf16 a[20:23], v[188:191], v[160:163], a[20:23]\n"
      "v_mfma_f32_16x16x32_bf16 a[24:27], v[192:195], v[160:163], a[24:27]\n"
      "buffer_load_dwordx4 v[144:147], v252, s[44:47], s83 offen offset:2048 sc1\n"
      "v_mfma_f32_16x16x32_bf16 a[28:31], v[196:199], v[160:163], a[28:31]\n"
      "v_mfma_f32_16x16x32_bf16 a[32:35], v[200:203], v[160:163], a[32:35]\n"
      "v_mfma_f32_16x16x32_bf16 a[36:39], v[204:207], v[160:163], a[36:39]\n"
      "buffer_load_dwordx4 v[148:151], v252, s[44:47], s83 offen offset:3072 sc1\n"
      "s_waitcnt vmcnt(34)\n"
      "v_mfma_f32_16x16x32_bf16 a[40:43], v[188:191], v[164:167], a[40:43]\n"
      "v_mfma_f32_16x16x32_bf16 a[44:47], v[192:195], v[164:167], a[44:47]\n"
      "v_mfma_f32_16x16x32_bf16 a[48:51], v[196:199], v[164:167], a[48:51]\n"
      "buffer_load_dwordx4 v[152:155], v248, s[44:47], s83 offen offset:0 sc1\n"
      "v_mfma_f32_16x16x32_bf16 a[52:55], v[200:203], v[164:167], a[52:55]\n"
      "v_mfma_f32_16x16x32_bf16 a[56:59], v[204:207], v[164:167], a[56:59]\n"
      "v_mfma_f32_16x16x32_bf16 a[60:63], v[188:191], v[168:171], a[60:63]\n"
      "s_add_u32 s84, s68, 688128\n"
      "buffer_load_dwordx4 v[104:107], v244, s[40:43], s84 offen offset:0 sc1\n"
      "v_mfma_f32_16x16x32_bf16 a[64:67], v[192:195], v[168:171], a[64:67]\n"
      "v_mfma_f32_16x16x32_bf16 a[68:71], v[196:199], v[168:171], a[68:71]\n"
      "v_mfma_f32_16x16x32_bf16 a[72:75], v[200:203], v[168:171], a[72:75]\n"
      "buffer_load_dwordx4 v[108:111], v244, s[40:43], s84 offen offset:1024 sc1\n"
      "v_mfma_f32_16x16x32_bf16 a[76:79], v[204:207], v[168:171], a[76:79]\n"
      "s_waitcnt vmcnt(35)\n"
      "v_mfma_f32_16x16x32_bf16 a[80:83], v[188:191], v[172:175], a[80:83]\n"
      "v_mfma_f32_16x16x32_bf16 a[84:87], v[192:195], v[172:175], a[84:87]\n"
      "buffer_load_dwordx4 v[112:115], v245, s[40:43], s84 offen offset:0 sc1\n"
      "v_mfma_f32_16x16x32_bf16 a[88:91], v[196:199], v[172:175], a[88:91]\n"
      "v_mfma_f32_16x16x32_bf16 a[92:95], v[200:203], v[172:175], a[92:95]\n"
      "v_mfma_f32_16x16x32_bf16 a[96:99], v[204:207], v[172:175], a[96:99]\n"
      "buffer_load_dwordx4 v[116:119], v245, s[40:43], s84 offen offset:1024 sc1\n"
      "v_mfma_f32_16x16x32_bf16 a[100:103], v[188:191], v[176:179], a[100:103]\n"
      "v_mfma_f32_16x16x32_bf16 a[104:107], v[192:195], v[176:179], a[104:107]\n"
      "v_mfma_f32_16x16x32_bf16 a[108:111], v[196:199], v[176:179], a[108:111]\n"
      "buffer_load_dwordx4 v[120:123], v246, s[40:43], s84 offen offset:0 sc1\n"
      "v_mfma_f32_16x16x32_bf16 a[112:115], v[200:203], v[176:179], a[112:115]\n"
      "v_mfma_f32_16x16x32_bf16 a[116:119], v[204:207], v[176:179], a[116:119]\n"
      "s_waitcnt vmcnt(36)\n"
      "v_mfma_f32_16x16x32_bf16 a[120:123], v[188:191], v[180:183], a[120:123]\n"
      "buffer_load_dwordx4 v[124:127], v246, s[40:43], s84 offen offset:1024 sc1\n"
      "v_mfma_f32_16x16x32_bf16 a[124:127], v[192:195], v[180:183], a[124:127]\n"
      "v_mfma_f32_16x16x32_bf16 a[128:131], v[196:199], v[180:183], a[128:131]\n"
      "v_mfma_f32_16x16x32_bf16 a[132:135], v[200:203], v[180:183], a[132:135]\n"
      "buffer_load_dwordx4 v[128:131], v247, s[40:43], s84 offen offset:0 sc1\n"
      "v_mfma_f32_16x16x32_bf16 a[136:139], v[204:207], v[180:183], a[136:139]\n"
      "v_mfma_f32_16x16x32_bf16 v[208:211], v[188:191], v[184:187], v[208:211]\n"
      "v_mfma_f32_16x16x32_bf16 v[212:215], v[192:195], v[184:187], v[212:215]\n"
      "buffer_load_dwordx4 v[132:135], v247, s[40:43], s84 offen offset:1024 sc1\n"
      "v_mfma_f32_16x16x32_bf16 v[216:219], v[196:199], v[184:187], v[216:219]\n"
      "v_mfma_f32_16x16x32_bf16 v[220:223], v[200:203], v[184:187], v[220:223]\n"
      "v_mfma_f32_16x16x32_bf16 v[224:227], v[204:207], v[184:187], v[224:227]\n"
      "s_waitcnt vmcnt(32)\n"
      "v_mfma_f32_16x16x32_bf16 a[0:3], v[32:35], v[0:3], a[0:3]\n"
      "s_add_u32 s83, s69, 471040\n"
      "buffer_load_dwordx4 v[188:191], v252, s[44:47], s83 offen offset:0 sc1\n"
      "v_mfma_f32_16x16x32_bf16 a[4:7], v[36:39], v[0:3], a[4:7]\n"
      "v_mfma_f32_16x16x32_bf16 a[8:11], v[40:43], v[0:3], a[8:11]\n"
      "v_mfma_f32_16x16x32_bf16 a[12:15], v[44:47], v[0:3], a[12:15]\n"
      "buffer_load_dwordx4 v[192:195], v252, s[44:47], s83 offen offset:1024 sc1\n"
      "v_mfma_f32_16x16x32_bf16 a[16:19], v[48:51], v[0:3], a[16:19]\n"
      "v_mfma_f32_16x16x32_bf16 a[20:23], v[32:35], v[4:7], a[20:23]\n"
      "v_mfma_f32_16x16x32_bf16 a[24:27], v[36:39], v[4:7], a[24:27]\n"
      "buffer_load_dwordx4 v[196:199], v252, s[44:47], s83 offen offset:2048 sc1\n"
      "v_mfma_f32_16x16x32_bf16 a[28:31], v[40:43], v[4:7], a[28:31]\n"
      "v_mfma_f32_16x16x32_bf16 a[32:35], v[44:47], v[4:7], a[32:35]\n"
      "v_mfma_f32_16x16x32_bf16 a[36:39], v[48:51], v[4:7], a[36:39]\n"
      "buffer_load_dwordx4 v[200:203], v252, s[44:47], s83 offen offset:3072 sc1\n"
      "s_waitcnt vmcnt(34)\n"
      "v_mfma_f32_16x16x32_bf16 a[40:43], v[32:35], v[8:11], a[40:43]\n"
      "v_mfma_f32_16x16x32_bf16 a[44:47], v[36:39], v[8:11], a[44:47]\n"
      "v_mfma_f32_16x16x32_bf16 a[48:51], v[40:43], v[8:11], a[48:51]\n"
      "buffer_load_dwordx4 v[204:207], v248, s[44:47], s83 offen offset:0 sc1\n"
      "v_mfma_f32_16x16x32_bf16 a[52:55], v[44:47], v[8:11], a[52:55]\n"
      "v_mfma_f32_16x16x32_bf16 a[56:59], v[48:51], v[8:11], a[56:59]\n"
      "v_mfma_f32_16x16x32_bf16 a[60:63], v[32:35], v[12:15], a[60:63]\n"
      "s_add_u32 s84, s68, 753664\n"
      "buffer_load_dwordx4 v[156:159], v244, s[40:43], s84 offen offset:0 sc1\n"
      "v_mfma_f32_16x16x32_bf16 a[64:67], v[36:39], v[12:15], a[64:67]\n"
      "v_mfma_f32_16x16x32_bf16 a[68:71], v[40:43], v[12:15], a[68:71]\n"
      "v_mfma_f32_16x16x32_bf16 a[72:75], v[44:47], v[12:15], a[72:75]\n"
      "buffer_load_dwordx4 v[160:163], v244, s[40:43], s84 offen offset:1024 sc1\n"
      "v_mfma_f32_16x16x32_bf16 a[76:79], v[48:51], v[12:15], a[76:79]\n"
      "s_waitcnt vmcnt(35)\n"
      "v_mfma_f32_16x16x32_bf16 a[80:83], v[32:35], v[16:19], a[80:83]\n"
      "v_mfma_f32_16x16x32_bf16 a[84:87], v[36:39], v[16:19], a[84:87]\n"
      "buffer_load_dwordx4 v[164:167], v245, s[40:43], s84 offen offset:0 sc1\n"
      "v_mfma_f32_16x16x32_bf16 a[88:91], v[40:43], v[16:19], a[88:91]\n"
      "v_mfma_f32_16x16x32_bf16 a[92:95], v[44:47], v[16:19], a[92:95]\n"
      "v_mfma_f32_16x16x32_bf16 a[96:99], v[48:51], v[16:19], a[96:99]\n"
      "buffer_load_dwordx4 v[168:171], v245, s[40:43], s84 offen offset:1024 sc1\n"
      "v_mfma_f32_16x16x32_bf16 a[100:103], v[32:35], v[20:23], a[100:103]\n"
      "v_mfma_f32_16x16x32_bf16 a[104:107], v[36:39], v[20:23], a[104:107]\n"
      "v_mfma_f32_16x16x32_bf16 a[108:111], v[40:43], v[20:23], a[108:111]\n"
      "buffer_load_dwordx4 v[172:175], v246, s[40:43], s84 offen offset:0 sc1\n"
      "v_mfma_f32_16x16x32_bf16 a[112:115], v[44:47], v[20:23], a[112:115]\n"
      "v_mfma_f32_16x16x32_bf16 a[116:119], v[48:51], v[20:23], a[116:119]\n"
      "s_waitcnt vmcnt(36)\n"
      "v_mfma_f32_16x16x32_bf16 a[120:123], v[32:35], v[24:27], a[120:123]\n"
      "buffer_load_dwordx4 v[176:179], v246, s[40:43], s84 offen offset:1024 sc1\n"
      "v_mfma_f32_16x16x32_bf16 a[124:127], v[36:39], v[24:27], a[124:127]\n"
      "v_mfma_f32_16x16x32_bf16 a[128:131], v[40:43], v[24:27], a[128:131]\n"
      "v_mfma_f32_16x16x32_bf16 a[132:135], v[44:47], v[24:27], a[132:135]\n"
      "buffer_load_dwordx4 v[180:183], v247, s[40:43], s84 offen offset:0 sc1\n"
      "v_mfma_f32_16x16x32_bf16 a[136:139], v[48:51], v[24:27], a[136:139]\n"
      "v_mfma_f32_16x16x32_bf16 v[208:211], v[32:35], v[28:31], v[208:211]\n"
      "v_mfma_f32_16x16x32_bf16 v[212:215], v[36:39], v[28:31], v[212:215]\n"
      "buffer_load_dwordx4 v[184:187], v247, s[40:43], s84 offen offset:1024 sc1\n"
      "v_mfma_f32_16x16x32_bf16 v[216:219], v[40:43], v[28:31], v[216:219]\n"
      "v_mfma_f32_16x16x32_bf16 v[220:223], v[44:47], v[28:31], v[220:223]\n"
      "v_mfma_f32_16x16x32_bf16 v[224:227], v[48:51], v[28:31], v[224:227]\n"
      "s_waitcnt vmcnt(32)\n"
      "v_mfma_f32_16x16x32_bf16 a[0:3], v[84:87], v[52:55], a[0:3]\n"
      "s_add_u32 s83, s69, 512000\n"
      "buffer_load_dwordx4 v[32:35], v252, s[44:47], s83 offen offset:0 sc1\n"
      "v_mfma_f32_16x16x32_bf16 a[4:7], v[88:91], v[52:55], a[4:7]\n"
      "v_mfma_f32_16x16x32_bf16 a[8:11], v[92:95], v[52:55], a[8:11]\n"
      "v_mfma_f32_16x16x32_bf16 a[12:15], v[96:99], v[52:55], a[12:15]\n"
      "buffer_load_dwordx4 v[36:39], v252, s[44:47], s83 offen offset:1024 sc1\n"
      "v_mfma_f32_16x16x32_bf16 a[16:19], v[100:103], v[52:55], a[16:19]\n"
      "v_mfma_f32_16x16x32_bf16 a[20:23], v[84:87], v[56:59], a[20:23]\n"
      "v_mfma_f32_16x16x32_bf16 a[24:27], v[88:91], v[56:59], a[24:27]\n"
      "buffer_load_dwordx4 v[40:43], v252, s[44:47], s83 offen offset:2048 sc1\n"
      "v_mfma_f32_16x16x32_bf16 a[28:31], v[92:95], v[56:59], a[28:31]\n"
      "v_mfma_f32_16x16x32_bf16 a[32:35], v[96:99], v[56:59], a[32:35]\n"
      "v_mfma_f32_16x16x32_bf16 a[36:39], v[100:103], v[56:59], a[36:39]\n"
      "buffer_load_dwordx4 v[44:47], v252, s[44:47], s83 offen offset:3072 sc1\n"
      "s_waitcnt vmcnt(34)\n"
      "v_mfma_f32_16x16x32_bf16 a[40:43], v[84:87], v[60:63], a[40:43]\n"
      "v_mfma_f32_16x16x32_bf16 a[44:47], v[88:91], v[60:63], a[44:47]\n"
      "v_mfma_f32_16x16x32_bf16 a[48:51], v[92:95], v[60:63], a[48:51]\n"
      "buffer_load_dwordx4 v[48:51], v248, s[44:47], s83 offen offset:0 sc1\n"
      "v_mfma_f32_16x16x32_bf16 a[52:55], v[96:99], v[60:63], a[52:55]\n"
      "v_mfma_f32_16x16x32_bf16 a[56:59], v[100:103], v[60:63], a[56:59]\n"
      "v_mfma_f32_16x16x32_bf16 a[60:63], v[84:87], v[64:67], a[60:63]\n"
      "s_add_u32 s84, s68, 819200\n"
      "buffer_load_dwordx4 v[0:3], v244, s[40:43], s84 offen offset:0 sc1\n"
      "v_mfma_f32_16x16x32_bf16 a[64:67], v[88:91], v[64:67], a[64:67]\n"
      "v_mfma_f32_16x16x32_bf16 a[68:71], v[92:95], v[64:67], a[68:71]\n"
      "v_mfma_f32_16x16x32_bf16 a[72:75], v[96:99], v[64:67], a[72:75]\n"
      "buffer_load_dwordx4 v[4:7], v244, s[40:43], s84 offen offset:1024 sc1\n"
      "v_mfma_f32_16x16x32_bf16 a[76:79], v[100:103], v[64:67], a[76:79]\n"
      "s_waitcnt vmcnt(35)\n"
      "v_mfma_f32_16x16x32_bf16 a[80:83], v[84:87], v[68:71], a[80:83]\n"
      "v_mfma_f32_16x16x32_bf16 a[84:87], v[88:91], v[68:71], a[84:87]\n"
      "buffer_load_dwordx4 v[8:11], v245, s[40:43], s84 offen offset:0 sc1\n"
      "v_mfma_f32_16x16x32_bf16 a[88:91], v[92:95], v[68:71], a[88:91]\n"
      "v_mfma_f32_16x16x32_bf16 a[92:95], v[96:99], v[68:71], a[92:95]\n"
      "v_mfma_f32_16x16x32_bf16 a[96:99], v[100:103], v[68:71], a[96:99]\n"
      "buffer_load_dwordx4 v[12:15], v245, s[40:43], s84 offen offset:1024 sc1\n"
      "v_mfma_f32_16x16x32_bf16 a[100:103], v[84:87], v[72:75], a[100:103]\n"
      "v_mfma_f32_16x16x32_bf16 a[104:107], v[88:91], v[72:75], a[104:107]\n"
      "v_mfma_f32_16x16x32_bf16 a[108:111], v[92:95], v[72:75], a[108:111]\n"
      "buffer_load_dwordx4 v[16:19], v246, s[40:43], s84 offen offset:0 sc1\n"
      "v_mfma_f32_16x16x32_bf16 a[112:115], v[96:99], v[72:75], a[112:115]\n"
      "v_mfma_f32_16x16x32_bf16 a[116:119], v[100:103], v[72:75], a[116:119]\n"
      "s_waitcnt vmcnt(36)\n"
      "v_mfma_f32_16x16x32_bf16 a[120:123], v[84:87], v[76:79], a[120:123]\n"
      "buffer_load_dwordx4 v[20:23], v246, s[40:43], s84 offen offset:1024 sc1\n"
      "v_mfma_f32_16x16x32_bf16 a[124:127], v[88:91], v[76:79], a[124:127]\n"
      "v_mfma_f32_16x16x32_bf16 a[128:131], v[92:95], v[76:79], a[128:131]\n"
      "v_mfma_f32_16x16x32_bf16 a[132:135], v[96:99], v[76:79], a[132:135]\n"
      "buffer_load_dwordx4 v[24:27], v247, s[40:43], s84 offen offset:0 sc1\n"
      "v_mfma_f32_16x16x32_bf16 a[136:139], v[100:103], v[76:79], a[136:139]\n"
      "v_mfma_f32_16x16x32_bf16 v[208:211], v[84:87], v[80:83], v[208:211]\n"
      "v_mfma_f32_16x16x32_bf16 v[212:215], v[88:91], v[80:83], v[212:215]\n"
      "buffer_load_dwordx4 v[28:31], v247, s[40:43], s84 offen offset:1024 sc1\n"
      "v_mfma_f32_16x16x32_bf16 v[216:219], v[92:95], v[80:83], v[216:219]\n"
      "v_mfma_f32_16x16x32_bf16 v[220:223], v[96:99], v[80:83], v[220:223]\n"
      "v_mfma_f32_16x16x32_bf16 v[224:227], v[100:103], v[80:83], v[224:227]\n"
      "s_waitcnt vmcnt(32)\n"
      "v_mfma_f32_16x16x32_bf16 a[0:3], v[136:139], v[104:107], a[0:3]\n"
      "s_add_u32 s83, s69, 552960\n"
      "buffer_load_dwordx4 v[84:87], v252, s[44:47], s83 offen offset:0 sc1\n"
      "v_mfma_f32_16x16x32_bf16 a[4:7], v[140:143], v[104:107], a[4:7]\n"
      "v_mfma_f32_16x16x32_bf16 a[8:11], v[144:147], v[104:107], a[8:11]\n"
      "v_mfma_f32_16x16x32_bf16 a[12:15], v[148:151], v[104:107], a[12:15]\n"
      "buffer_load_dwordx4 v[88:91], v252, s[44:47], s83 offen offset:1024 sc1\n"
      "v_mfma_f32_16x16x32_bf16 a[16:19], v[152:155], v[104:107], a[16:19]\n"
      "v_mfma_f32_16x16x32_bf16 a[20:23], v[136:139], v[108:111], a[20:23]\n"
      "v_mfma_f32_16x16x32_bf16 a[24:27], v[140:143], v[108:111], a[24:27]\n"
      "buffer_load_dwordx4 v[92:95], v252, s[44:47], s83 offen offset:2048 sc1\n"
      "v_mfma_f32_16x16x32_bf16 a[28:31], v[144:147], v[108:111], a[28:31]\n"
      "v_mfma_f32_16x16x32_bf16 a[32:35], v[148:151], v[108:111], a[32:35]\n"
      "v_mfma_f32_16x16x32_bf16 a[36:39], v[152:155], v[108:111], a[36:39]\n"
      "buffer_load_dwordx4 v[96:99], v252, s[44:47], s83 offen offset:3072 sc1\n"
      "s_waitcnt vmcnt(34)\n"
      "v_mfma_f32_16x16x32_bf16 a[40:43], v[136:139], v[112:115], a[40:43]\n"
      "v_mfma_f32_16x16x32_bf16 a[44:47], v[140:143], v[112:115], a[44:47]\n"
      "v_mfma_f32_16x16x32_bf16 a[48:51], v[144:147], v[112:115], a[48:51]\n"
      "buffer_load_dwordx4 v[100:103], v248, s[44:47], s83 offen offset:0 sc1\n"
      "v_mfma_f32_16x16x32_bf16 a[52:55], v[148:151], v[112:115], a[52:55]\n"
      "v_mfma_f32_16x16x32_bf16 a[56:59], v[152:155], v[112:115], a[56:59]\n"
      "v_mfma_f32_16x16x32_bf16 a[60:63], v[136:139], v[116:119], a[60:63]\n"
      "s_add_u32 s84, s68, 884736\n"
      "buffer_load_dwordx4 v[52:55], v244, s[40:43], s84 offen offset:0 sc1\n"
      "v_mfma_f32_16x16x32_bf16 a[64:67], v[140:143], v[116:119], a[64:67]\n"
      "v_mfma_f32_16x16x32_bf16 a[68:71], v[144:147], v[116:119], a[68:71]\n"
      "v_mfma_f32_16x16x32_bf16 a[72:75], v[148:151], v[116:119], a[72:75]\n"
      "buffer_load_dwordx4 v[56:59], v244, s[40:43], s84 offen offset:1024 sc1\n"
      "v_mfma_f32_16x16x32_bf16 a[76:79], v[152:155], v[116:119], a[76:79]\n"
      "s_waitcnt vmcnt(35)\n"
      "v_mfma_f32_16x16x32_bf16 a[80:83], v[136:139], v[120:123], a[80:83]\n"
      "v_mfma_f32_16x16x32_bf16 a[84:87], v[140:143], v[120:123], a[84:87]\n"
      "buffer_load_dwordx4 v[60:63], v245, s[40:43], s84 offen offset:0 sc1\n"
      "v_mfma_f32_16x16x32_bf16 a[88:91], v[144:147], v[120:123], a[88:91]\n"
      "v_mfma_f32_16x16x32_bf16 a[92:95], v[148:151], v[120:123], a[92:95]\n"
      "v_mfma_f32_16x16x32_bf16 a[96:99], v[152:155], v[120:123], a[96:99]\n"
      "buffer_load_dwordx4 v[64:67], v245, s[40:43], s84 offen offset:1024 sc1\n"
      "v_mfma_f32_16x16x32_bf16 a[100:103], v[136:139], v[124:127], a[100:103]\n"
      "v_mfma_f32_16x16x32_bf16 a[104:107], v[140:143], v[124:127], a[104:107]\n"
      "v_mfma_f32_16x16x32_bf16 a[108:111], v[144:147], v[124:127], a[108:111]\n"
      "buffer_load_dwordx4 v[68:71], v246, s[40:43], s84 offen offset:0 sc1\n"
      "v_mfma_f32_16x16x32_bf16 a[112:115], v[148:151], v[124:127], a[112:115]\n"
      "v_mfma_f32_16x16x32_bf16 a[116:119], v[152:155], v[124:127], a[116:119]\n"
      "s_waitcnt vmcnt(36)\n"
      "v_mfma_f32_16x16x32_bf16 a[120:123], v[136:139], v[128:131], a[120:123]\n"
      "buffer_load_dwordx4 v[72:75], v246, s[40:43], s84 offen offset:1024 sc1\n"
      "v_mfma_f32_16x16x32_bf16 a[124:127], v[140:143], v[128:131], a[124:127]\n"
      "v_mfma_f32_16x16x32_bf16 a[128:131], v[144:147], v[128:131], a[128:131]\n"
      "v_mfma_f32_16x16x32_bf16 a[132:135], v[148:151], v[128:131], a[132:135]\n"
      "buffer_load_dwordx4 v[76:79], v247, s[40:43], s84 offen offset:0 sc1\n"
      "v_mfma_f32_16x16x32_bf16 a[136:139], v[152:155], v[128:131], a[136:139]\n"
      "v_mfma_f32_16x16x32_bf16 v[208:211], v[136:139], v[132:135], v[208:211]\n"
      "v_mfma_f32_16x16x32_bf16 v[212:215], v[140:143], v[132:135], v[212:215]\n"
      "buffer_load_dwordx4 v[80:83], v247, s[40:43], s84 offen offset:1024 sc1\n"
      "v_mfma_f32_16x16x32_bf16 v[216:219], v[144:147], v[132:135], v[216:219]\n"
      "v_mfma_f32_16x16x32_bf16 v[220:223], v[148:151], v[132:135], v[220:223]\n"
      "v_mfma_f32_16x16x32_bf16 v[224:227], v[152:155], v[132:135], v[224:227]\n"
      "s_waitcnt vmcnt(32)\n"
      "v_mfma_f32_16x16x32_bf16 a[0:3], v[188:191], v[156:159], a[0:3]\n"
      "s_add_u32 s83, s69, 593920\n"
      "buffer_load_dwordx4 v[136:139], v252, s[44:47], s83 offen offset:0 sc1\n"
      "v_mfma_f32_16x16x32_bf16 a[4:7], v[192:195], v[156:159], a[4:7]\n"
      "v_mfma_f32_16x16x32_bf16 a[8:11], v[196:199], v[156:159], a[8:11]\n"
      "v_mfma_f32_16x16x32_bf16 a[12:15], v[200:203], v[156:159], a[12:15]\n"
      "buffer_load_dwordx4 v[140:143], v252, s[44:47], s83 offen offset:1024 sc1\n"
      "v_mfma_f32_16x16x32_bf16 a[16:19], v[204:207], v[156:159], a[16:19]\n"
      "v_mfma_f32_16x16x32_bf16 a[20:23], v[188:191], v[160:163], a[20:23]\n"
      "v_mfma_f32_16x16x32_bf16 a[24:27], v[192:195], v[160:163], a[24:27]\n"
      "buffer_load_dwordx4 v[144:147], v252, s[44:47], s83 offen offset:2048 sc1\n"
      "v_mfma_f32_16x16x32_bf16 a[28:31], v[196:199], v[160:163], a[28:31]\n"
      "v_mfma_f32_16x16x32_bf16 a[32:35], v[200:203], v[160:163], a[32:35]\n"
      "v_mfma_f32_16x16x32_bf16 a[36:39], v[204:207], v[160:163], a[36:39]\n"
      "buffer_load_dwordx4 v[148:151], v252, s[44:47], s83 offen offset:3072 sc1\n"
      "s_waitcnt vmcnt(34)\n"
      "v_mfma_f32_16x16x32_bf16 a[40:43], v[188:191], v[164:167], a[40:43]\n"
      "v_mfma_f32_16x16x32_bf16 a[44:47], v[192:195], v[164:167], a[44:47]\n"
      "v_mfma_f32_16x16x32_bf16 a[48:51], v[196:199], v[164:167], a[48:51]\n"
      "buffer_load_dwordx4 v[152:155], v248, s[44:47], s83 offen offset:0 sc1\n"
      "v_mfma_f32_16x16x32_bf16 a[52:55], v[200:203], v[164:167], a[52:55]\n"
      "v_mfma_f32_16x16x32_bf16 a[56:59], v[204:207], v[164:167], a[56:59]\n"
      "v_mfma_f32_16x16x32_bf16 a[60:63], v[188:191], v[168:171], a[60:63]\n"
      "s_add_u32 s84, s68, 950272\n"
      "buffer_load_dwordx4 v[104:107], v244, s[40:43], s84 offen offset:0 sc1\n"
      "v_mfma_f32_16x16x32_bf16 a[64:67], v[192:195], v[168:171], a[64:67]\n"
      "v_mfma_f32_16x16x32_bf16 a[68:71], v[196:199], v[168:171], a[68:71]\n"
      "v_mfma_f32_16x16x32_bf16 a[72:75], v[200:203], v[168:171], a[72:75]\n"
      "buffer_load_dwordx4 v[108:111], v244, s[40:43], s84 offen offset:1024 sc1\n"
      "v_mfma_f32_16x16x32_bf16 a[76:79], v[204:207], v[168:171], a[76:79]\n"
      "s_waitcnt vmcnt(35)\n"
      "v_mfma_f32_16x16x32_bf16 a[80:83], v[188:191], v[172:175], a[80:83]\n"
      "v_mfma_f32_16x16x32_bf16 a[84:87], v[192:195], v[172:175], a[84:87]\n"
      "buffer_load_dwordx4 v[112:115], v245, s[40:43], s84 offen offset:0 sc1\n"
      "v_mfma_f32_16x16x32_bf16 a[88:91], v[196:199], v[172:175], a[88:91]\n"
      "v_mfma_f32_16x16x32_bf16 a[92:95], v[200:203], v[172:175], a[92:95]\n"
      "v_mfma_f32_16x16x32_bf16 a[96:99], v[204:207], v[172:175], a[96:99]\n"
      "buffer_load_dwordx4 v[116:119], v245, s[40:43], s84 offen offset:1024 sc1\n"
      "v_mfma_f32_16x16x32_bf16 a[100:103], v[188:191], v[176:179], a[100:103]\n"
      "v_mfma_f32_16x16x32_bf16 a[104:107], v[192:195], v[176:179], a[104:107]\n"
      "v_mfma_f32_16x16x32_bf16 a[108:111], v[196:199], v[176:179], a[108:111]\n"
      "buffer_load_dwordx4 v[120:123], v246, s[40:43], s84 offen offset:0 sc1\n"
      "v_mfma_f32_16x16x32_bf16 a[112:115], v[200:203], v[176:179], a[112:115]\n"
      "v_mfma_f32_16x16x32_bf16 a[116:119], v[204:207], v[176:179], a[116:119]\n"
      "s_waitcnt vmcnt(36)\n"
      "v_mfma_f32_16x16x32_bf16 a[120:123], v[188:191], v[180:183], a[120:123]\n"
      "buffer_load_dwordx4 v[124:127], v246, s[40:43], s84 offen offset:1024 sc1\n"
      "v_mfma_f32_16x16x32_bf16 a[124:127], v[192:195], v[180:183], a[124:127]\n"
      "v_mfma_f32_16x16x32_bf16 a[128:131], v[196:199], v[180:183], a[128:131]\n"
      "v_mfma_f32_16x16x32_bf16 a[132:135], v[200:203], v[180:183], a[132:135]\n"
      "buffer_load_dwordx4 v[128:131], v247, s[40:43], s84 offen offset:0 sc1\n"
      "v_mfma_f32_16x16x32_bf16 a[136:139], v[204:207], v[180:183], a[136:139]\n"
      "v_mfma_f32_16x16x32_bf16 v[208:211], v[188:191], v[184:187], v[208:211]\n"
      "v_mfma_f32_16x16x32_bf16 v[212:215], v[192:195], v[184:187], v[212:215]\n"
      "buffer_load_dwordx4 v[132:135], v247, s[40:43], s84 offen offset:1024 sc1\n"
      "v_mfma_f32_16x16x32_bf16 v[216:219], v[196:199], v[184:187], v[216:219]\n"
      "v_mfma_f32_16x16x32_bf16 v[220:223], v[200:203], v[184:187], v[220:223]\n"
      "v_mfma_f32_16x16x32_bf16 v[224:227], v[204:207], v[184:187], v[224:227]\n"
      "s_waitcnt vmcnt(32)\n"
      "v_mfma_f32_16x16x32_bf16 a[0:3], v[32:35], v[0:3], a[0:3]\n"
      "s_add_u32 s83, s69, 634880\n"
      "buffer_load_dwordx4 v[188:191], v252, s[44:47], s83 offen offset:0 sc1\n"
      "v_mfma_f32_16x16x32_bf16 a[4:7], v[36:39], v[0:3], a[4:7]\n"
      "v_mfma_f32_16x16x32_bf16 a[8:11], v[40:43], v[0:3], a[8:11]\n"
      "v_mfma_f32_16x16x32_bf16 a[12:15], v[44:47], v[0:3], a[12:15]\n"
      "buffer_load_dwordx4 v[192:195], v252, s[44:47], s83 offen offset:1024 sc1\n"
      "v_mfma_f32_16x16x32_bf16 a[16:19], v[48:51], v[0:3], a[16:19]\n"
      "v_mfma_f32_16x16x32_bf16 a[20:23], v[32:35], v[4:7], a[20:23]\n"
      "v_mfma_f32_16x16x32_bf16 a[24:27], v[36:39], v[4:7], a[24:27]\n"
      "buffer_load_dwordx4 v[196:199], v252, s[44:47], s83 offen offset:2048 sc1\n"
      "v_mfma_f32_16x16x32_bf16 a[28:31], v[40:43], v[4:7], a[28:31]\n"
      "v_mfma_f32_16x16x32_bf16 a[32:35], v[44:47], v[4:7], a[32:35]\n"
      "v_mfma_f32_16x16x32_bf16 a[36:39], v[48:51], v[4:7], a[36:39]\n"
      "buffer_load_dwordx4 v[200:203], v252, s[44:47], s83 offen offset:3072 sc1\n"
      "s_waitcnt vmcnt(34)\n"
      "v_mfma_f32_16x16x32_bf16 a[40:43], v[32:35], v[8:11], a[40:43]\n"
      "v_mfma_f32_16x16x32_bf16 a[44:47], v[36:39], v[8:11], a[44:47]\n"
      "v_mfma_f32_16x16x32_bf16 a[48:51], v[40:43], v[8:11], a[48:51]\n"
      "buffer_load_dwordx4 v[204:207], v248, s[44:47], s83 offen offset:0 sc1\n"
      "v_mfma_f32_16x16x32_bf16 a[52:55], v[44:47], v[8:11], a[52:55]\n"
      "v_mfma_f32_16x16x32_bf16 a[56:59], v[48:51], v[8:11], a[56:59]\n"
      "v_mfma_f32_16x16x32_bf16 a[60:63], v[32:35], v[12:15], a[60:63]\n"
      "s_add_u32 s84, s68, 1015808\n"
      "buffer_load_dwordx4 v[156:159], v244, s[40:43], s84 offen offset:0 sc1\n"
      "v_mfma_f32_16x16x32_bf16 a[64:67], v[36:39], v[12:15], a[64:67]\n"
      "v_mfma_f32_16x16x32_bf16 a[68:71], v[40:43], v[12:15], a[68:71]\n"
      "v_mfma_f32_16x16x32_bf16 a[72:75], v[44:47], v[12:15], a[72:75]\n"
      "buffer_load_dwordx4 v[160:163], v244, s[40:43], s84 offen offset:1024 sc1\n"
      "v_mfma_f32_16x16x32_bf16 a[76:79], v[48:51], v[12:15], a[76:79]\n"
      "s_waitcnt vmcnt(35)\n"
      "v_mfma_f32_16x16x32_bf16 a[80:83], v[32:35], v[16:19], a[80:83]\n"
      "v_mfma_f32_16x16x32_bf16 a[84:87], v[36:39], v[16:19], a[84:87]\n"
      "buffer_load_dwordx4 v[164:167], v245, s[40:43], s84 offen offset:0 sc1\n"
      "v_mfma_f32_16x16x32_bf16 a[88:91], v[40:43], v[16:19], a[88:91]\n"
      "v_mfma_f32_16x16x32_bf16 a[92:95], v[44:47], v[16:19], a[92:95]\n"
      "v_mfma_f32_16x16x32_bf16 a[96:99], v[48:51], v[16:19], a[96:99]\n"
      "buffer_load_dwordx4 v[168:171], v245, s[40:43], s84 offen offset:1024 sc1\n"
      "v_mfma_f32_16x16x32_bf16 a[100:103], v[32:35], v[20:23], a[100:103]\n"
      "v_mfma_f32_16x16x32_bf16 a[104:107], v[36:39], v[20:23], a[104:107]\n"
      "v_mfma_f32_16x16x32_bf16 a[108:111], v[40:43], v[20:23], a[108:111]\n"
      "buffer_load_dwordx4 v[172:175], v246, s[40:43], s84 offen offset:0 sc1\n"
      "v_mfma_f32_16x16x32_bf16 a[112:115], v[44:47], v[20:23], a[112:115]\n"
      "v_mfma_f32_16x16x32_bf16 a[116:119], v[48:51], v[20:23], a[116:119]\n"
      "s_waitcnt vmcnt(36)\n"
      "v_mfma_f32_16x16x32_bf16 a[120:123], v[32:35], v[24:27], a[120:123]\n"
      "buffer_load_dwordx4 v[176:179], v246, s[40:43], s84 offen offset:1024 sc1\n"
      "v_mfma_f32_16x16x32_bf16 a[124:127], v[36:39], v[24:27], a[124:127]\n"
      "v_mfma_f32_16x16x32_bf16 a[128:131], v[40:43], v[24:27], a[128:131]\n"
      "v_mfma_f32_16x16x32_bf16 a[132:135], v[44:47], v[24:27], a[132:135]\n"
      "buffer_load_dwordx4 v[180:183], v247, s[40:43], s84 offen offset:0 sc1\n"
      "v_mfma_f32_16x16x32_bf16 a[136:139], v[48:51], v[24:27], a[136:139]\n"
      "v_mfma_f32_16x16x32_bf16 v[208:211], v[32:35], v[28:31], v[208:211]\n"
      "v_mfma_f32_16x16x32_bf16 v[212:215], v[36:39], v[28:31], v[212:215]\n"
      "buffer_load_dwordx4 v[184:187], v247, s[40:43], s84 offen offset:1024 sc1\n"
      "v_mfma_f32_16x16x32_bf16 v[216:219], v[40:43], v[28:31], v[216:219]\n"
      "v_mfma_f32_16x16x32_bf16 v[220:223], v[44:47], v[28:31], v[220:223]\n"
      "v_mfma_f32_16x16x32_bf16 v[224:227], v[48:51], v[28:31], v[224:227]\n"
      "s_waitcnt vmcnt(32)\n"
      "v_mfma_f32_16x16x32_bf16 a[0:3], v[84:87], v[52:55], a[0:3]\n"
      "v_mfma_f32_16x16x32_bf16 a[4:7], v[88:91], v[52:55], a[4:7]\n"
      "v_mfma_f32_16x16x32_bf16 a[8:11], v[92:95], v[52:55], a[8:11]\n"
      "v_mfma_f32_16x16x32_bf16 a[12:15], v[96:99], v[52:55], a[12:15]\n"
      "v_mfma_f32_16x16x32_bf16 a[16:19], v[100:103], v[52:55], a[16:19]\n"
      "v_mfma_f32_16x16x32_bf16 a[20:23], v[84:87], v[56:59], a[20:23]\n"
      "v_mfma_f32_16x16x32_bf16 a[24:27], v[88:91], v[56:59], a[24:27]\n"
      "v_mfma_f32_16x16x32_bf16 a[28:31], v[92:95], v[56:59], a[28:31]\n"
      "v_mfma_f32_16x16x32_bf16 a[32:35], v[96:99], v[56:59], a[32:35]\n"
      "v_mfma_f32_16x16x32_bf16 a[36:39], v[100:103], v[56:59], a[36:39]\n"
      "s_waitcnt vmcnt(30)\n"
      "v_mfma_f32_16x16x32_bf16 a[40:43], v[84:87], v[60:63], a[40:43]\n"
      "v_mfma_f32_16x16x32_bf16 a[44:47], v[88:91], v[60:63], a[44:47]\n"
      "v_mfma_f32_16x16x32_bf16 a[48:51], v[92:95], v[60:63], a[48:51]\n"
      "v_mfma_f32_16x16x32_bf16 a[52:55], v[96:99], v[60:63], a[52:55]\n"
      "v_mfma_f32_16x16x32_bf16 a[56:59], v[100:103], v[60:63], a[56:59]\n"
      "v_mfma_f32_16x16x32_bf16 a[60:63], v[84:87], v[64:67], a[60:63]\n"
      "v_mfma_f32_16x16x32_bf16 a[64:67], v[88:91], v[64:67], a[64:67]\n"
      "v_mfma_f32_16x16x32_bf16 a[68:71], v[92:95], v[64:67], a[68:71]\n"
      "v_mfma_f32_16x16x32_bf16 a[72:75], v[96:99], v[64:67], a[72:75]\n"
      "v_mfma_f32_16x16x32_bf16 a[76:79], v[100:103], v[64:67], a[76:79]\n"
      "s_waitcnt vmcnt(28)\n"
      "v_mfma_f32_16x16x32_bf16 a[80:83], v[84:87], v[68:71], a[80:83]\n"
      "v_mfma_f32_16x16x32_bf16 a[84:87], v[88:91], v[68:71], a[84:87]\n"
      "v_mfma_f32_16x16x32_bf16 a[88:91], v[92:95], v[68:71], a[88:91]\n"
      "v_mfma_f32_16x16x32_bf16 a[92:95], v[96:99], v[68:71], a[92:95]\n"
      "v_mfma_f32_16x16x32_bf16 a[96:99], v[100:103], v[68:71], a[96:99]\n"
      "v_mfma_f32_16x16x32_bf16 a[100:103], v[84:87], v[72:75], a[100:103]\n"
      "v_mfma_f32_16x16x32_bf16 a[104:107], v[88:91], v[72:75], a[104:107]\n"
      "v_mfma_f32_16x16x32_bf16 a[108:111], v[92:95], v[72:75], a[108:111]\n"
      "v_mfma_f32_16x16x32_bf16 a[112:115], v[96:99], v[72:75], a[112:115]\n"
      "v_mfma_f32_16x16x32_bf16 a[116:119], v[100:103], v[72:75], a[116:119]\n"
      "s_waitcnt vmcnt(26)\n"
      "v_mfma_f32_16x16x32_bf16 a[120:123], v[84:87], v[76:79], a[120:123]\n"
      "v_mfma_f32_16x16x32_bf16 a[124:127], v[88:91], v[76:79], a[124:127]\n"
      "v_mfma_f32_16x16x32_bf16 a[128:131], v[92:95], v[76:79], a[128:131]\n"
      "v_mfma_f32_16x16x32_bf16 a[132:135], v[96:99], v[76:79], a[132:135]\n"
      "v_mfma_f32_16x16x32_bf16 a[136:139], v[100:103], v[76:79], a[136:139]\n"
      "v_mfma_f32_16x16x32_bf16 v[208:211], v[84:87], v[80:83], v[208:211]\n"
      "v_mfma_f32_16x16x32_bf16 v[212:215], v[88:91], v[80:83], v[212:215]\n"
      "v_mfma_f32_16x16x32_bf16 v[216:219], v[92:95], v[80:83], v[216:219]\n"
      "v_mfma_f32_16x16x32_bf16 v[220:223], v[96:99], v[80:83], v[220:223]\n"
      "v_mfma_f32_16x16x32_bf16 v[224:227], v[100:103], v[80:83], v[224:227]\n"
      "s_waitcnt vmcnt(19)\n"
      "v_mfma_f32_16x16x32_bf16 a[0:3], v[136:139], v[104:107], a[0:3]\n"
      "s_add_u32 s87, s80, 1\n"
      "s_and_b32 s87, s87, 3\n"
      "s_mul_i32 s87, s87, 30720\n"
      "s_add_u32 s87, s87, 0\n"
      "v_add_u32 v240, s87, v252\n"
      "v_mfma_f32_16x16x32_bf16 a[4:7], v[140:143], v[104:107], a[4:7]\n"
      "s_add_u32 s87, s80, 2\n"
      "s_and_b32 s87, s87, 3\n"
      "s_mul_i32 s87, s87, 30720\n"
      "s_add_u32 s87, s87, 10240\n"
      "v_add_u32 v241, s87, v252\n"
      "v_mfma_f32_16x16x32_bf16 a[8:11], v[144:147], v[104:107], a[8:11]\n"
      "s_add_u32 s87, s80, 3\n"
      "s_and_b32 s87, s87, 3\n"
      "s_mul_i32 s87, s87, 30720\n"
      "s_add_u32 s87, s87, 20480\n"
      "v_add_u32 v242, s87, v252\n"
      "v_mfma_f32_16x16x32_bf16 a[12:15], v[148:151], v[104:107], a[12:15]\n"
      "v_mfma_f32_16x16x32_bf16 a[16:19], v[152:155], v[104:107], a[16:19]\n"
      "s_mul_i32 s87, s80, 30720\n"
      "v_add_u32 v243, s87, v252\n"
      "v_and_b32 v245, 15, v255\n"
      "v_lshrrev_b32 v246, 4, v255\n"
      "v_mul_u32_u24 v245, 2560, v245\n"
      "v_lshl_add_u32 v245, v246, 3, v245\n"
      "s_mul_i32 s81, s80, 81920\n"
      "s_add_u32 s82, s81, 40960\n"
      "v_mfma_f32_16x16x32_bf16 a[20:23], v[136:139], v[108:111], a[20:23]\n"
      "v_mfma_f32_16x16x32_bf16 a[24:27], v[140:143], v[108:111], a[24:27]\n"
      "v_mfma_f32_16x16x32_bf16 a[28:31], v[144:147], v[108:111], a[28:31]\n"
      "v_mfma_f32_16x16x32_bf16 a[32:35], v[148:151], v[108:111], a[32:35]\n"
      "v_mfma_f32_16x16x32_bf16 a[36:39], v[152:155], v[108:111], a[36:39]\n"
      "s_waitcnt vmcnt(17)\n"
      "v_mfma_f32_16x16x32_bf16 a[40:43], v[136:139], v[112:115], a[40:43]\n"
      "v_mfma_f32_16x16x32_bf16 a[44:47], v[140:143], v[112:115], a[44:47]\n"
      "v_mfma_f32_16x16x32_bf16 a[48:51], v[144:147], v[112:115], a[48:51]\n"
      "v_mfma_f32_16x16x32_bf16 a[52:55], v[148:151], v[112:115], a[52:55]\n"
      "v_mfma_f32_16x16x32_bf16 a[56:59], v[152:155], v[112:115], a[56:59]\n"
      "v_mfma_f32_16x16x32_bf16 a[60:63], v[136:139], v[116:119], a[60:63]\n"
      "v_mfma_f32_16x16x32_bf16 a[64:67], v[140:143], v[116:119], a[64:67]\n"
      "v_mfma_f32_16x16x32_bf16 a[68:71], v[144:147], v[116:119], a[68:71]\n"
      "v_mfma_f32_16x16x32_bf16 a[72:75], v[148:151], v[116:119], a[72:75]\n"
      "v_mfma_f32_16x16x32_bf16 a[76:79], v[152:155], v[116:119], a[76:79]\n"
      "s_waitcnt vmcnt(15)\n"
      "v_mfma_f32_16x16x32_bf16 a[80:83], v[136:139], v[120:123], a[80:83]\n"
      "v_mfma_f32_16x16x32_bf16 a[84:87], v[140:143], v[120:123], a[84:87]\n"
      "v_mfma_f32_16x16x32_bf16 a[88:91], v[144:147], v[120:123], a[88:91]\n"
      "v_mfma_f32_16x16x32_bf16 a[92:95], v[148:151], v[120:123], a[92:95]\n"
      "v_mfma_f32_16x16x32_bf16 a[96:99], v[152:155], v[120:123], a[96:99]\n"
      "v_mfma_f32_16x16x32_bf16 a[100:103], v[136:139], v[124:127], a[100:103]\n"
      "v_mfma_f32_16x16x32_bf16 a[104:107], v[140:143], v[124:127], a[104:107]\n"
      "v_mfma_f32_16x16x32_bf16 a[108:111], v[144:147], v[124:127], a[108:111]\n"
      "v_mfma_f32_16x16x32_bf16 a[112:115], v[148:151], v[124:127], a[112:115]\n"
      "v_mfma_f32_16x16x32_bf16 a[116:119], v[152:155], v[124:127], a[116:119]\n"
      "s_waitcnt vmcnt(13)\n"
      "v_mfma_f32_16x16x32_bf16 a[120:123], v[136:139], v[128:131], a[120:123]\n"
      "v_mfma_f32_16x16x32_bf16 a[124:127], v[140:143], v[128:131], a[124:127]\n"
      "v_mfma_f32_16x16x32_bf16 a[128:131], v[144:147], v[128:131], a[128:131]\n"
      "v_mfma_f32_16x16x32_bf16 a[132:135], v[148:151], v[128:131], a[132:135]\n"
      "v_mfma_f32_16x16x32_bf16 a[136:139], v[152:155], v[128:131], a[136:139]\n"
      "v_mfma_f32_16x16x32_bf16 v[208:211], v[136:139], v[132:135], v[208:211]\n"
      "v_mfma_f32_16x16x32_bf16 v[212:215], v[140:143], v[132:135], v[212:215]\n"
      "v_mfma_f32_16x16x32_bf16 v[216:219], v[144:147], v[132:135], v[216:219]\n"
      "v_mfma_f32_16x16x32_bf16 v[220:223], v[148:151], v[132:135], v[220:223]\n"
      "v_mfma_f32_16x16x32_bf16 v[224:227], v[152:155], v[132:135], v[224:227]\n"
      "s_waitcnt vmcnt(6)\n"
      "v_mfma_f32_16x16x32_bf16 a[0:3], v[188:191], v[156:159], a[0:3]\n"
      "v_mfma_f32_16x16x32_bf16 a[4:7], v[192:195], v[156:159], a[4:7]\n"
      "v_mfma_f32_16x16x32_bf16 a[8:11], v[196:199], v[156:159], a[8:11]\n"
      "v_mfma_f32_16x16x32_bf16 a[12:15], v[200:203], v[156:159], a[12:15]\n"
      "v_mfma_f32_16x16x32_bf16 a[16:19], v[204:207], v[156:159], a[16:19]\n"
      "v_mfma_f32_16x16x32_bf16 a[20:23], v[188:191], v[160:163], a[20:23]\n"
      "v_mfma_f32_16x16x32_bf16 a[24:27], v[192:195], v[160:163], a[24:27]\n"
      "v_mfma_f32_16x16x32_bf16 a[28:31], v[196:199], v[160:163], a[28:31]\n"
      "v_mfma_f32_16x16x32_bf16 a[32:35], v[200:203], v[160:163], a[32:35]\n"
      "v_mfma_f32_16x16x32_bf16 a[36:39], v[204:207], v[160:163], a[36:39]\n"
      "s_waitcnt vmcnt(4)\n"
      "v_mfma_f32_16x16x32_bf16 a[40:43], v[188:191], v[164:167], a[40:43]\n"
      "ds_write_b128 v240, a[0:3] offset:0\n"
      "v_mfma_f32_16x16x32_bf16 a[44:47], v[192:195], v[164:167], a[44:47]\n"
      "ds_write_b128 v240, a[4:7] offset:1024\n"
      "v_mfma_f32_16x16x32_bf16 a[48:51], v[196:199], v[164:167], a[48:51]\n"
      "ds_write_b128 v240, a[8:11] offset:2048\n"
      "v_mfma_f32_16x16x32_bf16 a[52:55], v[200:203], v[164:167], a[52:55]\n"
      "ds_write_b128 v240, a[12:15] offset:3072\n"
      "v_mfma_f32_16x16x32_bf16 a[56:59], v[204:207], v[164:167], a[56:59]\n"
      "ds_write_b128 v240, a[16:19] offset:4096\n"
      "v_mfma_f32_16x16x32_bf16 a[60:63], v[188:191], v[168:171], a[60:63]\n"
      "ds_write_b128 v240, a[20:23] offset:5120\n"
      "v_mfma_f32_16x16x32_bf16 a[64:67], v[192:195], v[168:171], a[64:67]\n"
      "ds_write_b128 v240, a[24:27] offset:6144\n"
      "v_mfma_f32_16x16x32_bf16 a[68:71], v[196:199], v[168:171], a[68:71]\n"
      "ds_write_b128 v240, a[28:31] offset:7168\n"
      "v_mfma_f32_16x16x32_bf16 a[72:75], v[200:203], v[168:171], a[72:75]\n"
      "ds_write_b128 v240, a[32:35] offset:8192\n"
      "v_mfma_f32_16x16x32_bf16 a[76:79], v[204:207], v[168:171], a[76:79]\n"
      "ds_write_b128 v240, a[36:39] offset:9216\n"
      "s_waitcnt vmcnt(2)\n"
      "v_mfma_f32_16x16x32_bf16 a[80:83], v[188:191], v[172:175], a[80:83]\n"
      "ds_write_b128 v241, a[40:43] offset:0\n"
      "v_mfma_f32_16x16x32_bf16 a[84:87], v[192:195], v[172:175], a[84:87]\n"
      "ds_write_b128 v241, a[44:47] offset:1024\n"
      "v_mfma_f32_16x16x32_bf16 a[88:91], v[196:199], v[172:175], a[88:91]\n"
      "ds_write_b128 v241, a[48:51] offset:2048\n"
      "v_mfma_f32_16x16x32_bf16 a[92:95], v[200:203], v[172:175], a[92:95]\n"
      "ds_write_b128 v241, a[52:55] offset:3072\n"
      "v_mfma_f32_16x16x32_bf16 a[96:99], v[204:207], v[172:175], a[96:99]\n"
      "ds_write_b128 v241, a[56:59] offset:4096\n"
      "v_mfma_f32_16x16x32_bf16 a[100:103], v[188:191], v[176:179], a[100:103]\n"
      "ds_write_b128 v241, a[60:63] offset:5120\n"
      "v_mfma_f32_16x16x32_bf16 a[104:107], v[192:195], v[176:179], a[104:107]\n"
      "ds_write_b128 v241, a[64:67] offset:6144\n"
      "v_mfma_f32_16x16x32_bf16 a[108:111], v[196:199], v[176:179], a[108:111]\n"
      "ds_write_b128 v241, a[68:71] offset:7168\n"
      "v_mfma_f32_16x16x32_bf16 a[112:115], v[200:203], v[176:179], a[112:115]\n"
      "ds_write_b128 v241, a[72:75] offset:8192\n"
      "v_mfma_f32_16x16x32_bf16 a[116:119], v[204:207], v[176:179], a[116:119]\n"
      "ds_write_b128 v241, a[76:79] offset:9216\n"
      "s_waitcnt vmcnt(0)\n"
      "v_mfma_f32_16x16x32_bf16 a[120:123], v[188:191], v[180:183], a[120:123]\n"
      "ds_write_b128 v242, a[80:83] offset:0\n"
      "v_mfma_f32_16x16x32_bf16 a[124:127], v[192:195], v[180:183], a[124:127]\n"
      "ds_write_b128 v242, a[84:87] offset:1024\n"
      "v_mfma_f32_16x16x32_bf16 a[128:131], v[196:199], v[180:183], a[128:131]\n"
      "ds_write_b128 v242, a[88:91] offset:2048\n"
      "v_mfma_f32_16x16x32_bf16 a[132:135], v[200:203], v[180:183], a[132:135]\n"
      "ds_write_b128 v242, a[92:95] offset:3072\n"
      "v_mfma_f32_16x16x32_bf16 a[136:139], v[204:207], v[180:183], a[136:139]\n"
      "ds_write_b128 v242, a[96:99] offset:4096\n"
      "v_mfma_f32_16x16x32_bf16 v[208:211], v[188:191], v[184:187], v[208:211]\n"
      "ds_write_b128 v242, a[100:103] offset:5120\n"
      "v_mfma_f32_16x16x32_bf16 v[212:215], v[192:195], v[184:187], v[212:215]\n"
      "ds_write_b128 v242, a[104:107] offset:6144\n"
      "v_mfma_f32_16x16x32_bf16 v[216:219], v[196:199], v[184:187], v[216:219]\n"
      "ds_write_b128 v242, a[108:111] offset:7168\n"
      "v_mfma_f32_16x16x32_bf16 v[220:223], v[200:203], v[184:187], v[220:223]\n"
      "ds_write_b128 v242, a[112:115] offset:8192\n"
      "v_mfma_f32_16x16x32_bf16 v[224:227], v[204:207], v[184:187], v[224:227]\n"
      "ds_write_b128 v242, a[116:119] offset:9216\n"
      "s_waitcnt lgkmcnt(0)\n"
      "s_barrier\n"
      "ds_read_b128 v[0:3], v243 offset:0\n"
      "ds_read_b128 v[4:7], v243 offset:10240\n"
      "ds_read_b128 v[8:11], v243 offset:20480\n"
      "ds_read_b128 v[16:19], v243 offset:1024\n"
      "ds_read_b128 v[20:23], v243 offset:11264\n"
      "ds_read_b128 v[24:27], v243 offset:21504\n"
      "ds_read_b128 v[32:35], v243 offset:2048\n"
      "ds_read_b128 v[36:39], v243 offset:12288\n"
      "ds_read_b128 v[40:43], v243 offset:22528\n"
      "ds_read_b128 v[48:51], v243 offset:3072\n"
      "ds_read_b128 v[52:55], v243 offset:13312\n"
      "ds_read_b128 v[56:59], v243 offset:23552\n"
      "ds_read_b128 v[64:67], v243 offset:4096\n"
      "ds_read_b128 v[68:71], v243 offset:14336\n"
      "ds_read_b128 v[72:75], v243 offset:24576\n"
      "ds_read_b128 v[80:83], v243 offset:5120\n"
      "ds_read_b128 v[84:87], v243 offset:15360\n"
      "ds_read_b128 v[88:91], v243 offset:25600\n"
      "ds_read_b128 v[96:99], v243 offset:6144\n"
      "ds_read_b128 v[100:103], v243 offset:16384\n"
      "ds_read_b128 v[104:107], v243 offset:26624\n"
      "ds_read_b128 v[112:115], v243 offset:7168\n"
      "ds_read_b128 v[116:119], v243 offset:17408\n"
      "ds_read_b128 v[120:123], v243 offset:27648\n"
      "ds_read_b128 v[128:131], v243 offset:8192\n"
      "ds_read_b128 v[132:135], v243 offset:18432\n"
      "ds_read_b128 v[136:139], v243 offset:28672\n"
      "ds_read_b128 v[144:147], v243 offset:9216\n"
      "ds_read_b128 v[148:151], v243 offset:19456\n"
      "ds_read_b128 v[152:155], v243 offset:29696\n"
      "v_accvgpr_read_b32 v12, a120\n"
      "v_accvgpr_read_b32 v13, a121\n"
      "v_accvgpr_read_b32 v14, a122\n"
      "v_accvgpr_read_b32 v15, a123\n"
      "v_accvgpr_read_b32 v28, a124\n"
      "v_accvgpr_read_b32 v29, a125\n"
      "v_accvgpr_read_b32 v30, a126\n"
      "v_accvgpr_read_b32 v31, a127\n"
      "v_accvgpr_read_b32 v44, a128\n"
      "v_accvgpr_read_b32 v45, a129\n"
      "v_accvgpr_read_b32 v46, a130\n"
      "v_accvgpr_read_b32 v47, a131\n"
      "v_accvgpr_read_b32 v60, a132\n"
      "v_accvgpr_read_b32 v61, a133\n"
      "v_accvgpr_read_b32 v62, a134\n"
      "v_accvgpr_read_b32 v63, a135\n"
      "v_accvgpr_read_b32 v76, a136\n"
      "v_accvgpr_read_b32 v77, a137\n"
      "v_accvgpr_read_b32 v78, a138\n"
      "v_accvgpr_read_b32 v79, a139\n"
      "s_waitcnt lgkmcnt(15)\n"
      "v_pk_add_f32 v[0:1], v[0:1], v[4:5]\n"
      "v_pk_add_f32 v[2:3], v[2:3], v[6:7]\n"
      "v_pk_add_f32 v[8:9], v[8:9], v[12:13]\n"
      "v_pk_add_f32 v[10:11], v[10:11], v[14:15]\n"
      "v_pk_add_f32 v[0:1], v[0:1], v[8:9]\n"
      "v_pk_add_f32 v[2:3], v[2:3], v[10:11]\n"
      "v_cvt_pk_bf16_f32 v0, v0, v1\n"
      "v_cvt_pk_bf16_f32 v1, v2, v3\n"
      "buffer_store_dwordx2 v[0:1], v245, s[48:51], s81 offen offset:0\n"
      "s_waitcnt lgkmcnt(15)\n"
      "v_pk_add_f32 v[16:17], v[16:17], v[20:21]\n"
      "v_pk_add_f32 v[18:19], v[18:19], v[22:23]\n"
      "v_pk_add_f32 v[24:25], v[24:25], v[28:29]\n"
      "v_pk_add_f32 v[26:27], v[26:27], v[30:31]\n"
      "v_pk_add_f32 v[16:17], v[16:17], v[24:25]\n"
      "v_pk_add_f32 v[18:19], v[18:19], v[26:27]\n"
      "v_cvt_pk_bf16_f32 v16, v16, v17\n"
      "v_cvt_pk_bf16_f32 v17, v18, v19\n"
      "buffer_store_dwordx2 v[16:17], v245, s[48:51], s81 offen offset:32\n"
      "s_waitcnt lgkmcnt(15)\n"
      "v_pk_add_f32 v[32:33], v[32:33], v[36:37]\n"
      "v_pk_add_f32 v[34:35], v[34:35], v[38:39]\n"
      "v_pk_add_f32 v[40:41], v[40:41], v[44:45]\n"
      "v_pk_add_f32 v[42:43], v[42:43], v[46:47]\n"
      "v_pk_add_f32 v[32:33], v[32:33], v[40:41]\n"
      "v_pk_add_f32 v[34:35], v[34:35], v[42:43]\n"
      "v_cvt_pk_bf16_f32 v32, v32, v33\n"
      "v_cvt_pk_bf16_f32 v33, v34, v35\n"
      "buffer_store_dwordx2 v[32:33], v245, s[48:51], s81 offen offset:64\n"
      "s_waitcnt lgkmcnt(15)\n"
      "v_pk_add_f32 v[48:49], v[48:49], v[52:53]\n"
      "v_pk_add_f32 v[50:51], v[50:51], v[54:55]\n"
      "v_pk_add_f32 v[56:57], v[56:57], v[60:61]\n"
      "v_pk_add_f32 v[58:59], v[58:59], v[62:63]\n"
      "v_pk_add_f32 v[48:49], v[48:49], v[56:57]\n"
      "v_pk_add_f32 v[50:51], v[50:51], v[58:59]\n"
      "v_cvt_pk_bf16_f32 v48, v48, v49\n"
      "v_cvt_pk_bf16_f32 v49, v50, v51\n"
      "buffer_store_dwordx2 v[48:49], v245, s[48:51], s81 offen offset:96\n"
      "s_waitcnt lgkmcnt(15)\n"
      "v_pk_add_f32 v[64:65], v[64:65], v[68:69]\n"
      "v_pk_add_f32 v[66:67], v[66:67], v[70:71]\n"
      "v_pk_add_f32 v[72:73], v[72:73], v[76:77]\n"
      "v_pk_add_f32 v[74:75], v[74:75], v[78:79]\n"
      "v_pk_add_f32 v[64:65], v[64:65], v[72:73]\n"
      "v_pk_add_f32 v[66:67], v[66:67], v[74:75]\n"
      "v_cvt_pk_bf16_f32 v64, v64, v65\n"
      "v_cvt_pk_bf16_f32 v65, v66, v67\n"
      "buffer_store_dwordx2 v[64:65], v245, s[48:51], s81 offen offset:128\n"
      "s_waitcnt lgkmcnt(12)\n"
      "v_pk_add_f32 v[80:81], v[80:81], v[84:85]\n"
      "v_pk_add_f32 v[82:83], v[82:83], v[86:87]\n"
      "v_pk_add_f32 v[88:89], v[88:89], v[208:209]\n"
      "v_pk_add_f32 v[90:91], v[90:91], v[210:211]\n"
      "v_pk_add_f32 v[80:81], v[80:81], v[88:89]\n"
      "v_pk_add_f32 v[82:83], v[82:83], v[90:91]\n"
      "v_cvt_pk_bf16_f32 v80, v80, v81\n"
      "v_cvt_pk_bf16_f32 v81, v82, v83\n"
      "buffer_store_dwordx2 v[80:81], v245, s[48:51], s82 offen offset:0\n"
      "s_waitcnt lgkmcnt(9)\n"
      "v_pk_add_f32 v[96:97], v[96:97], v[100:101]\n"
      "v_pk_add_f32 v[98:99], v[98:99], v[102:103]\n"
      "v_pk_add_f32 v[104:105], v[104:105], v[212:213]\n"
      "v_pk_add_f32 v[106:107], v[106:107], v[214:215]\n"
      "v_pk_add_f32 v[96:97], v[96:97], v[104:105]\n"
      "v_pk_add_f32 v[98:99], v[98:99], v[106:107]\n"
      "v_cvt_pk_bf16_f32 v96, v96, v97\n"
      "v_cvt_pk_bf16_f32 v97, v98, v99\n"
      "buffer_store_dwordx2 v[96:97], v245, s[48:51], s82 offen offset:32\n"
      "s_waitcnt lgkmcnt(6)\n"
      "v_pk_add_f32 v[112:113], v[112:113], v[116:117]\n"
      "v_pk_add_f32 v[114:115], v[114:115], v[118:119]\n"
      "v_pk_add_f32 v[120:121], v[120:121], v[216:217]\n"
      "v_pk_add_f32 v[122:123], v[122:123], v[218:219]\n"
      "v_pk_add_f32 v[112:113], v[112:113], v[120:121]\n"
      "v_pk_add_f32 v[114:115], v[114:115], v[122:123]\n"
      "v_cvt_pk_bf16_f32 v112, v112, v113\n"
      "v_cvt_pk_bf16_f32 v113, v114, v115\n"
      "buffer_store_dwordx2 v[112:113], v245, s[48:51], s82 offen offset:64\n"
      "s_waitcnt lgkmcnt(3)\n"
      "v_pk_add_f32 v[128:129], v[128:129], v[132:133]\n"
      "v_pk_add_f32 v[130:131], v[130:131], v[134:135]\n"
      "v_pk_add_f32 v[136:137], v[136:137], v[220:221]\n"
      "v_pk_add_f32 v[138:139], v[138:139], v[222:223]\n"
      "v_pk_add_f32 v[128:129], v[128:129], v[136:137]\n"
      "v_pk_add_f32 v[130:131], v[130:131], v[138:139]\n"
      "v_cvt_pk_bf16_f32 v128, v128, v129\n"
      "v_cvt_pk_bf16_f32 v129, v130, v131\n"
      "buffer_store_dwordx2 v[128:129], v245, s[48:51], s82 offen offset:96\n"
      "s_waitcnt lgkmcnt(0)\n"
      "v_pk_add_f32 v[144:145], v[144:145], v[148:149]\n"
      "v_pk_add_f32 v[146:147], v[146:147], v[150:151]\n"
      "v_pk_add_f32 v[152:153], v[152:153], v[224:225]\n"
      "v_pk_add_f32 v[154:155], v[154:155], v[226:227]\n"
      "v_pk_add_f32 v[144:145], v[144:145], v[152:153]\n"
      "v_pk_add_f32 v[146:147], v[146:147], v[154:155]\n"
      "v_cvt_pk_bf16_f32 v144, v144, v145\n"
      "v_cvt_pk_bf16_f32 v145, v146, v147\n"
      "buffer_store_dwordx2 v[144:145], v245, s[48:51], s82 offen offset:128\n"
      "s_endpgm\n"
      "STUB0_%=:\n"
      "s_call_b64 s[76:77], SLOW0_%=\n"
      "s_branch BACK0_%=\n"
      "STUB1_%=:\n"
      "s_call_b64 s[76:77], SLOW1_%=\n"
      "s_branch BACK1_%=\n"
      "STUB2_%=:\n"
      "s_call_b64 s[76:77], SLOWP_%=\n"
      "s_branch BACK2_%=\n"
      "STUB3_%=:\n"
      "s_call_b64 s[76:77], SLOW0_%=\n"
      "s_branch BACK3_%=\n"
      "STUB4_%=:\n"
      "s_call_b64 s[76:77], SLOWP_%=\n"
      "s_branch BACK4_%=\n"
      "STUB5_%=:\n"
      "s_call_b64 s[76:77], SLOW1_%=\n"
      "s_branch BACK5_%=\n"
      "STUB6_%=:\n"
      "s_call_b64 s[76:77], SLOWP_%=\n"
      "s_branch BACK6_%=\n"
      "STUB7_%=:\n"
      "s_call_b64 s[76:77], SLOW0_%=\n"
      "s_branch BACK7_%=\n"
      "STUB8_%=:\n"
      "s_call_b64 s[76:77], SLOWP_%=\n"
      "s_branch BACK8_%=\n"
      "STUB9_%=:\n"
      "s_call_b64 s[76:77], SLOW1_%=\n"
      "s_branch BACK9_%=\n"
      "STUB10_%=:\n"
      "s_call_b64 s[76:77], SLOWP_%=\n"
      "s_branch BACK10_%=\n"
      "STUB11_%=:\n"
      "s_call_b64 s[76:77], SLOW0_%=\n"
      "s_branch BACK11_%=\n"
      "STUB12_%=:\n"
      "s_call_b64 s[76:77], SLOWP_%=\n"
      "s_branch BACK12_%=\n"
      "STUB13_%=:\n"
      "s_call_b64 s[76:77], SLOW1_%=\n"
      "s_branch BACK13_%=\n"
      "STUB14_%=:\n"
      "s_call_b64 s[76:77], SLOWP_%=\n"
      "s_branch BACK14_%=\n"
      "STUB15_%=:\n"
      "s_call_b64 s[76:77], SLOW0_%=\n"
      "s_branch BACK15_%=\n"
      "STUB16_%=:\n"
      "s_call_b64 s[76:77], SLOWP_%=\n"
      "s_branch BACK16_%=\n"
      "STUB17_%=:\n"
      "s_call_b64 s[76:77], SLOW1_%=\n"
      "s_branch BACK17_%=\n"
      "STUB18_%=:\n"
      "s_call_b64 s[76:77], SLOW0_%=\n"
      "s_branch BACK18_%=\n"
      "STUB19_%=:\n"
      "s_call_b64 s[76:77], SLOW1_%=\n"
      "s_branch BACK19_%=\n"
      "STUB20_%=:\n"
      "s_call_b64 s[76:77], SLOWP_%=\n"
      "s_branch BACK20_%=\n"
      "STUB21_%=:\n"
      "s_call_b64 s[76:77], SLOW0_%=\n"
      "s_branch BACK21_%=\n"
      "STUB22_%=:\n"
      "s_call_b64 s[76:77], SLOWP_%=\n"
      "s_branch BACK22_%=\n"
      "STUB23_%=:\n"
      "s_call_b64 s[76:77], SLOW1_%=\n"
      "s_branch BACK23_%=\n"
      "STUB24_%=:\n"
      "s_call_b64 s[76:77], SLOWP_%=\n"
      "s_branch BACK24_%=\n"
      "STUB25_%=:\n"
      "s_call_b64 s[76:77], SLOW0_%=\n"
      "s_branch BACK25_%=\n"
      "STUB26_%=:\n"
      "s_call_b64 s[76:77], SLOWP_%=\n"
      "s_branch BACK26_%=\n"
      "STUB27_%=:\n"
      "s_call_b64 s[76:77], SLOW1_%=\n"
      "s_branch BACK27_%=\n"
      "STUB28_%=:\n"
      "s_call_b64 s[76:77], SLOWP_%=\n"
      "s_branch BACK28_%=\n"
      "STUB29_%=:\n"
      "s_call_b64 s[76:77], SLOW0_%=\n"
      "s_branch BACK29_%=\n"
      "STUB30_%=:\n"
      "s_call_b64 s[76:77], SLOWP_%=\n"
      "s_branch BACK30_%=\n"
      "STUB31_%=:\n"
      "s_call_b64 s[76:77], SLOW1_%=\n"
      "s_branch BACK31_%=\n"
      "STUB32_%=:\n"
      "s_call_b64 s[76:77], SLOW0_%=\n"
      "s_branch BACK32_%=\n"
      "STUB33_%=:\n"
      "s_call_b64 s[76:77], SLOW1_%=\n"
      "s_branch BACK33_%=\n"
      "STUB34_%=:\n"
      "s_call_b64 s[76:77], SLOW0_%=\n"
      "s_branch BACK34_%=\n"
      "STUB35_%=:\n"
      "s_call_b64 s[76:77], SLOW1_%=\n"
      "s_branch BACK35_%=\n"
      "STUB36_%=:\n"
      "s_call_b64 s[76:77], SLOW0_%=\n"
      "s_branch BACK36_%=\n"
      "STUB37_%=:\n"
      "s_call_b64 s[76:77], SLOW1_%=\n"
      "s_branch BACK37_%=\n"
      "STUB38_%=:\n"
      "s_call_b64 s[76:77], SLOW0_%=\n"
      "s_branch BACK38_%=\n"
      "STUB39_%=:\n"
      "s_call_b64 s[76:77], SLOW1_%=\n"
      "s_branch BACK39_%=\n"
      "SIG0_%=:\n"
      "v_lshlrev_b32 v190, 11, v255\n"
      "global_store_dword v190, v188, %14 sc1\n"
      "global_store_dword v190, v189, %14 offset:1024 sc1\n"
      "s_branch SIG0B_%=\n"
      "POL0_%=:\n"
      "v_mov_b32 v183, 0\n"
      "global_load_dwordx2 v[186:187], v184, %13 sc1\n"
      "global_load_dword v185, v183, %12 sc1\n"
      "s_branch POL0B_%=\n"
      "NONCE_%=:\n"
      "v_readfirstlane_b32 s87, v131\n"
      "NONCL_%=:\n"
      "s_add_u32 s87, s87, 1\n"
      "v_cmp_eq_u32 vcc, s87, v128\n"
      "v_cmp_eq_u32 s[92:93], s87, v129\n"
      "s_or_b64 s[92:93], s[92:93], vcc\n"
      "v_cmp_eq_u32 vcc, s87, v130\n"
      "s_or_b64 s[92:93], s[92:93], vcc\n"
      "s_cbranch_scc1 NONCL_%=\n"
      "v_mov_b32 v132, s87\n"
      "s_mov_b64 exec, 1\n"
      "global_store_dword v133, v132, %12 sc1\n"
      "s_mov_b64 exec, -1\n"
      "s_branch NONCEB_%=\n"
      "WOD_%=:\n"
      "v_add_u32 v244, 0, v255\n"
      "v_mul_u32_u24 v245, 6554, v244\n"
      "v_lshrrev_b32 v245, 16, v245\n"
      "v_mul_u32_u24 v245, 1120, v245\n"
      "v_lshl_add_u32 v244, v244, 4, v245\n"
      "s_add_u32 m0, s79, 130432\n"
      "s_nop 0\n"
      "buffer_load_dwordx4 v244, s[60:63], s85 offen offset:640 nt lds\n"
      "v_add_u32 v244, 64, v255\n"
      "v_mul_u32_u24 v245, 6554, v244\n"
      "v_lshrrev_b32 v245, 16, v245\n"
      "v_mul_u32_u24 v245, 1120, v245\n"
      "v_lshl_add_u32 v244, v244, 4, v245\n"
      "s_add_u32 m0, s79, 131456\n"
      "s_nop 0\n"
      "buffer_load_dwordx4 v244, s[60:63], s85 offen offset:640 nt lds\n"
      "v_add_u32 v244, 128, v255\n"
      "v_mul_u32_u24 v245, 6554, v244\n"
      "v_lshrrev_b32 v245, 16, v245\n"
      "v_mul_u32_u24 v245, 1120, v245\n"
      "v_lshl_add_u32 v244, v244, 4, v245\n"
      "s_add_u32 m0, s79, 132480\n"
      "s_nop 0\n"
      "buffer_load_dwordx4 v244, s[60:63], s85 offen offset:640 nt lds\n"
      "v_add_u32 v244, 192, v255\n"
      "v_mul_u32_u24 v245, 6554, v244\n"
      "v_lshrrev_b32 v245, 16, v245\n"
      "v_mul_u32_u24 v245, 1120, v245\n"
      "v_lshl_add_u32 v244, v244, 4, v245\n"
      "s_add_u32 m0, s79, 133504\n"
      "s_nop 0\n"
      "buffer_load_dwordx4 v244, s[60:63], s85 offen offset:640 nt lds\n"
      "v_add_u32 v244, 256, v255\n"
      "v_mul_u32_u24 v245, 6554, v244\n"
      "v_lshrrev_b32 v245, 16, v245\n"
      "v_mul_u32_u24 v245, 1120, v245\n"
      "v_lshl_add_u32 v244, v244, 4, v245\n"
      "s_add_u32 m0, s79, 134528\n"
      "s_nop 0\n"
      "buffer_load_dwordx4 v244, s[60:63], s85 offen offset:640 nt lds\n"
      "s_mov_b32 s92, -1\n"
      "s_add_u32 s86, s86, 2621440\n"
      "s_branch WODB_%=\n"
      "SPIN0_%=:\n"
      "s_add_u32 s91, s91, 1\n"
      "s_cmp_ge_u32 s91, 0x20000\n"
      "s_cbranch_scc1 SPB0_%=\n"
      "s_sleep 8\n"
      "global_load_dwordx2 v[186:187], v184, %13 sc1\n"
      "s_waitcnt vmcnt(0)\n"
      "v_cmp_ne_u32 vcc, s94, v186\n"
      "v_cmp_ne_u32 s[92:93], s94, v187\n"
      "s_or_b64 vcc, vcc, s[92:93]\n"
      "s_cmp_eq_u64 vcc, 0\n"
      "s_cbranch_scc0 SPIN0_%=\n"
      "s_branch SPB0_%=\n"
      "SPIN1_%=:\n"
      "s_add_u32 s91, s91, 1\n"
      "s_cmp_ge_u32 s91, 0x20000\n"
      "s_cbranch_scc1 SPB1_%=\n"
      "s_sleep 8\n"
      "global_load_dword v250, v249, %13 offset:512 sc1\n"
      "s_waitcnt vmcnt(0)\n"
      "v_cmp_ne_u32 vcc, s94, v250\n"
      "s_cmp_eq_u64 vcc, 0\n"
      "s_cbranch_scc0 SPIN1_%=\n"
      "s_branch SPB1_%=\n"
      "SLOWP_%=:\n"
      "s_memrealtime s[82:83]\n"
      "s_waitcnt lgkmcnt(0)\n"
      "s_sub_u32 s81, s95, s82\n"
      "s_sub_u32 s81, s81, 1\n"
      "s_cmp_lt_u32 s81, 1000\n"
      "s_cbranch_scc1 SLOWP_%=\n"
      "s_mov_b32 s82, s95\n"
      "s_setpc_b64 s[76:77]\n"
      "SLOW0_%=:\n"
      "s_nop 15\n"
      "v_max3_f32 v244, v0, v1, v2\n"
      "v_max3_f32 v242, v3, v4, v5\n"
      "v_max3_f32 v244, v244, v6, v7\n"
      "v_max_f32 v244, v244, v242\n"
      "v_mov_b32 v242, v244\n"
      "s_nop 1\n"
      "v_permlane16_swap_b32 v242, v244\n"
      "s_nop 0\n"
      "v_max_f32 v244, v244, v242\n"
      "v_mov_b32 v242, v244\n"
      "s_nop 1\n"
      "v_permlane32_swap_b32 v242, v244\n"
      "s_nop 0\n"
      "v_max_f32 v244, v244, v242\n"
      "v_max_f32 v244, 0, v244\n"
      "v_exp_f32 v242, -v244\n"
      "v_sub_f32 v0, v0, v244\n"
      "v_sub_f32 v1, v1, v244\n"
      "v_sub_f32 v2, v2, v244\n"
      "v_sub_f32 v3, v3, v244\n"
      "v_sub_f32 v4, v4, v244\n"
      "v_sub_f32 v5, v5, v244\n"
      "v_sub_f32 v6, v6, v244\n"
      "v_sub_f32 v7, v7, v244\n"
      "v_sub_f32 v64, v64, v244\n"
      "v_mul_f32 v212, v212, v242\n"
      "v_sub_f32 v65, v65, v244\n"
      "v_mul_f32 v213, v213, v242\n"
      "v_sub_f32 v66, v66, v244\n"
      "v_mul_f32 v214, v214, v242\n"
      "v_sub_f32 v67, v67, v244\n"
      "v_mul_f32 v215, v215, v242\n"
      "v_accvgpr_read_b32 v243, a0\n"
      "s_nop 0\n"
      "v_mul_f32 v243, v243, v242\n"
      "s_nop 0\n"
      "v_accvgpr_write_b32 a0, v243\n"
      "v_accvgpr_read_b32 v243, a1\n"
      "s_nop 0\n"
      "v_mul_f32 v243, v243, v242\n"
      "s_nop 0\n"
      "v_accvgpr_write_b32 a1, v243\n"
      "v_accvgpr_read_b32 v243, a2\n"
      "s_nop 0\n"
      "v_mul_f32 v243, v243, v242\n"
      "s_nop 0\n"
      "v_accvgpr_write_b32 a2, v243\n"
      "v_accvgpr_read_b32 v243, a3\n"
      "s_nop 0\n"
      "v_mul_f32 v243, v243, v242\n"
      "s_nop 0\n"
      "v_accvgpr_write_b32 a3, v243\n"
      "v_accvgpr_read_b32 v243, a4\n"
      "s_nop 0\n"
      "v_mul_f32 v243, v243, v242\n"
      "s_nop 0\n"
      "v_accvgpr_write_b32 a4, v243\n"
      "v_accvgpr_read_b32 v243, a5\n"
      "s_nop 0\n"
      "v_mul_f32 v243, v243, v242\n"
      "s_nop 0\n"
      "v_accvgpr_write_b32 a5, v243\n"
      "v_accvgpr_read_b32 v243, a6\n"
      "s_nop 0\n"
      "v_mul_f32 v243, v243, v242\n"
      "s_nop 0\n"
      "v_accvgpr_write_b32 a6, v243\n"
      "v_accvgpr_read_b32 v243, a7\n"
      "s_nop 0\n"
      "v_mul_f32 v243, v243, v242\n"
      "s_nop 0\n"
      "v_accvgpr_write_b32 a7, v243\n"
      "v_accvgpr_read_b32 v243, a8\n"
      "s_nop 0\n"
      "v_mul_f32 v243, v243, v242\n"
      "s_nop 0\n"
      "v_accvgpr_write_b32 a8, v243\n"
      "v_accvgpr_read_b32 v243, a9\n"
      "s_nop 0\n"
      "v_mul_f32 v243, v243, v242\n"
      "s_nop 0\n"
      "v_accvgpr_write_b32 a9, v243\n"
      "v_accvgpr_read_b32 v243, a10\n"
      "s_nop 0\n"
      "v_mul_f32 v243, v243, v242\n"
      "s_nop 0\n"
      "v_accvgpr_write_b32 a10, v243\n"
      "v_accvgpr_read_b32 v243, a11\n"
      "s_nop 0\n"
      "v_mul_f32 v243, v243, v242\n"
      "s_nop 0\n"
      "v_accvgpr_write_b32 a11, v243\n"
      "v_accvgpr_read_b32 v243, a12\n"
      "s_nop 0\n"
      "v_mul_f32 v243, v243, v242\n"
      "s_nop 0\n"
      "v_accvgpr_write_b32 a12, v243\n"
      "v_accvgpr_read_b32 v243, a13\n"
      "s_nop 0\n"
      "v_mul_f32 v243, v243, v242\n"
      "s_nop 0\n"
      "v_accvgpr_write_b32 a13, v243\n"
      "v_accvgpr_read_b32 v243, a14\n"
      "s_nop 0\n"
      "v_mul_f32 v243, v243, v242\n"
      "s_nop 0\n"
      "v_accvgpr_write_b32 a14, v243\n"
      "v_accvgpr_read_b32 v243, a15\n"
      "s_nop 0\n"
      "v_mul_f32 v243, v243, v242\n"
      "s_nop 0\n"
      "v_accvgpr_write_b32 a15, v243\n"
      "v_accvgpr_read_b32 v243, a16\n"
      "s_nop 0\n"
      "v_mul_f32 v243, v243, v242\n"
      "s_nop 0\n"
      "v_accvgpr_write_b32 a16, v243\n"
      "v_accvgpr_read_b32 v243, a17\n"
      "s_nop 0\n"
      "v_mul_f32 v243, v243, v242\n"
      "s_nop 0\n"
      "v_accvgpr_write_b32 a17, v243\n"
      "v_accvgpr_read_b32 v243, a18\n"
      "s_nop 0\n"
      "v_mul_f32 v243, v243, v242\n"
      "s_nop 0\n"
      "v_accvgpr_write_b32 a18, v243\n"
      "v_accvgpr_read_b32 v243, a19\n"
      "s_nop 0\n"
      "v_mul_f32 v243, v243, v242\n"
      "s_nop 0\n"
      "v_accvgpr_write_b32 a19, v243\n"
      "v_accvgpr_read_b32 v243, a20\n"
      "s_nop 0\n"
      "v_mul_f32 v243, v243, v242\n"
      "s_nop 0\n"
      "v_accvgpr_write_b32 a20, v243\n"
      "v_accvgpr_read_b32 v243, a21\n"
      "s_nop 0\n"
      "v_mul_f32 v243, v243, v242\n"
      "s_nop 0\n"
      "v_accvgpr_write_b32 a21, v243\n"
      "v_accvgpr_read_b32 v243, a22\n"
      "s_nop 0\n"
      "v_mul_f32 v243, v243, v242\n"
      "s_nop 0\n"
      "v_accvgpr_write_b32 a22, v243\n"
      "v_accvgpr_read_b32 v243, a23\n"
      "s_nop 0\n"
      "v_mul_f32 v243, v243, v242\n"
      "s_nop 0\n"
      "v_accvgpr_write_b32 a23, v243\n"
      "v_accvgpr_read_b32 v243, a24\n"
      "s_nop 0\n"
      "v_mul_f32 v243, v243, v242\n"
      "s_nop 0\n"
      "v_accvgpr_write_b32 a24, v243\n"
      "v_accvgpr_read_b32 v243, a25\n"
      "s_nop 0\n"
      "v_mul_f32 v243, v243, v242\n"
      "s_nop 0\n"
      "v_accvgpr_write_b32 a25, v243\n"
      "v_accvgpr_read_b32 v243, a26\n"
      "s_nop 0\n"
      "v_mul_f32 v243, v243, v242\n"
      "s_nop 0\n"
      "v_accvgpr_write_b32 a26, v243\n"
      "v_accvgpr_read_b32 v243, a27\n"
      "s_nop 0\n"
      "v_mul_f32 v243, v243, v242\n"
      "s_nop 0\n"
      "v_accvgpr_write_b32 a27, v243\n"
      "v_accvgpr_read_b32 v243, a28\n"
      "s_nop 0\n"
      "v_mul_f32 v243, v243, v242\n"
      "s_nop 0\n"
      "v_accvgpr_write_b32 a28, v243\n"
      "v_accvgpr_read_b32 v243, a29\n"
      "s_nop 0\n"
      "v_mul_f32 v243, v243, v242\n"
      "s_nop 0\n"
      "v_accvgpr_write_b32 a29, v243\n"
      "v_accvgpr_read_b32 v243, a30\n"
      "s_nop 0\n"
      "v_mul_f32 v243, v243, v242\n"
      "s_nop 0\n"
      "v_accvgpr_write_b32 a30, v243\n"
      "v_accvgpr_read_b32 v243, a31\n"
      "s_nop 0\n"
      "v_mul_f32 v243, v243, v242\n"
      "s_nop 0\n"
      "v_accvgpr_write_b32 a31, v243\n"
      "v_max3_f32 v245, v8, v9, v10\n"
      "v_max3_f32 v242, v11, v12, v13\n"
      "v_max3_f32 v245, v245, v14, v15\n"
      "v_max_f32 v245, v245, v242\n"
      "v_mov_b32 v242, v245\n"
      "s_nop 1\n"
      "v_permlane16_swap_b32 v242, v245\n"
      "s_nop 0\n"
      "v_max_f32 v245, v245, v242\n"
      "v_mov_b32 v242, v245\n"
      "s_nop 1\n"
      "v_permlane32_swap_b32 v242, v245\n"
      "s_nop 0\n"
      "v_max_f32 v245, v245, v242\n"
      "v_max_f32 v245, 0, v245\n"
      "v_exp_f32 v242, -v245\n"
      "v_sub_f32 v8, v8, v245\n"
      "v_sub_f32 v9, v9, v245\n"
      "v_sub_f32 v10, v10, v245\n"
      "v_sub_f32 v11, v11, v245\n"
      "v_sub_f32 v12, v12, v245\n"
      "v_sub_f32 v13, v13, v245\n"
      "v_sub_f32 v14, v14, v245\n"
      "v_sub_f32 v15, v15, v245\n"
      "v_sub_f32 v68, v68, v245\n"
      "v_mul_f32 v216, v216, v242\n"
      "v_sub_f32 v69, v69, v245\n"
      "v_mul_f32 v217, v217, v242\n"
      "v_sub_f32 v70, v70, v245\n"
      "v_mul_f32 v218, v218, v242\n"
      "v_sub_f32 v71, v71, v245\n"
      "v_mul_f32 v219, v219, v242\n"
      "v_accvgpr_read_b32 v243, a32\n"
      "s_nop 0\n"
      "v_mul_f32 v243, v243, v242\n"
      "s_nop 0\n"
      "v_accvgpr_write_b32 a32, v243\n"
      "v_accvgpr_read_b32 v243, a33\n"
      "s_nop 0\n"
      "v_mul_f32 v243, v243, v242\n"
      "s_nop 0\n"
      "v_accvgpr_write_b32 a33, v243\n"
      "v_accvgpr_read_b32 v243, a34\n"
      "s_nop 0\n"
      "v_mul_f32 v243, v243, v242\n"
      "s_nop 0\n"
      "v_accvgpr_write_b32 a34, v243\n"
      "v_accvgpr_read_b32 v243, a35\n"
      "s_nop 0\n"
      "v_mul_f32 v243, v243, v242\n"
      "s_nop 0\n"
      "v_accvgpr_write_b32 a35, v243\n"
      "v_accvgpr_read_b32 v243, a36\n"
      "s_nop 0\n"
      "v_mul_f32 v243, v243, v242\n"
      "s_nop 0\n"
      "v_accvgpr_write_b32 a36, v243\n"
      "v_accvgpr_read_b32 v243, a37\n"
      "s_nop 0\n"
      "v_mul_f32 v243, v243, v242\n"
      "s_nop 0\n"
      "v_accvgpr_write_b32 a37, v243\n"
      "v_accvgpr_read_b32 v243, a38\n"
      "s_nop 0\n"
      "v_mul_f32 v243, v243, v242\n"
      "s_nop 0\n"
      "v_accvgpr_write_b32 a38, v243\n"
      "v_accvgpr_read_b32 v243, a39\n"
      "s_nop 0\n"
      "v_mul_f32 v243, v243, v242\n"
      "s_nop 0\n"
      "v_accvgpr_write_b32 a39, v243\n"
      "v_accvgpr_read_b32 v243, a40\n"
      "s_nop 0\n"
      "v_mul_f32 v243, v243, v242\n"
      "s_nop 0\n"
      "v_accvgpr_write_b32 a40, v243\n"
      "v_accvgpr_read_b32 v243, a41\n"
      "s_nop 0\n"
      "v_mul_f32 v243, v243, v242\n"
      "s_nop 0\n"
      "v_accvgpr_write_b32 a41, v243\n"
      "v_accvgpr_read_b32 v243, a42\n"
      "s_nop 0\n"
      "v_mul_f32 v243, v243, v242\n"
      "s_nop 0\n"
      "v_accvgpr_write_b32 a42, v243\n"
      "v_accvgpr_read_b32 v243, a43\n"
      "s_nop 0\n"
      "v_mul_f32 v243, v243, v242\n"
      "s_nop 0\n"
      "v_accvgpr_write_b32 a43, v243\n"
      "v_accvgpr_read_b32 v243, a44\n"
      "s_nop 0\n"
      "v_mul_f32 v243, v243, v242\n"
      "s_nop 0\n"
      "v_accvgpr_write_b32 a44, v243\n"
      "v_accvgpr_read_b32 v243, a45\n"
      "s_nop 0\n"
      "v_mul_f32 v243, v243, v242\n"
      "s_nop 0\n"
      "v_accvgpr_write_b32 a45, v243\n"
      "v_accvgpr_read_b32 v243, a46\n"
      "s_nop 0\n"
      "v_mul_f32 v243, v243, v242\n"
      "s_nop 0\n"
      "v_accvgpr_write_b32 a46, v243\n"
      "v_accvgpr_read_b32 v243, a47\n"
      "s_nop 0\n"
      "v_mul_f32 v243, v243, v242\n"
      "s_nop 0\n"
      "v_accvgpr_write_b32 a47, v243\n"
      "v_accvgpr_read_b32 v243, a48\n"
      "s_nop 0\n"
      "v_mul_f32 v243, v243, v242\n"
      "s_nop 0\n"
      "v_accvgpr_write_b32 a48, v243\n"
      "v_accvgpr_read_b32 v243, a49\n"
      "s_nop 0\n"
      "v_mul_f32 v243, v243, v242\n"
      "s_nop 0\n"
      "v_accvgpr_write_b32 a49, v243\n"
      "v_accvgpr_read_b32 v243, a50\n"
      "s_nop 0\n"
      "v_mul_f32 v243, v243, v242\n"
      "s_nop 0\n"
      "v_accvgpr_write_b32 a50, v243\n"
      "v_accvgpr_read_b32 v243, a51\n"
      "s_nop 0\n"
      "v_mul_f32 v243, v243, v242\n"
      "s_nop 0\n"
      "v_accvgpr_write_b32 a51, v243\n"
      "v_accvgpr_read_b32 v243, a52\n"
      "s_nop 0\n"
      "v_mul_f32 v243, v243, v242\n"
      "s_nop 0\n"
      "v_accvgpr_write_b32 a52, v243\n"
      "v_accvgpr_read_b32 v243, a53\n"
      "s_nop 0\n"
      "v_mul_f32 v243, v243, v242\n"
      "s_nop 0\n"
      "v_accvgpr_write_b32 a53, v243\n"
      "v_accvgpr_read_b32 v243, a54\n"
      "s_nop 0\n"
      "v_mul_f32 v243, v243, v242\n"
      "s_nop 0\n"
      "v_accvgpr_write_b32 a54, v243\n"
      "v_accvgpr_read_b32 v243, a55\n"
      "s_nop 0\n"
      "v_mul_f32 v243, v243, v242\n"
      "s_nop 0\n"
      "v_accvgpr_write_b32 a55, v243\n"
      "v_accvgpr_read_b32 v243, a56\n"
      "s_nop 0\n"
      "v_mul_f32 v243, v243, v242\n"
      "s_nop 0\n"
      "v_accvgpr_write_b32 a56, v243\n"
      "v_accvgpr_read_b32 v243, a57\n"
      "s_nop 0\n"
      "v_mul_f32 v243, v243, v242\n"
      "s_nop 0\n"
      "v_accvgpr_write_b32 a57, v243\n"
      "v_accvgpr_read_b32 v243, a58\n"
      "s_nop 0\n"
      "v_mul_f32 v243, v243, v242\n"
      "s_nop 0\n"
      "v_accvgpr_write_b32 a58, v243\n"
      "v_accvgpr_read_b32 v243, a59\n"
      "s_nop 0\n"
      "v_mul_f32 v243, v243, v242\n"
      "s_nop 0\n"
      "v_accvgpr_write_b32 a59, v243\n"
      "v_accvgpr_read_b32 v243, a60\n"
      "s_nop 0\n"
      "v_mul_f32 v243, v243, v242\n"
      "s_nop 0\n"
      "v_accvgpr_write_b32 a60, v243\n"
      "v_accvgpr_read_b32 v243, a61\n"
      "s_nop 0\n"
      "v_mul_f32 v243, v243, v242\n"
      "s_nop 0\n"
      "v_accvgpr_write_b32 a61, v243\n"
      "v_accvgpr_read_b32 v243, a62\n"
      "s_nop 0\n"
      "v_mul_f32 v243, v243, v242\n"
      "s_nop 0\n"
      "v_accvgpr_write_b32 a62, v243\n"
      "v_accvgpr_read_b32 v243, a63\n"
      "s_nop 0\n"
      "v_mul_f32 v243, v243, v242\n"
      "s_nop 0\n"
      "v_accvgpr_write_b32 a63, v243\n"
      "v_max3_f32 v246, v16, v17, v18\n"
      "v_max3_f32 v242, v19, v20, v21\n"
      "v_max3_f32 v246, v246, v22, v23\n"
      "v_max_f32 v246, v246, v242\n"
      "v_mov_b32 v242, v246\n"
      "s_nop 1\n"
      "v_permlane16_swap_b32 v242, v246\n"
      "s_nop 0\n"
      "v_max_f32 v246, v246, v242\n"
      "v_mov_b32 v242, v246\n"
      "s_nop 1\n"
      "v_permlane32_swap_b32 v242, v246\n"
      "s_nop 0\n"
      "v_max_f32 v246, v246, v242\n"
      "v_max_f32 v246, 0, v246\n"
      "v_exp_f32 v242, -v246\n"
      "v_sub_f32 v16, v16, v246\n"
      "v_sub_f32 v17, v17, v246\n"
      "v_sub_f32 v18, v18, v246\n"
      "v_sub_f32 v19, v19, v246\n"
      "v_sub_f32 v20, v20, v246\n"
      "v_sub_f32 v21, v21, v246\n"
      "v_sub_f32 v22, v22, v246\n"
      "v_sub_f32 v23, v23, v246\n"
      "v_sub_f32 v72, v72, v246\n"
      "v_mul_f32 v220, v220, v242\n"
      "v_sub_f32 v73, v73, v246\n"
      "v_mul_f32 v221, v221, v242\n"
      "v_sub_f32 v74, v74, v246\n"
      "v_mul_f32 v222, v222, v242\n"
      "v_sub_f32 v75, v75, v246\n"
      "v_mul_f32 v223, v223, v242\n"
      "v_accvgpr_read_b32 v243, a64\n"
      "s_nop 0\n"
      "v_mul_f32 v243, v243, v242\n"
      "s_nop 0\n"
      "v_accvgpr_write_b32 a64, v243\n"
      "v_accvgpr_read_b32 v243, a65\n"
      "s_nop 0\n"
      "v_mul_f32 v243, v243, v242\n"
      "s_nop 0\n"
      "v_accvgpr_write_b32 a65, v243\n"
      "v_accvgpr_read_b32 v243, a66\n"
      "s_nop 0\n"
      "v_mul_f32 v243, v243, v242\n"
      "s_nop 0\n"
      "v_accvgpr_write_b32 a66, v243\n"
      "v_accvgpr_read_b32 v243, a67\n"
      "s_nop 0\n"
      "v_mul_f32 v243, v243, v242\n"
      "s_nop 0\n"
      "v_accvgpr_write_b32 a67, v243\n"
      "v_accvgpr_read_b32 v243, a68\n"
      "s_nop 0\n"
      "v_mul_f32 v243, v243, v242\n"
      "s_nop 0\n"
      "v_accvgpr_write_b32 a68, v243\n"
      "v_accvgpr_read_b32 v243, a69\n"
      "s_nop 0\n"
      "v_mul_f32 v243, v243, v242\n"
      "s_nop 0\n"
      "v_accvgpr_write_b32 a69, v243\n"
      "v_accvgpr_read_b32 v243, a70\n"
      "s_nop 0\n"
      "v_mul_f32 v243, v243, v242\n"
      "s_nop 0\n"
      "v_accvgpr_write_b32 a70, v243\n"
      "v_accvgpr_read_b32 v243, a71\n"
      "s_nop 0\n"
      "v_mul_f32 v243, v243, v242\n"
      "s_nop 0\n"
      "v_accvgpr_write_b32 a71, v243\n"
      "v_accvgpr_read_b32 v243, a72\n"
      "s_nop 0\n"
      "v_mul_f32 v243, v243, v242\n"
      "s_nop 0\n"
      "v_accvgpr_write_b32 a72, v243\n"
      "v_accvgpr_read_b32 v243, a73\n"
      "s_nop 0\n"
      "v_mul_f32 v243, v243, v242\n"
      "s_nop 0\n"
      "v_accvgpr_write_b32 a73, v243\n"
      "v_accvgpr_read_b32 v243, a74\n"
      "s_nop 0\n"
      "v_mul_f32 v243, v243, v242\n"
      "s_nop 0\n"
      "v_accvgpr_write_b32 a74, v243\n"
      "v_accvgpr_read_b32 v243, a75\n"
      "s_nop 0\n"
      "v_mul_f32 v243, v243, v242\n"
      "s_nop 0\n"
      "v_accvgpr_write_b32 a75, v243\n"
      "v_accvgpr_read_b32 v243, a76\n"
      "s_nop 0\n"
      "v_mul_f32 v243, v243, v242\n"
      "s_nop 0\n"
      "v_accvgpr_write_b32 a76, v243\n"
      "v_accvgpr_read_b32 v243, a77\n"
      "s_nop 0\n"
      "v_mul_f32 v243, v243, v242\n"
      "s_nop 0\n"
      "v_accvgpr_write_b32 a77, v243\n"
      "v_accvgpr_read_b32 v243, a78\n"
      "s_nop 0\n"
      "v_mul_f32 v243, v243, v242\n"
      "s_nop 0\n"
      "v_accvgpr_write_b32 a78, v243\n"
      "v_accvgpr_read_b32 v243, a79\n"
      "s_nop 0\n"
      "v_mul_f32 v243, v243, v242\n"
      "s_nop 0\n"
      "v_accvgpr_write_b32 a79, v243\n"
      "v_accvgpr_read_b32 v243, a80\n"
      "s_nop 0\n"
      "v_mul_f32 v243, v243, v242\n"
      "s_nop 0\n"
      "v_accvgpr_write_b32 a80, v243\n"
      "v_accvgpr_read_b32 v243, a81\n"
      "s_nop 0\n"
      "v_mul_f32 v243, v243, v242\n"
      "s_nop 0\n"
      "v_accvgpr_write_b32 a81, v243\n"
      "v_accvgpr_read_b32 v243, a82\n"
      "s_nop 0\n"
      "v_mul_f32 v243, v243, v242\n"
      "s_nop 0\n"
      "v_accvgpr_write_b32 a82, v243\n"
      "v_accvgpr_read_b32 v243, a83\n"
      "s_nop 0\n"
      "v_mul_f32 v243, v243, v242\n"
      "s_nop 0\n"
      "v_accvgpr_write_b32 a83, v243\n"
      "v_accvgpr_read_b32 v243, a84\n"
      "s_nop 0\n"
      "v_mul_f32 v243, v243, v242\n"
      "s_nop 0\n"
      "v_accvgpr_write_b32 a84, v243\n"
      "v_accvgpr_read_b32 v243, a85\n"
      "s_nop 0\n"
      "v_mul_f32 v243, v243, v242\n"
      "s_nop 0\n"
      "v_accvgpr_write_b32 a85, v243\n"
      "v_accvgpr_read_b32 v243, a86\n"
      "s_nop 0\n"
      "v_mul_f32 v243, v243, v242\n"
      "s_nop 0\n"
      "v_accvgpr_write_b32 a86, v243\n"
      "v_accvgpr_read_b32 v243, a87\n"
      "s_nop 0\n"
      "v_mul_f32 v243, v243, v242\n"
      "s_nop 0\n"
      "v_accvgpr_write_b32 a87, v243\n"
      "v_accvgpr_read_b32 v243, a88\n"
      "s_nop 0\n"
      "v_mul_f32 v243, v243, v242\n"
      "s_nop 0\n"
      "v_accvgpr_write_b32 a88, v243\n"
      "v_accvgpr_read_b32 v243, a89\n"
      "s_nop 0\n"
      "v_mul_f32 v243, v243, v242\n"
      "s_nop 0\n"
      "v_accvgpr_write_b32 a89, v243\n"
      "v_accvgpr_read_b32 v243, a90\n"
      "s_nop 0\n"
      "v_mul_f32 v243, v243, v242\n"
      "s_nop 0\n"
      "v_accvgpr_write_b32 a90, v243\n"
      "v_accvgpr_read_b32 v243, a91\n"
      "s_nop 0\n"
      "v_mul_f32 v243, v243, v242\n"
      "s_nop 0\n"
      "v_accvgpr_write_b32 a91, v243\n"
      "v_accvgpr_read_b32 v243, a92\n"
      "s_nop 0\n"
      "v_mul_f32 v243, v243, v242\n"
      "s_nop 0\n"
      "v_accvgpr_write_b32 a92, v243\n"
      "v_accvgpr_read_b32 v243, a93\n"
      "s_nop 0\n"
      "v_mul_f32 v243, v243, v242\n"
      "s_nop 0\n"
      "v_accvgpr_write_b32 a93, v243\n"
      "v_accvgpr_read_b32 v243, a94\n"
      "s_nop 0\n"
      "v_mul_f32 v243, v243, v242\n"
      "s_nop 0\n"
      "v_accvgpr_write_b32 a94, v243\n"
      "v_accvgpr_read_b32 v243, a95\n"
      "s_nop 0\n"
      "v_mul_f32 v243, v243, v242\n"
      "s_nop 0\n"
      "v_accvgpr_write_b32 a95, v243\n"
      "v_max3_f32 v247, v24, v25, v26\n"
      "v_max3_f32 v242, v27, v28, v29\n"
      "v_max3_f32 v247, v247, v30, v31\n"
      "v_max_f32 v247, v247, v242\n"
      "v_mov_b32 v242, v247\n"
      "s_nop 1\n"
      "v_permlane16_swap_b32 v242, v247\n"
      "s_nop 0\n"
      "v_max_f32 v247, v247, v242\n"
      "v_mov_b32 v242, v247\n"
      "s_nop 1\n"
      "v_permlane32_swap_b32 v242, v247\n"
      "s_nop 0\n"
      "v_max_f32 v247, v247, v242\n"
      "v_max_f32 v247, 0, v247\n"
      "v_exp_f32 v242, -v247\n"
      "v_sub_f32 v24, v24, v247\n"
      "v_sub_f32 v25, v25, v247\n"
      "v_sub_f32 v26, v26, v247\n"
      "v_sub_f32 v27, v27, v247\n"
      "v_sub_f32 v28, v28, v247\n"
      "v_sub_f32 v29, v29, v247\n"
      "v_sub_f32 v30, v30, v247\n"
      "v_sub_f32 v31, v31, v247\n"
      "v_sub_f32 v76, v76, v247\n"
      "v_mul_f32 v224, v224, v242\n"
      "v_sub_f32 v77, v77, v247\n"
      "v_mul_f32 v225, v225, v242\n"
      "v_sub_f32 v78, v78, v247\n"
      "v_mul_f32 v226, v226, v242\n"
      "v_sub_f32 v79, v79, v247\n"
      "v_mul_f32 v227, v227, v242\n"
      "v_accvgpr_read_b32 v243, a96\n"
      "s_nop 0\n"
      "v_mul_f32 v243, v243, v242\n"
      "s_nop 0\n"
      "v_accvgpr_write_b32 a96, v243\n"
      "v_accvgpr_read_b32 v243, a97\n"
      "s_nop 0\n"
      "v_mul_f32 v243, v243, v242\n"
      "s_nop 0\n"
      "v_accvgpr_write_b32 a97, v243\n"
      "v_accvgpr_read_b32 v243, a98\n"
      "s_nop 0\n"
      "v_mul_f32 v243, v243, v242\n"
      "s_nop 0\n"
      "v_accvgpr_write_b32 a98, v243\n"
      "v_accvgpr_read_b32 v243, a99\n"
      "s_nop 0\n"
      "v_mul_f32 v243, v243, v242\n"
      "s_nop 0\n"
      "v_accvgpr_write_b32 a99, v243\n"
      "v_accvgpr_read_b32 v243, a100\n"
      "s_nop 0\n"
      "v_mul_f32 v243, v243, v242\n"
      "s_nop 0\n"
      "v_accvgpr_write_b32 a100, v243\n"
      "v_accvgpr_read_b32 v243, a101\n"
      "s_nop 0\n"
      "v_mul_f32 v243, v243, v242\n"
      "s_nop 0\n"
      "v_accvgpr_write_b32 a101, v243\n"
      "v_accvgpr_read_b32 v243, a102\n"
      "s_nop 0\n"
      "v_mul_f32 v243, v243, v242\n"
      "s_nop 0\n"
      "v_accvgpr_write_b32 a102, v243\n"
      "v_accvgpr_read_b32 v243, a103\n"
      "s_nop 0\n"
      "v_mul_f32 v243, v243, v242\n"
      "s_nop 0\n"
      "v_accvgpr_write_b32 a103, v243\n"
      "v_accvgpr_read_b32 v243, a104\n"
      "s_nop 0\n"
      "v_mul_f32 v243, v243, v242\n"
      "s_nop 0\n"
      "v_accvgpr_write_b32 a104, v243\n"
      "v_accvgpr_read_b32 v243, a105\n"
      "s_nop 0\n"
      "v_mul_f32 v243, v243, v242\n"
      "s_nop 0\n"
      "v_accvgpr_write_b32 a105, v243\n"
      "v_accvgpr_read_b32 v243, a106\n"
      "s_nop 0\n"
      "v_mul_f32 v243, v243, v242\n"
      "s_nop 0\n"
      "v_accvgpr_write_b32 a106, v243\n"
      "v_accvgpr_read_b32 v243, a107\n"
      "s_nop 0\n"
      "v_mul_f32 v243, v243, v242\n"
      "s_nop 0\n"
      "v_accvgpr_write_b32 a107, v243\n"
      "v_accvgpr_read_b32 v243, a108\n"
      "s_nop 0\n"
      "v_mul_f32 v243, v243, v242\n"
      "s_nop 0\n"
      "v_accvgpr_write_b32 a108, v243\n"
      "v_accvgpr_read_b32 v243, a109\n"
      "s_nop 0\n"
      "v_mul_f32 v243, v243, v242\n"
      "s_nop 0\n"
      "v_accvgpr_write_b32 a109, v243\n"
      "v_accvgpr_read_b32 v243, a110\n"
      "s_nop 0\n"
      "v_mul_f32 v243, v243, v242\n"
      "s_nop 0\n"
      "v_accvgpr_write_b32 a110, v243\n"
      "v_accvgpr_read_b32 v243, a111\n"
      "s_nop 0\n"
      "v_mul_f32 v243, v243, v242\n"
      "s_nop 0\n"
      "v_accvgpr_write_b32 a111, v243\n"
      "v_accvgpr_read_b32 v243, a112\n"
      "s_nop 0\n"
      "v_mul_f32 v243, v243, v242\n"
      "s_nop 0\n"
      "v_accvgpr_write_b32 a112, v243\n"
      "v_accvgpr_read_b32 v243, a113\n"
      "s_nop 0\n"
      "v_mul_f32 v243, v243, v242\n"
      "s_nop 0\n"
      "v_accvgpr_write_b32 a113, v243\n"
      "v_accvgpr_read_b32 v243, a114\n"
      "s_nop 0\n"
      "v_mul_f32 v243, v243, v242\n"
      "s_nop 0\n"
      "v_accvgpr_write_b32 a114, v243\n"
      "v_accvgpr_read_b32 v243, a115\n"
      "s_nop 0\n"
      "v_mul_f32 v243, v243, v242\n"
      "s_nop 0\n"
      "v_accvgpr_write_b32 a115, v243\n"
      "v_accvgpr_read_b32 v243, a116\n"
      "s_nop 0\n"
      "v_mul_f32 v243, v243, v242\n"
      "s_nop 0\n"
      "v_accvgpr_write_b32 a116, v243\n"
      "v_accvgpr_read_b32 v243, a117\n"
      "s_nop 0\n"
      "v_mul_f32 v243, v243, v242\n"
      "s_nop 0\n"
      "v_accvgpr_write_b32 a117, v243\n"
      "v_accvgpr_read_b32 v243, a118\n"
      "s_nop 0\n"
      "v_mul_f32 v243, v243, v242\n"
      "s_nop 0\n"
      "v_accvgpr_write_b32 a118, v243\n"
      "v_accvgpr_read_b32 v243, a119\n"
      "s_nop 0\n"
      "v_mul_f32 v243, v243, v242\n"
      "s_nop 0\n"
      "v_accvgpr_write_b32 a119, v243\n"
      "v_accvgpr_read_b32 v243, a120\n"
      "s_nop 0\n"
      "v_mul_f32 v243, v243, v242\n"
      "s_nop 0\n"
      "v_accvgpr_write_b32 a120, v243\n"
      "v_accvgpr_read_b32 v243, a121\n"
      "s_nop 0\n"
      "v_mul_f32 v243, v243, v242\n"
      "s_nop 0\n"
      "v_accvgpr_write_b32 a121, v243\n"
      "v_accvgpr_read_b32 v243, a122\n"
      "s_nop 0\n"
      "v_mul_f32 v243, v243, v242\n"
      "s_nop 0\n"
      "v_accvgpr_write_b32 a122, v243\n"
      "v_accvgpr_read_b32 v243, a123\n"
      "s_nop 0\n"
      "v_mul_f32 v243, v243, v242\n"
      "s_nop 0\n"
      "v_accvgpr_write_b32 a123, v243\n"
      "v_accvgpr_read_b32 v243, a124\n"
      "s_nop 0\n"
      "v_mul_f32 v243, v243, v242\n"
      "s_nop 0\n"
      "v_accvgpr_write_b32 a124, v243\n"
      "v_accvgpr_read_b32 v243, a125\n"
      "s_nop 0\n"
      "v_mul_f32 v243, v243, v242\n"
      "s_nop 0\n"
      "v_accvgpr_write_b32 a125, v243\n"
      "v_accvgpr_read_b32 v243, a126\n"
      "s_nop 0\n"
      "v_mul_f32 v243, v243, v242\n"
      "s_nop 0\n"
      "v_accvgpr_write_b32 a126, v243\n"
      "v_accvgpr_read_b32 v243, a127\n"
      "s_nop 0\n"
      "v_mul_f32 v243, v243, v242\n"
      "s_nop 0\n"
      "v_accvgpr_write_b32 a127, v243\n"
      "v_exp_f32 v232, v0\n"
      "s_nop 0\n"
      "v_exp_f32 v233, v1\n"
      "s_nop 0\n"
      "v_cvt_pk_bf16_f32 v96, v232, v233\n"
      "s_nop 0\n"
      "v_exp_f32 v234, v2\n"
      "s_nop 0\n"
      "v_exp_f32 v235, v3\n"
      "s_nop 0\n"
      "v_cvt_pk_bf16_f32 v97, v234, v235\n"
      "s_nop 0\n"
      "v_exp_f32 v236, v4\n"
      "s_nop 0\n"
      "v_exp_f32 v237, v5\n"
      "s_nop 0\n"
      "v_cvt_pk_bf16_f32 v98, v236, v237\n"
      "s_nop 0\n"
      "v_exp_f32 v238, v6\n"
      "s_nop 0\n"
      "v_exp_f32 v239, v7\n"
      "s_nop 0\n"
      "v_cvt_pk_bf16_f32 v99, v238, v239\n"
      "s_nop 0\n"
      "v_exp_f32 v232, v8\n"
      "s_nop 0\n"
      "v_exp_f32 v233, v9\n"
      "s_nop 0\n"
      "v_cvt_pk_bf16_f32 v100, v232, v233\n"
      "s_nop 0\n"
      "v_exp_f32 v234, v10\n"
      "s_nop 0\n"
      "v_exp_f32 v235, v11\n"
      "s_nop 0\n"
      "v_cvt_pk_bf16_f32 v101, v234, v235\n"
      "s_nop 0\n"
      "v_exp_f32 v236, v12\n"
      "s_nop 0\n"
      "v_exp_f32 v237, v13\n"
      "s_nop 0\n"
      "v_cvt_pk_bf16_f32 v102, v236, v237\n"
      "s_nop 0\n"
      "v_exp_f32 v238, v14\n"
      "s_nop 0\n"
      "v_exp_f32 v239, v15\n"
      "s_nop 0\n"
      "v_cvt_pk_bf16_f32 v103, v238, v239\n"
      "s_nop 0\n"
      "v_exp_f32 v232, v16\n"
      "s_nop 0\n"
      "v_exp_f32 v233, v17\n"
      "s_nop 0\n"
      "v_cvt_pk_bf16_f32 v104, v232, v233\n"
      "s_nop 0\n"
      "v_exp_f32 v234, v18\n"
      "s_nop 0\n"
      "v_exp_f32 v235, v19\n"
      "s_nop 0\n"
      "v_cvt_pk_bf16_f32 v105, v234, v235\n"
      "s_nop 0\n"
      "v_exp_f32 v236, v20\n"
      "s_nop 0\n"
      "v_exp_f32 v237, v21\n"
      "s_nop 0\n"
      "v_cvt_pk_bf16_f32 v106, v236, v237\n"
      "s_nop 0\n"
      "v_exp_f32 v238, v22\n"
      "s_nop 0\n"
      "v_exp_f32 v239, v23\n"
      "s_nop 0\n"
      "v_cvt_pk_bf16_f32 v107, v238, v239\n"
      "s_nop 0\n"
      "v_exp_f32 v232, v24\n"
      "s_nop 0\n"
      "v_exp_f32 v233, v25\n"
      "s_nop 0\n"
      "v_cvt_pk_bf16_f32 v108, v232, v233\n"
      "s_nop 0\n"
      "v_exp_f32 v234, v26\n"
      "s_nop 0\n"
      "v_exp_f32 v235, v27\n"
      "s_nop 0\n"
      "v_cvt_pk_bf16_f32 v109, v234, v235\n"
      "s_nop 0\n"
      "v_exp_f32 v236, v28\n"
      "s_nop 0\n"
      "v_exp_f32 v237, v29\n"
      "s_nop 0\n"
      "v_cvt_pk_bf16_f32 v110, v236, v237\n"
      "s_nop 0\n"
      "v_exp_f32 v238, v30\n"
      "s_nop 0\n"
      "v_exp_f32 v239, v31\n"
      "s_nop 0\n"
      "v_cvt_pk_bf16_f32 v111, v238, v239\n"
      "s_nop 0\n"
      "s_nop 1\n"
      "v_mfma_f32_16x16x32_bf16 v[80:83], v[228:231], v[96:99], v[212:215]\n"
      "v_mfma_f32_16x16x32_bf16 v[84:87], v[228:231], v[100:103], v[216:219]\n"
      "v_mfma_f32_16x16x32_bf16 v[88:91], v[228:231], v[104:107], v[220:223]\n"
      "v_mfma_f32_16x16x32_bf16 v[92:95], v[228:231], v[108:111], v[224:227]\n"
      "s_cmp_lg_u32 s75, 0\n"
      "s_cbranch_scc1 SLX0_%=\n"
      "v_sub_f32 v32, v32, v244\n"
      "v_sub_f32 v33, v33, v244\n"
      "v_sub_f32 v34, v34, v244\n"
      "v_sub_f32 v35, v35, v244\n"
      "v_sub_f32 v36, v36, v244\n"
      "v_sub_f32 v37, v37, v244\n"
      "v_sub_f32 v38, v38, v244\n"
      "v_sub_f32 v39, v39, v244\n"
      "v_sub_f32 v40, v40, v245\n"
      "v_sub_f32 v41, v41, v245\n"
      "v_sub_f32 v42, v42, v245\n"
      "v_sub_f32 v43, v43, v245\n"
      "v_sub_f32 v44, v44, v245\n"
      "v_sub_f32 v45, v45, v245\n"
      "v_sub_f32 v46, v46, v245\n"
      "v_sub_f32 v47, v47, v245\n"
      "v_sub_f32 v48, v48, v246\n"
      "v_sub_f32 v49, v49, v246\n"
      "v_sub_f32 v50, v50, v246\n"
      "v_sub_f32 v51, v51, v246\n"
      "v_sub_f32 v52, v52, v246\n"
      "v_sub_f32 v53, v53, v246\n"
      "v_sub_f32 v54, v54, v246\n"
      "v_sub_f32 v55, v55, v246\n"
      "v_sub_f32 v56, v56, v247\n"
      "v_sub_f32 v57, v57, v247\n"
      "v_sub_f32 v58, v58, v247\n"
      "v_sub_f32 v59, v59, v247\n"
      "v_sub_f32 v60, v60, v247\n"
      "v_sub_f32 v61, v61, v247\n"
      "v_sub_f32 v62, v62, v247\n"
      "v_sub_f32 v63, v63, v247\n"
      "v_exp_f32 v232, v32\n"
      "s_nop 0\n"
      "v_exp_f32 v233, v33\n"
      "s_nop 0\n"
      "v_cvt_pk_bf16_f32 v112, v232, v233\n"
      "s_nop 0\n"
      "v_exp_f32 v234, v34\n"
      "s_nop 0\n"
      "v_exp_f32 v235, v35\n"
      "s_nop 0\n"
      "v_cvt_pk_bf16_f32 v113, v234, v235\n"
      "s_nop 0\n"
      "v_exp_f32 v236, v36\n"
      "s_nop 0\n"
      "v_exp_f32 v237, v37\n"
      "s_nop 0\n"
      "v_cvt_pk_bf16_f32 v114, v236, v237\n"
      "s_nop 0\n"
      "v_exp_f32 v238, v38\n"
      "s_nop 0\n"
      "v_exp_f32 v239, v39\n"
      "s_nop 0\n"
      "v_cvt_pk_bf16_f32 v115, v238, v239\n"
      "s_nop 0\n"
      "v_exp_f32 v232, v40\n"
      "s_nop 0\n"
      "v_exp_f32 v233, v41\n"
      "s_nop 0\n"
      "v_cvt_pk_bf16_f32 v116, v232, v233\n"
      "s_nop 0\n"
      "SLX0_%=:\n"
      "s_nop 7\n"
      "s_setpc_b64 s[76:77]\n"
      "SLOW1_%=:\n"
      "s_nop 15\n"
      "v_max3_f32 v244, v32, v33, v34\n"
      "v_max3_f32 v242, v35, v36, v37\n"
      "v_max3_f32 v244, v244, v38, v39\n"
      "v_max_f32 v244, v244, v242\n"
      "v_mov_b32 v242, v244\n"
      "s_nop 1\n"
      "v_permlane16_swap_b32 v242, v244\n"
      "s_nop 0\n"
      "v_max_f32 v244, v244, v242\n"
      "v_mov_b32 v242, v244\n"
      "s_nop 1\n"
      "v_permlane32_swap_b32 v242, v244\n"
      "s_nop 0\n"
      "v_max_f32 v244, v244, v242\n"
      "v_max_f32 v244, 0, v244\n"
      "v_exp_f32 v242, -v244\n"
      "v_sub_f32 v32, v32, v244\n"
      "v_sub_f32 v33, v33, v244\n"
      "v_sub_f32 v34, v34, v244\n"
      "v_sub_f32 v35, v35, v244\n"
      "v_sub_f32 v36, v36, v244\n"
      "v_sub_f32 v37, v37, v244\n"
      "v_sub_f32 v38, v38, v244\n"
      "v_sub_f32 v39, v39, v244\n"
      "v_sub_f32 v64, v64, v244\n"
      "v_mul_f32 v80, v80, v242\n"
      "v_sub_f32 v65, v65, v244\n"
      "v_mul_f32 v81, v81, v242\n"
      "v_sub_f32 v66, v66, v244\n"
      "v_mul_f32 v82, v82, v242\n"
      "v_sub_f32 v67, v67, v244\n"
      "v_mul_f32 v83, v83, v242\n"
      "v_accvgpr_read_b32 v243, a0\n"
      "s_nop 0\n"
      "v_mul_f32 v243, v243, v242\n"
      "s_nop 0\n"
      "v_accvgpr_write_b32 a0, v243\n"
      "v_accvgpr_read_b32 v243, a1\n"
      "s_nop 0\n"
      "v_mul_f32 v243, v243, v242\n"
      "s_nop 0\n"
      "v_accvgpr_write_b32 a1, v243\n"
      "v_accvgpr_read_b32 v243, a2\n"
      "s_nop 0\n"
      "v_mul_f32 v243, v243, v242\n"
      "s_nop 0\n"
      "v_accvgpr_write_b32 a2, v243\n"
      "v_accvgpr_read_b32 v243, a3\n"
      "s_nop 0\n"
      "v_mul_f32 v243, v243, v242\n"
      "s_nop 0\n"
      "v_accvgpr_write_b32 a3, v243\n"
      "v_accvgpr_read_b32 v243, a4\n"
      "s_nop 0\n"
      "v_mul_f32 v243, v243, v242\n"
      "s_nop 0\n"
      "v_accvgpr_write_b32 a4, v243\n"
      "v_accvgpr_read_b32 v243, a5\n"
      "s_nop 0\n"
      "v_mul_f32 v243, v243, v242\n"
      "s_nop 0\n"
      "v_accvgpr_write_b32 a5, v243\n"
      "v_accvgpr_read_b32 v243, a6\n"
      "s_nop 0\n"
      "v_mul_f32 v243, v243, v242\n"
      "s_nop 0\n"
      "v_accvgpr_write_b32 a6, v243\n"
      "v_accvgpr_read_b32 v243, a7\n"
      "s_nop 0\n"
      "v_mul_f32 v243, v243, v242\n"
      "s_nop 0\n"
      "v_accvgpr_write_b32 a7, v243\n"
      "v_accvgpr_read_b32 v243, a8\n"
      "s_nop 0\n"
      "v_mul_f32 v243, v243, v242\n"
      "s_nop 0\n"
      "v_accvgpr_write_b32 a8, v243\n"
      "v_accvgpr_read_b32 v243, a9\n"
      "s_nop 0\n"
      "v_mul_f32 v243, v243, v242\n"
      "s_nop 0\n"
      "v_accvgpr_write_b32 a9, v243\n"
      "v_accvgpr_read_b32 v243, a10\n"
      "s_nop 0\n"
      "v_mul_f32 v243, v243, v242\n"
      "s_nop 0\n"
      "v_accvgpr_write_b32 a10, v243\n"
      "v_accvgpr_read_b32 v243, a11\n"
      "s_nop 0\n"
      "v_mul_f32 v243, v243, v242\n"
      "s_nop 0\n"
      "v_accvgpr_write_b32 a11, v243\n"
      "v_accvgpr_read_b32 v243, a12\n"
      "s_nop 0\n"
      "v_mul_f32 v243, v243, v242\n"
      "s_nop 0\n"
      "v_accvgpr_write_b32 a12, v243\n"
      "v_accvgpr_read_b32 v243, a13\n"
      "s_nop 0\n"
      "v_mul_f32 v243, v243, v242\n"
      "s_nop 0\n"
      "v_accvgpr_write_b32 a13, v243\n"
      "v_accvgpr_read_b32 v243, a14\n"
      "s_nop 0\n"
      "v_mul_f32 v243, v243, v242\n"
      "s_nop 0\n"
      "v_accvgpr_write_b32 a14, v243\n"
      "v_accvgpr_read_b32 v243, a15\n"
      "s_nop 0\n"
      "v_mul_f32 v243, v243, v242\n"
      "s_nop 0\n"
      "v_accvgpr_write_b32 a15, v243\n"
      "v_accvgpr_read_b32 v243, a16\n"
      "s_nop 0\n"
      "v_mul_f32 v243, v243, v242\n"
      "s_nop 0\n"
      "v_accvgpr_write_b32 a16, v243\n"
      "v_accvgpr_read_b32 v243, a17\n"
      "s_nop 0\n"
      "v_mul_f32 v243, v243, v242\n"
      "s_nop 0\n"
      "v_accvgpr_write_b32 a17, v243\n"
      "v_accvgpr_read_b32 v243, a18\n"
      "s_nop 0\n"
      "v_mul_f32 v243, v243, v242\n"
      "s_nop 0\n"
      "v_accvgpr_write_b32 a18, v243\n"
      "v_accvgpr_read_b32 v243, a19\n"
      "s_nop 0\n"
      "v_mul_f32 v243, v243, v242\n"
      "s_nop 0\n"
      "v_accvgpr_write_b32 a19, v243\n"
      "v_accvgpr_read_b32 v243, a20\n"
      "s_nop 0\n"
      "v_mul_f32 v243, v243, v242\n"
      "s_nop 0\n"
      "v_accvgpr_write_b32 a20, v243\n"
      "v_accvgpr_read_b32 v243, a21\n"
      "s_nop 0\n"
      "v_mul_f32 v243, v243, v242\n"
      "s_nop 0\n"
      "v_accvgpr_write_b32 a21, v243\n"
      "v_accvgpr_read_b32 v243, a22\n"
      "s_nop 0\n"
      "v_mul_f32 v243, v243, v242\n"
      "s_nop 0\n"
      "v_accvgpr_write_b32 a22, v243\n"
      "v_accvgpr_read_b32 v243, a23\n"
      "s_nop 0\n"
      "v_mul_f32 v243, v243, v242\n"
      "s_nop 0\n"
      "v_accvgpr_write_b32 a23, v243\n"
      "v_accvgpr_read_b32 v243, a24\n"
      "s_nop 0\n"
      "v_mul_f32 v243, v243, v242\n"
      "s_nop 0\n"
      "v_accvgpr_write_b32 a24, v243\n"
      "v_accvgpr_read_b32 v243, a25\n"
      "s_nop 0\n"
      "v_mul_f32 v243, v243, v242\n"
      "s_nop 0\n"
      "v_accvgpr_write_b32 a25, v243\n"
      "v_accvgpr_read_b32 v243, a26\n"
      "s_nop 0\n"
      "v_mul_f32 v243, v243, v242\n"
      "s_nop 0\n"
      "v_accvgpr_write_b32 a26, v243\n"
      "v_accvgpr_read_b32 v243, a27\n"
      "s_nop 0\n"
      "v_mul_f32 v243, v243, v242\n"
      "s_nop 0\n"
      "v_accvgpr_write_b32 a27, v243\n"
      "v_accvgpr_read_b32 v243, a28\n"
      "s_nop 0\n"
      "v_mul_f32 v243, v243, v242\n"
      "s_nop 0\n"
      "v_accvgpr_write_b32 a28, v243\n"
      "v_accvgpr_read_b32 v243, a29\n"
      "s_nop 0\n"
      "v_mul_f32 v243, v243, v242\n"
      "s_nop 0\n"
      "v_accvgpr_write_b32 a29, v243\n"
      "v_accvgpr_read_b32 v243, a30\n"
      "s_nop 0\n"
      "v_mul_f32 v243, v243, v242\n"
      "s_nop 0\n"
      "v_accvgpr_write_b32 a30, v243\n"
      "v_accvgpr_read_b32 v243, a31\n"
      "s_nop 0\n"
      "v_mul_f32 v243, v243, v242\n"
      "s_nop 0\n"
      "v_accvgpr_write_b32 a31, v243\n"
      "v_max3_f32 v245, v40, v41, v42\n"
      "v_max3_f32 v242, v43, v44, v45\n"
      "v_max3_f32 v245, v245, v46, v47\n"
      "v_max_f32 v245, v245, v242\n"
      "v_mov_b32 v242, v245\n"
      "s_nop 1\n"
      "v_permlane16_swap_b32 v242, v245\n"
      "s_nop 0\n"
      "v_max_f32 v245, v245, v242\n"
      "v_mov_b32 v242, v245\n"
      "s_nop 1\n"
      "v_permlane32_swap_b32 v242, v245\n"
      "s_nop 0\n"
      "v_max_f32 v245, v245, v242\n"
      "v_max_f32 v245, 0, v245\n"
      "v_exp_f32 v242, -v245\n"
      "v_sub_f32 v40, v40, v245\n"
      "v_sub_f32 v41, v41, v245\n"
      "v_sub_f32 v42, v42, v245\n"
      "v_sub_f32 v43, v43, v245\n"
      "v_sub_f32 v44, v44, v245\n"
      "v_sub_f32 v45, v45, v245\n"
      "v_sub_f32 v46, v46, v245\n"
      "v_sub_f32 v47, v47, v245\n"
      "v_sub_f32 v68, v68, v245\n"
      "v_mul_f32 v84, v84, v242\n"
      "v_sub_f32 v69, v69, v245\n"
      "v_mul_f32 v85, v85, v242\n"
      "v_sub_f32 v70, v70, v245\n"
      "v_mul_f32 v86, v86, v242\n"
      "v_sub_f32 v71, v71, v245\n"
      "v_mul_f32 v87, v87, v242\n"
      "v_accvgpr_read_b32 v243, a32\n"
      "s_nop 0\n"
      "v_mul_f32 v243, v243, v242\n"
      "s_nop 0\n"
      "v_accvgpr_write_b32 a32, v243\n"
      "v_accvgpr_read_b32 v243, a33\n"
      "s_nop 0\n"
      "v_mul_f32 v243, v243, v242\n"
      "s_nop 0\n"
      "v_accvgpr_write_b32 a33, v243\n"
      "v_accvgpr_read_b32 v243, a34\n"
      "s_nop 0\n"
      "v_mul_f32 v243, v243, v242\n"
      "s_nop 0\n"
      "v_accvgpr_write_b32 a34, v243\n"
      "v_accvgpr_read_b32 v243, a35\n"
      "s_nop 0\n"
      "v_mul_f32 v243, v243, v242\n"
      "s_nop 0\n"
      "v_accvgpr_write_b32 a35, v243\n"
      "v_accvgpr_read_b32 v243, a36\n"
      "s_nop 0\n"
      "v_mul_f32 v243, v243, v242\n"
      "s_nop 0\n"
      "v_accvgpr_write_b32 a36, v243\n"
      "v_accvgpr_read_b32 v243, a37\n"
      "s_nop 0\n"
      "v_mul_f32 v243, v243, v242\n"
      "s_nop 0\n"
      "v_accvgpr_write_b32 a37, v243\n"
      "v_accvgpr_read_b32 v243, a38\n"
      "s_nop 0\n"
      "v_mul_f32 v243, v243, v242\n"
      "s_nop 0\n"
      "v_accvgpr_write_b32 a38, v243\n"
      "v_accvgpr_read_b32 v243, a39\n"
      "s_nop 0\n"
      "v_mul_f32 v243, v243, v242\n"
      "s_nop 0\n"
      "v_accvgpr_write_b32 a39, v243\n"
      "v_accvgpr_read_b32 v243, a40\n"
      "s_nop 0\n"
      "v_mul_f32 v243, v243, v242\n"
      "s_nop 0\n"
      "v_accvgpr_write_b32 a40, v243\n"
      "v_accvgpr_read_b32 v243, a41\n"
      "s_nop 0\n"
      "v_mul_f32 v243, v243, v242\n"
      "s_nop 0\n"
      "v_accvgpr_write_b32 a41, v243\n"
      "v_accvgpr_read_b32 v243, a42\n"
      "s_nop 0\n"
      "v_mul_f32 v243, v243, v242\n"
      "s_nop 0\n"
      "v_accvgpr_write_b32 a42, v243\n"
      "v_accvgpr_read_b32 v243, a43\n"
      "s_nop 0\n"
      "v_mul_f32 v243, v243, v242\n"
      "s_nop 0\n"
      "v_accvgpr_write_b32 a43, v243\n"
      "v_accvgpr_read_b32 v243, a44\n"
      "s_nop 0\n"
      "v_mul_f32 v243, v243, v242\n"
      "s_nop 0\n"
      "v_accvgpr_write_b32 a44, v243\n"
      "v_accvgpr_read_b32 v243, a45\n"
      "s_nop 0\n"
      "v_mul_f32 v243, v243, v242\n"
      "s_nop 0\n"
      "v_accvgpr_write_b32 a45, v243\n"
      "v_accvgpr_read_b32 v243, a46\n"
      "s_nop 0\n"
      "v_mul_f32 v243, v243, v242\n"
      "s_nop 0\n"
      "v_accvgpr_write_b32 a46, v243\n"
      "v_accvgpr_read_b32 v243, a47\n"
      "s_nop 0\n"
      "v_mul_f32 v243, v243, v242\n"
      "s_nop 0\n"
      "v_accvgpr_write_b32 a47, v243\n"
      "v_accvgpr_read_b32 v243, a48\n"
      "s_nop 0\n"
      "v_mul_f32 v243, v243, v242\n"
      "s_nop 0\n"
      "v_accvgpr_write_b32 a48, v243\n"
      "v_accvgpr_read_b32 v243, a49\n"
      "s_nop 0\n"
      "v_mul_f32 v243, v243, v242\n"
      "s_nop 0\n"
      "v_accvgpr_write_b32 a49, v243\n"
      "v_accvgpr_read_b32 v243, a50\n"
      "s_nop 0\n"
      "v_mul_f32 v243, v243, v242\n"
      "s_nop 0\n"
      "v_accvgpr_write_b32 a50, v243\n"
      "v_accvgpr_read_b32 v243, a51\n"
      "s_nop 0\n"
      "v_mul_f32 v243, v243, v242\n"
      "s_nop 0\n"
      "v_accvgpr_write_b32 a51, v243\n"
      "v_accvgpr_read_b32 v243, a52\n"
      "s_nop 0\n"
      "v_mul_f32 v243, v243, v242\n"
      "s_nop 0\n"
      "v_accvgpr_write_b32 a52, v243\n"
      "v_accvgpr_read_b32 v243, a53\n"
      "s_nop 0\n"
      "v_mul_f32 v243, v243, v242\n"
      "s_nop 0\n"
      "v_accvgpr_write_b32 a53, v243\n"
      "v_accvgpr_read_b32 v243, a54\n"
      "s_nop 0\n"
      "v_mul_f32 v243, v243, v242\n"
      "s_nop 0\n"
      "v_accvgpr_write_b32 a54, v243\n"
      "v_accvgpr_read_b32 v243, a55\n"
      "s_nop 0\n"
      "v_mul_f32 v243, v243, v242\n"
      "s_nop 0\n"
      "v_accvgpr_write_b32 a55, v243\n"
      "v_accvgpr_read_b32 v243, a56\n"
      "s_nop 0\n"
      "v_mul_f32 v243, v243, v242\n"
      "s_nop 0\n"
      "v_accvgpr_write_b32 a56, v243\n"
      "v_accvgpr_read_b32 v243, a57\n"
      "s_nop 0\n"
      "v_mul_f32 v243, v243, v242\n"
      "s_nop 0\n"
      "v_accvgpr_write_b32 a57, v243\n"
      "v_accvgpr_read_b32 v243, a58\n"
      "s_nop 0\n"
      "v_mul_f32 v243, v243, v242\n"
      "s_nop 0\n"
      "v_accvgpr_write_b32 a58, v243\n"
      "v_accvgpr_read_b32 v243, a59\n"
      "s_nop 0\n"
      "v_mul_f32 v243, v243, v242\n"
      "s_nop 0\n"
      "v_accvgpr_write_b32 a59, v243\n"
      "v_accvgpr_read_b32 v243, a60\n"
      "s_nop 0\n"
      "v_mul_f32 v243, v243, v242\n"
      "s_nop 0\n"
      "v_accvgpr_write_b32 a60, v243\n"
      "v_accvgpr_read_b32 v243, a61\n"
      "s_nop 0\n"
      "v_mul_f32 v243, v243, v242\n"
      "s_nop 0\n"
      "v_accvgpr_write_b32 a61, v243\n"
      "v_accvgpr_read_b32 v243, a62\n"
      "s_nop 0\n"
      "v_mul_f32 v243, v243, v242\n"
      "s_nop 0\n"
      "v_accvgpr_write_b32 a62, v243\n"
      "v_accvgpr_read_b32 v243, a63\n"
      "s_nop 0\n"
      "v_mul_f32 v243, v243, v242\n"
      "s_nop 0\n"
      "v_accvgpr_write_b32 a63, v243\n"
      "v_max3_f32 v246, v48, v49, v50\n"
      "v_max3_f32 v242, v51, v52, v53\n"
      "v_max3_f32 v246, v246, v54, v55\n"
      "v_max_f32 v246, v246, v242\n"
      "v_mov_b32 v242, v246\n"
      "s_nop 1\n"
      "v_permlane16_swap_b32 v242, v246\n"
      "s_nop 0\n"
      "v_max_f32 v246, v246, v242\n"
      "v_mov_b32 v242, v246\n"
      "s_nop 1\n"
      "v_permlane32_swap_b32 v242, v246\n"
      "s_nop 0\n"
      "v_max_f32 v246, v246, v242\n"
      "v_max_f32 v246, 0, v246\n"
      "v_exp_f32 v242, -v246\n"
      "v_sub_f32 v48, v48, v246\n"
      "v_sub_f32 v49, v49, v246\n"
      "v_sub_f32 v50, v50, v246\n"
      "v_sub_f32 v51, v51, v246\n"
      "v_sub_f32 v52, v52, v246\n"
      "v_sub_f32 v53, v53, v246\n"
      "v_sub_f32 v54, v54, v246\n"
      "v_sub_f32 v55, v55, v246\n"
      "v_sub_f32 v72, v72, v246\n"
      "v_mul_f32 v88, v88, v242\n"
      "v_sub_f32 v73, v73, v246\n"
      "v_mul_f32 v89, v89, v242\n"
      "v_sub_f32 v74, v74, v246\n"
      "v_mul_f32 v90, v90, v242\n"
      "v_sub_f32 v75, v75, v246\n"
      "v_mul_f32 v91, v91, v242\n"
      "v_accvgpr_read_b32 v243, a64\n"
      "s_nop 0\n"
      "v_mul_f32 v243, v243, v242\n"
      "s_nop 0\n"
      "v_accvgpr_write_b32 a64, v243\n"
      "v_accvgpr_read_b32 v243, a65\n"
      "s_nop 0\n"
      "v_mul_f32 v243, v243, v242\n"
      "s_nop 0\n"
      "v_accvgpr_write_b32 a65, v243\n"
      "v_accvgpr_read_b32 v243, a66\n"
      "s_nop 0\n"
      "v_mul_f32 v243, v243, v242\n"
      "s_nop 0\n"
      "v_accvgpr_write_b32 a66, v243\n"
      "v_accvgpr_read_b32 v243, a67\n"
      "s_nop 0\n"
      "v_mul_f32 v243, v243, v242\n"
      "s_nop 0\n"
      "v_accvgpr_write_b32 a67, v243\n"
      "v_accvgpr_read_b32 v243, a68\n"
      "s_nop 0\n"
      "v_mul_f32 v243, v243, v242\n"
      "s_nop 0\n"
      "v_accvgpr_write_b32 a68, v243\n"
      "v_accvgpr_read_b32 v243, a69\n"
      "s_nop 0\n"
      "v_mul_f32 v243, v243, v242\n"
      "s_nop 0\n"
      "v_accvgpr_write_b32 a69, v243\n"
      "v_accvgpr_read_b32 v243, a70\n"
      "s_nop 0\n"
      "v_mul_f32 v243, v243, v242\n"
      "s_nop 0\n"
      "v_accvgpr_write_b32 a70, v243\n"
      "v_accvgpr_read_b32 v243, a71\n"
      "s_nop 0\n"
      "v_mul_f32 v243, v243, v242\n"
      "s_nop 0\n"
      "v_accvgpr_write_b32 a71, v243\n"
      "v_accvgpr_read_b32 v243, a72\n"
      "s_nop 0\n"
      "v_mul_f32 v243, v243, v242\n"
      "s_nop 0\n"
      "v_accvgpr_write_b32 a72, v243\n"
      "v_accvgpr_read_b32 v243, a73\n"
      "s_nop 0\n"
      "v_mul_f32 v243, v243, v242\n"
      "s_nop 0\n"
      "v_accvgpr_write_b32 a73, v243\n"
      "v_accvgpr_read_b32 v243, a74\n"
      "s_nop 0\n"
      "v_mul_f32 v243, v243, v242\n"
      "s_nop 0\n"
      "v_accvgpr_write_b32 a74, v243\n"
      "v_accvgpr_read_b32 v243, a75\n"
      "s_nop 0\n"
      "v_mul_f32 v243, v243, v242\n"
      "s_nop 0\n"
      "v_accvgpr_write_b32 a75, v243\n"
      "v_accvgpr_read_b32 v243, a76\n"
      "s_nop 0\n"
      "v_mul_f32 v243, v243, v242\n"
      "s_nop 0\n"
      "v_accvgpr_write_b32 a76, v243\n"
      "v_accvgpr_read_b32 v243, a77\n"
      "s_nop 0\n"
      "v_mul_f32 v243, v243, v242\n"
      "s_nop 0\n"
      "v_accvgpr_write_b32 a77, v243\n"
      "v_accvgpr_read_b32 v243, a78\n"
      "s_nop 0\n"
      "v_mul_f32 v243, v243, v242\n"
      "s_nop 0\n"
      "v_accvgpr_write_b32 a78, v243\n"
      "v_accvgpr_read_b32 v243, a79\n"
      "s_nop 0\n"
      "v_mul_f32 v243, v243, v242\n"
      "s_nop 0\n"
      "v_accvgpr_write_b32 a79, v243\n"
      "v_accvgpr_read_b32 v243, a80\n"
      "s_nop 0\n"
      "v_mul_f32 v243, v243, v242\n"
      "s_nop 0\n"
      "v_accvgpr_write_b32 a80, v243\n"
      "v_accvgpr_read_b32 v243, a81\n"
      "s_nop 0\n"
      "v_mul_f32 v243, v243, v242\n"
      "s_nop 0\n"
      "v_accvgpr_write_b32 a81, v243\n"
      "v_accvgpr_read_b32 v243, a82\n"
      "s_nop 0\n"
      "v_mul_f32 v243, v243, v242\n"
      "s_nop 0\n"
      "v_accvgpr_write_b32 a82, v243\n"
      "v_accvgpr_read_b32 v243, a83\n"
      "s_nop 0\n"
      "v_mul_f32 v243, v243, v242\n"
      "s_nop 0\n"
      "v_accvgpr_write_b32 a83, v243\n"
      "v_accvgpr_read_b32 v243, a84\n"
      "s_nop 0\n"
      "v_mul_f32 v243, v243, v242\n"
      "s_nop 0\n"
      "v_accvgpr_write_b32 a84, v243\n"
      "v_accvgpr_read_b32 v243, a85\n"
      "s_nop 0\n"
      "v_mul_f32 v243, v243, v242\n"
      "s_nop 0\n"
      "v_accvgpr_write_b32 a85, v243\n"
      "v_accvgpr_read_b32 v243, a86\n"
      "s_nop 0\n"
      "v_mul_f32 v243, v243, v242\n"
      "s_nop 0\n"
      "v_accvgpr_write_b32 a86, v243\n"
      "v_accvgpr_read_b32 v243, a87\n"
      "s_nop 0\n"
      "v_mul_f32 v243, v243, v242\n"
      "s_nop 0\n"
      "v_accvgpr_write_b32 a87, v243\n"
      "v_accvgpr_read_b32 v243, a88\n"
      "s_nop 0\n"
      "v_mul_f32 v243, v243, v242\n"
      "s_nop 0\n"
      "v_accvgpr_write_b32 a88, v243\n"
      "v_accvgpr_read_b32 v243, a89\n"
      "s_nop 0\n"
      "v_mul_f32 v243, v243, v242\n"
      "s_nop 0\n"
      "v_accvgpr_write_b32 a89, v243\n"
      "v_accvgpr_read_b32 v243, a90\n"
      "s_nop 0\n"
      "v_mul_f32 v243, v243, v242\n"
      "s_nop 0\n"
      "v_accvgpr_write_b32 a90, v243\n"
      "v_accvgpr_read_b32 v243, a91\n"
      "s_nop 0\n"
      "v_mul_f32 v243, v243, v242\n"
      "s_nop 0\n"
      "v_accvgpr_write_b32 a91, v243\n"
      "v_accvgpr_read_b32 v243, a92\n"
      "s_nop 0\n"
      "v_mul_f32 v243, v243, v242\n"
      "s_nop 0\n"
      "v_accvgpr_write_b32 a92, v243\n"
      "v_accvgpr_read_b32 v243, a93\n"
      "s_nop 0\n"
      "v_mul_f32 v243, v243, v242\n"
      "s_nop 0\n"
      "v_accvgpr_write_b32 a93, v243\n"
      "v_accvgpr_read_b32 v243, a94\n"
      "s_nop 0\n"
      "v_mul_f32 v243, v243, v242\n"
      "s_nop 0\n"
      "v_accvgpr_write_b32 a94, v243\n"
      "v_accvgpr_read_b32 v243, a95\n"
      "s_nop 0\n"
      "v_mul_f32 v243, v243, v242\n"
      "s_nop 0\n"
      "v_accvgpr_write_b32 a95, v243\n"
      "v_max3_f32 v247, v56, v57, v58\n"
      "v_max3_f32 v242, v59, v60, v61\n"
      "v_max3_f32 v247, v247, v62, v63\n"
      "v_max_f32 v247, v247, v242\n"
      "v_mov_b32 v242, v247\n"
      "s_nop 1\n"
      "v_permlane16_swap_b32 v242, v247\n"
      "s_nop 0\n"
      "v_max_f32 v247, v247, v242\n"
      "v_mov_b32 v242, v247\n"
      "s_nop 1\n"
      "v_permlane32_swap_b32 v242, v247\n"
      "s_nop 0\n"
      "v_max_f32 v247, v247, v242\n"
      "v_max_f32 v247, 0, v247\n"
      "v_exp_f32 v242, -v247\n"
      "v_sub_f32 v56, v56, v247\n"
      "v_sub_f32 v57, v57, v247\n"
      "v_sub_f32 v58, v58, v247\n"
      "v_sub_f32 v59, v59, v247\n"
      "v_sub_f32 v60, v60, v247\n"
      "v_sub_f32 v61, v61, v247\n"
      "v_sub_f32 v62, v62, v247\n"
      "v_sub_f32 v63, v63, v247\n"
      "v_sub_f32 v76, v76, v247\n"
      "v_mul_f32 v92, v92, v242\n"
      "v_sub_f32 v77, v77, v247\n"
      "v_mul_f32 v93, v93, v242\n"
      "v_sub_f32 v78, v78, v247\n"
      "v_mul_f32 v94, v94, v242\n"
      "v_sub_f32 v79, v79, v247\n"
      "v_mul_f32 v95, v95, v242\n"
      "v_accvgpr_read_b32 v243, a96\n"
      "s_nop 0\n"
      "v_mul_f32 v243, v243, v242\n"
      "s_nop 0\n"
      "v_accvgpr_write_b32 a96, v243\n"
      "v_accvgpr_read_b32 v243, a97\n"
      "s_nop 0\n"
      "v_mul_f32 v243, v243, v242\n"
      "s_nop 0\n"
      "v_accvgpr_write_b32 a97, v243\n"
      "v_accvgpr_read_b32 v243, a98\n"
      "s_nop 0\n"
      "v_mul_f32 v243, v243, v242\n"
      "s_nop 0\n"
      "v_accvgpr_write_b32 a98, v243\n"
      "v_accvgpr_read_b32 v243, a99\n"
      "s_nop 0\n"
      "v_mul_f32 v243, v243, v242\n"
      "s_nop 0\n"
      "v_accvgpr_write_b32 a99, v243\n"
      "v_accvgpr_read_b32 v243, a100\n"
      "s_nop 0\n"
      "v_mul_f32 v243, v243, v242\n"
      "s_nop 0\n"
      "v_accvgpr_write_b32 a100, v243\n"
      "v_accvgpr_read_b32 v243, a101\n"
      "s_nop 0\n"
      "v_mul_f32 v243, v243, v242\n"
      "s_nop 0\n"
      "v_accvgpr_write_b32 a101, v243\n"
      "v_accvgpr_read_b32 v243, a102\n"
      "s_nop 0\n"
      "v_mul_f32 v243, v243, v242\n"
      "s_nop 0\n"
      "v_accvgpr_write_b32 a102, v243\n"
      "v_accvgpr_read_b32 v243, a103\n"
      "s_nop 0\n"
      "v_mul_f32 v243, v243, v242\n"
      "s_nop 0\n"
      "v_accvgpr_write_b32 a103, v243\n"
      "v_accvgpr_read_b32 v243, a104\n"
      "s_nop 0\n"
      "v_mul_f32 v243, v243, v242\n"
      "s_nop 0\n"
      "v_accvgpr_write_b32 a104, v243\n"
      "v_accvgpr_read_b32 v243, a105\n"
      "s_nop 0\n"
      "v_mul_f32 v243, v243, v242\n"
      "s_nop 0\n"
      "v_accvgpr_write_b32 a105, v243\n"
      "v_accvgpr_read_b32 v243, a106\n"
      "s_nop 0\n"
      "v_mul_f32 v243, v243, v242\n"
      "s_nop 0\n"
      "v_accvgpr_write_b32 a106, v243\n"
      "v_accvgpr_read_b32 v243, a107\n"
      "s_nop 0\n"
      "v_mul_f32 v243, v243, v242\n"
      "s_nop 0\n"
      "v_accvgpr_write_b32 a107, v243\n"
      "v_accvgpr_read_b32 v243, a108\n"
      "s_nop 0\n"
      "v_mul_f32 v243, v243, v242\n"
      "s_nop 0\n"
      "v_accvgpr_write_b32 a108, v243\n"
      "v_accvgpr_read_b32 v243, a109\n"
      "s_nop 0\n"
      "v_mul_f32 v243, v243, v242\n"
      "s_nop 0\n"
      "v_accvgpr_write_b32 a109, v243\n"
      "v_accvgpr_read_b32 v243, a110\n"
      "s_nop 0\n"
      "v_mul_f32 v243, v243, v242\n"
      "s_nop 0\n"
      "v_accvgpr_write_b32 a110, v243\n"
      "v_accvgpr_read_b32 v243, a111\n"
      "s_nop 0\n"
      "v_mul_f32 v243, v243, v242\n"
      "s_nop 0\n"
      "v_accvgpr_write_b32 a111, v243\n"
      "v_accvgpr_read_b32 v243, a112\n"
      "s_nop 0\n"
      "v_mul_f32 v243, v243, v242\n"
      "s_nop 0\n"
      "v_accvgpr_write_b32 a112, v243\n"
      "v_accvgpr_read_b32 v243, a113\n"
      "s_nop 0\n"
      "v_mul_f32 v243, v243, v242\n"
      "s_nop 0\n"
      "v_accvgpr_write_b32 a113, v243\n"
      "v_accvgpr_read_b32 v243, a114\n"
      "s_nop 0\n"
      "v_mul_f32 v243, v243, v242\n"
      "s_nop 0\n"
      "v_accvgpr_write_b32 a114, v243\n"
      "v_accvgpr_read_b32 v243, a115\n"
      "s_nop 0\n"
      "v_mul_f32 v243, v243, v242\n"
      "s_nop 0\n"
      "v_accvgpr_write_b32 a115, v243\n"
      "v_accvgpr_read_b32 v243, a116\n"
      "s_nop 0\n"
      "v_mul_f32 v243, v243, v242\n"
      "s_nop 0\n"
      "v_accvgpr_write_b32 a116, v243\n"
      "v_accvgpr_read_b32 v243, a117\n"
      "s_nop 0\n"
      "v_mul_f32 v243, v243, v242\n"
      "s_nop 0\n"
      "v_accvgpr_write_b32 a117, v243\n"
      "v_accvgpr_read_b32 v243, a118\n"
      "s_nop 0\n"
      "v_mul_f32 v243, v243, v242\n"
      "s_nop 0\n"
      "v_accvgpr_write_b32 a118, v243\n"
      "v_accvgpr_read_b32 v243, a119\n"
      "s_nop 0\n"
      "v_mul_f32 v243, v243, v242\n"
      "s_nop 0\n"
      "v_accvgpr_write_b32 a119, v243\n"
      "v_accvgpr_read_b32 v243, a120\n"
      "s_nop 0\n"
      "v_mul_f32 v243, v243, v242\n"
      "s_nop 0\n"
      "v_accvgpr_write_b32 a120, v243\n"
      "v_accvgpr_read_b32 v243, a121\n"
      "s_nop 0\n"
      "v_mul_f32 v243, v243, v242\n"
      "s_nop 0\n"
      "v_accvgpr_write_b32 a121, v243\n"
      "v_accvgpr_read_b32 v243, a122\n"
      "s_nop 0\n"
      "v_mul_f32 v243, v243, v242\n"
      "s_nop 0\n"
      "v_accvgpr_write_b32 a122, v243\n"
      "v_accvgpr_read_b32 v243, a123\n"
      "s_nop 0\n"
      "v_mul_f32 v243, v243, v242\n"
      "s_nop 0\n"
      "v_accvgpr_write_b32 a123, v243\n"
      "v_accvgpr_read_b32 v243, a124\n"
      "s_nop 0\n"
      "v_mul_f32 v243, v243, v242\n"
      "s_nop 0\n"
      "v_accvgpr_write_b32 a124, v243\n"
      "v_accvgpr_read_b32 v243, a125\n"
      "s_nop 0\n"
      "v_mul_f32 v243, v243, v242\n"
      "s_nop 0\n"
      "v_accvgpr_write_b32 a125, v243\n"
      "v_accvgpr_read_b32 v243, a126\n"
      "s_nop 0\n"
      "v_mul_f32 v243, v243, v242\n"
      "s_nop 0\n"
      "v_accvgpr_write_b32 a126, v243\n"
      "v_accvgpr_read_b32 v243, a127\n"
      "s_nop 0\n"
      "v_mul_f32 v243, v243, v242\n"
      "s_nop 0\n"
      "v_accvgpr_write_b32 a127, v243\n"
      "v_exp_f32 v232, v32\n"
      "s_nop 0\n"
      "v_exp_f32 v233, v33\n"
      "s_nop 0\n"
      "v_cvt_pk_bf16_f32 v112, v232, v233\n"
      "s_nop 0\n"
      "v_exp_f32 v234, v34\n"
      "s_nop 0\n"
      "v_exp_f32 v235, v35\n"
      "s_nop 0\n"
      "v_cvt_pk_bf16_f32 v113, v234, v235\n"
      "s_nop 0\n"
      "v_exp_f32 v236, v36\n"
      "s_nop 0\n"
      "v_exp_f32 v237, v37\n"
      "s_nop 0\n"
      "v_cvt_pk_bf16_f32 v114, v236, v237\n"
      "s_nop 0\n"
      "v_exp_f32 v238, v38\n"
      "s_nop 0\n"
      "v_exp_f32 v239, v39\n"
      "s_nop 0\n"
      "v_cvt_pk_bf16_f32 v115, v238, v239\n"
      "s_nop 0\n"
      "v_exp_f32 v232, v40\n"
      "s_nop 0\n"
      "v_exp_f32 v233, v41\n"
      "s_nop 0\n"
      "v_cvt_pk_bf16_f32 v116, v232, v233\n"
      "s_nop 0\n"
      "v_exp_f32 v234, v42\n"
      "s_nop 0\n"
      "v_exp_f32 v235, v43\n"
      "s_nop 0\n"
      "v_cvt_pk_bf16_f32 v117, v234, v235\n"
      "s_nop 0\n"
      "v_exp_f32 v236, v44\n"
      "s_nop 0\n"
      "v_exp_f32 v237, v45\n"
      "s_nop 0\n"
      "v_cvt_pk_bf16_f32 v118, v236, v237\n"
      "s_nop 0\n"
      "v_exp_f32 v238, v46\n"
      "s_nop 0\n"
      "v_exp_f32 v239, v47\n"
      "s_nop 0\n"
      "v_cvt_pk_bf16_f32 v119, v238, v239\n"
      "s_nop 0\n"
      "v_exp_f32 v232, v48\n"
      "s_nop 0\n"
      "v_exp_f32 v233, v49\n"
      "s_nop 0\n"
      "v_cvt_pk_bf16_f32 v120, v232, v233\n"
      "s_nop 0\n"
      "v_exp_f32 v234, v50\n"
      "s_nop 0\n"
      "v_exp_f32 v235, v51\n"
      "s_nop 0\n"
      "v_cvt_pk_bf16_f32 v121, v234, v235\n"
      "s_nop 0\n"
      "v_exp_f32 v236, v52\n"
      "s_nop 0\n"
      "v_exp_f32 v237, v53\n"
      "s_nop 0\n"
      "v_cvt_pk_bf16_f32 v122, v236, v237\n"
      "s_nop 0\n"
      "v_exp_f32 v238, v54\n"
      "s_nop 0\n"
      "v_exp_f32 v239, v55\n"
      "s_nop 0\n"
      "v_cvt_pk_bf16_f32 v123, v238, v239\n"
      "s_nop 0\n"
      "v_exp_f32 v232, v56\n"
      "s_nop 0\n"
      "v_exp_f32 v233, v57\n"
      "s_nop 0\n"
      "v_cvt_pk_bf16_f32 v124, v232, v233\n"
      "s_nop 0\n"
      "v_exp_f32 v234, v58\n"
      "s_nop 0\n"
      "v_exp_f32 v235, v59\n"
      "s_nop 0\n"
      "v_cvt_pk_bf16_f32 v125, v234, v235\n"
      "s_nop 0\n"
      "v_exp_f32 v236, v60\n"
      "s_nop 0\n"
      "v_exp_f32 v237, v61\n"
      "s_nop 0\n"
      "v_cvt_pk_bf16_f32 v126, v236, v237\n"
      "s_nop 0\n"
      "v_exp_f32 v238, v62\n"
      "s_nop 0\n"
      "v_exp_f32 v239, v63\n"
      "s_nop 0\n"
      "v_cvt_pk_bf16_f32 v127, v238, v239\n"
      "s_nop 0\n"
      "s_nop 1\n"
      "v_mfma_f32_16x16x32_bf16 v[212:215], v[228:231], v[112:115], v[80:83]\n"
      "v_mfma_f32_16x16x32_bf16 v[216:219], v[228:231], v[116:119], v[84:87]\n"
      "v_mfma_f32_16x16x32_bf16 v[220:223], v[228:231], v[120:123], v[88:91]\n"
      "v_mfma_f32_16x16x32_bf16 v[224:227], v[228:231], v[124:127], v[92:95]\n"
      "s_cmp_lg_u32 s75, 0\n"
      "s_cbranch_scc1 SLX1_%=\n"
      "v_sub_f32 v0, v0, v244\n"
      "v_sub_f32 v1, v1, v244\n"
      "v_sub_f32 v2, v2, v244\n"
      "v_sub_f32 v3, v3, v244\n"
      "v_sub_f32 v4, v4, v244\n"
      "v_sub_f32 v5, v5, v244\n"
      "v_sub_f32 v6, v6, v244\n"
      "v_sub_f32 v7, v7, v244\n"
      "v_sub_f32 v8, v8, v245\n"
      "v_sub_f32 v9, v9, v245\n"
      "v_sub_f32 v10, v10, v245\n"
      "v_sub_f32 v11, v11, v245\n"
      "v_sub_f32 v12, v12, v245\n"
      "v_sub_f32 v13, v13, v245\n"
      "v_sub_f32 v14, v14, v245\n"
      "v_sub_f32 v15, v15, v245\n"
      "v_sub_f32 v16, v16, v246\n"
      "v_sub_f32 v17, v17, v246\n"
      "v_sub_f32 v18, v18, v246\n"
      "v_sub_f32 v19, v19, v246\n"
      "v_sub_f32 v20, v20, v246\n"
      "v_sub_f32 v21, v21, v246\n"
      "v_sub_f32 v22, v22, v246\n"
      "v_sub_f32 v23, v23, v246\n"
      "v_sub_f32 v24, v24, v247\n"
      "v_sub_f32 v25, v25, v247\n"
      "v_sub_f32 v26, v26, v247\n"
      "v_sub_f32 v27, v27, v247\n"
      "v_sub_f32 v28, v28, v247\n"
      "v_sub_f32 v29, v29, v247\n"
      "v_sub_f32 v30, v30, v247\n"
      "v_sub_f32 v31, v31, v247\n"
      "v_exp_f32 v232, v0\n"
      "s_nop 0\n"
      "v_exp_f32 v233, v1\n"
      "s_nop 0\n"
      "v_cvt_pk_bf16_f32 v96, v232, v233\n"
      "s_nop 0\n"
      "v_exp_f32 v234, v2\n"
      "s_nop 0\n"
      "v_exp_f32 v235, v3\n"
      "s_nop 0\n"
      "v_cvt_pk_bf16_f32 v97, v234, v235\n"
      "s_nop 0\n"
      "v_exp_f32 v236, v4\n"
      "s_nop 0\n"
      "v_exp_f32 v237, v5\n"
      "s_nop 0\n"
      "v_cvt_pk_bf16_f32 v98, v236, v237\n"
      "s_nop 0\n"
      "v_exp_f32 v238, v6\n"
      "s_nop 0\n"
      "v_exp_f32 v239, v7\n"
      "s_nop 0\n"
      "v_cvt_pk_bf16_f32 v99, v238, v239\n"
      "s_nop 0\n"
      "v_exp_f32 v232, v8\n"
      "s_nop 0\n"
      "v_exp_f32 v233, v9\n"
      "s_nop 0\n"
      "v_cvt_pk_bf16_f32 v100, v232, v233\n"
      "s_nop 0\n"
      "SLX1_%=:\n"
      "s_nop 7\n"
      "s_setpc_b64 s[76:77]\n"
      "END_%=:\n"
      "s_waitcnt vmcnt(0)\n"
      :: "s"(wave), "s"(pk), "s"(pv), "s"(px), "s"(pw), "s"(po), "s"(ps), "s"(pd), "s"(qo), "s"(qw), "s"(pc), "s"(pn), "s"(pm), "s"(pr), "s"(p0), "s"(p1), "s"(m1) : CLOB);
}

extern "C" void kernel_launch(void* const* d_in, const int* in_sizes, int n_in, void* d_out, int out_size, void* d_ws, size_t ws_size,
                              hipStream_t stream) {
  char* ows = (char*)d_ws + ((ws_size - (size_t)48 * 1048576u) & ~(size_t)4095);
  char* wws = ows + 36u * 1048576u;
  (void)hipFuncSetAttribute((const void*)k_attn, hipFuncAttributeMaxDynamicSharedMemorySize, 163840);
  hipLaunchKernelGGL(k_attn, dim3(256), dim3(256), 163840, stream, (const char*)d_in[0], (const char*)d_in[1], (const char*)d_in[2],
                     (const char*)d_in[3], (const char*)d_in[4], (const char*)d_in[5], (const char*)d_in[6], (const char*)d_in[7],
                     (const char*)d_in[8], (const char*)d_in[9], ows, wws, (char*)d_out, (char*)d_ws);
}
```
